```python
import torch

def _softmax(x, axis=-1):
    return torch.softmax(x, dim=axis)
def _gelu(x):
    return torch.nn.functional.gelu(x, approximate="tanh")
def _silu(x):
    return torch.nn.functional.silu(x)
def _sigmoid(x):
    return torch.sigmoid(x)
def _layernorm(x, axis=-1, eps=1e-5):
    m = x.mean(dim=axis, keepdim=True)
    v = x.var(dim=axis, keepdim=True, unbiased=False)
    return (x - m) / torch.sqrt(v + eps)
def _scatter_add(tgt, idx, src, axis=0):
    return tgt.clone().index_add_(axis, idx, src)
def _segment_sum(src, seg, n):
    out = torch.zeros((n,) + src.shape[1:], dtype=src.dtype)
    return out.index_add_(0, seg, src)
def _pad(x, pw):
    flat = tuple(v for pair in reversed(pw) for v in pair)
    return torch.nn.functional.pad(x, flat)

kernel_name = 'concat_heads__mha_block_cached__gqa_block_cached__near_ridge'

def setup_inputs():

    _w = (lambda a: (lambda t: t.to(torch.bfloat16) if t.is_floating_point() else t)(torch.as_tensor(a)))
    def _A_setup():
        B, Sq, D = 8, 144, 576
        H, Sk, Dh = 32, 512, 128
        return {
            "x": _w((lambda *s: torch.randn(*s, dtype=torch.bfloat16) * 0.8)(B, Sq, D)),
            "k_cache": _w((lambda *s: torch.randn(*s, dtype=torch.bfloat16) * 0.8)(B, H, Sk, Dh)),
            "v_cache": _w((lambda *s: torch.randn(*s, dtype=torch.bfloat16) * 0.05)(B, H, Sk, Dh)),
            "wq": _w((lambda *s: torch.randn(*s, dtype=torch.bfloat16) * 0.8)(D, H * Dh) * (D ** -0.5)),
            "wo": _w((lambda *s: torch.randn(*s, dtype=torch.bfloat16) * 0.05)(H * Dh, D) * ((H * Dh) ** -0.5)),
        }
    _A_in = _A_setup()

    def _B_setup():
        B, Sq, D = 8, 144, 576
        Hq, Hkv, Sk, Dh = 32, 32, 512, 128
        assert Hq % Hkv == 0, (Hq, Hkv)
        return {
            "x": _w((lambda *s: torch.randn(*s, dtype=torch.bfloat16) * 0.8)(B, Sq, D)),
            "k_cache": _w((lambda *s: torch.randn(*s, dtype=torch.bfloat16) * 0.8)(B, Hkv, Sk, Dh)),
            "v_cache": _w((lambda *s: torch.randn(*s, dtype=torch.bfloat16) * 0.05)(B, Hkv, Sk, Dh)),
            "wq": _w((lambda *s: torch.randn(*s, dtype=torch.bfloat16) * 0.8)(D, Hq * Dh) * (D ** -0.5)),
            "wo": _w((lambda *s: torch.randn(*s, dtype=torch.bfloat16) * 0.05)(Hq * Dh, D) * ((Hq * Dh) ** -0.5)),
        }
    _B_in = _B_setup()

    return {
        "A_x": _A_in["x"],
        "A_k_cache": _A_in["k_cache"],
        "A_v_cache": _A_in["v_cache"],
        "A_wq": _A_in["wq"],
        "A_wo": _A_in["wo"],
        "B_x": _B_in["x"],
        "B_k_cache": _B_in["k_cache"],
        "B_v_cache": _B_in["v_cache"],
        "B_wq": _B_in["wq"],
        "B_wo": _B_in["wo"],
    }

def reference(A_x, A_k_cache, A_v_cache, A_wq, A_wo, B_x, B_k_cache, B_v_cache, B_wq, B_wo):
    def _A_ref(x, k_cache, v_cache, wq, wo):
        B, Sq, D = x.shape
        H, Dh = 32, 128
        q = (lambda x, *d: x.permute(*d))((x @ wq).reshape(B, Sq, H, Dh), 0, 2, 1, 3)
        scores = torch.einsum("bhqd,bhkd->bhqk", q, k_cache) * (Dh ** -0.5)
        p = _softmax(scores, axis=-1)
        o = torch.einsum("bhqk,bhkd->bhqd", p, v_cache)
        return (lambda x, *d: x.permute(*d))(o, 0, 2, 1, 3).reshape(B, Sq, H * Dh) @ wo

    def _B_ref(x, k_cache, v_cache, wq, wo):
        B, Sq, D = x.shape
        Hq, Dh = 32, 128
        Hkv = k_cache.shape[1]
        rep = Hq // Hkv
        q = (lambda x, *d: x.permute(*d))((x @ wq).reshape(B, Sq, Hq, Dh), 0, 2, 1, 3)
        k = (lambda x, n, axis: torch.repeat_interleave(x, n, dim=axis))(k_cache, rep, axis=1)
        v = (lambda x, n, axis: torch.repeat_interleave(x, n, dim=axis))(v_cache, rep, axis=1)
        scores = torch.einsum("bhqd,bhkd->bhqk", q, k) * (Dh ** -0.5)
        p = _softmax(scores, axis=-1)
        o = torch.einsum("bhqk,bhkd->bhqd", p, v)
        return (lambda x, *d: x.permute(*d))(o, 0, 2, 1, 3).reshape(B, Sq, Hq * Dh) @ wo

    a = _A_ref(A_x, A_k_cache, A_v_cache, A_wq, A_wo)
    b = _B_ref(B_x, B_k_cache, B_v_cache, B_wq, B_wo)
    return torch.concatenate([a, b], axis=-1)
```

```cpp
#include <hip/hip_runtime.h>
#include <stdint.h>

#define MF "v_mfma_f32_16x16x32_bf16 "
#define DT "ds_read_b64_tr_b16 "
#define CV "v_cvt_pk_bf16_f32 "
#define BL "buffer_load_dwordx4 "
#define DR "ds_read_b128 "
#define EX "v_exp_f32 "
#define WT "s_waitcnt "
#define PM "v_pk_mul_f32 "

struct KArgs { const void* in[10]; void* out; void* ws; };

__global__ void __launch_bounds__(256, 1) fused_block_kernel(KArgs args) {
    __shared__ __attribute__((aligned(16))) char smem[163840];
    uint64_t ka = (uint64_t)__builtin_amdgcn_kernarg_segment_ptr();
    unsigned tid = threadIdx.x;
    asm volatile(
"v_lshrrev_b32 v2,6,%0\n"
"v_and_b32 v0,63,%0\n"
"s_mov_b64 s[98:99],0\n"
"v_readfirstlane_b32 s36,v2\n"
"LR1_%=:\n"
"s_and_b32 s66,%1,7\n"
"s_lshr_b32 s37,s66,2\n"
"s_and_b32 s38,s66,3\n"
"s_mul_i32 s67,s37,40\n"
"s_add_u32 s60,%2,s67\n"
"s_addc_u32 s61,%3,0\n"
"s_load_dwordx8 s[68:75],s[60:61],0\n"
"s_load_dwordx2 s[24:25],s[60:61],32\n"
"s_mov_b32 s62,%2\n"
"s_mov_b32 s63,%3\n"
"s_load_dwordx4 s[76:79],s[62:63],80\n"
"s_lshr_b32 s39,%1,5\n"
"s_bfe_u32 s40,%1,0x20003\n"
"s_lshl_b32 s41,s37,3\n"
"s_add_u32 s41,s41,s39\n"
"s_lshl_b32 s41,s41,2\n"
"s_add_u32 s41,s41,s38\n"
"s_lshl_b32 s41,s41,2\n"
"s_add_u32 s41,s41,s40\n"
"v_lshlrev_b32 v1,4,v0\n"
"s_lshl_b32 s52,s38,2\n"
"s_add_u32 s52,s52,s40\n"
"v_lshrrev_b32 v2,5,v0\n"
"v_lshlrev_b32 v2,15,v2\n"
"v_and_b32 v3,0x1f0,v1\n"
"v_add_u32 v86,v2,v3\n"
"s_lshl_b32 s66,s36,16\n"
"s_lshl_b32 s67,s52,9\n"
"s_add_u32 s66,s66,s67\n"
"v_add_u32 v86,s66,v86\n"
"s_mul_i32 s66,s39,144\n"
"s_lshl_b32 s67,s36,5\n"
"s_add_u32 s67,s67,s66\n"
"v_lshrrev_b32 v2,3,v0\n"
"v_add_u32 v2,s67,v2\n"
"v_mul_u32_u24 v87,1152,v2\n"
"v_lshrrev_b32 v2,4,v0\n"
"v_xor_b32 v3,v2,v0\n"
"v_and_b32 v3,7,v3\n"
"v_lshl_add_u32 v87,v3,4,v87\n"
"v_xor_b32 v90,64,v87\n"
"v_add_u32 v90,0x2000,v90\n"
"s_lshl_b32 s67,s36,2\n"
"s_add_u32 s67,s67,s66\n"
"s_add_u32 s67,s67,128\n"
"v_lshrrev_b32 v2,5,v0\n"
"v_add_u32 v2,s67,v2\n"
"v_mul_u32_u24 v88,1152,v2\n"
"s_lshl_b32 s67,s36,3\n"
"v_xor_b32 v2,s67,v0\n"
"v_and_b32 v2,31,v2\n"
"v_lshl_add_u32 v88,v2,2,v88\n"
"v_xor_b32 v91,16,v88\n"
"v_add_u32 v91,2304,v91\n"
"v_and_b32 v2,3,v0\n"
"s_movk_i32 s42,7136\n"
"s_movk_i32 s43,14272\n"
"s_movk_i32 s44,21408\n"
"s_movk_i32 s45,17408\n"
"s_mul_i32 s46,s36,4224\n"
"s_add_u32 s46,s46,67584\n"
"s_mul_i32 s47,s36,4608\n"
"s_mul_i32 s48,s36,2112\n"
"s_and_b32 s66,s41,0xfc\n"
"v_or_b32 v26,s66,v2\n"
"v_lshlrev_b32 v26,9,v26\n"
"v_add_u32 v26,33554432,v26\n"
"s_and_b32 s66,s41,0xf3\n"
"v_lshl_or_b32 v27,v2,2,s66\n"
"v_lshlrev_b32 v27,9,v27\n"
"v_add_u32 v27,33554432,v27\n"
"s_mov_b32 s11,0x20000\n"
"s_mov_b32 s15,0x20000\n"
"s_mov_b32 s19,0x20000\n"
"s_mov_b32 s23,0x20000\n"
"s_mov_b32 s27,0x20000\n"
"s_mov_b32 s31,0x20000\n"
"s_mov_b32 s35,0x20000\n"
"s_mov_b32 s10,1327104\n"
"s_mov_b32 s14,33555456\n"
"s_mov_b32 s18,33555456\n"
"s_mov_b32 s22,4718592\n"
"s_mov_b32 s26,4722688\n"
"s_mov_b32 s30,0x2200000\n"
"s_mov_b32 s34,2654208\n"
"s_mov_b32 s50,0\n"
WT "lgkmcnt(0)\n"
"s_mov_b64 s[8:9],s[68:69]\n"
"s_mov_b64 s[20:21],s[74:75]\n"
"s_mov_b64 s[28:29],s[78:79]\n"
"s_mov_b64 s[32:33],s[76:77]\n"
"s_sub_u32 s12,s70,1024\n"
"s_subb_u32 s13,s71,0\n"
"s_sub_u32 s16,s72,1024\n"
"s_subb_u32 s17,s73,0\n"
"s_sub_u32 s24,s24,4096\n"
"s_subb_u32 s25,s25,0\n"
"s_cmp_gt_u32 s98,0\n"
"s_cbranch_scc1 Lf2_%=\n"
"buffer_load_dword v28,v26,s[28:31],0 offen sc1\n"
"buffer_load_dword v29,v26,s[28:31],0 offen offset:128 sc1\n"
"buffer_load_dword v30,v27,s[28:31],0 offen offset:256 sc1\n"
"Lf2_%=:\n"
"s_add_u32 m0,s46,33792\n"
"v_add_u32 v89,0,v86\n"
BL "v89,s[20:23],0 offen offset:0 lds\n"
BL "v89,s[20:23],s42 offen offset:1056 lds\n"
BL "v89,s[20:23],s43 offen offset:2112 lds\n"
BL "v89,s[20:23],s44 offen offset:3168 lds\n"
"s_add_u32 m0,s47,31232\n"
"s_movk_i32 s67,0\n"
BL "v87,s[8:11],s67 offen offset:0 lds\n"
BL "v90,s[8:11],s67 offen offset:1024 lds\n"
"s_add_u32 m0,s47,30720\n"
"s_movk_i32 s67,0\n"
"buffer_load_dword v88,s[8:11],s67 offen lds\n"
"s_add_u32 m0,s47,31232\n"
"s_movk_i32 s67,16384\n"
BL "v87,s[8:11],s67 offen offset:2048 lds\n"
BL "v90,s[8:11],s67 offen offset:3072 lds\n"
"s_add_u32 m0,s47,30976\n"
"s_movk_i32 s67,0\n"
"buffer_load_dword v91,s[8:11],s67 offen lds\n"
"s_lshl_b32 s66,s36,5\n"
"s_lshl_b32 s67,s39,2\n"
"s_add_u32 s66,s66,s67\n"
"s_lshl_b32 s66,s66,13\n"
"s_lshl_b32 s67,s52,9\n"
"s_add_u32 s66,s66,s67\n"
"v_lshrrev_b32 v3,2,v0\n"
"v_lshlrev_b32 v3,13,v3\n"
"v_lshl_add_u32 v3,v2,7,v3\n"
"v_add_u32 v93,s66,v3\n"
"v_cmp_gt_u32 vcc,16,v0\n"
"v_bfrev_b32 v3,-2\n"
"s_mul_i32 s66,s39,144\n"
"s_mul_i32 s67,s40,36\n"
"v_cndmask_b32 v93,v3,v93,vcc\n"
"s_add_u32 s66,s66,s67\n"
"s_and_b32 s67,s36,1\n"
"s_mul_i32 s67,s67,18\n"
"s_add_u32 s66,s66,s67\n"
"v_add_u32 v94,s66,v0\n"
"v_mul_u32_u24 v94,1152,v94\n"
"v_cmp_gt_u32 vcc,18,v0\n"
"s_lshr_b32 s67,s36,1\n"
"s_lshl_b32 s67,s67,7\n"
"v_add_u32 v94,s67,v94\n"
"v_cndmask_b32 v94,v3,v94,vcc\n"
"s_add_u32 m0,s46,50688\n"
"v_add_u32 v89,262144,v86\n"
BL "v89,s[20:23],0 offen offset:0 lds\n"
BL "v89,s[20:23],s42 offen offset:1056 lds\n"
BL "v89,s[20:23],s43 offen offset:2112 lds\n"
BL "v89,s[20:23],s44 offen offset:3168 lds\n"
"s_add_u32 m0,s47,49664\n"
"s_movk_i32 s67,128\n"
BL "v87,s[8:11],s67 offen offset:0 lds\n"
BL "v90,s[8:11],s67 offen offset:1024 lds\n"
"s_add_u32 m0,s47,49152\n"
"s_movk_i32 s67,128\n"
"buffer_load_dword v88,s[8:11],s67 offen lds\n"
"s_mov_b32 s77,524288\n"
"buffer_load_dword v95,v93,s[20:23],s77 offen\n"
"buffer_load_dword v95,v94,s[8:11],0 offen offset:128\n"
"s_add_u32 m0,s46,0\n"
"v_add_u32 v89,524288,v86\n"
BL "v89,s[20:23],0 offen offset:0 lds\n"
BL "v89,s[20:23],s42 offen offset:1056 lds\n"
BL "v89,s[20:23],s43 offen offset:2112 lds\n"
BL "v89,s[20:23],s44 offen offset:3168 lds\n"
"s_add_u32 m0,s47,49664\n"
"s_movk_i32 s67,16512\n"
BL "v87,s[8:11],s67 offen offset:2048 lds\n"
BL "v90,s[8:11],s67 offen offset:3072 lds\n"
"s_add_u32 m0,s47,49408\n"
"s_movk_i32 s67,128\n"
"buffer_load_dword v91,s[8:11],s67 offen lds\n"
"s_getpc_b64 s[60:61]\n"
"s_bfe_u32 s66,%1,0x20003\n"
"s_lshl_b32 s66,s66,2\n"
"s_add_u32 s66,s66,s36\n"
"s_lshl_b32 s66,s66,13\n"
"v_lshl_add_u32 v2,v0,7,s66\n"
"v_min_u32 v2,111616,v2\n"
"global_load_dword v92,v2,s[60:61]\n"
"v_lshrrev_b32 v2,2,v0\n"
"v_mul_u32_u24 v80,1056,v2\n"
"v_and_b32 v3,3,v0\n"
"v_lshl_add_u32 v80,v3,3,v80\n"
"v_mul_u32_u24 v25,288,v2\n"
"v_lshl_add_u32 v25,v3,3,v25\n"
"v_and_b32 v2,7,v2\n"
"v_mul_u32_u24 v5,1056,v2\n"
"v_lshl_add_u32 v5,v3,3,v5\n"
"v_lshrrev_b32 v2,5,v0\n"
"v_lshl_add_u32 v5,v2,8,v5\n"
"v_add_u32 v5,8448,v5\n"
"s_add_u32 s67,s36,s38\n"
"s_add_u32 s66,s67,0\n"
"s_and_b32 s66,s66,3\n"
"s_lshl_b32 s66,s66,6\n"
"s_add_u32 s66,s66,84480\n"
"v_add_u32 v21,s66,v25\n"
"s_add_u32 s66,s67,1\n"
"s_and_b32 s66,s66,3\n"
"s_lshl_b32 s66,s66,6\n"
"s_add_u32 s66,s66,84480\n"
"v_add_u32 v22,s66,v25\n"
"s_add_u32 s66,s67,2\n"
"s_and_b32 s66,s66,3\n"
"s_lshl_b32 s66,s66,6\n"
"s_add_u32 s66,s66,84480\n"
"v_add_u32 v23,s66,v25\n"
"s_add_u32 s66,s67,3\n"
"s_and_b32 s66,s66,3\n"
"s_lshl_b32 s66,s66,6\n"
"s_add_u32 s66,s66,84480\n"
"v_add_u32 v24,s66,v25\n"
"v_add_u32 v25,84736,v25\n"
"s_add_u32 s66,s36,3\n"
"s_and_b32 s66,s66,3\n"
"s_lshl_b32 s66,s66,7\n"
"s_add_u32 s66,s66,67584\n"
"v_add_u32 v83,s66,v80\n"
"s_add_u32 s66,s36,2\n"
"s_and_b32 s66,s66,3\n"
"s_lshl_b32 s66,s66,7\n"
"s_add_u32 s66,s66,67584\n"
"v_add_u32 v82,s66,v80\n"
"s_add_u32 s66,s36,1\n"
"s_and_b32 s66,s66,3\n"
"s_lshl_b32 s66,s66,7\n"
"s_add_u32 s66,s66,67584\n"
"v_add_u32 v81,s66,v80\n"
"s_add_u32 s66,s36,0\n"
"s_and_b32 s66,s66,3\n"
"s_lshl_b32 s66,s66,7\n"
"s_add_u32 s66,s66,67584\n"
"v_add_u32 v80,s66,v80\n"
"v_bfe_u32 v3,v0,1,3\n"
"v_lshrrev_b32 v4,4,v0\n"
"v_xor_b32 v3,v3,v4\n"
"v_lshlrev_b32 v3,4,v3\n"
"v_bfe_u32 v2,v0,2,2\n"
"v_mul_u32_u24 v99,4608,v2\n"
"v_add_u32 v99,v99,v3\n"
"v_and_b32 v2,3,v0\n"
"v_lshl_add_u32 v99,v2,7,v99\n"
"v_add_u32 v85,135168,v99\n"
"v_and_b32 v2,15,v0\n"
"v_lshl_add_u32 v98,v2,7,v3\n"
"v_add_u32 v98,s47,v98\n"
"v_add_u32 v84,135168,v98\n"
"v_xor_b32 v100,64,v84\n"
"v_xor_b32 v101,64,v85\n"
"v_xor_b32 v102,64,v98\n"
"v_xor_b32 v103,64,v99\n"
"v_and_b32 v3,1,v0\n"
"v_xor_b32 v4,v4,v3\n"
"v_lshlrev_b32 v4,4,v4\n"
"v_lshl_add_u32 v4,v3,8,v4\n"
"v_lshrrev_b32 v2,1,v2\n"
"v_mul_u32_u24 v2,1056,v2\n"
"v_add_u32 v4,v4,v2\n"
"s_lshl_b32 s66,s39,4\n"
"s_add_u32 s66,s66,s52\n"
"s_lshl_b32 s66,s66,18\n"
"s_add_u32 s66,s66,1024\n"
"v_bfe_u32 v2,v0,4,1\n"
"v_xor_b32 v3,v2,v0\n"
"v_and_b32 v8,12,v3\n"
"v_and_b32 v9,1,v3\n"
"v_lshl_or_b32 v8,v9,1,v8\n"
"v_bfe_u32 v9,v3,1,1\n"
"v_or_b32 v8,v8,v9\n"
"v_lshlrev_b32 v8,4,v8\n"
"v_lshl_add_u32 v8,v2,8,v8\n"
"v_lshrrev_b32 v2,5,v0\n"
"v_lshl_add_u32 v8,v2,12,v8\n"
"s_lshl_b32 s67,s36,10\n"
"s_add_u32 s67,s67,s66\n"
"v_add_u32 v8,s67,v8\n"
"v_subrev_u32 v9,544,v8\n"
"v_lshrrev_b32 v2,4,v0\n"
"v_and_b32 v10,0xf0,v1\n"
"v_lshl_add_u32 v10,v2,11,v10\n"
"s_lshl_b32 s67,s36,9\n"
"s_add_u32 s67,s67,s66\n"
"v_add_u32 v10,s67,v10\n"
"v_subrev_u32 v11,800,v10\n"
"s_mul_i32 s66,s36,36864\n"
"s_mul_i32 s67,s40,288\n"
"s_add_u32 s66,s66,s67\n"
"v_add_u32 v2,0,v0\n"
"v_mul_u32_u24 v3,3641,v2\n"
"v_lshrrev_b32 v3,16,v3\n"
"v_mul_u32_u24 v3,864,v3\n"
"v_lshl_add_u32 v2,v2,4,v3\n"
"s_add_u32 s67,s66,4096\n"
"v_add_u32 v12,s67,v2\n"
"v_add_u32 v2,64,v0\n"
"v_mul_u32_u24 v3,3641,v2\n"
"v_lshrrev_b32 v3,16,v3\n"
"v_mul_u32_u24 v3,864,v3\n"
"v_lshl_add_u32 v2,v2,4,v3\n"
"s_add_u32 s67,s66,3072\n"
"v_add_u32 v13,s67,v2\n"
"v_add_u32 v2,128,v0\n"
"v_mul_u32_u24 v3,3641,v2\n"
"v_lshrrev_b32 v3,16,v3\n"
"v_mul_u32_u24 v3,864,v3\n"
"v_lshl_add_u32 v2,v2,4,v3\n"
"s_add_u32 s67,s66,2048\n"
"v_add_u32 v14,s67,v2\n"
"v_add_u32 v2,192,v0\n"
"v_mul_u32_u24 v3,3641,v2\n"
"v_lshrrev_b32 v3,16,v3\n"
"v_mul_u32_u24 v3,864,v3\n"
"v_lshl_add_u32 v2,v2,4,v3\n"
"s_add_u32 s67,s66,4096\n"
"v_add_u32 v15,s67,v2\n"
"v_add_u32 v2,256,v0\n"
"v_mul_u32_u24 v3,3641,v2\n"
"v_lshrrev_b32 v3,16,v3\n"
"v_mul_u32_u24 v3,864,v3\n"
"v_lshl_add_u32 v2,v2,4,v3\n"
"s_add_u32 s67,s66,3072\n"
"v_add_u32 v16,s67,v2\n"
"v_add_u32 v2,320,v0\n"
"v_mul_u32_u24 v3,3641,v2\n"
"v_lshrrev_b32 v3,16,v3\n"
"v_mul_u32_u24 v3,864,v3\n"
"v_lshl_add_u32 v2,v2,4,v3\n"
"s_add_u32 s67,s66,2048\n"
"v_add_u32 v17,s67,v2\n"
"v_add_u32 v2,384,v0\n"
"v_mul_u32_u24 v3,3641,v2\n"
"v_lshrrev_b32 v3,16,v3\n"
"v_mul_u32_u24 v3,864,v3\n"
"v_lshl_add_u32 v2,v2,4,v3\n"
"s_add_u32 s67,s66,4096\n"
"v_add_u32 v18,s67,v2\n"
"v_add_u32 v2,448,v0\n"
"v_mul_u32_u24 v3,3641,v2\n"
"v_lshrrev_b32 v3,16,v3\n"
"v_mul_u32_u24 v3,864,v3\n"
"v_lshl_add_u32 v2,v2,4,v3\n"
"s_add_u32 s67,s66,3072\n"
"v_add_u32 v19,s67,v2\n"
"v_add_u32 v2,512,v0\n"
"v_mul_u32_u24 v3,3641,v2\n"
"v_lshrrev_b32 v3,16,v3\n"
"v_mul_u32_u24 v3,864,v3\n"
"v_lshl_add_u32 v2,v2,4,v3\n"
"s_add_u32 s67,s66,2048\n"
"v_add_u32 v20,s67,v2\n"
"s_add_u32 s66,s40,0\n"
"s_and_b32 s66,s66,3\n"
"s_lshl_b32 s67,s38,2\n"
"s_add_u32 s67,s67,s66\n"
"s_mul_i32 s68,s67,294912\n"
"s_and_b32 s67,s41,0xfc\n"
"s_or_b32 s67,s67,s66\n"
"s_mul_i32 s56,s67,73728\n"
"s_sub_u32 s66,s38,0\n"
"s_and_b32 s66,s66,3\n"
"s_lshl_b32 s66,s66,2\n"
"s_and_b32 s67,s41,0xf3\n"
"s_or_b32 s67,s67,s66\n"
"s_mul_i32 s67,s67,45056\n"
"s_add_u32 s72,s67,20971520\n"
"s_add_u32 s66,s40,1\n"
"s_and_b32 s66,s66,3\n"
"s_lshl_b32 s67,s38,2\n"
"s_add_u32 s67,s67,s66\n"
"s_mul_i32 s69,s67,294912\n"
"s_and_b32 s67,s41,0xfc\n"
"s_or_b32 s67,s67,s66\n"
"s_mul_i32 s57,s67,73728\n"
"s_sub_u32 s66,s38,1\n"
"s_and_b32 s66,s66,3\n"
"s_lshl_b32 s66,s66,2\n"
"s_and_b32 s67,s41,0xf3\n"
"s_or_b32 s67,s67,s66\n"
"s_mul_i32 s67,s67,45056\n"
"s_add_u32 s73,s67,20971520\n"
"s_add_u32 s66,s40,2\n"
"s_and_b32 s66,s66,3\n"
"s_lshl_b32 s67,s38,2\n"
"s_add_u32 s67,s67,s66\n"
"s_mul_i32 s70,s67,294912\n"
"s_and_b32 s67,s41,0xfc\n"
"s_or_b32 s67,s67,s66\n"
"s_mul_i32 s58,s67,73728\n"
"s_sub_u32 s66,s38,2\n"
"s_and_b32 s66,s66,3\n"
"s_lshl_b32 s66,s66,2\n"
"s_and_b32 s67,s41,0xf3\n"
"s_or_b32 s67,s67,s66\n"
"s_mul_i32 s67,s67,45056\n"
"s_add_u32 s74,s67,20971520\n"
"s_add_u32 s66,s40,3\n"
"s_and_b32 s66,s66,3\n"
"s_lshl_b32 s67,s38,2\n"
"s_add_u32 s67,s67,s66\n"
"s_mul_i32 s71,s67,294912\n"
"s_and_b32 s67,s41,0xfc\n"
"s_or_b32 s67,s67,s66\n"
"s_mul_i32 s59,s67,73728\n"
"s_sub_u32 s66,s38,3\n"
"s_and_b32 s66,s66,3\n"
"s_lshl_b32 s66,s66,2\n"
"s_and_b32 s67,s41,0xf3\n"
"s_or_b32 s67,s67,s66\n"
"s_mul_i32 s67,s67,45056\n"
"s_add_u32 s75,s67,20971520\n"
"s_mov_b32 s62,0x11111111\n"
"s_lshl_b32 s64,s62,s38\n"
"s_lshl_b32 s62,s62,s40\n"
"s_mov_b32 s63,s62\n"
"s_mov_b32 s65,s64\n"
"s_mul_i32 s76,s36,9216\n"
"s_mov_b32 s53,0x41000000\n"
"s_mov_b32 s54,0x3e0293ee\n"
"s_mov_b32 s55,s54\n"
"s_mov_b32 s49,16896\n"
"s_mov_b32 s51,0\n"
"v_mov_b32 v2,0x3f803f80\n"
"s_lshr_b32 s41,%1,4\n"
"s_xor_b32 s41,s41,%1\n"
"s_and_b32 s41,s41,15\n"
"s_lshl_b32 s41,s41,13\n"
"s_mov_b32 s78,s41\n"
"v_accvgpr_write_b32 a48,v2\n"
"v_accvgpr_write_b32 a49,v2\n"
"v_accvgpr_write_b32 a50,v2\n"
"v_accvgpr_write_b32 a51,v2\n"
"s_lshr_b32 s66,%1,3\n"
"s_lshl_b32 s66,s66,2\n"
"s_add_u32 s66,s66,s36\n"
"s_mul_i32 s66,s66,9216\n"
"s_mul_i32 s67,s38,1179648\n"
"s_add_u32 s66,s66,s67\n"
"s_add_u32 s66,s66,4096\n"
"v_cmp_gt_u32 vcc,36,v0\n"
"v_lshl_add_u32 v97,v0,8,s66\n"
"v_bfrev_b32 v3,-2\n"
"v_cndmask_b32 v97,v3,v97,vcc\n"
WT "vmcnt(17)\n"
"s_barrier\n"
DR "v[32:35],v98 offset:31232\n"
DR "v[36:39],v98 offset:33280\n"
DR "v[40:43],v99 offset:30720\n"
DT "a[128:129],v80 offset:33792\n"
DT "a[130:131],v80 offset:34304\n"
DT "a[132:133],v80 offset:33824\n"
DT "a[134:135],v80 offset:34336\n"
DT "a[136:137],v80 offset:33856\n"
DT "a[138:139],v80 offset:34368\n"
DT "a[140:141],v80 offset:33888\n"
DT "a[142:143],v80 offset:34400\n"
DT "a[144:145],v81 offset:33792\n"
DT "a[146:147],v81 offset:34304\n"
DT "a[148:149],v81 offset:33824\n"
DT "a[150:151],v81 offset:34336\n"
DT "a[152:153],v81 offset:33856\n"
DT "a[154:155],v81 offset:34368\n"
DT "a[156:157],v81 offset:33888\n"
DT "a[158:159],v81 offset:34400\n"
DT "a[160:161],v82 offset:33792\n"
DT "a[162:163],v82 offset:34304\n"
DT "a[164:165],v82 offset:33824\n"
DT "a[166:167],v82 offset:34336\n"
DT "a[168:169],v82 offset:33856\n"
DT "a[170:171],v82 offset:34368\n"
DT "a[172:173],v82 offset:33888\n"
DT "a[174:175],v82 offset:34400\n"
DT "a[176:177],v83 offset:33792\n"
DT "a[178:179],v83 offset:34304\n"
DT "a[180:181],v83 offset:33824\n"
DT "a[182:183],v83 offset:34336\n"
DT "a[184:185],v83 offset:33856\n"
DT "a[186:187],v83 offset:34368\n"
DT "a[188:189],v83 offset:33888\n"
DT "a[190:191],v83 offset:34400\n"
WT "lgkmcnt(0)\n"
MF "v[112:115],a[128:131],v[32:35],0\n"
WT "vmcnt(13)\n"
"s_barrier\n"
MF "v[176:179],a[128:131],v[36:39],0\n"
"s_add_u32 m0,s46,16896\n"
DR "v[44:47],v102 offset:31232\n"
MF "v[240:243],a[128:131],v[40:43],0\n"
DR "v[48:51],v102 offset:33280\n"
"v_add_u32 v89,786432,v86\n"
MF "v[116:119],a[132:135],v[32:35],0\n"
DR "v[52:55],v103 offset:30720\n"
BL "v89,s[20:23],0 offen offset:0 lds\n"
MF "v[180:183],a[132:135],v[36:39],0\n"
DT "a[192:193],v80 offset:50688\n"
MF "v[244:247],a[132:135],v[40:43],0\n"
DT "a[194:195],v80 offset:51200\n"
MF "v[120:123],a[136:139],v[32:35],0\n"
DT "a[196:197],v80 offset:50720\n"
BL "v89,s[20:23],s42 offen offset:1056 lds\n"
MF "v[184:187],a[136:139],v[36:39],0\n"
DT "a[198:199],v80 offset:51232\n"
MF "v[248:251],a[136:139],v[40:43],0\n"
DT "a[200:201],v80 offset:50752\n"
MF "v[124:127],a[140:143],v[32:35],0\n"
WT "lgkmcnt(8)\n"
DT "a[202:203],v80 offset:51264\n"
MF "v[188:191],a[140:143],v[36:39],0\n"
DT "a[204:205],v80 offset:50784\n"
BL "v89,s[20:23],s43 offen offset:2112 lds\n"
MF "v[252:255],a[140:143],v[40:43],0\n"
DT "a[206:207],v80 offset:51296\n"
MF "v[128:131],a[144:147],v[32:35],0\n"
DT "a[208:209],v81 offset:50688\n"
MF "v[192:195],a[144:147],v[36:39],0\n"
DT "a[210:211],v81 offset:51200\n"
MF "v[132:135],a[148:151],v[32:35],0\n"
DT "a[212:213],v81 offset:50720\n"
BL "v89,s[20:23],s44 offen offset:3168 lds\n"
MF "v[196:199],a[148:151],v[36:39],0\n"
"s_add_u32 m0,s47,135680\n"
DT "a[214:215],v81 offset:51232\n"
MF "v[136:139],a[152:155],v[32:35],0\n"
"s_movk_i32 s67,256\n"
DT "a[216:217],v81 offset:50752\n"
MF "v[200:203],a[152:155],v[36:39],0\n"
DT "a[218:219],v81 offset:51264\n"
BL "v87,s[8:11],s67 offen offset:0 lds\n"
MF "v[140:143],a[156:159],v[32:35],0\n"
DT "a[220:221],v81 offset:50784\n"
MF "v[204:207],a[156:159],v[36:39],0\n"
DT "a[222:223],v81 offset:51296\n"
MF "v[144:147],a[160:163],v[32:35],0\n"
DT "a[224:225],v82 offset:50688\n"
MF "v[208:211],a[160:163],v[36:39],0\n"
DT "a[226:227],v82 offset:51200\n"
BL "v90,s[8:11],s67 offen offset:1024 lds\n"
MF "v[148:151],a[164:167],v[32:35],0\n"
DT "a[228:229],v82 offset:50720\n"
MF "v[212:215],a[164:167],v[36:39],0\n"
"s_add_u32 m0,s47,135168\n"
DT "a[230:231],v82 offset:51232\n"
MF "v[152:155],a[168:171],v[32:35],0\n"
"s_movk_i32 s67,256\n"
DT "a[232:233],v82 offset:50752\n"
MF "v[216:219],a[168:171],v[36:39],0\n"
DT "a[234:235],v82 offset:51264\n"
"buffer_load_dword v88,s[8:11],s67 offen lds\n"
MF "v[156:159],a[172:175],v[32:35],0\n"
DT "a[236:237],v82 offset:50784\n"
MF "v[220:223],a[172:175],v[36:39],0\n"
"s_mov_b32 s77,1572864\n"
DT "a[238:239],v82 offset:51296\n"
MF "v[160:163],a[176:179],v[32:35],0\n"
DT "a[240:241],v83 offset:50688\n"
"buffer_load_dword v95,v93,s[20:23],s77 offen\n"
MF "v[224:227],a[176:179],v[36:39],0\n"
DT "a[242:243],v83 offset:51200\n"
MF "v[164:167],a[180:183],v[32:35],0\n"
DT "a[244:245],v83 offset:50720\n"
MF "v[228:231],a[180:183],v[36:39],0\n"
DT "a[246:247],v83 offset:51232\n"
MF "v[168:171],a[184:187],v[32:35],0\n"
DT "a[248:249],v83 offset:50752\n"
"buffer_load_dword v95,v94,s[8:11],0 offen offset:384\n"
MF "v[232:235],a[184:187],v[36:39],0\n"
DT "a[250:251],v83 offset:51264\n"
MF "v[172:175],a[188:191],v[32:35],0\n"
DT "a[252:253],v83 offset:50784\n"
MF "v[236:239],a[188:191],v[36:39],0\n"
WT "lgkmcnt(8)\n"
DT "a[254:255],v83 offset:51296\n"
MF "v[112:115],a[192:195],v[44:47],v[112:115]\n"
WT "vmcnt(10)\n"
"s_barrier\n"
MF "v[176:179],a[192:195],v[48:51],v[176:179]\n"
"s_add_u32 m0,s46,33792\n"
DR "v[32:35],v98 offset:49664\n"
MF "v[240:243],a[192:195],v[52:55],v[240:243]\n"
DR "v[36:39],v98 offset:51712\n"
"v_add_u32 v89,1048576,v86\n"
MF "v[116:119],a[196:199],v[44:47],v[116:119]\n"
DR "v[40:43],v99 offset:49152\n"
BL "v89,s[20:23],0 offen offset:0 lds\n"
MF "v[180:183],a[196:199],v[48:51],v[180:183]\n"
DT "a[128:129],v80 offset:0\n"
MF "v[244:247],a[196:199],v[52:55],v[244:247]\n"
DT "a[130:131],v80 offset:512\n"
MF "v[120:123],a[200:203],v[44:47],v[120:123]\n"
DT "a[132:133],v80 offset:32\n"
MF "v[184:187],a[200:203],v[48:51],v[184:187]\n"
DT "a[134:135],v80 offset:544\n"
BL "v89,s[20:23],s42 offen offset:1056 lds\n"
MF "v[248:251],a[200:203],v[52:55],v[248:251]\n"
DT "a[136:137],v80 offset:64\n"
MF "v[124:127],a[204:207],v[44:47],v[124:127]\n"
WT "lgkmcnt(8)\n"
DT "a[138:139],v80 offset:576\n"
MF "v[188:191],a[204:207],v[48:51],v[188:191]\n"
DT "a[140:141],v80 offset:96\n"
MF "v[252:255],a[204:207],v[52:55],v[252:255]\n"
DT "a[142:143],v80 offset:608\n"
MF "v[128:131],a[208:211],v[44:47],v[128:131]\n"
DT "a[144:145],v81 offset:0\n"
BL "v89,s[20:23],s43 offen offset:2112 lds\n"
MF "v[192:195],a[208:211],v[48:51],v[192:195]\n"
DT "a[146:147],v81 offset:512\n"
MF "v[132:135],a[212:215],v[44:47],v[132:135]\n"
DT "a[148:149],v81 offset:32\n"
MF "v[196:199],a[212:215],v[48:51],v[196:199]\n"
DT "a[150:151],v81 offset:544\n"
MF "v[136:139],a[216:219],v[44:47],v[136:139]\n"
DT "a[152:153],v81 offset:64\n"
MF "v[200:203],a[216:219],v[48:51],v[200:203]\n"
DT "a[154:155],v81 offset:576\n"
BL "v89,s[20:23],s44 offen offset:3168 lds\n"
MF "v[140:143],a[220:223],v[44:47],v[140:143]\n"
DT "a[156:157],v81 offset:96\n"
MF "v[204:207],a[220:223],v[48:51],v[204:207]\n"
"s_add_u32 m0,s47,135680\n"
DT "a[158:159],v81 offset:608\n"
MF "v[144:147],a[224:227],v[44:47],v[144:147]\n"
"s_movk_i32 s67,16640\n"
DT "a[160:161],v82 offset:0\n"
MF "v[208:211],a[224:227],v[48:51],v[208:211]\n"
DT "a[162:163],v82 offset:512\n"
BL "v87,s[8:11],s67 offen offset:2048 lds\n"
MF "v[148:151],a[228:231],v[44:47],v[148:151]\n"
DT "a[164:165],v82 offset:32\n"
MF "v[212:215],a[228:231],v[48:51],v[212:215]\n"
DT "a[166:167],v82 offset:544\n"
MF "v[152:155],a[232:235],v[44:47],v[152:155]\n"
DT "a[168:169],v82 offset:64\n"
MF "v[216:219],a[232:235],v[48:51],v[216:219]\n"
DT "a[170:171],v82 offset:576\n"
MF "v[156:159],a[236:239],v[44:47],v[156:159]\n"
DT "a[172:173],v82 offset:96\n"
BL "v90,s[8:11],s67 offen offset:3072 lds\n"
MF "v[220:223],a[236:239],v[48:51],v[220:223]\n"
DT "a[174:175],v82 offset:608\n"
MF "v[160:163],a[240:243],v[44:47],v[160:163]\n"
DT "a[176:177],v83 offset:0\n"
MF "v[224:227],a[240:243],v[48:51],v[224:227]\n"
"s_add_u32 m0,s47,135424\n"
DT "a[178:179],v83 offset:512\n"
MF "v[164:167],a[244:247],v[44:47],v[164:167]\n"
"s_movk_i32 s67,256\n"
DT "a[180:181],v83 offset:32\n"
MF "v[228:231],a[244:247],v[48:51],v[228:231]\n"
DT "a[182:183],v83 offset:544\n"
"buffer_load_dword v91,s[8:11],s67 offen lds\n"
MF "v[168:171],a[248:251],v[44:47],v[168:171]\n"
DT "a[184:185],v83 offset:64\n"
MF "v[232:235],a[248:251],v[48:51],v[232:235]\n"
DT "a[186:187],v83 offset:576\n"
MF "v[172:175],a[252:255],v[44:47],v[172:175]\n"
DT "a[188:189],v83 offset:96\n"
MF "v[236:239],a[252:255],v[48:51],v[236:239]\n"
WT "lgkmcnt(8)\n"
DT "a[190:191],v83 offset:608\n"
MF "v[112:115],a[128:131],v[32:35],v[112:115]\n"
WT "vmcnt(12)\n"
"s_barrier\n"
MF "v[176:179],a[128:131],v[36:39],v[176:179]\n"
"s_add_u32 m0,s46,50688\n"
DR "v[44:47],v102 offset:49664\n"
MF "v[240:243],a[128:131],v[40:43],v[240:243]\n"
DR "v[48:51],v102 offset:51712\n"
"v_add_u32 v89,1310720,v86\n"
MF "v[116:119],a[132:135],v[32:35],v[116:119]\n"
DR "v[52:55],v103 offset:49152\n"
BL "v89,s[20:23],0 offen offset:0 lds\n"
MF "v[180:183],a[132:135],v[36:39],v[180:183]\n"
DT "a[192:193],v80 offset:16896\n"
MF "v[244:247],a[132:135],v[40:43],v[244:247]\n"
DT "a[194:195],v80 offset:17408\n"
MF "v[120:123],a[136:139],v[32:35],v[120:123]\n"
DT "a[196:197],v80 offset:16928\n"
MF "v[184:187],a[136:139],v[36:39],v[184:187]\n"
DT "a[198:199],v80 offset:17440\n"
BL "v89,s[20:23],s42 offen offset:1056 lds\n"
MF "v[248:251],a[136:139],v[40:43],v[248:251]\n"
DT "a[200:201],v80 offset:16960\n"
MF "v[124:127],a[140:143],v[32:35],v[124:127]\n"
WT "lgkmcnt(8)\n"
DT "a[202:203],v80 offset:17472\n"
MF "v[188:191],a[140:143],v[36:39],v[188:191]\n"
DT "a[204:205],v80 offset:16992\n"
MF "v[252:255],a[140:143],v[40:43],v[252:255]\n"
DT "a[206:207],v80 offset:17504\n"
MF "v[128:131],a[144:147],v[32:35],v[128:131]\n"
DT "a[208:209],v81 offset:16896\n"
BL "v89,s[20:23],s43 offen offset:2112 lds\n"
MF "v[192:195],a[144:147],v[36:39],v[192:195]\n"
DT "a[210:211],v81 offset:17408\n"
MF "v[132:135],a[148:151],v[32:35],v[132:135]\n"
DT "a[212:213],v81 offset:16928\n"
MF "v[196:199],a[148:151],v[36:39],v[196:199]\n"
DT "a[214:215],v81 offset:17440\n"
MF "v[136:139],a[152:155],v[32:35],v[136:139]\n"
DT "a[216:217],v81 offset:16960\n"
MF "v[200:203],a[152:155],v[36:39],v[200:203]\n"
DT "a[218:219],v81 offset:17472\n"
BL "v89,s[20:23],s44 offen offset:3168 lds\n"
MF "v[140:143],a[156:159],v[32:35],v[140:143]\n"
DT "a[220:221],v81 offset:16992\n"
MF "v[204:207],a[156:159],v[36:39],v[204:207]\n"
"s_add_u32 m0,s47,31232\n"
DT "a[222:223],v81 offset:17504\n"
MF "v[144:147],a[160:163],v[32:35],v[144:147]\n"
"s_movk_i32 s67,384\n"
DT "a[224:225],v82 offset:16896\n"
MF "v[208:211],a[160:163],v[36:39],v[208:211]\n"
DT "a[226:227],v82 offset:17408\n"
BL "v87,s[8:11],s67 offen offset:0 lds\n"
MF "v[148:151],a[164:167],v[32:35],v[148:151]\n"
DT "a[228:229],v82 offset:16928\n"
MF "v[212:215],a[164:167],v[36:39],v[212:215]\n"
DT "a[230:231],v82 offset:17440\n"
MF "v[152:155],a[168:171],v[32:35],v[152:155]\n"
DT "a[232:233],v82 offset:16960\n"
MF "v[216:219],a[168:171],v[36:39],v[216:219]\n"
DT "a[234:235],v82 offset:17472\n"
MF "v[156:159],a[172:175],v[32:35],v[156:159]\n"
DT "a[236:237],v82 offset:16992\n"
BL "v90,s[8:11],s67 offen offset:1024 lds\n"
MF "v[220:223],a[172:175],v[36:39],v[220:223]\n"
DT "a[238:239],v82 offset:17504\n"
MF "v[160:163],a[176:179],v[32:35],v[160:163]\n"
DT "a[240:241],v83 offset:16896\n"
MF "v[224:227],a[176:179],v[36:39],v[224:227]\n"
"s_add_u32 m0,s47,30720\n"
DT "a[242:243],v83 offset:17408\n"
MF "v[164:167],a[180:183],v[32:35],v[164:167]\n"
"s_movk_i32 s67,384\n"
DT "a[244:245],v83 offset:16928\n"
MF "v[228:231],a[180:183],v[36:39],v[228:231]\n"
DT "a[246:247],v83 offset:17440\n"
"buffer_load_dword v88,s[8:11],s67 offen lds\n"
MF "v[168:171],a[184:187],v[32:35],v[168:171]\n"
DT "a[248:249],v83 offset:16960\n"
MF "v[232:235],a[184:187],v[36:39],v[232:235]\n"
DT "a[250:251],v83 offset:17472\n"
MF "v[172:175],a[188:191],v[32:35],v[172:175]\n"
DT "a[252:253],v83 offset:16992\n"
MF "v[236:239],a[188:191],v[36:39],v[236:239]\n"
WT "lgkmcnt(8)\n"
DT "a[254:255],v83 offset:17504\n"
MF "v[112:115],a[192:195],v[44:47],v[112:115]\n"
WT "vmcnt(7)\n"
"s_barrier\n"
MF "v[176:179],a[192:195],v[48:51],v[176:179]\n"
"s_add_u32 m0,s46,0\n"
DR "v[32:35],v84 offset:512\n"
MF "v[240:243],a[192:195],v[52:55],v[240:243]\n"
DR "v[36:39],v84 offset:2560\n"
"v_add_u32 v89,1572864,v86\n"
MF "v[116:119],a[196:199],v[44:47],v[116:119]\n"
DR "v[40:43],v85 offset:0\n"
BL "v89,s[20:23],0 offen offset:0 lds\n"
MF "v[180:183],a[196:199],v[48:51],v[180:183]\n"
DT "a[128:129],v80 offset:33792\n"
MF "v[244:247],a[196:199],v[52:55],v[244:247]\n"
DT "a[130:131],v80 offset:34304\n"
MF "v[120:123],a[200:203],v[44:47],v[120:123]\n"
DT "a[132:133],v80 offset:33824\n"
BL "v89,s[20:23],s42 offen offset:1056 lds\n"
MF "v[184:187],a[200:203],v[48:51],v[184:187]\n"
DT "a[134:135],v80 offset:34336\n"
MF "v[248:251],a[200:203],v[52:55],v[248:251]\n"
DT "a[136:137],v80 offset:33856\n"
MF "v[124:127],a[204:207],v[44:47],v[124:127]\n"
WT "lgkmcnt(8)\n"
DT "a[138:139],v80 offset:34368\n"
MF "v[188:191],a[204:207],v[48:51],v[188:191]\n"
DT "a[140:141],v80 offset:33888\n"
BL "v89,s[20:23],s43 offen offset:2112 lds\n"
MF "v[252:255],a[204:207],v[52:55],v[252:255]\n"
DT "a[142:143],v80 offset:34400\n"
MF "v[128:131],a[208:211],v[44:47],v[128:131]\n"
DT "a[144:145],v81 offset:33792\n"
MF "v[192:195],a[208:211],v[48:51],v[192:195]\n"
DT "a[146:147],v81 offset:34304\n"
MF "v[132:135],a[212:215],v[44:47],v[132:135]\n"
DT "a[148:149],v81 offset:33824\n"
BL "v89,s[20:23],s44 offen offset:3168 lds\n"
MF "v[196:199],a[212:215],v[48:51],v[196:199]\n"
"s_add_u32 m0,s47,31232\n"
DT "a[150:151],v81 offset:34336\n"
MF "v[136:139],a[216:219],v[44:47],v[136:139]\n"
"s_movk_i32 s67,16768\n"
DT "a[152:153],v81 offset:33856\n"
MF "v[200:203],a[216:219],v[48:51],v[200:203]\n"
DT "a[154:155],v81 offset:34368\n"
BL "v87,s[8:11],s67 offen offset:2048 lds\n"
MF "v[140:143],a[220:223],v[44:47],v[140:143]\n"
DT "a[156:157],v81 offset:33888\n"
MF "v[204:207],a[220:223],v[48:51],v[204:207]\n"
DT "a[158:159],v81 offset:34400\n"
MF "v[144:147],a[224:227],v[44:47],v[144:147]\n"
DT "a[160:161],v82 offset:33792\n"
MF "v[208:211],a[224:227],v[48:51],v[208:211]\n"
DT "a[162:163],v82 offset:34304\n"
BL "v90,s[8:11],s67 offen offset:3072 lds\n"
MF "v[148:151],a[228:231],v[44:47],v[148:151]\n"
DT "a[164:165],v82 offset:33824\n"
MF "v[212:215],a[228:231],v[48:51],v[212:215]\n"
"s_add_u32 m0,s47,30976\n"
DT "a[166:167],v82 offset:34336\n"
MF "v[152:155],a[232:235],v[44:47],v[152:155]\n"
"s_movk_i32 s67,384\n"
DT "a[168:169],v82 offset:33856\n"
MF "v[216:219],a[232:235],v[48:51],v[216:219]\n"
DT "a[170:171],v82 offset:34368\n"
"buffer_load_dword v91,s[8:11],s67 offen lds\n"
MF "v[156:159],a[236:239],v[44:47],v[156:159]\n"
DT "a[172:173],v82 offset:33888\n"
MF "v[220:223],a[236:239],v[48:51],v[220:223]\n"
"s_mov_b32 s77,2621440\n"
DT "a[174:175],v82 offset:34400\n"
MF "v[160:163],a[240:243],v[44:47],v[160:163]\n"
DT "a[176:177],v83 offset:33792\n"
"buffer_load_dword v95,v93,s[20:23],s77 offen\n"
MF "v[224:227],a[240:243],v[48:51],v[224:227]\n"
DT "a[178:179],v83 offset:34304\n"
MF "v[164:167],a[244:247],v[44:47],v[164:167]\n"
DT "a[180:181],v83 offset:33824\n"
MF "v[228:231],a[244:247],v[48:51],v[228:231]\n"
DT "a[182:183],v83 offset:34336\n"
MF "v[168:171],a[248:251],v[44:47],v[168:171]\n"
DT "a[184:185],v83 offset:33856\n"
"buffer_load_dword v95,v94,s[8:11],0 offen offset:640\n"
MF "v[232:235],a[248:251],v[48:51],v[232:235]\n"
DT "a[186:187],v83 offset:34368\n"
MF "v[172:175],a[252:255],v[44:47],v[172:175]\n"
DT "a[188:189],v83 offset:33888\n"
MF "v[236:239],a[252:255],v[48:51],v[236:239]\n"
WT "lgkmcnt(8)\n"
DT "a[190:191],v83 offset:34400\n"
MF "v[112:115],a[128:131],v[32:35],v[112:115]\n"
WT "vmcnt(12)\n"
"s_barrier\n"
MF "v[176:179],a[128:131],v[36:39],v[176:179]\n"
"s_add_u32 m0,s46,16896\n"
DR "v[44:47],v100 offset:512\n"
MF "v[240:243],a[128:131],v[40:43],v[240:243]\n"
DR "v[48:51],v100 offset:2560\n"
"v_add_u32 v89,1835008,v86\n"
MF "v[116:119],a[132:135],v[32:35],v[116:119]\n"
DR "v[52:55],v101 offset:0\n"
BL "v89,s[20:23],0 offen offset:0 lds\n"
MF "v[180:183],a[132:135],v[36:39],v[180:183]\n"
DT "a[192:193],v80 offset:50688\n"
MF "v[244:247],a[132:135],v[40:43],v[244:247]\n"
DT "a[194:195],v80 offset:51200\n"
MF "v[120:123],a[136:139],v[32:35],v[120:123]\n"
DT "a[196:197],v80 offset:50720\n"
MF "v[184:187],a[136:139],v[36:39],v[184:187]\n"
DT "a[198:199],v80 offset:51232\n"
BL "v89,s[20:23],s42 offen offset:1056 lds\n"
MF "v[248:251],a[136:139],v[40:43],v[248:251]\n"
DT "a[200:201],v80 offset:50752\n"
MF "v[124:127],a[140:143],v[32:35],v[124:127]\n"
WT "lgkmcnt(8)\n"
DT "a[202:203],v80 offset:51264\n"
MF "v[188:191],a[140:143],v[36:39],v[188:191]\n"
DT "a[204:205],v80 offset:50784\n"
MF "v[252:255],a[140:143],v[40:43],v[252:255]\n"
DT "a[206:207],v80 offset:51296\n"
MF "v[128:131],a[144:147],v[32:35],v[128:131]\n"
DT "a[208:209],v81 offset:50688\n"
BL "v89,s[20:23],s43 offen offset:2112 lds\n"
MF "v[192:195],a[144:147],v[36:39],v[192:195]\n"
DT "a[210:211],v81 offset:51200\n"
MF "v[132:135],a[148:151],v[32:35],v[132:135]\n"
DT "a[212:213],v81 offset:50720\n"
MF "v[196:199],a[148:151],v[36:39],v[196:199]\n"
DT "a[214:215],v81 offset:51232\n"
MF "v[136:139],a[152:155],v[32:35],v[136:139]\n"
DT "a[216:217],v81 offset:50752\n"
MF "v[200:203],a[152:155],v[36:39],v[200:203]\n"
DT "a[218:219],v81 offset:51264\n"
BL "v89,s[20:23],s44 offen offset:3168 lds\n"
MF "v[140:143],a[156:159],v[32:35],v[140:143]\n"
DT "a[220:221],v81 offset:50784\n"
MF "v[204:207],a[156:159],v[36:39],v[204:207]\n"
"s_add_u32 m0,s47,49664\n"
DT "a[222:223],v81 offset:51296\n"
MF "v[144:147],a[160:163],v[32:35],v[144:147]\n"
"s_movk_i32 s67,512\n"
DT "a[224:225],v82 offset:50688\n"
MF "v[208:211],a[160:163],v[36:39],v[208:211]\n"
DT "a[226:227],v82 offset:51200\n"
BL "v87,s[8:11],s67 offen offset:0 lds\n"
MF "v[148:151],a[164:167],v[32:35],v[148:151]\n"
DT "a[228:229],v82 offset:50720\n"
MF "v[212:215],a[164:167],v[36:39],v[212:215]\n"
DT "a[230:231],v82 offset:51232\n"
MF "v[152:155],a[168:171],v[32:35],v[152:155]\n"
DT "a[232:233],v82 offset:50752\n"
MF "v[216:219],a[168:171],v[36:39],v[216:219]\n"
DT "a[234:235],v82 offset:51264\n"
MF "v[156:159],a[172:175],v[32:35],v[156:159]\n"
DT "a[236:237],v82 offset:50784\n"
BL "v90,s[8:11],s67 offen offset:1024 lds\n"
MF "v[220:223],a[172:175],v[36:39],v[220:223]\n"
DT "a[238:239],v82 offset:51296\n"
MF "v[160:163],a[176:179],v[32:35],v[160:163]\n"
DT "a[240:241],v83 offset:50688\n"
MF "v[224:227],a[176:179],v[36:39],v[224:227]\n"
"s_add_u32 m0,s47,49152\n"
DT "a[242:243],v83 offset:51200\n"
MF "v[164:167],a[180:183],v[32:35],v[164:167]\n"
"s_movk_i32 s67,512\n"
DT "a[244:245],v83 offset:50720\n"
MF "v[228:231],a[180:183],v[36:39],v[228:231]\n"
DT "a[246:247],v83 offset:51232\n"
"buffer_load_dword v88,s[8:11],s67 offen lds\n"
MF "v[168:171],a[184:187],v[32:35],v[168:171]\n"
DT "a[248:249],v83 offset:50752\n"
MF "v[232:235],a[184:187],v[36:39],v[232:235]\n"
DT "a[250:251],v83 offset:51264\n"
MF "v[172:175],a[188:191],v[32:35],v[172:175]\n"
DT "a[252:253],v83 offset:50784\n"
MF "v[236:239],a[188:191],v[36:39],v[236:239]\n"
WT "lgkmcnt(8)\n"
DT "a[254:255],v83 offset:51296\n"
MF "v[112:115],a[192:195],v[44:47],v[112:115]\n"
WT "vmcnt(9)\n"
"s_barrier\n"
MF "v[176:179],a[192:195],v[48:51],v[176:179]\n"
"s_add_u32 m0,s46,33792\n"
DR "v[32:35],v98 offset:31232\n"
MF "v[240:243],a[192:195],v[52:55],v[240:243]\n"
DR "v[36:39],v98 offset:33280\n"
"v_add_u32 v89,2097152,v86\n"
MF "v[116:119],a[196:199],v[44:47],v[116:119]\n"
DR "v[40:43],v99 offset:30720\n"
BL "v89,s[20:23],0 offen offset:0 lds\n"
MF "v[180:183],a[196:199],v[48:51],v[180:183]\n"
DT "a[128:129],v80 offset:0\n"
MF "v[244:247],a[196:199],v[52:55],v[244:247]\n"
DT "a[130:131],v80 offset:512\n"
MF "v[120:123],a[200:203],v[44:47],v[120:123]\n"
DT "a[132:133],v80 offset:32\n"
MF "v[184:187],a[200:203],v[48:51],v[184:187]\n"
DT "a[134:135],v80 offset:544\n"
BL "v89,s[20:23],s42 offen offset:1056 lds\n"
MF "v[248:251],a[200:203],v[52:55],v[248:251]\n"
DT "a[136:137],v80 offset:64\n"
MF "v[124:127],a[204:207],v[44:47],v[124:127]\n"
WT "lgkmcnt(8)\n"
DT "a[138:139],v80 offset:576\n"
MF "v[188:191],a[204:207],v[48:51],v[188:191]\n"
DT "a[140:141],v80 offset:96\n"
MF "v[252:255],a[204:207],v[52:55],v[252:255]\n"
DT "a[142:143],v80 offset:608\n"
MF "v[128:131],a[208:211],v[44:47],v[128:131]\n"
DT "a[144:145],v81 offset:0\n"
BL "v89,s[20:23],s43 offen offset:2112 lds\n"
MF "v[192:195],a[208:211],v[48:51],v[192:195]\n"
DT "a[146:147],v81 offset:512\n"
MF "v[132:135],a[212:215],v[44:47],v[132:135]\n"
DT "a[148:149],v81 offset:32\n"
MF "v[196:199],a[212:215],v[48:51],v[196:199]\n"
DT "a[150:151],v81 offset:544\n"
MF "v[136:139],a[216:219],v[44:47],v[136:139]\n"
DT "a[152:153],v81 offset:64\n"
MF "v[200:203],a[216:219],v[48:51],v[200:203]\n"
DT "a[154:155],v81 offset:576\n"
BL "v89,s[20:23],s44 offen offset:3168 lds\n"
MF "v[140:143],a[220:223],v[44:47],v[140:143]\n"
DT "a[156:157],v81 offset:96\n"
MF "v[204:207],a[220:223],v[48:51],v[204:207]\n"
"s_add_u32 m0,s47,49664\n"
DT "a[158:159],v81 offset:608\n"
MF "v[144:147],a[224:227],v[44:47],v[144:147]\n"
"s_movk_i32 s67,16896\n"
DT "a[160:161],v82 offset:0\n"
MF "v[208:211],a[224:227],v[48:51],v[208:211]\n"
DT "a[162:163],v82 offset:512\n"
BL "v87,s[8:11],s67 offen offset:2048 lds\n"
MF "v[148:151],a[228:231],v[44:47],v[148:151]\n"
DT "a[164:165],v82 offset:32\n"
MF "v[212:215],a[228:231],v[48:51],v[212:215]\n"
DT "a[166:167],v82 offset:544\n"
MF "v[152:155],a[232:235],v[44:47],v[152:155]\n"
DT "a[168:169],v82 offset:64\n"
MF "v[216:219],a[232:235],v[48:51],v[216:219]\n"
DT "a[170:171],v82 offset:576\n"
MF "v[156:159],a[236:239],v[44:47],v[156:159]\n"
DT "a[172:173],v82 offset:96\n"
BL "v90,s[8:11],s67 offen offset:3072 lds\n"
MF "v[220:223],a[236:239],v[48:51],v[220:223]\n"
DT "a[174:175],v82 offset:608\n"
MF "v[160:163],a[240:243],v[44:47],v[160:163]\n"
DT "a[176:177],v83 offset:0\n"
MF "v[224:227],a[240:243],v[48:51],v[224:227]\n"
"s_add_u32 m0,s47,49408\n"
DT "a[178:179],v83 offset:512\n"
MF "v[164:167],a[244:247],v[44:47],v[164:167]\n"
"s_movk_i32 s67,512\n"
DT "a[180:181],v83 offset:32\n"
MF "v[228:231],a[244:247],v[48:51],v[228:231]\n"
DT "a[182:183],v83 offset:544\n"
"buffer_load_dword v91,s[8:11],s67 offen lds\n"
MF "v[168:171],a[248:251],v[44:47],v[168:171]\n"
DT "a[184:185],v83 offset:64\n"
MF "v[232:235],a[248:251],v[48:51],v[232:235]\n"
DT "a[186:187],v83 offset:576\n"
MF "v[172:175],a[252:255],v[44:47],v[172:175]\n"
DT "a[188:189],v83 offset:96\n"
MF "v[236:239],a[252:255],v[48:51],v[236:239]\n"
WT "lgkmcnt(8)\n"
DT "a[190:191],v83 offset:608\n"
MF "v[112:115],a[128:131],v[32:35],v[112:115]\n"
WT "vmcnt(10)\n"
"s_barrier\n"
MF "v[176:179],a[128:131],v[36:39],v[176:179]\n"
"s_add_u32 m0,s46,50688\n"
DR "v[44:47],v102 offset:31232\n"
MF "v[240:243],a[128:131],v[40:43],v[240:243]\n"
DR "v[48:51],v102 offset:33280\n"
"v_add_u32 v89,2359296,v86\n"
MF "v[116:119],a[132:135],v[32:35],v[116:119]\n"
DR "v[52:55],v103 offset:30720\n"
BL "v89,s[20:23],0 offen offset:0 lds\n"
MF "v[180:183],a[132:135],v[36:39],v[180:183]\n"
DT "a[192:193],v80 offset:16896\n"
MF "v[244:247],a[132:135],v[40:43],v[244:247]\n"
DT "a[194:195],v80 offset:17408\n"
MF "v[120:123],a[136:139],v[32:35],v[120:123]\n"
DT "a[196:197],v80 offset:16928\n"
BL "v89,s[20:23],s42 offen offset:1056 lds\n"
MF "v[184:187],a[136:139],v[36:39],v[184:187]\n"
DT "a[198:199],v80 offset:17440\n"
MF "v[248:251],a[136:139],v[40:43],v[248:251]\n"
DT "a[200:201],v80 offset:16960\n"
MF "v[124:127],a[140:143],v[32:35],v[124:127]\n"
WT "lgkmcnt(8)\n"
DT "a[202:203],v80 offset:17472\n"
MF "v[188:191],a[140:143],v[36:39],v[188:191]\n"
DT "a[204:205],v80 offset:16992\n"
BL "v89,s[20:23],s43 offen offset:2112 lds\n"
MF "v[252:255],a[140:143],v[40:43],v[252:255]\n"
DT "a[206:207],v80 offset:17504\n"
MF "v[128:131],a[144:147],v[32:35],v[128:131]\n"
DT "a[208:209],v81 offset:16896\n"
MF "v[192:195],a[144:147],v[36:39],v[192:195]\n"
DT "a[210:211],v81 offset:17408\n"
MF "v[132:135],a[148:151],v[32:35],v[132:135]\n"
DT "a[212:213],v81 offset:16928\n"
BL "v89,s[20:23],s44 offen offset:3168 lds\n"
MF "v[196:199],a[148:151],v[36:39],v[196:199]\n"
"s_add_u32 m0,s47,135680\n"
DT "a[214:215],v81 offset:17440\n"
MF "v[136:139],a[152:155],v[32:35],v[136:139]\n"
"s_movk_i32 s67,640\n"
DT "a[216:217],v81 offset:16960\n"
MF "v[200:203],a[152:155],v[36:39],v[200:203]\n"
DT "a[218:219],v81 offset:17472\n"
BL "v87,s[8:11],s67 offen offset:0 lds\n"
MF "v[140:143],a[156:159],v[32:35],v[140:143]\n"
DT "a[220:221],v81 offset:16992\n"
MF "v[204:207],a[156:159],v[36:39],v[204:207]\n"
DT "a[222:223],v81 offset:17504\n"
MF "v[144:147],a[160:163],v[32:35],v[144:147]\n"
DT "a[224:225],v82 offset:16896\n"
MF "v[208:211],a[160:163],v[36:39],v[208:211]\n"
DT "a[226:227],v82 offset:17408\n"
BL "v90,s[8:11],s67 offen offset:1024 lds\n"
MF "v[148:151],a[164:167],v[32:35],v[148:151]\n"
DT "a[228:229],v82 offset:16928\n"
MF "v[212:215],a[164:167],v[36:39],v[212:215]\n"
"s_add_u32 m0,s47,135168\n"
DT "a[230:231],v82 offset:17440\n"
MF "v[152:155],a[168:171],v[32:35],v[152:155]\n"
"s_movk_i32 s67,640\n"
DT "a[232:233],v82 offset:16960\n"
MF "v[216:219],a[168:171],v[36:39],v[216:219]\n"
DT "a[234:235],v82 offset:17472\n"
"buffer_load_dword v88,s[8:11],s67 offen lds\n"
MF "v[156:159],a[172:175],v[32:35],v[156:159]\n"
DT "a[236:237],v82 offset:16992\n"
MF "v[220:223],a[172:175],v[36:39],v[220:223]\n"
"s_mov_b32 s77,3670016\n"
DT "a[238:239],v82 offset:17504\n"
MF "v[160:163],a[176:179],v[32:35],v[160:163]\n"
DT "a[240:241],v83 offset:16896\n"
"buffer_load_dword v95,v93,s[20:23],s77 offen\n"
MF "v[224:227],a[176:179],v[36:39],v[224:227]\n"
DT "a[242:243],v83 offset:17408\n"
MF "v[164:167],a[180:183],v[32:35],v[164:167]\n"
DT "a[244:245],v83 offset:16928\n"
MF "v[228:231],a[180:183],v[36:39],v[228:231]\n"
DT "a[246:247],v83 offset:17440\n"
MF "v[168:171],a[184:187],v[32:35],v[168:171]\n"
DT "a[248:249],v83 offset:16960\n"
"buffer_load_dword v95,v94,s[8:11],0 offen offset:896\n"
MF "v[232:235],a[184:187],v[36:39],v[232:235]\n"
DT "a[250:251],v83 offset:17472\n"
MF "v[172:175],a[188:191],v[32:35],v[172:175]\n"
DT "a[252:253],v83 offset:16992\n"
MF "v[236:239],a[188:191],v[36:39],v[236:239]\n"
WT "lgkmcnt(8)\n"
DT "a[254:255],v83 offset:17504\n"
MF "v[112:115],a[192:195],v[44:47],v[112:115]\n"
WT "vmcnt(9)\n"
"s_barrier\n"
MF "v[176:179],a[192:195],v[48:51],v[176:179]\n"
"s_add_u32 m0,s46,0\n"
DR "v[32:35],v98 offset:49664\n"
MF "v[240:243],a[192:195],v[52:55],v[240:243]\n"
DR "v[36:39],v98 offset:51712\n"
"v_add_u32 v89,2621440,v86\n"
MF "v[116:119],a[196:199],v[44:47],v[116:119]\n"
DR "v[40:43],v99 offset:49152\n"
BL "v89,s[20:23],0 offen offset:0 lds\n"
MF "v[180:183],a[196:199],v[48:51],v[180:183]\n"
DT "a[128:129],v80 offset:33792\n"
MF "v[244:247],a[196:199],v[52:55],v[244:247]\n"
DT "a[130:131],v80 offset:34304\n"
MF "v[120:123],a[200:203],v[44:47],v[120:123]\n"
DT "a[132:133],v80 offset:33824\n"
MF "v[184:187],a[200:203],v[48:51],v[184:187]\n"
DT "a[134:135],v80 offset:34336\n"
BL "v89,s[20:23],s42 offen offset:1056 lds\n"
MF "v[248:251],a[200:203],v[52:55],v[248:251]\n"
DT "a[136:137],v80 offset:33856\n"
MF "v[124:127],a[204:207],v[44:47],v[124:127]\n"
WT "lgkmcnt(8)\n"
DT "a[138:139],v80 offset:34368\n"
MF "v[188:191],a[204:207],v[48:51],v[188:191]\n"
DT "a[140:141],v80 offset:33888\n"
MF "v[252:255],a[204:207],v[52:55],v[252:255]\n"
DT "a[142:143],v80 offset:34400\n"
MF "v[128:131],a[208:211],v[44:47],v[128:131]\n"
DT "a[144:145],v81 offset:33792\n"
BL "v89,s[20:23],s43 offen offset:2112 lds\n"
MF "v[192:195],a[208:211],v[48:51],v[192:195]\n"
DT "a[146:147],v81 offset:34304\n"
MF "v[132:135],a[212:215],v[44:47],v[132:135]\n"
DT "a[148:149],v81 offset:33824\n"
MF "v[196:199],a[212:215],v[48:51],v[196:199]\n"
DT "a[150:151],v81 offset:34336\n"
MF "v[136:139],a[216:219],v[44:47],v[136:139]\n"
DT "a[152:153],v81 offset:33856\n"
MF "v[200:203],a[216:219],v[48:51],v[200:203]\n"
DT "a[154:155],v81 offset:34368\n"
BL "v89,s[20:23],s44 offen offset:3168 lds\n"
MF "v[140:143],a[220:223],v[44:47],v[140:143]\n"
DT "a[156:157],v81 offset:33888\n"
MF "v[204:207],a[220:223],v[48:51],v[204:207]\n"
"s_add_u32 m0,s47,135680\n"
DT "a[158:159],v81 offset:34400\n"
MF "v[144:147],a[224:227],v[44:47],v[144:147]\n"
"s_movk_i32 s67,17024\n"
DT "a[160:161],v82 offset:33792\n"
MF "v[208:211],a[224:227],v[48:51],v[208:211]\n"
DT "a[162:163],v82 offset:34304\n"
BL "v87,s[8:11],s67 offen offset:2048 lds\n"
MF "v[148:151],a[228:231],v[44:47],v[148:151]\n"
DT "a[164:165],v82 offset:33824\n"
MF "v[212:215],a[228:231],v[48:51],v[212:215]\n"
DT "a[166:167],v82 offset:34336\n"
MF "v[152:155],a[232:235],v[44:47],v[152:155]\n"
DT "a[168:169],v82 offset:33856\n"
MF "v[216:219],a[232:235],v[48:51],v[216:219]\n"
DT "a[170:171],v82 offset:34368\n"
MF "v[156:159],a[236:239],v[44:47],v[156:159]\n"
DT "a[172:173],v82 offset:33888\n"
BL "v90,s[8:11],s67 offen offset:3072 lds\n"
MF "v[220:223],a[236:239],v[48:51],v[220:223]\n"
DT "a[174:175],v82 offset:34400\n"
MF "v[160:163],a[240:243],v[44:47],v[160:163]\n"
DT "a[176:177],v83 offset:33792\n"
MF "v[224:227],a[240:243],v[48:51],v[224:227]\n"
"s_add_u32 m0,s47,135424\n"
DT "a[178:179],v83 offset:34304\n"
MF "v[164:167],a[244:247],v[44:47],v[164:167]\n"
"s_movk_i32 s67,640\n"
DT "a[180:181],v83 offset:33824\n"
MF "v[228:231],a[244:247],v[48:51],v[228:231]\n"
DT "a[182:183],v83 offset:34336\n"
"buffer_load_dword v91,s[8:11],s67 offen lds\n"
MF "v[168:171],a[248:251],v[44:47],v[168:171]\n"
DT "a[184:185],v83 offset:33856\n"
MF "v[232:235],a[248:251],v[48:51],v[232:235]\n"
DT "a[186:187],v83 offset:34368\n"
MF "v[172:175],a[252:255],v[44:47],v[172:175]\n"
DT "a[188:189],v83 offset:33888\n"
MF "v[236:239],a[252:255],v[48:51],v[236:239]\n"
WT "lgkmcnt(8)\n"
DT "a[190:191],v83 offset:34400\n"
MF "v[112:115],a[128:131],v[32:35],v[112:115]\n"
WT "vmcnt(12)\n"
"s_barrier\n"
MF "v[176:179],a[128:131],v[36:39],v[176:179]\n"
"s_add_u32 m0,s46,16896\n"
DR "v[44:47],v102 offset:49664\n"
MF "v[240:243],a[128:131],v[40:43],v[240:243]\n"
DR "v[48:51],v102 offset:51712\n"
"v_add_u32 v89,2883584,v86\n"
MF "v[116:119],a[132:135],v[32:35],v[116:119]\n"
DR "v[52:55],v103 offset:49152\n"
BL "v89,s[20:23],0 offen offset:0 lds\n"
MF "v[180:183],a[132:135],v[36:39],v[180:183]\n"
DT "a[192:193],v80 offset:50688\n"
MF "v[244:247],a[132:135],v[40:43],v[244:247]\n"
DT "a[194:195],v80 offset:51200\n"
MF "v[120:123],a[136:139],v[32:35],v[120:123]\n"
DT "a[196:197],v80 offset:50720\n"
MF "v[184:187],a[136:139],v[36:39],v[184:187]\n"
DT "a[198:199],v80 offset:51232\n"
BL "v89,s[20:23],s42 offen offset:1056 lds\n"
MF "v[248:251],a[136:139],v[40:43],v[248:251]\n"
DT "a[200:201],v80 offset:50752\n"
MF "v[124:127],a[140:143],v[32:35],v[124:127]\n"
WT "lgkmcnt(8)\n"
DT "a[202:203],v80 offset:51264\n"
MF "v[188:191],a[140:143],v[36:39],v[188:191]\n"
DT "a[204:205],v80 offset:50784\n"
MF "v[252:255],a[140:143],v[40:43],v[252:255]\n"
DT "a[206:207],v80 offset:51296\n"
MF "v[128:131],a[144:147],v[32:35],v[128:131]\n"
DT "a[208:209],v81 offset:50688\n"
BL "v89,s[20:23],s43 offen offset:2112 lds\n"
MF "v[192:195],a[144:147],v[36:39],v[192:195]\n"
DT "a[210:211],v81 offset:51200\n"
MF "v[132:135],a[148:151],v[32:35],v[132:135]\n"
DT "a[212:213],v81 offset:50720\n"
MF "v[196:199],a[148:151],v[36:39],v[196:199]\n"
DT "a[214:215],v81 offset:51232\n"
MF "v[136:139],a[152:155],v[32:35],v[136:139]\n"
DT "a[216:217],v81 offset:50752\n"
MF "v[200:203],a[152:155],v[36:39],v[200:203]\n"
DT "a[218:219],v81 offset:51264\n"
BL "v89,s[20:23],s44 offen offset:3168 lds\n"
MF "v[140:143],a[156:159],v[32:35],v[140:143]\n"
DT "a[220:221],v81 offset:50784\n"
MF "v[204:207],a[156:159],v[36:39],v[204:207]\n"
"s_add_u32 m0,s47,31232\n"
DT "a[222:223],v81 offset:51296\n"
MF "v[144:147],a[160:163],v[32:35],v[144:147]\n"
"s_movk_i32 s67,768\n"
DT "a[224:225],v82 offset:50688\n"
MF "v[208:211],a[160:163],v[36:39],v[208:211]\n"
DT "a[226:227],v82 offset:51200\n"
BL "v87,s[8:11],s67 offen offset:0 lds\n"
MF "v[148:151],a[164:167],v[32:35],v[148:151]\n"
DT "a[228:229],v82 offset:50720\n"
MF "v[212:215],a[164:167],v[36:39],v[212:215]\n"
DT "a[230:231],v82 offset:51232\n"
MF "v[152:155],a[168:171],v[32:35],v[152:155]\n"
DT "a[232:233],v82 offset:50752\n"
MF "v[216:219],a[168:171],v[36:39],v[216:219]\n"
DT "a[234:235],v82 offset:51264\n"
MF "v[156:159],a[172:175],v[32:35],v[156:159]\n"
DT "a[236:237],v82 offset:50784\n"
BL "v90,s[8:11],s67 offen offset:1024 lds\n"
MF "v[220:223],a[172:175],v[36:39],v[220:223]\n"
DT "a[238:239],v82 offset:51296\n"
MF "v[160:163],a[176:179],v[32:35],v[160:163]\n"
DT "a[240:241],v83 offset:50688\n"
MF "v[224:227],a[176:179],v[36:39],v[224:227]\n"
"s_add_u32 m0,s47,30720\n"
DT "a[242:243],v83 offset:51200\n"
MF "v[164:167],a[180:183],v[32:35],v[164:167]\n"
"s_movk_i32 s67,768\n"
DT "a[244:245],v83 offset:50720\n"
MF "v[228:231],a[180:183],v[36:39],v[228:231]\n"
DT "a[246:247],v83 offset:51232\n"
"buffer_load_dword v88,s[8:11],s67 offen lds\n"
MF "v[168:171],a[184:187],v[32:35],v[168:171]\n"
DT "a[248:249],v83 offset:50752\n"
MF "v[232:235],a[184:187],v[36:39],v[232:235]\n"
DT "a[250:251],v83 offset:51264\n"
MF "v[172:175],a[188:191],v[32:35],v[172:175]\n"
DT "a[252:253],v83 offset:50784\n"
MF "v[236:239],a[188:191],v[36:39],v[236:239]\n"
WT "lgkmcnt(8)\n"
DT "a[254:255],v83 offset:51296\n"
MF "v[112:115],a[192:195],v[44:47],v[112:115]\n"
WT "vmcnt(7)\n"
"s_barrier\n"
MF "v[176:179],a[192:195],v[48:51],v[176:179]\n"
"s_add_u32 m0,s46,33792\n"
DR "v[32:35],v84 offset:512\n"
MF "v[240:243],a[192:195],v[52:55],v[240:243]\n"
DR "v[36:39],v84 offset:2560\n"
"v_add_u32 v89,3145728,v86\n"
MF "v[116:119],a[196:199],v[44:47],v[116:119]\n"
DR "v[40:43],v85 offset:0\n"
BL "v89,s[20:23],0 offen offset:0 lds\n"
MF "v[180:183],a[196:199],v[48:51],v[180:183]\n"
DT "a[128:129],v80 offset:0\n"
MF "v[244:247],a[196:199],v[52:55],v[244:247]\n"
DT "a[130:131],v80 offset:512\n"
MF "v[120:123],a[200:203],v[44:47],v[120:123]\n"
DT "a[132:133],v80 offset:32\n"
MF "v[184:187],a[200:203],v[48:51],v[184:187]\n"
DT "a[134:135],v80 offset:544\n"
BL "v89,s[20:23],s42 offen offset:1056 lds\n"
MF "v[248:251],a[200:203],v[52:55],v[248:251]\n"
DT "a[136:137],v80 offset:64\n"
MF "v[124:127],a[204:207],v[44:47],v[124:127]\n"
WT "lgkmcnt(8)\n"
DT "a[138:139],v80 offset:576\n"
MF "v[188:191],a[204:207],v[48:51],v[188:191]\n"
DT "a[140:141],v80 offset:96\n"
MF "v[252:255],a[204:207],v[52:55],v[252:255]\n"
DT "a[142:143],v80 offset:608\n"
BL "v89,s[20:23],s43 offen offset:2112 lds\n"
MF "v[128:131],a[208:211],v[44:47],v[128:131]\n"
DT "a[144:145],v81 offset:0\n"
MF "v[192:195],a[208:211],v[48:51],v[192:195]\n"
DT "a[146:147],v81 offset:512\n"
MF "v[132:135],a[212:215],v[44:47],v[132:135]\n"
DT "a[148:149],v81 offset:32\n"
MF "v[196:199],a[212:215],v[48:51],v[196:199]\n"
DT "a[150:151],v81 offset:544\n"
BL "v89,s[20:23],s44 offen offset:3168 lds\n"
MF "v[136:139],a[216:219],v[44:47],v[136:139]\n"
DT "a[152:153],v81 offset:64\n"
MF "v[200:203],a[216:219],v[48:51],v[200:203]\n"
"s_add_u32 m0,s47,31232\n"
DT "a[154:155],v81 offset:576\n"
MF "v[140:143],a[220:223],v[44:47],v[140:143]\n"
"s_movk_i32 s67,17152\n"
DT "a[156:157],v81 offset:96\n"
MF "v[204:207],a[220:223],v[48:51],v[204:207]\n"
DT "a[158:159],v81 offset:608\n"
BL "v87,s[8:11],s67 offen offset:2048 lds\n"
MF "v[144:147],a[224:227],v[44:47],v[144:147]\n"
DT "a[160:161],v82 offset:0\n"
MF "v[208:211],a[224:227],v[48:51],v[208:211]\n"
DT "a[162:163],v82 offset:512\n"
MF "v[148:151],a[228:231],v[44:47],v[148:151]\n"
DT "a[164:165],v82 offset:32\n"
MF "v[212:215],a[228:231],v[48:51],v[212:215]\n"
DT "a[166:167],v82 offset:544\n"
BL "v90,s[8:11],s67 offen offset:3072 lds\n"
MF "v[152:155],a[232:235],v[44:47],v[152:155]\n"
DT "a[168:169],v82 offset:64\n"
MF "v[216:219],a[232:235],v[48:51],v[216:219]\n"
"s_add_u32 m0,s47,30976\n"
DT "a[170:171],v82 offset:576\n"
MF "v[156:159],a[236:239],v[44:47],v[156:159]\n"
"s_movk_i32 s67,768\n"
DT "a[172:173],v82 offset:96\n"
MF "v[220:223],a[236:239],v[48:51],v[220:223]\n"
DT "a[174:175],v82 offset:608\n"
"buffer_load_dword v91,s[8:11],s67 offen lds\n"
MF "v[160:163],a[240:243],v[44:47],v[160:163]\n"
DT "a[176:177],v83 offset:0\n"
MF "v[224:227],a[240:243],v[48:51],v[224:227]\n"
DT "a[178:179],v83 offset:512\n"
MF "v[164:167],a[244:247],v[44:47],v[164:167]\n"
DT "a[180:181],v83 offset:32\n"
MF "v[228:231],a[244:247],v[48:51],v[228:231]\n"
DT "a[182:183],v83 offset:544\n"
"buffer_load_dword v95,v97,s[24:27],0 offen offset:0\n"
MF "v[168:171],a[248:251],v[44:47],v[168:171]\n"
DT "a[184:185],v83 offset:64\n"
MF "v[232:235],a[248:251],v[48:51],v[232:235]\n"
DT "a[186:187],v83 offset:576\n"
MF "v[172:175],a[252:255],v[44:47],v[172:175]\n"
DT "a[188:189],v83 offset:96\n"
MF "v[236:239],a[252:255],v[48:51],v[236:239]\n"
WT "lgkmcnt(8)\n"
DT "a[190:191],v83 offset:608\n"
MF "v[112:115],a[128:131],v[32:35],v[112:115]\n"
WT "vmcnt(11)\n"
"s_barrier\n"
MF "v[176:179],a[128:131],v[36:39],v[176:179]\n"
"s_add_u32 m0,s46,50688\n"
DR "v[44:47],v100 offset:512\n"
MF "v[240:243],a[128:131],v[40:43],v[240:243]\n"
DR "v[48:51],v100 offset:2560\n"
"v_add_u32 v89,3407872,v86\n"
MF "v[116:119],a[132:135],v[32:35],v[116:119]\n"
DR "v[52:55],v101 offset:0\n"
BL "v89,s[20:23],0 offen offset:0 lds\n"
MF "v[180:183],a[132:135],v[36:39],v[180:183]\n"
DT "a[192:193],v80 offset:16896\n"
MF "v[244:247],a[132:135],v[40:43],v[244:247]\n"
DT "a[194:195],v80 offset:17408\n"
MF "v[120:123],a[136:139],v[32:35],v[120:123]\n"
DT "a[196:197],v80 offset:16928\n"
MF "v[184:187],a[136:139],v[36:39],v[184:187]\n"
DT "a[198:199],v80 offset:17440\n"
BL "v89,s[20:23],s42 offen offset:1056 lds\n"
MF "v[248:251],a[136:139],v[40:43],v[248:251]\n"
DT "a[200:201],v80 offset:16960\n"
MF "v[124:127],a[140:143],v[32:35],v[124:127]\n"
WT "lgkmcnt(8)\n"
DT "a[202:203],v80 offset:17472\n"
MF "v[188:191],a[140:143],v[36:39],v[188:191]\n"
DT "a[204:205],v80 offset:16992\n"
MF "v[252:255],a[140:143],v[40:43],v[252:255]\n"
DT "a[206:207],v80 offset:17504\n"
MF "v[128:131],a[144:147],v[32:35],v[128:131]\n"
DT "a[208:209],v81 offset:16896\n"
BL "v89,s[20:23],s43 offen offset:2112 lds\n"
MF "v[192:195],a[144:147],v[36:39],v[192:195]\n"
DT "a[210:211],v81 offset:17408\n"
MF "v[132:135],a[148:151],v[32:35],v[132:135]\n"
DT "a[212:213],v81 offset:16928\n"
MF "v[196:199],a[148:151],v[36:39],v[196:199]\n"
DT "a[214:215],v81 offset:17440\n"
MF "v[136:139],a[152:155],v[32:35],v[136:139]\n"
DT "a[216:217],v81 offset:16960\n"
MF "v[200:203],a[152:155],v[36:39],v[200:203]\n"
DT "a[218:219],v81 offset:17472\n"
BL "v89,s[20:23],s44 offen offset:3168 lds\n"
MF "v[140:143],a[156:159],v[32:35],v[140:143]\n"
DT "a[220:221],v81 offset:16992\n"
MF "v[204:207],a[156:159],v[36:39],v[204:207]\n"
"s_add_u32 m0,s47,49664\n"
DT "a[222:223],v81 offset:17504\n"
MF "v[144:147],a[160:163],v[32:35],v[144:147]\n"
"s_movk_i32 s67,896\n"
DT "a[224:225],v82 offset:16896\n"
MF "v[208:211],a[160:163],v[36:39],v[208:211]\n"
DT "a[226:227],v82 offset:17408\n"
BL "v87,s[8:11],s67 offen offset:0 lds\n"
MF "v[148:151],a[164:167],v[32:35],v[148:151]\n"
DT "a[228:229],v82 offset:16928\n"
MF "v[212:215],a[164:167],v[36:39],v[212:215]\n"
DT "a[230:231],v82 offset:17440\n"
MF "v[152:155],a[168:171],v[32:35],v[152:155]\n"
DT "a[232:233],v82 offset:16960\n"
MF "v[216:219],a[168:171],v[36:39],v[216:219]\n"
DT "a[234:235],v82 offset:17472\n"
MF "v[156:159],a[172:175],v[32:35],v[156:159]\n"
DT "a[236:237],v82 offset:16992\n"
BL "v90,s[8:11],s67 offen offset:1024 lds\n"
MF "v[220:223],a[172:175],v[36:39],v[220:223]\n"
DT "a[238:239],v82 offset:17504\n"
MF "v[160:163],a[176:179],v[32:35],v[160:163]\n"
DT "a[240:241],v83 offset:16896\n"
MF "v[224:227],a[176:179],v[36:39],v[224:227]\n"
"s_add_u32 m0,s47,49152\n"
DT "a[242:243],v83 offset:17408\n"
MF "v[164:167],a[180:183],v[32:35],v[164:167]\n"
"s_movk_i32 s67,896\n"
DT "a[244:245],v83 offset:16928\n"
MF "v[228:231],a[180:183],v[36:39],v[228:231]\n"
DT "a[246:247],v83 offset:17440\n"
"buffer_load_dword v88,s[8:11],s67 offen lds\n"
MF "v[168:171],a[184:187],v[32:35],v[168:171]\n"
DT "a[248:249],v83 offset:16960\n"
MF "v[232:235],a[184:187],v[36:39],v[232:235]\n"
DT "a[250:251],v83 offset:17472\n"
MF "v[172:175],a[188:191],v[32:35],v[172:175]\n"
DT "a[252:253],v83 offset:16992\n"
MF "v[236:239],a[188:191],v[36:39],v[236:239]\n"
WT "lgkmcnt(8)\n"
DT "a[254:255],v83 offset:17504\n"
MF "v[112:115],a[192:195],v[44:47],v[112:115]\n"
WT "vmcnt(8)\n"
"s_barrier\n"
MF "v[176:179],a[192:195],v[48:51],v[176:179]\n"
"s_add_u32 m0,s46,0\n"
DR "v[32:35],v98 offset:31232\n"
MF "v[240:243],a[192:195],v[52:55],v[240:243]\n"
DR "v[36:39],v98 offset:33280\n"
"v_add_u32 v89,3670016,v86\n"
MF "v[116:119],a[196:199],v[44:47],v[116:119]\n"
DR "v[40:43],v99 offset:30720\n"
BL "v89,s[20:23],0 offen offset:0 lds\n"
MF "v[180:183],a[196:199],v[48:51],v[180:183]\n"
DT "a[128:129],v80 offset:33792\n"
MF "v[244:247],a[196:199],v[52:55],v[244:247]\n"
DT "a[130:131],v80 offset:34304\n"
MF "v[120:123],a[200:203],v[44:47],v[120:123]\n"
DT "a[132:133],v80 offset:33824\n"
MF "v[184:187],a[200:203],v[48:51],v[184:187]\n"
DT "a[134:135],v80 offset:34336\n"
BL "v89,s[20:23],s42 offen offset:1056 lds\n"
MF "v[248:251],a[200:203],v[52:55],v[248:251]\n"
DT "a[136:137],v80 offset:33856\n"
MF "v[124:127],a[204:207],v[44:47],v[124:127]\n"
WT "lgkmcnt(8)\n"
DT "a[138:139],v80 offset:34368\n"
MF "v[188:191],a[204:207],v[48:51],v[188:191]\n"
DT "a[140:141],v80 offset:33888\n"
MF "v[252:255],a[204:207],v[52:55],v[252:255]\n"
DT "a[142:143],v80 offset:34400\n"
MF "v[128:131],a[208:211],v[44:47],v[128:131]\n"
DT "a[144:145],v81 offset:33792\n"
BL "v89,s[20:23],s43 offen offset:2112 lds\n"
MF "v[192:195],a[208:211],v[48:51],v[192:195]\n"
DT "a[146:147],v81 offset:34304\n"
MF "v[132:135],a[212:215],v[44:47],v[132:135]\n"
DT "a[148:149],v81 offset:33824\n"
MF "v[196:199],a[212:215],v[48:51],v[196:199]\n"
DT "a[150:151],v81 offset:34336\n"
MF "v[136:139],a[216:219],v[44:47],v[136:139]\n"
DT "a[152:153],v81 offset:33856\n"
MF "v[200:203],a[216:219],v[48:51],v[200:203]\n"
DT "a[154:155],v81 offset:34368\n"
BL "v89,s[20:23],s44 offen offset:3168 lds\n"
MF "v[140:143],a[220:223],v[44:47],v[140:143]\n"
DT "a[156:157],v81 offset:33888\n"
MF "v[204:207],a[220:223],v[48:51],v[204:207]\n"
"s_add_u32 m0,s47,49664\n"
DT "a[158:159],v81 offset:34400\n"
MF "v[144:147],a[224:227],v[44:47],v[144:147]\n"
"s_movk_i32 s67,17280\n"
DT "a[160:161],v82 offset:33792\n"
MF "v[208:211],a[224:227],v[48:51],v[208:211]\n"
DT "a[162:163],v82 offset:34304\n"
BL "v87,s[8:11],s67 offen offset:2048 lds\n"
MF "v[148:151],a[228:231],v[44:47],v[148:151]\n"
DT "a[164:165],v82 offset:33824\n"
MF "v[212:215],a[228:231],v[48:51],v[212:215]\n"
DT "a[166:167],v82 offset:34336\n"
MF "v[152:155],a[232:235],v[44:47],v[152:155]\n"
DT "a[168:169],v82 offset:33856\n"
MF "v[216:219],a[232:235],v[48:51],v[216:219]\n"
DT "a[170:171],v82 offset:34368\n"
MF "v[156:159],a[236:239],v[44:47],v[156:159]\n"
DT "a[172:173],v82 offset:33888\n"
BL "v90,s[8:11],s67 offen offset:3072 lds\n"
MF "v[220:223],a[236:239],v[48:51],v[220:223]\n"
DT "a[174:175],v82 offset:34400\n"
MF "v[160:163],a[240:243],v[44:47],v[160:163]\n"
DT "a[176:177],v83 offset:33792\n"
MF "v[224:227],a[240:243],v[48:51],v[224:227]\n"
"s_add_u32 m0,s47,49408\n"
DT "a[178:179],v83 offset:34304\n"
MF "v[164:167],a[244:247],v[44:47],v[164:167]\n"
"s_movk_i32 s67,896\n"
DT "a[180:181],v83 offset:33824\n"
MF "v[228:231],a[244:247],v[48:51],v[228:231]\n"
DT "a[182:183],v83 offset:34336\n"
"buffer_load_dword v91,s[8:11],s67 offen lds\n"
MF "v[168:171],a[248:251],v[44:47],v[168:171]\n"
DT "a[184:185],v83 offset:33856\n"
MF "v[232:235],a[248:251],v[48:51],v[232:235]\n"
DT "a[186:187],v83 offset:34368\n"
MF "v[172:175],a[252:255],v[44:47],v[172:175]\n"
DT "a[188:189],v83 offset:33888\n"
MF "v[236:239],a[252:255],v[48:51],v[236:239]\n"
WT "lgkmcnt(8)\n"
DT "a[190:191],v83 offset:34400\n"
MF "v[112:115],a[128:131],v[32:35],v[112:115]\n"
WT "vmcnt(10)\n"
"s_barrier\n"
MF "v[176:179],a[128:131],v[36:39],v[176:179]\n"
"s_add_u32 m0,s46,16896\n"
DR "v[44:47],v102 offset:31232\n"
MF "v[240:243],a[128:131],v[40:43],v[240:243]\n"
DR "v[48:51],v102 offset:33280\n"
"v_add_u32 v89,3932160,v86\n"
MF "v[116:119],a[132:135],v[32:35],v[116:119]\n"
DR "v[52:55],v103 offset:30720\n"
BL "v89,s[20:23],0 offen offset:0 lds\n"
MF "v[180:183],a[132:135],v[36:39],v[180:183]\n"
DT "a[192:193],v80 offset:50688\n"
MF "v[244:247],a[132:135],v[40:43],v[244:247]\n"
DT "a[194:195],v80 offset:51200\n"
MF "v[120:123],a[136:139],v[32:35],v[120:123]\n"
DT "a[196:197],v80 offset:50720\n"
MF "v[184:187],a[136:139],v[36:39],v[184:187]\n"
DT "a[198:199],v80 offset:51232\n"
BL "v89,s[20:23],s42 offen offset:1056 lds\n"
MF "v[248:251],a[136:139],v[40:43],v[248:251]\n"
DT "a[200:201],v80 offset:50752\n"
MF "v[124:127],a[140:143],v[32:35],v[124:127]\n"
WT "lgkmcnt(8)\n"
DT "a[202:203],v80 offset:51264\n"
MF "v[188:191],a[140:143],v[36:39],v[188:191]\n"
DT "a[204:205],v80 offset:50784\n"
MF "v[252:255],a[140:143],v[40:43],v[252:255]\n"
DT "a[206:207],v80 offset:51296\n"
BL "v89,s[20:23],s43 offen offset:2112 lds\n"
MF "v[128:131],a[144:147],v[32:35],v[128:131]\n"
DT "a[208:209],v81 offset:50688\n"
MF "v[192:195],a[144:147],v[36:39],v[192:195]\n"
DT "a[210:211],v81 offset:51200\n"
MF "v[132:135],a[148:151],v[32:35],v[132:135]\n"
DT "a[212:213],v81 offset:50720\n"
MF "v[196:199],a[148:151],v[36:39],v[196:199]\n"
DT "a[214:215],v81 offset:51232\n"
BL "v89,s[20:23],s44 offen offset:3168 lds\n"
MF "v[136:139],a[152:155],v[32:35],v[136:139]\n"
DT "a[216:217],v81 offset:50752\n"
MF "v[200:203],a[152:155],v[36:39],v[200:203]\n"
"s_add_u32 m0,s47,135680\n"
DT "a[218:219],v81 offset:51264\n"
MF "v[140:143],a[156:159],v[32:35],v[140:143]\n"
"s_movk_i32 s67,1024\n"
DT "a[220:221],v81 offset:50784\n"
MF "v[204:207],a[156:159],v[36:39],v[204:207]\n"
DT "a[222:223],v81 offset:51296\n"
BL "v87,s[8:11],s67 offen offset:0 lds\n"
MF "v[144:147],a[160:163],v[32:35],v[144:147]\n"
DT "a[224:225],v82 offset:50688\n"
MF "v[208:211],a[160:163],v[36:39],v[208:211]\n"
DT "a[226:227],v82 offset:51200\n"
MF "v[148:151],a[164:167],v[32:35],v[148:151]\n"
DT "a[228:229],v82 offset:50720\n"
MF "v[212:215],a[164:167],v[36:39],v[212:215]\n"
DT "a[230:231],v82 offset:51232\n"
BL "v90,s[8:11],s67 offen offset:1024 lds\n"
MF "v[152:155],a[168:171],v[32:35],v[152:155]\n"
DT "a[232:233],v82 offset:50752\n"
MF "v[216:219],a[168:171],v[36:39],v[216:219]\n"
"s_add_u32 m0,s47,135168\n"
DT "a[234:235],v82 offset:51264\n"
MF "v[156:159],a[172:175],v[32:35],v[156:159]\n"
"s_movk_i32 s67,1024\n"
DT "a[236:237],v82 offset:50784\n"
MF "v[220:223],a[172:175],v[36:39],v[220:223]\n"
DT "a[238:239],v82 offset:51296\n"
"buffer_load_dword v88,s[8:11],s67 offen lds\n"
MF "v[160:163],a[176:179],v[32:35],v[160:163]\n"
DT "a[240:241],v83 offset:50688\n"
MF "v[224:227],a[176:179],v[36:39],v[224:227]\n"
DT "a[242:243],v83 offset:51200\n"
MF "v[164:167],a[180:183],v[32:35],v[164:167]\n"
DT "a[244:245],v83 offset:50720\n"
MF "v[228:231],a[180:183],v[36:39],v[228:231]\n"
DT "a[246:247],v83 offset:51232\n"
"buffer_load_dword v95,v97,s[24:27],0 offen offset:128\n"
MF "v[168:171],a[184:187],v[32:35],v[168:171]\n"
DT "a[248:249],v83 offset:50752\n"
MF "v[232:235],a[184:187],v[36:39],v[232:235]\n"
DT "a[250:251],v83 offset:51264\n"
MF "v[172:175],a[188:191],v[32:35],v[172:175]\n"
DT "a[252:253],v83 offset:50784\n"
MF "v[236:239],a[188:191],v[36:39],v[236:239]\n"
WT "lgkmcnt(8)\n"
DT "a[254:255],v83 offset:51296\n"
MF "v[112:115],a[192:195],v[44:47],v[112:115]\n"
WT "vmcnt(8)\n"
"s_barrier\n"
MF "v[176:179],a[192:195],v[48:51],v[176:179]\n"
"s_add_u32 m0,s46,33792\n"
DR "v[32:35],v98 offset:49664\n"
MF "v[240:243],a[192:195],v[52:55],v[240:243]\n"
DR "v[36:39],v98 offset:51712\n"
"v_add_u32 v89,4194304,v86\n"
MF "v[116:119],a[196:199],v[44:47],v[116:119]\n"
DR "v[40:43],v99 offset:49152\n"
BL "v89,s[20:23],0 offen offset:0 lds\n"
MF "v[180:183],a[196:199],v[48:51],v[180:183]\n"
DT "a[128:129],v80 offset:0\n"
MF "v[244:247],a[196:199],v[52:55],v[244:247]\n"
DT "a[130:131],v80 offset:512\n"
MF "v[120:123],a[200:203],v[44:47],v[120:123]\n"
DT "a[132:133],v80 offset:32\n"
MF "v[184:187],a[200:203],v[48:51],v[184:187]\n"
DT "a[134:135],v80 offset:544\n"
BL "v89,s[20:23],s42 offen offset:1056 lds\n"
MF "v[248:251],a[200:203],v[52:55],v[248:251]\n"
DT "a[136:137],v80 offset:64\n"
MF "v[124:127],a[204:207],v[44:47],v[124:127]\n"
WT "lgkmcnt(8)\n"
DT "a[138:139],v80 offset:576\n"
MF "v[188:191],a[204:207],v[48:51],v[188:191]\n"
DT "a[140:141],v80 offset:96\n"
MF "v[252:255],a[204:207],v[52:55],v[252:255]\n"
DT "a[142:143],v80 offset:608\n"
MF "v[128:131],a[208:211],v[44:47],v[128:131]\n"
DT "a[144:145],v81 offset:0\n"
BL "v89,s[20:23],s43 offen offset:2112 lds\n"
MF "v[192:195],a[208:211],v[48:51],v[192:195]\n"
DT "a[146:147],v81 offset:512\n"
MF "v[132:135],a[212:215],v[44:47],v[132:135]\n"
DT "a[148:149],v81 offset:32\n"
MF "v[196:199],a[212:215],v[48:51],v[196:199]\n"
DT "a[150:151],v81 offset:544\n"
MF "v[136:139],a[216:219],v[44:47],v[136:139]\n"
DT "a[152:153],v81 offset:64\n"
MF "v[200:203],a[216:219],v[48:51],v[200:203]\n"
DT "a[154:155],v81 offset:576\n"
BL "v89,s[20:23],s44 offen offset:3168 lds\n"
MF "v[140:143],a[220:223],v[44:47],v[140:143]\n"
DT "a[156:157],v81 offset:96\n"
MF "v[204:207],a[220:223],v[48:51],v[204:207]\n"
"s_add_u32 m0,s47,135680\n"
DT "a[158:159],v81 offset:608\n"
MF "v[144:147],a[224:227],v[44:47],v[144:147]\n"
"s_movk_i32 s67,17408\n"
DT "a[160:161],v82 offset:0\n"
MF "v[208:211],a[224:227],v[48:51],v[208:211]\n"
DT "a[162:163],v82 offset:512\n"
BL "v87,s[8:11],s67 offen offset:2048 lds\n"
MF "v[148:151],a[228:231],v[44:47],v[148:151]\n"
DT "a[164:165],v82 offset:32\n"
MF "v[212:215],a[228:231],v[48:51],v[212:215]\n"
DT "a[166:167],v82 offset:544\n"
MF "v[152:155],a[232:235],v[44:47],v[152:155]\n"
DT "a[168:169],v82 offset:64\n"
MF "v[216:219],a[232:235],v[48:51],v[216:219]\n"
DT "a[170:171],v82 offset:576\n"
MF "v[156:159],a[236:239],v[44:47],v[156:159]\n"
DT "a[172:173],v82 offset:96\n"
BL "v90,s[8:11],s67 offen offset:3072 lds\n"
MF "v[220:223],a[236:239],v[48:51],v[220:223]\n"
DT "a[174:175],v82 offset:608\n"
MF "v[160:163],a[240:243],v[44:47],v[160:163]\n"
DT "a[176:177],v83 offset:0\n"
MF "v[224:227],a[240:243],v[48:51],v[224:227]\n"
"s_add_u32 m0,s47,135424\n"
DT "a[178:179],v83 offset:512\n"
MF "v[164:167],a[244:247],v[44:47],v[164:167]\n"
"s_movk_i32 s67,1024\n"
DT "a[180:181],v83 offset:32\n"
MF "v[228:231],a[244:247],v[48:51],v[228:231]\n"
DT "a[182:183],v83 offset:544\n"
"buffer_load_dword v91,s[8:11],s67 offen lds\n"
MF "v[168:171],a[248:251],v[44:47],v[168:171]\n"
DT "a[184:185],v83 offset:64\n"
MF "v[232:235],a[248:251],v[48:51],v[232:235]\n"
DT "a[186:187],v83 offset:576\n"
MF "v[172:175],a[252:255],v[44:47],v[172:175]\n"
DT "a[188:189],v83 offset:96\n"
MF "v[236:239],a[252:255],v[48:51],v[236:239]\n"
WT "lgkmcnt(8)\n"
DT "a[190:191],v83 offset:608\n"
MF "v[112:115],a[128:131],v[32:35],v[112:115]\n"
WT "vmcnt(11)\n"
"s_barrier\n"
MF "v[176:179],a[128:131],v[36:39],v[176:179]\n"
"s_add_u32 m0,s46,50688\n"
DR "v[44:47],v102 offset:49664\n"
MF "v[240:243],a[128:131],v[40:43],v[240:243]\n"
DR "v[48:51],v102 offset:51712\n"
"v_add_u32 v89,4456448,v86\n"
MF "v[116:119],a[132:135],v[32:35],v[116:119]\n"
DR "v[52:55],v103 offset:49152\n"
BL "v89,s[20:23],0 offen offset:0 lds\n"
MF "v[180:183],a[132:135],v[36:39],v[180:183]\n"
DT "a[192:193],v80 offset:16896\n"
MF "v[244:247],a[132:135],v[40:43],v[244:247]\n"
DT "a[194:195],v80 offset:17408\n"
MF "v[120:123],a[136:139],v[32:35],v[120:123]\n"
DT "a[196:197],v80 offset:16928\n"
MF "v[184:187],a[136:139],v[36:39],v[184:187]\n"
DT "a[198:199],v80 offset:17440\n"
MF "v[248:251],a[136:139],v[40:43],v[248:251]\n"
DT "a[200:201],v80 offset:16960\n"
MF "v[124:127],a[140:143],v[32:35],v[124:127]\n"
WT "lgkmcnt(8)\n"
DT "a[202:203],v80 offset:17472\n"
MF "v[188:191],a[140:143],v[36:39],v[188:191]\n"
DT "a[204:205],v80 offset:16992\n"
MF "v[252:255],a[140:143],v[40:43],v[252:255]\n"
DT "a[206:207],v80 offset:17504\n"
BL "v89,s[20:23],s42 offen offset:1056 lds\n"
MF "v[128:131],a[144:147],v[32:35],v[128:131]\n"
DT "a[208:209],v81 offset:16896\n"
MF "v[192:195],a[144:147],v[36:39],v[192:195]\n"
DT "a[210:211],v81 offset:17408\n"
MF "v[132:135],a[148:151],v[32:35],v[132:135]\n"
DT "a[212:213],v81 offset:16928\n"
MF "v[196:199],a[148:151],v[36:39],v[196:199]\n"
DT "a[214:215],v81 offset:17440\n"
MF "v[136:139],a[152:155],v[32:35],v[136:139]\n"
DT "a[216:217],v81 offset:16960\n"
MF "v[200:203],a[152:155],v[36:39],v[200:203]\n"
DT "a[218:219],v81 offset:17472\n"
MF "v[140:143],a[156:159],v[32:35],v[140:143]\n"
DT "a[220:221],v81 offset:16992\n"
MF "v[204:207],a[156:159],v[36:39],v[204:207]\n"
DT "a[222:223],v81 offset:17504\n"
BL "v89,s[20:23],s43 offen offset:2112 lds\n"
MF "v[144:147],a[160:163],v[32:35],v[144:147]\n"
DT "a[224:225],v82 offset:16896\n"
MF "v[208:211],a[160:163],v[36:39],v[208:211]\n"
DT "a[226:227],v82 offset:17408\n"
MF "v[148:151],a[164:167],v[32:35],v[148:151]\n"
DT "a[228:229],v82 offset:16928\n"
MF "v[212:215],a[164:167],v[36:39],v[212:215]\n"
DT "a[230:231],v82 offset:17440\n"
MF "v[152:155],a[168:171],v[32:35],v[152:155]\n"
DT "a[232:233],v82 offset:16960\n"
MF "v[216:219],a[168:171],v[36:39],v[216:219]\n"
DT "a[234:235],v82 offset:17472\n"
MF "v[156:159],a[172:175],v[32:35],v[156:159]\n"
DT "a[236:237],v82 offset:16992\n"
MF "v[220:223],a[172:175],v[36:39],v[220:223]\n"
DT "a[238:239],v82 offset:17504\n"
BL "v89,s[20:23],s44 offen offset:3168 lds\n"
MF "v[160:163],a[176:179],v[32:35],v[160:163]\n"
DT "a[240:241],v83 offset:16896\n"
MF "v[224:227],a[176:179],v[36:39],v[224:227]\n"
DT "a[242:243],v83 offset:17408\n"
MF "v[164:167],a[180:183],v[32:35],v[164:167]\n"
DT "a[244:245],v83 offset:16928\n"
MF "v[228:231],a[180:183],v[36:39],v[228:231]\n"
DT "a[246:247],v83 offset:17440\n"
MF "v[168:171],a[184:187],v[32:35],v[168:171]\n"
DT "a[248:249],v83 offset:16960\n"
MF "v[232:235],a[184:187],v[36:39],v[232:235]\n"
DT "a[250:251],v83 offset:17472\n"
MF "v[172:175],a[188:191],v[32:35],v[172:175]\n"
DT "a[252:253],v83 offset:16992\n"
MF "v[236:239],a[188:191],v[36:39],v[236:239]\n"
WT "lgkmcnt(8)\n"
DT "a[254:255],v83 offset:17504\n"
MF "v[112:115],a[192:195],v[44:47],v[112:115]\n"
WT "vmcnt(4)\n"
"s_barrier\n"
MF "v[176:179],a[192:195],v[48:51],v[176:179]\n"
DR "v[32:35],v84 offset:512\n"
MF "v[240:243],a[192:195],v[52:55],v[240:243]\n"
"s_add_u32 m0,s48,0\n"
DR "v[36:39],v84 offset:2560\n"
MF "v[116:119],a[196:199],v[44:47],v[116:119]\n"
DR "v[40:43],v85 offset:0\n"
BL "v8,s[12:15],s78 offen nt lds\n"
MF "v[180:183],a[196:199],v[48:51],v[180:183]\n"
DT "a[128:129],v80 offset:33792\n"
MF "v[244:247],a[196:199],v[52:55],v[244:247]\n"
DT "a[130:131],v80 offset:34304\n"
MF "v[120:123],a[200:203],v[44:47],v[120:123]\n"
DT "a[132:133],v80 offset:33824\n"
MF "v[184:187],a[200:203],v[48:51],v[184:187]\n"
DT "a[134:135],v80 offset:34336\n"
MF "v[248:251],a[200:203],v[52:55],v[248:251]\n"
DT "a[136:137],v80 offset:33856\n"
BL "v9,s[12:15],s78 offen offset:1056 nt lds\n"
MF "v[124:127],a[204:207],v[44:47],v[124:127]\n"
WT "lgkmcnt(8)\n"
DT "a[138:139],v80 offset:34368\n"
MF "v[188:191],a[204:207],v[48:51],v[188:191]\n"
"s_add_u32 s51,s51,8192\n"
DT "a[140:141],v80 offset:33888\n"
MF "v[252:255],a[204:207],v[52:55],v[252:255]\n"
"s_mov_b32 s79,s78\n"
DT "a[142:143],v80 offset:34400\n"
MF "v[128:131],a[208:211],v[44:47],v[128:131]\n"
"s_xor_b32 s78,s51,s41\n"
DT "a[144:145],v81 offset:33792\n"
MF "v[192:195],a[208:211],v[48:51],v[192:195]\n"
"s_add_u32 m0,s48,16896\n"
DT "a[146:147],v81 offset:34304\n"
MF "v[132:135],a[212:215],v[44:47],v[132:135]\n"
DT "a[148:149],v81 offset:33824\n"
BL "v8,s[12:15],s78 offen nt lds\n"
MF "v[196:199],a[212:215],v[48:51],v[196:199]\n"
DT "a[150:151],v81 offset:34336\n"
MF "v[136:139],a[216:219],v[44:47],v[136:139]\n"
DT "a[152:153],v81 offset:33856\n"
MF "v[200:203],a[216:219],v[48:51],v[200:203]\n"
DT "a[154:155],v81 offset:34368\n"
MF "v[140:143],a[220:223],v[44:47],v[140:143]\n"
DT "a[156:157],v81 offset:33888\n"
MF "v[204:207],a[220:223],v[48:51],v[204:207]\n"
DT "a[158:159],v81 offset:34400\n"
BL "v9,s[12:15],s78 offen offset:1056 nt lds\n"
MF "v[144:147],a[224:227],v[44:47],v[144:147]\n"
DT "a[160:161],v82 offset:33792\n"
MF "v[208:211],a[224:227],v[48:51],v[208:211]\n"
DT "a[162:163],v82 offset:34304\n"
MF "v[148:151],a[228:231],v[44:47],v[148:151]\n"
DT "a[164:165],v82 offset:33824\n"
MF "v[212:215],a[228:231],v[48:51],v[212:215]\n"
DT "a[166:167],v82 offset:34336\n"
MF "v[152:155],a[232:235],v[44:47],v[152:155]\n"
"s_add_u32 m0,s48,25344\n"
DT "a[168:169],v82 offset:33856\n"
MF "v[216:219],a[232:235],v[48:51],v[216:219]\n"
DT "a[170:171],v82 offset:34368\n"
BL "v10,s[16:19],s79 offen nt lds\n"
MF "v[156:159],a[236:239],v[44:47],v[156:159]\n"
DT "a[172:173],v82 offset:33888\n"
MF "v[220:223],a[236:239],v[48:51],v[220:223]\n"
DT "a[174:175],v82 offset:34400\n"
MF "v[160:163],a[240:243],v[44:47],v[160:163]\n"
DT "a[176:177],v83 offset:33792\n"
MF "v[224:227],a[240:243],v[48:51],v[224:227]\n"
DT "a[178:179],v83 offset:34304\n"
MF "v[164:167],a[244:247],v[44:47],v[164:167]\n"
DT "a[180:181],v83 offset:33824\n"
BL "v11,s[16:19],s79 offen offset:1056 nt lds\n"
MF "v[228:231],a[244:247],v[48:51],v[228:231]\n"
"s_add_u32 s51,s51,8192\n"
"s_mov_b32 s79,s78\n"
"s_xor_b32 s78,s51,s41\n"
DT "a[182:183],v83 offset:34336\n"
MF "v[168:171],a[248:251],v[44:47],v[168:171]\n"
DT "a[184:185],v83 offset:33856\n"
MF "v[232:235],a[248:251],v[48:51],v[232:235]\n"
DT "a[186:187],v83 offset:34368\n"
MF "v[172:175],a[252:255],v[44:47],v[172:175]\n"
DT "a[188:189],v83 offset:33888\n"
MF "v[236:239],a[252:255],v[48:51],v[236:239]\n"
WT "lgkmcnt(8)\n"
DT "a[190:191],v83 offset:34400\n"
MF "v[112:115],a[128:131],v[32:35],v[112:115]\n"
WT "vmcnt(6)\n"
"s_barrier\n"
MF "v[176:179],a[128:131],v[36:39],v[176:179]\n"
DR "v[44:47],v100 offset:512\n"
MF "v[240:243],a[128:131],v[40:43],v[240:243]\n"
"s_add_u32 m0,s48,33792\n"
DR "v[48:51],v100 offset:2560\n"
MF "v[116:119],a[132:135],v[32:35],v[116:119]\n"
DR "v[52:55],v101 offset:0\n"
BL "v8,s[12:15],s78 offen nt lds\n"
MF "v[180:183],a[132:135],v[36:39],v[180:183]\n"
DT "a[192:193],v80 offset:50688\n"
MF "v[244:247],a[132:135],v[40:43],v[244:247]\n"
DT "a[194:195],v80 offset:51200\n"
MF "v[120:123],a[136:139],v[32:35],v[120:123]\n"
DT "a[196:197],v80 offset:50720\n"
MF "v[184:187],a[136:139],v[36:39],v[184:187]\n"
DT "a[198:199],v80 offset:51232\n"
MF "v[248:251],a[136:139],v[40:43],v[248:251]\n"
DT "a[200:201],v80 offset:50752\n"
BL "v9,s[12:15],s78 offen offset:1056 nt lds\n"
MF "v[124:127],a[140:143],v[32:35],v[124:127]\n"
WT "lgkmcnt(8)\n"
DT "a[202:203],v80 offset:51264\n"
MF "v[188:191],a[140:143],v[36:39],v[188:191]\n"
DT "a[204:205],v80 offset:50784\n"
MF "v[252:255],a[140:143],v[40:43],v[252:255]\n"
DT "a[206:207],v80 offset:51296\n"
MF "v[128:131],a[144:147],v[32:35],v[128:131]\n"
DT "a[208:209],v81 offset:50688\n"
MF "v[192:195],a[144:147],v[36:39],v[192:195]\n"
"s_add_u32 m0,s48,42240\n"
DT "a[210:211],v81 offset:51200\n"
MF "v[132:135],a[148:151],v[32:35],v[132:135]\n"
DT "a[212:213],v81 offset:50720\n"
BL "v10,s[16:19],s79 offen nt lds\n"
MF "v[196:199],a[148:151],v[36:39],v[196:199]\n"
DT "a[214:215],v81 offset:51232\n"
MF "v[136:139],a[152:155],v[32:35],v[136:139]\n"
DT "a[216:217],v81 offset:50752\n"
MF "v[200:203],a[152:155],v[36:39],v[200:203]\n"
DT "a[218:219],v81 offset:51264\n"
MF "v[140:143],a[156:159],v[32:35],v[140:143]\n"
DT "a[220:221],v81 offset:50784\n"
MF "v[204:207],a[156:159],v[36:39],v[204:207]\n"
DT "a[222:223],v81 offset:51296\n"
BL "v11,s[16:19],s79 offen offset:1056 nt lds\n"
MF "v[144:147],a[160:163],v[32:35],v[144:147]\n"
DT "a[224:225],v82 offset:50688\n"
MF "v[208:211],a[160:163],v[36:39],v[208:211]\n"
"s_add_u32 s51,s51,8192\n"
DT "a[226:227],v82 offset:51200\n"
MF "v[148:151],a[164:167],v[32:35],v[148:151]\n"
"s_mov_b32 s79,s78\n"
DT "a[228:229],v82 offset:50720\n"
MF "v[212:215],a[164:167],v[36:39],v[212:215]\n"
"s_xor_b32 s78,s51,s41\n"
DT "a[230:231],v82 offset:51232\n"
MF "v[152:155],a[168:171],v[32:35],v[152:155]\n"
"s_add_u32 m0,s48,50688\n"
DT "a[232:233],v82 offset:50752\n"
MF "v[216:219],a[168:171],v[36:39],v[216:219]\n"
DT "a[234:235],v82 offset:51264\n"
BL "v8,s[12:15],s78 offen nt lds\n"
MF "v[156:159],a[172:175],v[32:35],v[156:159]\n"
DT "a[236:237],v82 offset:50784\n"
MF "v[220:223],a[172:175],v[36:39],v[220:223]\n"
DT "a[238:239],v82 offset:51296\n"
MF "v[160:163],a[176:179],v[32:35],v[160:163]\n"
DT "a[240:241],v83 offset:50688\n"
MF "v[224:227],a[176:179],v[36:39],v[224:227]\n"
DT "a[242:243],v83 offset:51200\n"
MF "v[164:167],a[180:183],v[32:35],v[164:167]\n"
DT "a[244:245],v83 offset:50720\n"
BL "v9,s[12:15],s78 offen offset:1056 nt lds\n"
MF "v[228:231],a[180:183],v[36:39],v[228:231]\n"
DT "a[246:247],v83 offset:51232\n"
MF "v[168:171],a[184:187],v[32:35],v[168:171]\n"
DT "a[248:249],v83 offset:50752\n"
MF "v[232:235],a[184:187],v[36:39],v[232:235]\n"
DT "a[250:251],v83 offset:51264\n"
MF "v[172:175],a[188:191],v[32:35],v[172:175]\n"
DT "a[252:253],v83 offset:50784\n"
MF "v[236:239],a[188:191],v[36:39],v[236:239]\n"
WT "lgkmcnt(8)\n"
DT "a[254:255],v83 offset:51296\n"
MF "v[112:115],a[192:195],v[44:47],v[112:115]\n"
MF "v[176:179],a[192:195],v[48:51],v[176:179]\n"
MF "v[240:243],a[192:195],v[52:55],v[240:243]\n"
"s_add_u32 m0,s48,59136\n"
MF "v[116:119],a[196:199],v[44:47],v[116:119]\n"
BL "v10,s[16:19],s79 offen nt lds\n"
MF "v[180:183],a[196:199],v[48:51],v[180:183]\n"
MF "v[244:247],a[196:199],v[52:55],v[244:247]\n"
MF "v[120:123],a[200:203],v[44:47],v[120:123]\n"
MF "v[184:187],a[200:203],v[48:51],v[184:187]\n"
MF "v[248:251],a[200:203],v[52:55],v[248:251]\n"
BL "v11,s[16:19],s79 offen offset:1056 nt lds\n"
MF "v[124:127],a[204:207],v[44:47],v[124:127]\n"
WT "lgkmcnt(0)\n"
MF "v[188:191],a[204:207],v[48:51],v[188:191]\n"
"s_add_u32 s51,s51,8192\n"
MF "v[252:255],a[204:207],v[52:55],v[252:255]\n"
"s_mov_b32 s79,s78\n"
MF "v[128:131],a[208:211],v[44:47],v[128:131]\n"
"s_xor_b32 s78,s51,s41\n"
MF "v[192:195],a[208:211],v[48:51],v[192:195]\n"
"s_add_u32 m0,s48,67584\n"
MF "v[132:135],a[212:215],v[44:47],v[132:135]\n"
BL "v8,s[12:15],s78 offen nt lds\n"
MF "v[196:199],a[212:215],v[48:51],v[196:199]\n"
MF "v[136:139],a[216:219],v[44:47],v[136:139]\n"
MF "v[200:203],a[216:219],v[48:51],v[200:203]\n"
MF "v[140:143],a[220:223],v[44:47],v[140:143]\n"
MF "v[204:207],a[220:223],v[48:51],v[204:207]\n"
BL "v9,s[12:15],s78 offen offset:1056 nt lds\n"
MF "v[144:147],a[224:227],v[44:47],v[144:147]\n"
MF "v[208:211],a[224:227],v[48:51],v[208:211]\n"
MF "v[148:151],a[228:231],v[44:47],v[148:151]\n"
MF "v[212:215],a[228:231],v[48:51],v[212:215]\n"
MF "v[152:155],a[232:235],v[44:47],v[152:155]\n"
"s_add_u32 m0,s48,76032\n"
MF "v[216:219],a[232:235],v[48:51],v[216:219]\n"
BL "v10,s[16:19],s79 offen nt lds\n"
MF "v[156:159],a[236:239],v[44:47],v[156:159]\n"
MF "v[220:223],a[236:239],v[48:51],v[220:223]\n"
MF "v[160:163],a[240:243],v[44:47],v[160:163]\n"
MF "v[224:227],a[240:243],v[48:51],v[224:227]\n"
MF "v[164:167],a[244:247],v[44:47],v[164:167]\n"
BL "v11,s[16:19],s79 offen offset:1056 nt lds\n"
MF "v[228:231],a[244:247],v[48:51],v[228:231]\n"
"s_add_u32 s51,s51,8192\n"
"s_mov_b32 s79,s78\n"
"s_xor_b32 s78,s51,s41\n"
MF "v[168:171],a[248:251],v[44:47],v[168:171]\n"
MF "v[232:235],a[248:251],v[48:51],v[232:235]\n"
MF "v[172:175],a[252:255],v[44:47],v[172:175]\n"
MF "v[236:239],a[252:255],v[48:51],v[236:239]\n"
WT "lgkmcnt(8)\n"
"s_lshl_b32 s66,s36,11\n"
"s_add_u32 s66,s66,84480\n"
"v_add_u32 v2,s66,v1\n"
"s_mov_b32 s42,0\n"
PM "v[240:241],v[240:241],s[54:55]\n"
PM "v[242:243],v[242:243],s[54:55]\n"
PM "v[244:245],v[244:245],s[54:55]\n"
PM "v[246:247],v[246:247],s[54:55]\n"
CV "v96,v240,v241\n"
CV "v98,v244,v245\n"
CV "v97,v242,v243\n"
CV "v99,v246,v247\n"
PM "v[248:249],v[248:249],s[54:55]\n"
PM "v[250:251],v[250:251],s[54:55]\n"
PM "v[252:253],v[252:253],s[54:55]\n"
PM "v[254:255],v[254:255],s[54:55]\n"
CV "v100,v248,v249\n"
CV "v102,v252,v253\n"
CV "v101,v250,v251\n"
CV "v103,v254,v255\n"
"v_permlane16_swap_b32 v96,v98\n"
"v_permlane16_swap_b32 v97,v99\n"
"v_permlane16_swap_b32 v100,v102\n"
"v_permlane16_swap_b32 v101,v103\n"
"ds_write_b128 v2,v[96:99]\n"
"ds_write_b128 v2,v[100:103] offset:1024\n"
"s_cmp_eq_u32 s36,1\n"
"s_cbranch_scc1 Lw4_%=\n"
"s_cmp_eq_u32 s36,2\n"
"s_cbranch_scc1 Lw5_%=\n"
"s_cmp_eq_u32 s36,3\n"
"s_cbranch_scc1 Lw6_%=\n"
"Lw3_%=:\n"
PM "v[112:113],v[112:113],s[54:55]\n"
PM "v[114:115],v[114:115],s[54:55]\n"
PM "v[116:117],v[116:117],s[54:55]\n"
PM "v[118:119],v[118:119],s[54:55]\n"
CV "v32,v112,v113\n"
CV "v34,v116,v117\n"
CV "v33,v114,v115\n"
CV "v35,v118,v119\n"
PM "v[176:177],v[176:177],s[54:55]\n"
PM "v[178:179],v[178:179],s[54:55]\n"
PM "v[180:181],v[180:181],s[54:55]\n"
PM "v[182:183],v[182:183],s[54:55]\n"
CV "v48,v176,v177\n"
CV "v50,v180,v181\n"
CV "v49,v178,v179\n"
CV "v51,v182,v183\n"
"v_permlane16_swap_b32 v32,v34\n"
"v_permlane16_swap_b32 v33,v35\n"
PM "v[120:121],v[120:121],s[54:55]\n"
PM "v[122:123],v[122:123],s[54:55]\n"
PM "v[124:125],v[124:125],s[54:55]\n"
PM "v[126:127],v[126:127],s[54:55]\n"
CV "v36,v120,v121\n"
CV "v38,v124,v125\n"
CV "v37,v122,v123\n"
CV "v39,v126,v127\n"
"v_permlane16_swap_b32 v48,v50\n"
"v_permlane16_swap_b32 v49,v51\n"
PM "v[184:185],v[184:185],s[54:55]\n"
PM "v[186:187],v[186:187],s[54:55]\n"
PM "v[188:189],v[188:189],s[54:55]\n"
PM "v[190:191],v[190:191],s[54:55]\n"
CV "v52,v184,v185\n"
CV "v54,v188,v189\n"
CV "v53,v186,v187\n"
CV "v55,v190,v191\n"
"v_permlane16_swap_b32 v36,v38\n"
"v_permlane16_swap_b32 v37,v39\n"
PM "v[128:129],v[128:129],s[54:55]\n"
PM "v[130:131],v[130:131],s[54:55]\n"
PM "v[132:133],v[132:133],s[54:55]\n"
PM "v[134:135],v[134:135],s[54:55]\n"
CV "v40,v128,v129\n"
CV "v42,v132,v133\n"
CV "v41,v130,v131\n"
CV "v43,v134,v135\n"
"v_permlane16_swap_b32 v52,v54\n"
"v_permlane16_swap_b32 v53,v55\n"
PM "v[192:193],v[192:193],s[54:55]\n"
PM "v[194:195],v[194:195],s[54:55]\n"
PM "v[196:197],v[196:197],s[54:55]\n"
PM "v[198:199],v[198:199],s[54:55]\n"
CV "v56,v192,v193\n"
CV "v58,v196,v197\n"
CV "v57,v194,v195\n"
CV "v59,v198,v199\n"
"v_permlane16_swap_b32 v40,v42\n"
"v_permlane16_swap_b32 v41,v43\n"
PM "v[136:137],v[136:137],s[54:55]\n"
PM "v[138:139],v[138:139],s[54:55]\n"
PM "v[140:141],v[140:141],s[54:55]\n"
PM "v[142:143],v[142:143],s[54:55]\n"
CV "v44,v136,v137\n"
CV "v46,v140,v141\n"
CV "v45,v138,v139\n"
CV "v47,v142,v143\n"
"v_permlane16_swap_b32 v56,v58\n"
"v_permlane16_swap_b32 v57,v59\n"
PM "v[200:201],v[200:201],s[54:55]\n"
PM "v[202:203],v[202:203],s[54:55]\n"
PM "v[204:205],v[204:205],s[54:55]\n"
PM "v[206:207],v[206:207],s[54:55]\n"
CV "v60,v200,v201\n"
CV "v62,v204,v205\n"
CV "v61,v202,v203\n"
CV "v63,v206,v207\n"
"v_permlane16_swap_b32 v44,v46\n"
"v_permlane16_swap_b32 v45,v47\n"
PM "v[144:145],v[144:145],s[54:55]\n"
PM "v[146:147],v[146:147],s[54:55]\n"
PM "v[148:149],v[148:149],s[54:55]\n"
PM "v[150:151],v[150:151],s[54:55]\n"
CV "v104,v144,v145\n"
CV "v106,v148,v149\n"
CV "v105,v146,v147\n"
CV "v107,v150,v151\n"
"v_permlane16_swap_b32 v60,v62\n"
"v_permlane16_swap_b32 v61,v63\n"
PM "v[208:209],v[208:209],s[54:55]\n"
PM "v[210:211],v[210:211],s[54:55]\n"
PM "v[212:213],v[212:213],s[54:55]\n"
PM "v[214:215],v[214:215],s[54:55]\n"
CV "v108,v208,v209\n"
CV "v110,v212,v213\n"
CV "v109,v210,v211\n"
CV "v111,v214,v215\n"
"v_permlane16_swap_b32 v104,v106\n"
"v_permlane16_swap_b32 v105,v107\n"
"v_accvgpr_write_b32 a0,v104\n"
"v_accvgpr_write_b32 a1,v105\n"
"v_accvgpr_write_b32 a2,v106\n"
"v_accvgpr_write_b32 a3,v107\n"
PM "v[152:153],v[152:153],s[54:55]\n"
PM "v[154:155],v[154:155],s[54:55]\n"
PM "v[156:157],v[156:157],s[54:55]\n"
PM "v[158:159],v[158:159],s[54:55]\n"
CV "v104,v152,v153\n"
CV "v106,v156,v157\n"
CV "v105,v154,v155\n"
CV "v107,v158,v159\n"
"v_permlane16_swap_b32 v108,v110\n"
"v_permlane16_swap_b32 v109,v111\n"
"v_accvgpr_write_b32 a16,v108\n"
"v_accvgpr_write_b32 a17,v109\n"
"v_accvgpr_write_b32 a18,v110\n"
"v_accvgpr_write_b32 a19,v111\n"
PM "v[216:217],v[216:217],s[54:55]\n"
PM "v[218:219],v[218:219],s[54:55]\n"
PM "v[220:221],v[220:221],s[54:55]\n"
PM "v[222:223],v[222:223],s[54:55]\n"
CV "v108,v216,v217\n"
CV "v110,v220,v221\n"
CV "v109,v218,v219\n"
CV "v111,v222,v223\n"
"v_permlane16_swap_b32 v104,v106\n"
"v_permlane16_swap_b32 v105,v107\n"
"v_accvgpr_write_b32 a4,v104\n"
"v_accvgpr_write_b32 a5,v105\n"
"v_accvgpr_write_b32 a6,v106\n"
"v_accvgpr_write_b32 a7,v107\n"
PM "v[160:161],v[160:161],s[54:55]\n"
PM "v[162:163],v[162:163],s[54:55]\n"
PM "v[164:165],v[164:165],s[54:55]\n"
PM "v[166:167],v[166:167],s[54:55]\n"
CV "v104,v160,v161\n"
CV "v106,v164,v165\n"
CV "v105,v162,v163\n"
CV "v107,v166,v167\n"
"v_permlane16_swap_b32 v108,v110\n"
"v_permlane16_swap_b32 v109,v111\n"
"v_accvgpr_write_b32 a20,v108\n"
"v_accvgpr_write_b32 a21,v109\n"
"v_accvgpr_write_b32 a22,v110\n"
"v_accvgpr_write_b32 a23,v111\n"
PM "v[224:225],v[224:225],s[54:55]\n"
PM "v[226:227],v[226:227],s[54:55]\n"
PM "v[228:229],v[228:229],s[54:55]\n"
PM "v[230:231],v[230:231],s[54:55]\n"
CV "v108,v224,v225\n"
CV "v110,v228,v229\n"
CV "v109,v226,v227\n"
CV "v111,v230,v231\n"
"v_permlane16_swap_b32 v104,v106\n"
"v_permlane16_swap_b32 v105,v107\n"
"v_accvgpr_write_b32 a8,v104\n"
"v_accvgpr_write_b32 a9,v105\n"
"v_accvgpr_write_b32 a10,v106\n"
"v_accvgpr_write_b32 a11,v107\n"
PM "v[168:169],v[168:169],s[54:55]\n"
PM "v[170:171],v[170:171],s[54:55]\n"
PM "v[172:173],v[172:173],s[54:55]\n"
PM "v[174:175],v[174:175],s[54:55]\n"
CV "v104,v168,v169\n"
CV "v106,v172,v173\n"
CV "v105,v170,v171\n"
CV "v107,v174,v175\n"
"v_permlane16_swap_b32 v108,v110\n"
"v_permlane16_swap_b32 v109,v111\n"
"v_accvgpr_write_b32 a24,v108\n"
"v_accvgpr_write_b32 a25,v109\n"
"v_accvgpr_write_b32 a26,v110\n"
"v_accvgpr_write_b32 a27,v111\n"
PM "v[232:233],v[232:233],s[54:55]\n"
PM "v[234:235],v[234:235],s[54:55]\n"
PM "v[236:237],v[236:237],s[54:55]\n"
PM "v[238:239],v[238:239],s[54:55]\n"
CV "v108,v232,v233\n"
CV "v110,v236,v237\n"
CV "v109,v234,v235\n"
CV "v111,v238,v239\n"
"v_permlane16_swap_b32 v104,v106\n"
"v_permlane16_swap_b32 v105,v107\n"
"v_accvgpr_write_b32 a12,v104\n"
"v_accvgpr_write_b32 a13,v105\n"
"v_accvgpr_write_b32 a14,v106\n"
"v_accvgpr_write_b32 a15,v107\n"
"s_nop 1\n"
"v_permlane16_swap_b32 v108,v110\n"
"v_permlane16_swap_b32 v109,v111\n"
"v_accvgpr_write_b32 a28,v108\n"
"v_accvgpr_write_b32 a29,v109\n"
"v_accvgpr_write_b32 a30,v110\n"
"v_accvgpr_write_b32 a31,v111\n"
"s_lshl_b32 s66,s36,13\n"
"v_add_u32 v244,s66,v1\n"
"v_add_u32 v245,0x1000,v244\n"
"v_add_u32 v246,0x8000,v1\n"
"v_add_u32 v247,158208,v1\n"
"v_add_u32 v253,84480,v1\n"
"v_mov_b32 v252,162304\n"
"v_lshl_add_u32 v251,s36,2,v252\n"
"s_mov_b32 s43,1400897536\n"
"v_add_u32 v248,73728,v1\n"
"v_lshl_add_u32 v249,s36,10,v246\n"
WT "vmcnt(16)\n"
WT "lgkmcnt(0)\n"
"s_barrier\n"
DR "a[64:67],v4 offset:0\n"
DR "a[68:71],v4 offset:512\n"
DR "a[72:75],v4 offset:64\n"
DR "a[76:79],v4 offset:576\n"
DR "a[80:83],v4 offset:128\n"
DR "a[84:87],v4 offset:640\n"
DR "a[88:91],v4 offset:192\n"
DR "a[92:95],v4 offset:704\n"
"s_cmp_lg_u32 s98,0\n"
"s_cbranch_scc1 LS12_%=\n"
DR "v[64:67],v253 offset:0\n"
DR "v[68:71],v253 offset:1024\n"
DR "v[72:75],v253 offset:2048\n"
DR "v[76:79],v253 offset:3072\n"
DR "a[32:35],v253 offset:4096\n"
DR "a[36:39],v253 offset:5120\n"
DR "a[40:43],v253 offset:6144\n"
DR "a[44:47],v253 offset:7168\n"
"v_add_u32 v254,159744,v1\n"
WT "lgkmcnt(0)\n"
MF "v[96:99],a[64:67],v[64:67],0\n"
"v_mov_b64 v[136:137],0\n"
"v_mov_b64 v[138:139],0\n"
"v_mov_b64 v[140:141],0\n"
"v_mov_b64 v[142:143],0\n"
MF "v[100:103],a[68:71],v[64:67],0\n"
"v_mov_b64 v[144:145],0\n"
"v_mov_b64 v[146:147],0\n"
"v_mov_b64 v[148:149],0\n"
MF "v[96:99],a[72:75],v[68:71],v[96:99]\n"
"v_mov_b64 v[150:151],0\n"
"v_mov_b64 v[152:153],0\n"
"v_mov_b64 v[154:155],0\n"
MF "v[100:103],a[76:79],v[68:71],v[100:103]\n"
"v_mov_b64 v[156:157],0\n"
"v_mov_b64 v[158:159],0\n"
"v_mov_b64 v[160:161],0\n"
"v_mov_b64 v[162:163],0\n"
MF "v[96:99],a[80:83],v[72:75],v[96:99]\n"
"v_mov_b64 v[164:165],0\n"
"v_mov_b64 v[166:167],0\n"
"v_mov_b64 v[168:169],0\n"
MF "v[100:103],a[84:87],v[72:75],v[100:103]\n"
"v_mov_b64 v[170:171],0\n"
"v_mov_b64 v[172:173],0\n"
"v_mov_b64 v[174:175],0\n"
MF "v[96:99],a[88:91],v[76:79],v[96:99]\n"
"v_mov_b64 v[176:177],0\n"
"v_mov_b64 v[178:179],0\n"
"v_mov_b64 v[180:181],0\n"
"v_mov_b64 v[182:183],0\n"
MF "v[100:103],a[92:95],v[76:79],v[100:103]\n"
"v_mov_b64 v[184:185],0\n"
"v_mov_b64 v[186:187],0\n"
"v_mov_b64 v[188:189],0\n"
MF "v[80:83],a[64:67],v[32:35],0\n"
"v_mov_b64 v[190:191],0\n"
"v_mov_b64 v[192:193],0\n"
"v_mov_b64 v[194:195],0\n"
MF "v[84:87],a[68:71],v[32:35],0\n"
"v_mov_b64 v[196:197],0\n"
"v_mov_b64 v[198:199],0\n"
"v_mov_b64 v[200:201],0\n"
"v_mov_b64 v[202:203],0\n"
MF "v[80:83],a[72:75],v[36:39],v[80:83]\n"
"v_mov_b64 v[204:205],0\n"
"v_mov_b64 v[206:207],0\n"
"v_add_u32 v6,s49,v4\n"
MF "v[84:87],a[76:79],v[36:39],v[84:87]\n"
"v_add_u32 v7,s49,v5\n"
"s_mov_b32 s50,s42\n"
"s_mov_b32 s42,s49\n"
MF "v[80:83],a[80:83],v[40:43],v[80:83]\n"
"s_add_u32 s49,s49,16896\n"
"s_cmp_eq_u32 s49,84480\n"
"s_cselect_b32 s49,0,s49\n"
"v_max3_f32 v128,v96,v97,v98\n"
MF "v[84:87],a[84:87],v[40:43],v[84:87]\n"
"v_max3_f32 v128,v128,v99,v100\n"
"v_max3_f32 v128,v128,v101,v102\n"
"v_max_f32 v128,v128,v103\n"
MF "v[80:83],a[88:91],v[44:47],v[80:83]\n"
"v_mov_b32 v129,v128\n"
"s_nop 1\n"
"v_permlane32_swap_b32 v129,v128\n"
MF "v[84:87],a[92:95],v[44:47],v[84:87]\n"
"v_max_f32 v128,v129,v128\n"
"v_mov_b32 v129,v128\n"
"s_nop 1\n"
"v_permlane16_swap_b32 v129,v128\n"
MF "v[88:91],a[64:67],v[48:51],0\n"
"v_max_f32 v128,v129,v128\n"
"v_sub_f32 v96,v96,v128\n"
"v_sub_f32 v97,v97,v128\n"
MF "v[92:95],a[68:71],v[48:51],0\n"
"v_sub_f32 v98,v98,v128\n"
"v_sub_f32 v99,v99,v128\n"
"v_sub_f32 v100,v100,v128\n"
MF "v[88:91],a[72:75],v[52:55],v[88:91]\n"
"v_sub_f32 v101,v101,v128\n"
"v_sub_f32 v102,v102,v128\n"
"v_sub_f32 v103,v103,v128\n"
"v_sub_f32 v112,0,v128\n"
MF "v[92:95],a[76:79],v[52:55],v[92:95]\n"
"v_sub_f32 v113,0,v128\n"
"v_sub_f32 v114,0,v128\n"
"v_sub_f32 v115,0,v128\n"
MF "v[88:91],a[80:83],v[56:59],v[88:91]\n"
EX "v96,v96\n"
EX "v97,v97\n"
MF "v[92:95],a[84:87],v[56:59],v[92:95]\n"
EX "v98,v98\n"
EX "v99,v99\n"
MF "v[88:91],a[88:91],v[60:63],v[88:91]\n"
"s_nop 0\n"
CV "v124,v96,v97\n"
MF "v[92:95],a[92:95],v[60:63],v[92:95]\n"
CV "v125,v98,v99\n"
"ds_write_b64 v254,v[124:125]\n"
"v_xor_b32 v254,0x400,v254\n"
WT "vmcnt(12)\n"
WT "lgkmcnt(0)\n"
"s_barrier\n"
DR "a[64:67],v6 offset:0\n"
DR "a[68:71],v6 offset:512\n"
DR "a[72:75],v6 offset:64\n"
DR "a[76:79],v6 offset:576\n"
DR "a[80:83],v6 offset:128\n"
DR "a[84:87],v6 offset:640\n"
DR "a[88:91],v6 offset:192\n"
DR "a[92:95],v6 offset:704\n"
DT "a[96:97],v7 offset:0\n"
DT "a[98:99],v7 offset:512\n"
DT "a[100:101],v7 offset:32\n"
DT "a[102:103],v7 offset:544\n"
DT "a[104:105],v7 offset:64\n"
DT "a[106:107],v7 offset:576\n"
DT "a[108:109],v7 offset:96\n"
DT "a[110:111],v7 offset:608\n"
"v_max3_f32 v128,v80,v81,v82\n"
"v_max3_f32 v128,v128,v83,v84\n"
"v_max3_f32 v128,v128,v85,v86\n"
"v_max_f32 v128,v128,v87\n"
"v_mov_b32 v129,v128\n"
"s_nop 1\n"
"v_permlane32_swap_b32 v129,v128\n"
"v_max_f32 v128,v129,v128\n"
"v_mov_b32 v129,v128\n"
"s_nop 1\n"
"v_permlane16_swap_b32 v129,v128\n"
"v_max_f32 v128,v129,v128\n"
"v_sub_f32 v80,v80,v128\n"
"v_sub_f32 v81,v81,v128\n"
"v_sub_f32 v82,v82,v128\n"
"v_sub_f32 v83,v83,v128\n"
"v_sub_f32 v84,v84,v128\n"
"v_sub_f32 v85,v85,v128\n"
"v_sub_f32 v86,v86,v128\n"
"v_sub_f32 v87,v87,v128\n"
"v_sub_f32 v104,0,v128\n"
"v_sub_f32 v105,0,v128\n"
"v_sub_f32 v106,0,v128\n"
"v_sub_f32 v107,0,v128\n"
EX "v80,v80\n"
EX "v81,v81\n"
EX "v82,v82\n"
EX "v83,v83\n"
EX "v84,v84\n"
EX "v85,v85\n"
EX "v86,v86\n"
EX "v87,v87\n"
"v_max3_f32 v128,v88,v89,v90\n"
"v_max3_f32 v128,v128,v91,v92\n"
"v_max3_f32 v128,v128,v93,v94\n"
"v_max_f32 v128,v128,v95\n"
"v_mov_b32 v129,v128\n"
"s_nop 1\n"
"v_permlane32_swap_b32 v129,v128\n"
"v_max_f32 v128,v129,v128\n"
"v_mov_b32 v129,v128\n"
"s_nop 1\n"
"v_permlane16_swap_b32 v129,v128\n"
"v_max_f32 v128,v129,v128\n"
"v_sub_f32 v88,v88,v128\n"
"v_sub_f32 v89,v89,v128\n"
"v_sub_f32 v90,v90,v128\n"
"v_sub_f32 v91,v91,v128\n"
"v_sub_f32 v92,v92,v128\n"
"v_sub_f32 v93,v93,v128\n"
"v_sub_f32 v94,v94,v128\n"
"v_sub_f32 v95,v95,v128\n"
"v_sub_f32 v108,0,v128\n"
"v_sub_f32 v109,0,v128\n"
"v_sub_f32 v110,0,v128\n"
"v_sub_f32 v111,0,v128\n"
CV "v116,v80,v81\n"
CV "v117,v82,v83\n"
CV "v118,v84,v85\n"
CV "v119,v86,v87\n"
EX "v88,v88\n"
EX "v89,v89\n"
EX "v90,v90\n"
EX "v91,v91\n"
EX "v92,v92\n"
EX "v93,v93\n"
EX "v94,v94\n"
EX "v95,v95\n"
WT "lgkmcnt(8)\n"
"s_mov_b32 s52,163840\n"
"Ll16_%=:\n"
MF "v[96:99],a[64:67],v[64:67],v[112:115]\n"
"s_add_u32 m0,s50,s48\n"
DT "a[112:113],v7 offset:128\n"
CV "v120,v88,v89\n"
MF "v[96:99],a[72:75],v[68:71],v[96:99]\n"
DT "a[114:115],v7 offset:640\n"
BL "v8,s[12:15],s78 offen nt lds\n"
MF "v[96:99],a[80:83],v[72:75],v[96:99]\n"
DT "a[116:117],v7 offset:160\n"
CV "v121,v90,v91\n"
MF "v[96:99],a[88:91],v[76:79],v[96:99]\n"
DT "a[118:119],v7 offset:672\n"
CV "v122,v92,v93\n"
MF "v[80:83],a[64:67],v[32:35],v[104:107]\n"
DT "a[120:121],v7 offset:192\n"
CV "v123,v94,v95\n"
MF "v[84:87],a[68:71],v[32:35],v[104:107]\n"
DT "a[122:123],v7 offset:704\n"
BL "v9,s[12:15],s78 offen offset:1056 nt lds\n"
MF "v[80:83],a[72:75],v[36:39],v[80:83]\n"
"s_add_u32 m0,m0,8448\n"
DT "a[124:125],v7 offset:224\n"
MF "v[84:87],a[76:79],v[36:39],v[84:87]\n"
DT "a[126:127],v7 offset:736\n"
MF "v[80:83],a[80:83],v[40:43],v[80:83]\n"
EX "v96,v96\n"
MF "v[84:87],a[84:87],v[40:43],v[84:87]\n"
"s_mov_b32 s50,s42\n"
BL "v10,s[16:19],s79 offen nt lds\n"
EX "v97,v97\n"
MF "v[80:83],a[88:91],v[44:47],v[80:83]\n"
"s_mov_b32 s42,s49\n"
EX "v98,v98\n"
MF "v[84:87],a[92:95],v[44:47],v[84:87]\n"
EX "v99,v99\n"
MF "v[88:91],a[64:67],v[48:51],v[108:111]\n"
CV "v124,v96,v97\n"
CV "v125,v98,v99\n"
MF "v[92:95],a[68:71],v[48:51],v[108:111]\n"
"ds_write_b64 v254,v[124:125]\n"
BL "v11,s[16:19],s79 offen offset:1056 nt lds\n"
"v_xor_b32 v254,0x400,v254\n"
MF "v[88:91],a[72:75],v[52:55],v[88:91]\n"
"s_add_u32 s51,s51,8192\n"
EX "v80,v80\n"
MF "v[92:95],a[76:79],v[52:55],v[92:95]\n"
"s_mov_b32 s79,s78\n"
EX "v81,v81\n"
MF "v[88:91],a[80:83],v[56:59],v[88:91]\n"
"s_xor_b32 s78,s51,s41\n"
EX "v82,v82\n"
MF "v[92:95],a[84:87],v[56:59],v[92:95]\n"
EX "v83,v83\n"
MF "v[88:91],a[88:91],v[60:63],v[88:91]\n"
EX "v84,v84\n"
MF "v[92:95],a[92:95],v[60:63],v[92:95]\n"
WT "vmcnt(10) lgkmcnt(0)\n"
"s_barrier\n"
"v_add_u32 v6,s49,v4\n"
MF "v[136:139],a[96:99],v[116:119],v[136:139]\n"
DR "a[64:67],v6 offset:0\n"
EX "v85,v85\n"
MF "v[140:143],a[100:103],v[116:119],v[140:143]\n"
DR "a[68:71],v6 offset:512\n"
EX "v86,v86\n"
MF "v[144:147],a[104:107],v[116:119],v[144:147]\n"
DR "a[72:75],v6 offset:64\n"
EX "v87,v87\n"
MF "v[148:151],a[108:111],v[116:119],v[148:151]\n"
DR "a[76:79],v6 offset:576\n"
"v_add_u32 v7,s49,v5\n"
MF "v[152:155],a[112:115],v[116:119],v[152:155]\n"
DR "a[80:83],v6 offset:128\n"
EX "v88,v88\n"
MF "v[156:159],a[116:119],v[116:119],v[156:159]\n"
DR "a[84:87],v6 offset:640\n"
EX "v89,v89\n"
MF "v[160:163],a[120:123],v[116:119],v[160:163]\n"
"s_add_u32 s49,s49,16896\n"
DR "a[88:91],v6 offset:192\n"
EX "v90,v90\n"
MF "v[164:167],a[124:127],v[116:119],v[164:167]\n"
"s_cmp_eq_u32 s49,84480\n"
DR "a[92:95],v6 offset:704\n"
EX "v91,v91\n"
MF "v[168:171],a[48:51],v[116:119],v[168:171]\n"
"s_cselect_b32 s49,0,s49\n"
EX "v92,v92\n"
MF "v[172:175],a[96:99],v[120:123],v[172:175]\n"
EX "v93,v93\n"
MF "v[176:179],a[100:103],v[120:123],v[176:179]\n"
DT "a[96:97],v7 offset:0\n"
CV "v116,v80,v81\n"
MF "v[180:183],a[104:107],v[120:123],v[180:183]\n"
DT "a[98:99],v7 offset:512\n"
CV "v117,v82,v83\n"
MF "v[184:187],a[108:111],v[120:123],v[184:187]\n"
DT "a[100:101],v7 offset:32\n"
CV "v118,v84,v85\n"
MF "v[188:191],a[112:115],v[120:123],v[188:191]\n"
DT "a[102:103],v7 offset:544\n"
CV "v119,v86,v87\n"
MF "v[192:195],a[116:119],v[120:123],v[192:195]\n"
DT "a[104:105],v7 offset:64\n"
EX "v94,v94\n"
MF "v[196:199],a[120:123],v[120:123],v[196:199]\n"
DT "a[106:107],v7 offset:576\n"
EX "v95,v95\n"
MF "v[200:203],a[124:127],v[120:123],v[200:203]\n"
WT "lgkmcnt(6)\n"
DT "a[108:109],v7 offset:96\n"
MF "v[204:207],a[48:51],v[120:123],v[204:207]\n"
"s_cmp_lg_u32 s51,s52\n"
DT "a[110:111],v7 offset:608\n"
"s_cbranch_scc1 Ll16_%=\n"
MF "v[96:99],a[64:67],a[32:35],0\n"
"s_add_u32 m0,s50,s48\n"
DT "a[112:113],v7 offset:128\n"
CV "v120,v88,v89\n"
MF "v[100:103],a[68:71],a[32:35],0\n"
DT "a[114:115],v7 offset:640\n"
BL "v8,s[12:15],s78 offen nt lds\n"
MF "v[96:99],a[72:75],a[36:39],v[96:99]\n"
DT "a[116:117],v7 offset:160\n"
CV "v121,v90,v91\n"
MF "v[100:103],a[76:79],a[36:39],v[100:103]\n"
DT "a[118:119],v7 offset:672\n"
CV "v122,v92,v93\n"
MF "v[96:99],a[80:83],a[40:43],v[96:99]\n"
DT "a[120:121],v7 offset:192\n"
CV "v123,v94,v95\n"
MF "v[100:103],a[84:87],a[40:43],v[100:103]\n"
DT "a[122:123],v7 offset:704\n"
BL "v9,s[12:15],s78 offen offset:1056 nt lds\n"
MF "v[96:99],a[88:91],a[44:47],v[96:99]\n"
"s_add_u32 m0,m0,8448\n"
DT "a[124:125],v7 offset:224\n"
MF "v[100:103],a[92:95],a[44:47],v[100:103]\n"
DT "a[126:127],v7 offset:736\n"
MF "v[80:83],a[64:67],a[0:3],0\n"
MF "v[84:87],a[68:71],a[0:3],0\n"
"s_mov_b32 s50,s42\n"
BL "v10,s[16:19],s79 offen nt lds\n"
MF "v[80:83],a[72:75],a[4:7],v[80:83]\n"
"s_mov_b32 s42,s49\n"
MF "v[84:87],a[76:79],a[4:7],v[84:87]\n"
"v_max3_f32 v128,v96,v97,v98\n"
"v_max3_f32 v128,v128,v99,v100\n"
"v_max3_f32 v128,v128,v101,v102\n"
"v_max_f32 v128,v128,v103\n"
MF "v[80:83],a[80:83],a[8:11],v[80:83]\n"
"v_mov_b32 v129,v128\n"
"s_nop 1\n"
"v_permlane32_swap_b32 v129,v128\n"
MF "v[84:87],a[84:87],a[8:11],v[84:87]\n"
BL "v11,s[16:19],s79 offen offset:1056 nt lds\n"
"v_max_f32 v128,v129,v128\n"
"v_mov_b32 v129,v128\n"
"s_nop 1\n"
MF "v[80:83],a[88:91],a[12:15],v[80:83]\n"
"s_add_u32 s51,s51,8192\n"
"v_permlane16_swap_b32 v129,v128\n"
"v_max_f32 v128,v129,v128\n"
"v_sub_f32 v96,v96,v128\n"
MF "v[84:87],a[92:95],a[12:15],v[84:87]\n"
"s_mov_b32 s79,s78\n"
"v_sub_f32 v97,v97,v128\n"
"v_sub_f32 v98,v98,v128\n"
"v_sub_f32 v99,v99,v128\n"
MF "v[88:91],a[64:67],a[16:19],0\n"
"s_xor_b32 s78,s51,s41\n"
"v_sub_f32 v100,v100,v128\n"
"v_sub_f32 v101,v101,v128\n"
"v_sub_f32 v102,v102,v128\n"
MF "v[92:95],a[68:71],a[16:19],0\n"
"v_sub_f32 v103,v103,v128\n"
"v_sub_f32 v112,0,v128\n"
"v_sub_f32 v113,0,v128\n"
MF "v[88:91],a[72:75],a[20:23],v[88:91]\n"
"v_sub_f32 v114,0,v128\n"
"v_sub_f32 v115,0,v128\n"
EX "v96,v96\n"
MF "v[92:95],a[76:79],a[20:23],v[92:95]\n"
EX "v97,v97\n"
MF "v[88:91],a[80:83],a[24:27],v[88:91]\n"
EX "v98,v98\n"
EX "v99,v99\n"
MF "v[92:95],a[84:87],a[24:27],v[92:95]\n"
"s_nop 0\n"
CV "v124,v96,v97\n"
MF "v[88:91],a[88:91],a[28:31],v[88:91]\n"
CV "v125,v98,v99\n"
"ds_write_b64 v254,v[124:125]\n"
"v_xor_b32 v254,0x400,v254\n"
MF "v[92:95],a[92:95],a[28:31],v[92:95]\n"
WT "vmcnt(12)\n"
WT "lgkmcnt(0)\n"
"s_barrier\n"
"v_add_u32 v6,s49,v4\n"
MF "v[136:139],a[96:99],v[116:119],v[136:139]\n"
DR "a[64:67],v6 offset:0\n"
"v_max3_f32 v128,v80,v81,v82\n"
"v_max3_f32 v128,v128,v83,v84\n"
"v_max3_f32 v128,v128,v85,v86\n"
"v_max_f32 v128,v128,v87\n"
"v_mov_b32 v129,v128\n"
MF "v[140:143],a[100:103],v[116:119],v[140:143]\n"
DR "a[68:71],v6 offset:512\n"
"s_nop 1\n"
"v_permlane32_swap_b32 v129,v128\n"
"v_max_f32 v128,v129,v128\n"
"v_mov_b32 v129,v128\n"
MF "v[144:147],a[104:107],v[116:119],v[144:147]\n"
DR "a[72:75],v6 offset:64\n"
"s_nop 1\n"
"v_permlane16_swap_b32 v129,v128\n"
"v_max_f32 v128,v129,v128\n"
"v_sub_f32 v80,v80,v128\n"
"v_sub_f32 v81,v81,v128\n"
MF "v[148:151],a[108:111],v[116:119],v[148:151]\n"
DR "a[76:79],v6 offset:576\n"
"v_add_u32 v7,s49,v5\n"
"v_sub_f32 v82,v82,v128\n"
"v_sub_f32 v83,v83,v128\n"
"v_sub_f32 v84,v84,v128\n"
"v_sub_f32 v85,v85,v128\n"
MF "v[152:155],a[112:115],v[116:119],v[152:155]\n"
DR "a[80:83],v6 offset:128\n"
"v_sub_f32 v86,v86,v128\n"
"v_sub_f32 v87,v87,v128\n"
"v_sub_f32 v104,0,v128\n"
"v_sub_f32 v105,0,v128\n"
"v_sub_f32 v106,0,v128\n"
MF "v[156:159],a[116:119],v[116:119],v[156:159]\n"
DR "a[84:87],v6 offset:640\n"
"v_sub_f32 v107,0,v128\n"
EX "v80,v80\n"
EX "v81,v81\n"
MF "v[160:163],a[120:123],v[116:119],v[160:163]\n"
"s_add_u32 s49,s49,16896\n"
DR "a[88:91],v6 offset:192\n"
EX "v82,v82\n"
EX "v83,v83\n"
MF "v[164:167],a[124:127],v[116:119],v[164:167]\n"
"s_cmp_eq_u32 s49,84480\n"
DR "a[92:95],v6 offset:704\n"
EX "v84,v84\n"
EX "v85,v85\n"
MF "v[168:171],a[48:51],v[116:119],v[168:171]\n"
"s_cselect_b32 s49,0,s49\n"
EX "v86,v86\n"
EX "v87,v87\n"
MF "v[172:175],a[96:99],v[120:123],v[172:175]\n"
CV "v116,v80,v81\n"
CV "v117,v82,v83\n"
CV "v118,v84,v85\n"
CV "v119,v86,v87\n"
"v_max3_f32 v128,v88,v89,v90\n"
MF "v[176:179],a[100:103],v[120:123],v[176:179]\n"
DT "a[96:97],v7 offset:0\n"
"v_max3_f32 v128,v128,v91,v92\n"
"v_max3_f32 v128,v128,v93,v94\n"
"v_max_f32 v128,v128,v95\n"
"v_mov_b32 v129,v128\n"
"s_nop 1\n"
MF "v[180:183],a[104:107],v[120:123],v[180:183]\n"
DT "a[98:99],v7 offset:512\n"
"v_permlane32_swap_b32 v129,v128\n"
"v_max_f32 v128,v129,v128\n"
"v_mov_b32 v129,v128\n"
"s_nop 1\n"
"v_permlane16_swap_b32 v129,v128\n"
MF "v[184:187],a[108:111],v[120:123],v[184:187]\n"
DT "a[100:101],v7 offset:32\n"
"v_max_f32 v128,v129,v128\n"
"v_sub_f32 v88,v88,v128\n"
"v_sub_f32 v89,v89,v128\n"
"v_sub_f32 v90,v90,v128\n"
"v_sub_f32 v91,v91,v128\n"
MF "v[188:191],a[112:115],v[120:123],v[188:191]\n"
DT "a[102:103],v7 offset:544\n"
"v_sub_f32 v92,v92,v128\n"
"v_sub_f32 v93,v93,v128\n"
"v_sub_f32 v94,v94,v128\n"
"v_sub_f32 v95,v95,v128\n"
"v_sub_f32 v108,0,v128\n"
MF "v[192:195],a[116:119],v[120:123],v[192:195]\n"
DT "a[104:105],v7 offset:64\n"
"v_sub_f32 v109,0,v128\n"
"v_sub_f32 v110,0,v128\n"
"v_sub_f32 v111,0,v128\n"
EX "v88,v88\n"
MF "v[196:199],a[120:123],v[120:123],v[196:199]\n"
DT "a[106:107],v7 offset:576\n"
EX "v89,v89\n"
EX "v90,v90\n"
EX "v91,v91\n"
MF "v[200:203],a[124:127],v[120:123],v[200:203]\n"
WT "lgkmcnt(6)\n"
DT "a[108:109],v7 offset:96\n"
EX "v92,v92\n"
EX "v93,v93\n"
MF "v[204:207],a[48:51],v[120:123],v[204:207]\n"
DT "a[110:111],v7 offset:608\n"
EX "v94,v94\n"
EX "v95,v95\n"
"v_rcp_f32 v130,v168\n"
"s_add_u32 s66,s56,s99\n"
PM "v[136:137],v[136:137],v[130:131] op_sel_hi:[1,0]\n"
PM "v[138:139],v[138:139],v[130:131] op_sel_hi:[1,0]\n"
PM "v[140:141],v[140:141],v[130:131] op_sel_hi:[1,0]\n"
PM "v[142:143],v[142:143],v[130:131] op_sel_hi:[1,0]\n"
CV "v32,v136,v137\n"
CV "v33,v138,v139\n"
CV "v34,v140,v141\n"
CV "v35,v142,v143\n"
"buffer_store_dwordx4 v[32:35],v244,s[28:31],s66 offen offset:0 sc1\n"
PM "v[144:145],v[144:145],v[130:131] op_sel_hi:[1,0]\n"
PM "v[146:147],v[146:147],v[130:131] op_sel_hi:[1,0]\n"
PM "v[148:149],v[148:149],v[130:131] op_sel_hi:[1,0]\n"
PM "v[150:151],v[150:151],v[130:131] op_sel_hi:[1,0]\n"
CV "v36,v144,v145\n"
CV "v37,v146,v147\n"
CV "v38,v148,v149\n"
CV "v39,v150,v151\n"
"buffer_store_dwordx4 v[36:39],v244,s[28:31],s66 offen offset:1024 sc1\n"
PM "v[152:153],v[152:153],v[130:131] op_sel_hi:[1,0]\n"
PM "v[154:155],v[154:155],v[130:131] op_sel_hi:[1,0]\n"
PM "v[156:157],v[156:157],v[130:131] op_sel_hi:[1,0]\n"
PM "v[158:159],v[158:159],v[130:131] op_sel_hi:[1,0]\n"
CV "v40,v152,v153\n"
CV "v41,v154,v155\n"
CV "v42,v156,v157\n"
CV "v43,v158,v159\n"
"buffer_store_dwordx4 v[40:43],v244,s[28:31],s66 offen offset:2048 sc1\n"
PM "v[160:161],v[160:161],v[130:131] op_sel_hi:[1,0]\n"
PM "v[162:163],v[162:163],v[130:131] op_sel_hi:[1,0]\n"
PM "v[164:165],v[164:165],v[130:131] op_sel_hi:[1,0]\n"
PM "v[166:167],v[166:167],v[130:131] op_sel_hi:[1,0]\n"
CV "v44,v160,v161\n"
CV "v45,v162,v163\n"
CV "v46,v164,v165\n"
CV "v47,v166,v167\n"
"buffer_store_dwordx4 v[44:47],v244,s[28:31],s66 offen offset:3072 sc1\n"
"v_add_f32 v128,v168,v204\n"
"v_cmp_ngt_f32 vcc,s43,v128\n"
"s_cmp_lg_u64 vcc,0\n"
"s_cselect_b32 s66,1,0\n"
"v_mov_b32 v128,s66\n"
"ds_write_b32 v251,v128 offset:0\n"
"v_rcp_f32 v130,v204\n"
"s_add_u32 s66,s56,s99\n"
PM "v[172:173],v[172:173],v[130:131] op_sel_hi:[1,0]\n"
PM "v[174:175],v[174:175],v[130:131] op_sel_hi:[1,0]\n"
PM "v[176:177],v[176:177],v[130:131] op_sel_hi:[1,0]\n"
PM "v[178:179],v[178:179],v[130:131] op_sel_hi:[1,0]\n"
CV "v48,v172,v173\n"
CV "v49,v174,v175\n"
CV "v50,v176,v177\n"
CV "v51,v178,v179\n"
"buffer_store_dwordx4 v[48:51],v245,s[28:31],s66 offen offset:0 sc1\n"
PM "v[180:181],v[180:181],v[130:131] op_sel_hi:[1,0]\n"
PM "v[182:183],v[182:183],v[130:131] op_sel_hi:[1,0]\n"
PM "v[184:185],v[184:185],v[130:131] op_sel_hi:[1,0]\n"
PM "v[186:187],v[186:187],v[130:131] op_sel_hi:[1,0]\n"
CV "v52,v180,v181\n"
CV "v53,v182,v183\n"
CV "v54,v184,v185\n"
CV "v55,v186,v187\n"
"buffer_store_dwordx4 v[52:55],v245,s[28:31],s66 offen offset:1024 sc1\n"
PM "v[188:189],v[188:189],v[130:131] op_sel_hi:[1,0]\n"
PM "v[190:191],v[190:191],v[130:131] op_sel_hi:[1,0]\n"
PM "v[192:193],v[192:193],v[130:131] op_sel_hi:[1,0]\n"
PM "v[194:195],v[194:195],v[130:131] op_sel_hi:[1,0]\n"
CV "v56,v188,v189\n"
CV "v57,v190,v191\n"
CV "v58,v192,v193\n"
CV "v59,v194,v195\n"
"buffer_store_dwordx4 v[56:59],v245,s[28:31],s66 offen offset:2048 sc1\n"
PM "v[196:197],v[196:197],v[130:131] op_sel_hi:[1,0]\n"
PM "v[198:199],v[198:199],v[130:131] op_sel_hi:[1,0]\n"
PM "v[200:201],v[200:201],v[130:131] op_sel_hi:[1,0]\n"
PM "v[202:203],v[202:203],v[130:131] op_sel_hi:[1,0]\n"
CV "v60,v196,v197\n"
CV "v61,v198,v199\n"
CV "v62,v200,v201\n"
CV "v63,v202,v203\n"
"buffer_store_dwordx4 v[60:63],v245,s[28:31],s66 offen offset:3072 sc1\n"
"v_mov_b64 v[136:137],0\n"
"v_mov_b64 v[138:139],0\n"
"v_mov_b64 v[140:141],0\n"
"v_mov_b64 v[142:143],0\n"
"v_mov_b64 v[144:145],0\n"
"v_mov_b64 v[146:147],0\n"
"v_mov_b64 v[148:149],0\n"
"v_mov_b64 v[150:151],0\n"
"v_mov_b64 v[152:153],0\n"
"v_mov_b64 v[154:155],0\n"
"v_mov_b64 v[156:157],0\n"
"v_mov_b64 v[158:159],0\n"
"v_mov_b64 v[160:161],0\n"
"v_mov_b64 v[162:163],0\n"
"v_mov_b64 v[164:165],0\n"
"v_mov_b64 v[166:167],0\n"
"v_mov_b64 v[168:169],0\n"
"v_mov_b64 v[170:171],0\n"
"v_mov_b64 v[172:173],0\n"
"v_mov_b64 v[174:175],0\n"
"v_mov_b64 v[176:177],0\n"
"v_mov_b64 v[178:179],0\n"
"v_mov_b64 v[180:181],0\n"
"v_mov_b64 v[182:183],0\n"
"v_mov_b64 v[184:185],0\n"
"v_mov_b64 v[186:187],0\n"
"v_mov_b64 v[188:189],0\n"
"v_mov_b64 v[190:191],0\n"
"v_mov_b64 v[192:193],0\n"
"v_mov_b64 v[194:195],0\n"
"v_mov_b64 v[196:197],0\n"
"v_mov_b64 v[198:199],0\n"
"v_mov_b64 v[200:201],0\n"
"v_mov_b64 v[202:203],0\n"
"v_mov_b64 v[204:205],0\n"
"v_mov_b64 v[206:207],0\n"
"s_mov_b32 s52,204800\n"
"Ll17_%=:\n"
MF "v[96:99],a[64:67],a[32:35],v[112:115]\n"
"s_add_u32 m0,s50,s48\n"
DT "a[112:113],v7 offset:128\n"
CV "v120,v88,v89\n"
MF "v[96:99],a[72:75],a[36:39],v[96:99]\n"
DT "a[114:115],v7 offset:640\n"
BL "v8,s[12:15],s78 offen nt lds\n"
MF "v[96:99],a[80:83],a[40:43],v[96:99]\n"
DT "a[116:117],v7 offset:160\n"
CV "v121,v90,v91\n"
MF "v[96:99],a[88:91],a[44:47],v[96:99]\n"
DT "a[118:119],v7 offset:672\n"
CV "v122,v92,v93\n"
MF "v[80:83],a[64:67],a[0:3],v[104:107]\n"
DT "a[120:121],v7 offset:192\n"
CV "v123,v94,v95\n"
MF "v[84:87],a[68:71],a[0:3],v[104:107]\n"
DT "a[122:123],v7 offset:704\n"
BL "v9,s[12:15],s78 offen offset:1056 nt lds\n"
MF "v[80:83],a[72:75],a[4:7],v[80:83]\n"
"s_add_u32 m0,m0,8448\n"
DT "a[124:125],v7 offset:224\n"
MF "v[84:87],a[76:79],a[4:7],v[84:87]\n"
DT "a[126:127],v7 offset:736\n"
MF "v[80:83],a[80:83],a[8:11],v[80:83]\n"
EX "v96,v96\n"
MF "v[84:87],a[84:87],a[8:11],v[84:87]\n"
"s_mov_b32 s50,s42\n"
BL "v10,s[16:19],s79 offen nt lds\n"
EX "v97,v97\n"
MF "v[80:83],a[88:91],a[12:15],v[80:83]\n"
"s_mov_b32 s42,s49\n"
EX "v98,v98\n"
MF "v[84:87],a[92:95],a[12:15],v[84:87]\n"
EX "v99,v99\n"
MF "v[88:91],a[64:67],a[16:19],v[108:111]\n"
CV "v124,v96,v97\n"
CV "v125,v98,v99\n"
MF "v[92:95],a[68:71],a[16:19],v[108:111]\n"
"ds_write_b64 v254,v[124:125]\n"
BL "v11,s[16:19],s79 offen offset:1056 nt lds\n"
"v_xor_b32 v254,0x400,v254\n"
MF "v[88:91],a[72:75],a[20:23],v[88:91]\n"
"s_add_u32 s51,s51,8192\n"
EX "v80,v80\n"
MF "v[92:95],a[76:79],a[20:23],v[92:95]\n"
"s_mov_b32 s79,s78\n"
EX "v81,v81\n"
MF "v[88:91],a[80:83],a[24:27],v[88:91]\n"
"s_xor_b32 s78,s51,s41\n"
EX "v82,v82\n"
MF "v[92:95],a[84:87],a[24:27],v[92:95]\n"
EX "v83,v83\n"
MF "v[88:91],a[88:91],a[28:31],v[88:91]\n"
EX "v84,v84\n"
MF "v[92:95],a[92:95],a[28:31],v[92:95]\n"
WT "vmcnt(10) lgkmcnt(0)\n"
"s_barrier\n"
"v_add_u32 v6,s49,v4\n"
MF "v[136:139],a[96:99],v[116:119],v[136:139]\n"
DR "a[64:67],v6 offset:0\n"
EX "v85,v85\n"
MF "v[140:143],a[100:103],v[116:119],v[140:143]\n"
DR "a[68:71],v6 offset:512\n"
EX "v86,v86\n"
MF "v[144:147],a[104:107],v[116:119],v[144:147]\n"
DR "a[72:75],v6 offset:64\n"
EX "v87,v87\n"
MF "v[148:151],a[108:111],v[116:119],v[148:151]\n"
DR "a[76:79],v6 offset:576\n"
"v_add_u32 v7,s49,v5\n"
MF "v[152:155],a[112:115],v[116:119],v[152:155]\n"
DR "a[80:83],v6 offset:128\n"
EX "v88,v88\n"
MF "v[156:159],a[116:119],v[116:119],v[156:159]\n"
DR "a[84:87],v6 offset:640\n"
EX "v89,v89\n"
MF "v[160:163],a[120:123],v[116:119],v[160:163]\n"
"s_add_u32 s49,s49,16896\n"
DR "a[88:91],v6 offset:192\n"
EX "v90,v90\n"
MF "v[164:167],a[124:127],v[116:119],v[164:167]\n"
"s_cmp_eq_u32 s49,84480\n"
DR "a[92:95],v6 offset:704\n"
EX "v91,v91\n"
MF "v[168:171],a[48:51],v[116:119],v[168:171]\n"
"s_cselect_b32 s49,0,s49\n"
EX "v92,v92\n"
MF "v[172:175],a[96:99],v[120:123],v[172:175]\n"
EX "v93,v93\n"
MF "v[176:179],a[100:103],v[120:123],v[176:179]\n"
DT "a[96:97],v7 offset:0\n"
CV "v116,v80,v81\n"
MF "v[180:183],a[104:107],v[120:123],v[180:183]\n"
DT "a[98:99],v7 offset:512\n"
CV "v117,v82,v83\n"
MF "v[184:187],a[108:111],v[120:123],v[184:187]\n"
DT "a[100:101],v7 offset:32\n"
CV "v118,v84,v85\n"
MF "v[188:191],a[112:115],v[120:123],v[188:191]\n"
DT "a[102:103],v7 offset:544\n"
CV "v119,v86,v87\n"
MF "v[192:195],a[116:119],v[120:123],v[192:195]\n"
DT "a[104:105],v7 offset:64\n"
EX "v94,v94\n"
MF "v[196:199],a[120:123],v[120:123],v[196:199]\n"
DT "a[106:107],v7 offset:576\n"
EX "v95,v95\n"
MF "v[200:203],a[124:127],v[120:123],v[200:203]\n"
WT "lgkmcnt(6)\n"
DT "a[108:109],v7 offset:96\n"
MF "v[204:207],a[48:51],v[120:123],v[204:207]\n"
"s_cmp_lg_u32 s51,s52\n"
DT "a[110:111],v7 offset:608\n"
"s_cbranch_scc1 Ll17_%=\n"
DR "v[128:131],v252 offset:0\n"
DR "v[64:67],v253 offset:36864\n"
DR "v[68:71],v253 offset:37888\n"
DR "v[72:75],v253 offset:38912\n"
DR "v[76:79],v253 offset:39936\n"
WT "lgkmcnt(0)\n"
"v_or3_b32 v128,v128,v129,v130\n"
"v_or_b32 v128,v128,v131\n"
"v_cmp_ne_u32 vcc,0,v128\n"
"s_cbranch_vccnz Lr18_%=\n"
"v_add_u32 v3,1,v28\n"
"s_lshl_b64 exec,1,s40\n"
"buffer_store_dword v3,v26,s[28:31],0 offen offset:0 sc1\n"
"s_mov_b64 exec,-1\n"
"s_add_u32 m0,s76,84480\n"
"s_add_u32 s67,s68,0\n"
BL "v12,s[24:27],s67 offen offset:0 lds\n"
BL "v13,s[24:27],s67 offen offset:1024 lds\n"
BL "v14,s[24:27],s67 offen offset:2048 lds\n"
"s_add_u32 m0,s76,87552\n"
"s_add_u32 s67,s68,0\n"
BL "v15,s[24:27],s67 offen offset:0 lds\n"
BL "v16,s[24:27],s67 offen offset:1024 lds\n"
BL "v17,s[24:27],s67 offen offset:2048 lds\n"
"s_add_u32 m0,s76,90624\n"
"s_add_u32 s67,s68,0\n"
BL "v18,s[24:27],s67 offen offset:0 lds\n"
BL "v19,s[24:27],s67 offen offset:1024 lds\n"
BL "v20,s[24:27],s67 offen offset:2048 lds\n"
MF "v[96:99],a[64:67],a[32:35],v[112:115]\n"
"s_add_u32 m0,s50,s48\n"
DT "a[112:113],v7 offset:128\n"
CV "v120,v88,v89\n"
MF "v[96:99],a[72:75],a[36:39],v[96:99]\n"
DT "a[114:115],v7 offset:640\n"
BL "v8,s[12:15],s78 offen nt lds\n"
MF "v[96:99],a[80:83],a[40:43],v[96:99]\n"
DT "a[116:117],v7 offset:160\n"
CV "v121,v90,v91\n"
MF "v[96:99],a[88:91],a[44:47],v[96:99]\n"
DT "a[118:119],v7 offset:672\n"
CV "v122,v92,v93\n"
MF "v[80:83],a[64:67],a[0:3],v[104:107]\n"
DT "a[120:121],v7 offset:192\n"
CV "v123,v94,v95\n"
MF "v[84:87],a[68:71],a[0:3],v[104:107]\n"
DT "a[122:123],v7 offset:704\n"
MF "v[80:83],a[72:75],a[4:7],v[80:83]\n"
DT "a[124:125],v7 offset:224\n"
MF "v[84:87],a[76:79],a[4:7],v[84:87]\n"
DT "a[126:127],v7 offset:736\n"
MF "v[80:83],a[80:83],a[8:11],v[80:83]\n"
EX "v96,v96\n"
MF "v[84:87],a[84:87],a[8:11],v[84:87]\n"
"s_mov_b32 s50,s42\n"
BL "v9,s[12:15],s78 offen offset:1056 nt lds\n"
EX "v97,v97\n"
MF "v[80:83],a[88:91],a[12:15],v[80:83]\n"
"s_add_u32 m0,m0,8448\n"
"s_mov_b32 s42,s49\n"
EX "v98,v98\n"
MF "v[84:87],a[92:95],a[12:15],v[84:87]\n"
EX "v99,v99\n"
MF "v[88:91],a[64:67],a[16:19],v[108:111]\n"
CV "v124,v96,v97\n"
CV "v125,v98,v99\n"
MF "v[92:95],a[68:71],a[16:19],v[108:111]\n"
"ds_write_b64 v254,v[124:125]\n"
"v_xor_b32 v254,0x400,v254\n"
MF "v[88:91],a[72:75],a[20:23],v[88:91]\n"
EX "v80,v80\n"
MF "v[92:95],a[76:79],a[20:23],v[92:95]\n"
EX "v81,v81\n"
MF "v[88:91],a[80:83],a[24:27],v[88:91]\n"
EX "v82,v82\n"
MF "v[92:95],a[84:87],a[24:27],v[92:95]\n"
EX "v83,v83\n"
MF "v[88:91],a[88:91],a[28:31],v[88:91]\n"
BL "v10,s[16:19],s79 offen nt lds\n"
EX "v84,v84\n"
MF "v[92:95],a[92:95],a[28:31],v[92:95]\n"
WT "vmcnt(21)\n"
WT "lgkmcnt(0)\n"
"s_barrier\n"
"v_add_u32 v6,s49,v4\n"
MF "v[136:139],a[96:99],v[116:119],v[136:139]\n"
DR "a[64:67],v6 offset:0\n"
EX "v85,v85\n"
MF "v[140:143],a[100:103],v[116:119],v[140:143]\n"
DR "a[68:71],v6 offset:512\n"
EX "v86,v86\n"
MF "v[144:147],a[104:107],v[116:119],v[144:147]\n"
DR "a[72:75],v6 offset:64\n"
EX "v87,v87\n"
MF "v[148:151],a[108:111],v[116:119],v[148:151]\n"
DR "a[76:79],v6 offset:576\n"
"v_add_u32 v7,s49,v5\n"
MF "v[152:155],a[112:115],v[116:119],v[152:155]\n"
DR "a[80:83],v6 offset:128\n"
EX "v88,v88\n"
MF "v[156:159],a[116:119],v[116:119],v[156:159]\n"
DR "a[84:87],v6 offset:640\n"
EX "v89,v89\n"
MF "v[160:163],a[120:123],v[116:119],v[160:163]\n"
"s_add_u32 s49,s49,16896\n"
DR "a[88:91],v6 offset:192\n"
BL "v11,s[16:19],s79 offen offset:1056 nt lds\n"
EX "v90,v90\n"
MF "v[164:167],a[124:127],v[116:119],v[164:167]\n"
"s_add_u32 s51,s51,8192\n"
"s_mov_b32 s79,s78\n"
"s_xor_b32 s78,s51,s41\n"
"s_cmp_eq_u32 s49,84480\n"
DR "a[92:95],v6 offset:704\n"
EX "v91,v91\n"
MF "v[168:171],a[48:51],v[116:119],v[168:171]\n"
"s_cselect_b32 s49,0,s49\n"
EX "v92,v92\n"
MF "v[172:175],a[96:99],v[120:123],v[172:175]\n"
EX "v93,v93\n"
MF "v[176:179],a[100:103],v[120:123],v[176:179]\n"
DT "a[96:97],v7 offset:0\n"
CV "v116,v80,v81\n"
MF "v[180:183],a[104:107],v[120:123],v[180:183]\n"
DT "a[98:99],v7 offset:512\n"
CV "v117,v82,v83\n"
MF "v[184:187],a[108:111],v[120:123],v[184:187]\n"
DT "a[100:101],v7 offset:32\n"
CV "v118,v84,v85\n"
MF "v[188:191],a[112:115],v[120:123],v[188:191]\n"
DT "a[102:103],v7 offset:544\n"
CV "v119,v86,v87\n"
MF "v[192:195],a[116:119],v[120:123],v[192:195]\n"
DT "a[104:105],v7 offset:64\n"
EX "v94,v94\n"
MF "v[196:199],a[120:123],v[120:123],v[196:199]\n"
DT "a[106:107],v7 offset:576\n"
"buffer_load_dword v31,v26,s[28:31],0 offen offset:0 sc1\n"
EX "v95,v95\n"
MF "v[200:203],a[124:127],v[120:123],v[200:203]\n"
WT "lgkmcnt(6)\n"
DT "a[108:109],v7 offset:96\n"
MF "v[204:207],a[48:51],v[120:123],v[204:207]\n"
DT "a[110:111],v7 offset:608\n"
MF "v[96:99],a[64:67],a[32:35],v[112:115]\n"
"s_add_u32 m0,s50,s48\n"
DT "a[112:113],v7 offset:128\n"
CV "v120,v88,v89\n"
MF "v[96:99],a[72:75],a[36:39],v[96:99]\n"
DT "a[114:115],v7 offset:640\n"
BL "v8,s[12:15],s78 offen nt lds\n"
MF "v[96:99],a[80:83],a[40:43],v[96:99]\n"
DT "a[116:117],v7 offset:160\n"
CV "v121,v90,v91\n"
MF "v[96:99],a[88:91],a[44:47],v[96:99]\n"
DT "a[118:119],v7 offset:672\n"
CV "v122,v92,v93\n"
MF "v[80:83],a[64:67],a[0:3],v[104:107]\n"
DT "a[120:121],v7 offset:192\n"
CV "v123,v94,v95\n"
MF "v[84:87],a[68:71],a[0:3],v[104:107]\n"
DT "a[122:123],v7 offset:704\n"
BL "v9,s[12:15],s78 offen offset:1056 nt lds\n"
MF "v[80:83],a[72:75],a[4:7],v[80:83]\n"
"s_add_u32 m0,m0,8448\n"
DT "a[124:125],v7 offset:224\n"
MF "v[84:87],a[76:79],a[4:7],v[84:87]\n"
DT "a[126:127],v7 offset:736\n"
MF "v[80:83],a[80:83],a[8:11],v[80:83]\n"
EX "v96,v96\n"
MF "v[84:87],a[84:87],a[8:11],v[84:87]\n"
"s_mov_b32 s50,s42\n"
BL "v10,s[16:19],s79 offen nt lds\n"
EX "v97,v97\n"
MF "v[80:83],a[88:91],a[12:15],v[80:83]\n"
"s_mov_b32 s42,s49\n"
EX "v98,v98\n"
MF "v[84:87],a[92:95],a[12:15],v[84:87]\n"
EX "v99,v99\n"
MF "v[88:91],a[64:67],a[16:19],v[108:111]\n"
CV "v124,v96,v97\n"
CV "v125,v98,v99\n"
MF "v[92:95],a[68:71],a[16:19],v[108:111]\n"
"ds_write_b64 v254,v[124:125]\n"
BL "v11,s[16:19],s79 offen offset:1056 nt lds\n"
"v_xor_b32 v254,0x400,v254\n"
MF "v[88:91],a[72:75],a[20:23],v[88:91]\n"
"s_add_u32 s51,s51,8192\n"
EX "v80,v80\n"
MF "v[92:95],a[76:79],a[20:23],v[92:95]\n"
"s_mov_b32 s79,s78\n"
EX "v81,v81\n"
MF "v[88:91],a[80:83],a[24:27],v[88:91]\n"
"s_xor_b32 s78,s51,s41\n"
EX "v82,v82\n"
MF "v[92:95],a[84:87],a[24:27],v[92:95]\n"
EX "v83,v83\n"
MF "v[88:91],a[88:91],a[28:31],v[88:91]\n"
EX "v84,v84\n"
MF "v[92:95],a[92:95],a[28:31],v[92:95]\n"
WT "vmcnt(23)\n"
WT "vmcnt(9)\n"
WT "lgkmcnt(0)\n"
"s_barrier\n"
"v_add_u32 v6,s49,v4\n"
MF "v[136:139],a[96:99],v[116:119],v[136:139]\n"
DR "a[64:67],v6 offset:0\n"
EX "v85,v85\n"
MF "v[140:143],a[100:103],v[116:119],v[140:143]\n"
DR "a[68:71],v6 offset:512\n"
EX "v86,v86\n"
MF "v[144:147],a[104:107],v[116:119],v[144:147]\n"
DR "a[72:75],v6 offset:64\n"
EX "v87,v87\n"
MF "v[148:151],a[108:111],v[116:119],v[148:151]\n"
DR "a[76:79],v6 offset:576\n"
"v_add_u32 v7,s49,v5\n"
MF "v[152:155],a[112:115],v[116:119],v[152:155]\n"
DR "a[80:83],v6 offset:128\n"
EX "v88,v88\n"
MF "v[156:159],a[116:119],v[116:119],v[156:159]\n"
DR "a[84:87],v6 offset:640\n"
EX "v89,v89\n"
MF "v[160:163],a[120:123],v[116:119],v[160:163]\n"
"s_add_u32 s49,s49,16896\n"
DR "a[88:91],v6 offset:192\n"
EX "v90,v90\n"
MF "v[164:167],a[124:127],v[116:119],v[164:167]\n"
"s_cmp_eq_u32 s49,84480\n"
DR "a[92:95],v6 offset:704\n"
EX "v91,v91\n"
MF "v[168:171],a[48:51],v[116:119],v[168:171]\n"
"s_cselect_b32 s49,0,s49\n"
EX "v92,v92\n"
MF "v[172:175],a[96:99],v[120:123],v[172:175]\n"
EX "v93,v93\n"
MF "v[176:179],a[100:103],v[120:123],v[176:179]\n"
DT "a[96:97],v7 offset:0\n"
CV "v116,v80,v81\n"
MF "v[180:183],a[104:107],v[120:123],v[180:183]\n"
DT "a[98:99],v7 offset:512\n"
CV "v117,v82,v83\n"
MF "v[184:187],a[108:111],v[120:123],v[184:187]\n"
DT "a[100:101],v7 offset:32\n"
CV "v118,v84,v85\n"
MF "v[188:191],a[112:115],v[120:123],v[188:191]\n"
DT "a[102:103],v7 offset:544\n"
CV "v119,v86,v87\n"
MF "v[192:195],a[116:119],v[120:123],v[192:195]\n"
DT "a[104:105],v7 offset:64\n"
EX "v94,v94\n"
MF "v[196:199],a[120:123],v[120:123],v[196:199]\n"
DT "a[106:107],v7 offset:576\n"
EX "v95,v95\n"
MF "v[200:203],a[124:127],v[120:123],v[200:203]\n"
WT "lgkmcnt(6)\n"
DT "a[108:109],v7 offset:96\n"
MF "v[204:207],a[48:51],v[120:123],v[204:207]\n"
DT "a[110:111],v7 offset:608\n"
DT "a[212:213],v21 offset:0\n"
DT "a[214:215],v21 offset:4608\n"
DT "a[216:217],v21 offset:32\n"
DT "a[218:219],v21 offset:4640\n"
DT "a[220:221],v22 offset:0\n"
DT "a[222:223],v22 offset:4608\n"
DT "a[224:225],v22 offset:32\n"
DT "a[226:227],v22 offset:4640\n"
DT "a[228:229],v23 offset:0\n"
DT "a[230:231],v23 offset:4608\n"
DT "a[232:233],v23 offset:32\n"
DT "a[234:235],v23 offset:4640\n"
DT "a[236:237],v24 offset:0\n"
DT "a[238:239],v24 offset:4608\n"
DT "a[240:241],v24 offset:32\n"
DT "a[242:243],v24 offset:4640\n"
DT "a[244:245],v25 offset:0\n"
DT "a[246:247],v25 offset:4608\n"
WT "vmcnt(4)\n"
"v_cmp_eq_u32 vcc,v31,v28\n"
"s_andn2_b64 vcc,vcc,s[62:63]\n"
"s_cbranch_scc1 Lp19_%=\n"
"Lp20_%=:\n"
"v_add_u32 v3,2,v28\n"
"s_lshl_b64 exec,1,s40\n"
"buffer_store_dword v3,v26,s[28:31],0 offen offset:0 sc1\n"
"s_mov_b64 exec,-1\n"
MF "v[96:99],a[64:67],a[32:35],v[112:115]\n"
"s_add_u32 m0,s50,s48\n"
DT "a[112:113],v7 offset:128\n"
CV "v120,v88,v89\n"
MF "v[96:99],a[72:75],a[36:39],v[96:99]\n"
DT "a[114:115],v7 offset:640\n"
BL "v8,s[12:15],s78 offen nt lds\n"
MF "v[96:99],a[80:83],a[40:43],v[96:99]\n"
DT "a[116:117],v7 offset:160\n"
CV "v121,v90,v91\n"
MF "v[96:99],a[88:91],a[44:47],v[96:99]\n"
DT "a[118:119],v7 offset:672\n"
CV "v122,v92,v93\n"
MF "v[80:83],a[64:67],a[0:3],v[104:107]\n"
DT "a[120:121],v7 offset:192\n"
CV "v123,v94,v95\n"
MF "v[84:87],a[68:71],a[0:3],v[104:107]\n"
DT "a[122:123],v7 offset:704\n"
BL "v9,s[12:15],s78 offen offset:1056 nt lds\n"
MF "v[80:83],a[72:75],a[4:7],v[80:83]\n"
"s_add_u32 m0,m0,8448\n"
DT "a[124:125],v7 offset:224\n"
MF "v[84:87],a[76:79],a[4:7],v[84:87]\n"
DT "a[126:127],v7 offset:736\n"
MF "v[80:83],a[80:83],a[8:11],v[80:83]\n"
EX "v96,v96\n"
MF "v[84:87],a[84:87],a[8:11],v[84:87]\n"
"s_mov_b32 s50,s42\n"
BL "v10,s[16:19],s79 offen nt lds\n"
EX "v97,v97\n"
MF "v[80:83],a[88:91],a[12:15],v[80:83]\n"
"s_mov_b32 s42,s49\n"
EX "v98,v98\n"
MF "v[84:87],a[92:95],a[12:15],v[84:87]\n"
EX "v99,v99\n"
MF "v[88:91],a[64:67],a[16:19],v[108:111]\n"
CV "v124,v96,v97\n"
CV "v125,v98,v99\n"
MF "v[92:95],a[68:71],a[16:19],v[108:111]\n"
"ds_write_b64 v254,v[124:125]\n"
BL "v11,s[16:19],s79 offen offset:1056 nt lds\n"
"v_xor_b32 v254,0x400,v254\n"
MF "v[88:91],a[72:75],a[20:23],v[88:91]\n"
"s_add_u32 s51,s51,8192\n"
EX "v80,v80\n"
MF "v[92:95],a[76:79],a[20:23],v[92:95]\n"
"s_mov_b32 s79,s78\n"
EX "v81,v81\n"
MF "v[88:91],a[80:83],a[24:27],v[88:91]\n"
"s_xor_b32 s78,s51,s41\n"
EX "v82,v82\n"
MF "v[92:95],a[84:87],a[24:27],v[92:95]\n"
EX "v83,v83\n"
MF "v[88:91],a[88:91],a[28:31],v[88:91]\n"
EX "v84,v84\n"
MF "v[92:95],a[92:95],a[28:31],v[92:95]\n"
WT "vmcnt(24)\n"
WT "lgkmcnt(0)\n"
"s_barrier\n"
"v_add_u32 v6,s49,v4\n"
MF "v[136:139],a[96:99],v[116:119],v[136:139]\n"
DR "a[64:67],v6 offset:0\n"
EX "v85,v85\n"
MF "v[140:143],a[100:103],v[116:119],v[140:143]\n"
DR "a[68:71],v6 offset:512\n"
EX "v86,v86\n"
MF "v[144:147],a[104:107],v[116:119],v[144:147]\n"
DR "a[72:75],v6 offset:64\n"
EX "v87,v87\n"
MF "v[148:151],a[108:111],v[116:119],v[148:151]\n"
DR "a[76:79],v6 offset:576\n"
"v_add_u32 v7,s49,v5\n"
MF "v[152:155],a[112:115],v[116:119],v[152:155]\n"
DR "a[80:83],v6 offset:128\n"
EX "v88,v88\n"
MF "v[156:159],a[116:119],v[116:119],v[156:159]\n"
DR "a[84:87],v6 offset:640\n"
EX "v89,v89\n"
MF "v[160:163],a[120:123],v[116:119],v[160:163]\n"
"s_add_u32 s49,s49,16896\n"
DR "a[88:91],v6 offset:192\n"
EX "v90,v90\n"
MF "v[164:167],a[124:127],v[116:119],v[164:167]\n"
"s_cmp_eq_u32 s49,84480\n"
DR "a[92:95],v6 offset:704\n"
EX "v91,v91\n"
MF "v[168:171],a[48:51],v[116:119],v[168:171]\n"
"s_cselect_b32 s49,0,s49\n"
EX "v92,v92\n"
MF "v[172:175],a[96:99],v[120:123],v[172:175]\n"
EX "v93,v93\n"
MF "v[176:179],a[100:103],v[120:123],v[176:179]\n"
DT "a[96:97],v7 offset:0\n"
CV "v116,v80,v81\n"
MF "v[180:183],a[104:107],v[120:123],v[180:183]\n"
DT "a[98:99],v7 offset:512\n"
CV "v117,v82,v83\n"
MF "v[184:187],a[108:111],v[120:123],v[184:187]\n"
DT "a[100:101],v7 offset:32\n"
CV "v118,v84,v85\n"
MF "v[188:191],a[112:115],v[120:123],v[188:191]\n"
DT "a[102:103],v7 offset:544\n"
CV "v119,v86,v87\n"
MF "v[192:195],a[116:119],v[120:123],v[192:195]\n"
DT "a[104:105],v7 offset:64\n"
EX "v94,v94\n"
MF "v[196:199],a[120:123],v[120:123],v[196:199]\n"
DT "a[106:107],v7 offset:576\n"
EX "v95,v95\n"
MF "v[200:203],a[124:127],v[120:123],v[200:203]\n"
WT "lgkmcnt(6)\n"
DT "a[108:109],v7 offset:96\n"
MF "v[204:207],a[48:51],v[120:123],v[204:207]\n"
DT "a[110:111],v7 offset:608\n"
MF "a[128:131],a[212:215],v[32:35],0\n"
"s_add_u32 m0,s76,121344\n"
MF "a[164:167],a[212:215],v[48:51],0\n"
"s_add_u32 s67,s69,0\n"
MF "a[200:203],a[212:215],v[64:67],0\n"
DT "a[212:213],v21 offset:9216\n"
BL "v12,s[24:27],s67 offen offset:0 lds\n"
MF "a[132:135],a[216:219],v[32:35],0\n"
DT "a[214:215],v21 offset:13824\n"
MF "a[168:171],a[216:219],v[48:51],0\n"
MF "a[204:207],a[216:219],v[64:67],0\n"
DT "a[216:217],v21 offset:9248\n"
BL "v13,s[24:27],s67 offen offset:1024 lds\n"
MF "a[136:139],a[220:223],v[32:35],0\n"
DT "a[218:219],v21 offset:13856\n"
MF "a[172:175],a[220:223],v[48:51],0\n"
DT "a[220:221],v22 offset:9216\n"
MF "a[140:143],a[224:227],v[32:35],0\n"
DT "a[222:223],v22 offset:13824\n"
BL "v14,s[24:27],s67 offen offset:2048 lds\n"
MF "a[176:179],a[224:227],v[48:51],0\n"
"s_add_u32 m0,s76,124416\n"
DT "a[224:225],v22 offset:9248\n"
MF "a[144:147],a[228:231],v[32:35],0\n"
"s_add_u32 s67,s69,0\n"
DT "a[226:227],v22 offset:13856\n"
MF "a[180:183],a[228:231],v[48:51],0\n"
DT "a[228:229],v23 offset:9216\n"
BL "v15,s[24:27],s67 offen offset:0 lds\n"
MF "a[148:151],a[232:235],v[32:35],0\n"
DT "a[230:231],v23 offset:13824\n"
MF "a[184:187],a[232:235],v[48:51],0\n"
DT "a[232:233],v23 offset:9248\n"
MF "a[152:155],a[236:239],v[32:35],0\n"
DT "a[234:235],v23 offset:13856\n"
BL "v16,s[24:27],s67 offen offset:1024 lds\n"
MF "a[188:191],a[236:239],v[48:51],0\n"
DT "a[236:237],v24 offset:9216\n"
MF "a[156:159],a[240:243],v[32:35],0\n"
DT "a[238:239],v24 offset:13824\n"
MF "a[192:195],a[240:243],v[48:51],0\n"
DT "a[240:241],v24 offset:9248\n"
BL "v17,s[24:27],s67 offen offset:2048 lds\n"
MF "a[160:163],a[244:247],v[32:35],0\n"
DT "a[242:243],v24 offset:13856\n"
MF "a[196:199],a[244:247],v[48:51],0\n"
MF "a[208:211],a[244:247],v[64:67],0\n"
DT "a[244:245],v25 offset:9216\n"
DT "a[246:247],v25 offset:13824\n"
MF "v[96:99],a[64:67],a[32:35],v[112:115]\n"
"s_add_u32 m0,s50,s48\n"
DT "a[112:113],v7 offset:128\n"
CV "v120,v88,v89\n"
MF "v[96:99],a[72:75],a[36:39],v[96:99]\n"
DT "a[114:115],v7 offset:640\n"
BL "v8,s[12:15],s78 offen nt lds\n"
MF "v[96:99],a[80:83],a[40:43],v[96:99]\n"
DT "a[116:117],v7 offset:160\n"
CV "v121,v90,v91\n"
MF "v[96:99],a[88:91],a[44:47],v[96:99]\n"
DT "a[118:119],v7 offset:672\n"
CV "v122,v92,v93\n"
MF "v[80:83],a[64:67],a[0:3],v[104:107]\n"
DT "a[120:121],v7 offset:192\n"
CV "v123,v94,v95\n"
MF "v[84:87],a[68:71],a[0:3],v[104:107]\n"
DT "a[122:123],v7 offset:704\n"
BL "v9,s[12:15],s78 offen offset:1056 nt lds\n"
MF "v[80:83],a[72:75],a[4:7],v[80:83]\n"
"s_add_u32 m0,m0,8448\n"
DT "a[124:125],v7 offset:224\n"
MF "v[84:87],a[76:79],a[4:7],v[84:87]\n"
DT "a[126:127],v7 offset:736\n"
MF "v[80:83],a[80:83],a[8:11],v[80:83]\n"
EX "v96,v96\n"
MF "v[84:87],a[84:87],a[8:11],v[84:87]\n"
"s_mov_b32 s50,s42\n"
BL "v10,s[16:19],s79 offen nt lds\n"
EX "v97,v97\n"
MF "v[80:83],a[88:91],a[12:15],v[80:83]\n"
"s_mov_b32 s42,s49\n"
EX "v98,v98\n"
MF "v[84:87],a[92:95],a[12:15],v[84:87]\n"
EX "v99,v99\n"
MF "v[88:91],a[64:67],a[16:19],v[108:111]\n"
CV "v124,v96,v97\n"
CV "v125,v98,v99\n"
MF "v[92:95],a[68:71],a[16:19],v[108:111]\n"
"ds_write_b64 v254,v[124:125]\n"
BL "v11,s[16:19],s79 offen offset:1056 nt lds\n"
"v_xor_b32 v254,0x400,v254\n"
MF "v[88:91],a[72:75],a[20:23],v[88:91]\n"
"s_add_u32 s51,s51,8192\n"
EX "v80,v80\n"
MF "v[92:95],a[76:79],a[20:23],v[92:95]\n"
"s_mov_b32 s79,s78\n"
EX "v81,v81\n"
MF "v[88:91],a[80:83],a[24:27],v[88:91]\n"
"s_xor_b32 s78,s51,s41\n"
EX "v82,v82\n"
MF "v[92:95],a[84:87],a[24:27],v[92:95]\n"
EX "v83,v83\n"
MF "v[88:91],a[88:91],a[28:31],v[88:91]\n"
EX "v84,v84\n"
MF "v[92:95],a[92:95],a[28:31],v[92:95]\n"
WT "vmcnt(20)\n"
WT "lgkmcnt(0)\n"
"s_barrier\n"
"v_add_u32 v6,s49,v4\n"
MF "v[136:139],a[96:99],v[116:119],v[136:139]\n"
DR "a[64:67],v6 offset:0\n"
EX "v85,v85\n"
MF "v[140:143],a[100:103],v[116:119],v[140:143]\n"
DR "a[68:71],v6 offset:512\n"
EX "v86,v86\n"
MF "v[144:147],a[104:107],v[116:119],v[144:147]\n"
DR "a[72:75],v6 offset:64\n"
EX "v87,v87\n"
MF "v[148:151],a[108:111],v[116:119],v[148:151]\n"
DR "a[76:79],v6 offset:576\n"
"v_add_u32 v7,s49,v5\n"
MF "v[152:155],a[112:115],v[116:119],v[152:155]\n"
DR "a[80:83],v6 offset:128\n"
EX "v88,v88\n"
MF "v[156:159],a[116:119],v[116:119],v[156:159]\n"
DR "a[84:87],v6 offset:640\n"
EX "v89,v89\n"
MF "v[160:163],a[120:123],v[116:119],v[160:163]\n"
"s_add_u32 s49,s49,16896\n"
DR "a[88:91],v6 offset:192\n"
EX "v90,v90\n"
MF "v[164:167],a[124:127],v[116:119],v[164:167]\n"
"s_cmp_eq_u32 s49,84480\n"
DR "a[92:95],v6 offset:704\n"
EX "v91,v91\n"
MF "v[168:171],a[48:51],v[116:119],v[168:171]\n"
"s_cselect_b32 s49,0,s49\n"
EX "v92,v92\n"
MF "v[172:175],a[96:99],v[120:123],v[172:175]\n"
EX "v93,v93\n"
MF "v[176:179],a[100:103],v[120:123],v[176:179]\n"
DT "a[96:97],v7 offset:0\n"
CV "v116,v80,v81\n"
MF "v[180:183],a[104:107],v[120:123],v[180:183]\n"
DT "a[98:99],v7 offset:512\n"
CV "v117,v82,v83\n"
MF "v[184:187],a[108:111],v[120:123],v[184:187]\n"
DT "a[100:101],v7 offset:32\n"
CV "v118,v84,v85\n"
MF "v[188:191],a[112:115],v[120:123],v[188:191]\n"
DT "a[102:103],v7 offset:544\n"
CV "v119,v86,v87\n"
MF "v[192:195],a[116:119],v[120:123],v[192:195]\n"
DT "a[104:105],v7 offset:64\n"
EX "v94,v94\n"
MF "v[196:199],a[120:123],v[120:123],v[196:199]\n"
DT "a[106:107],v7 offset:576\n"
EX "v95,v95\n"
MF "v[200:203],a[124:127],v[120:123],v[200:203]\n"
WT "lgkmcnt(6)\n"
DT "a[108:109],v7 offset:96\n"
MF "v[204:207],a[48:51],v[120:123],v[204:207]\n"
DT "a[110:111],v7 offset:608\n"
MF "a[128:131],a[212:215],v[36:39],a[128:131]\n"
MF "a[164:167],a[212:215],v[52:55],a[164:167]\n"
"s_add_u32 s44,s57,0\n"
MF "a[200:203],a[212:215],v[68:71],a[200:203]\n"
DT "a[212:213],v21 offset:18432\n"
BL "v[32:35],v244,s[28:31],s44 offen offset:0 sc1\n"
MF "a[132:135],a[216:219],v[36:39],a[132:135]\n"
DT "a[214:215],v21 offset:23040\n"
MF "a[168:171],a[216:219],v[52:55],a[168:171]\n"
MF "a[204:207],a[216:219],v[68:71],a[204:207]\n"
DT "a[216:217],v21 offset:18464\n"
BL "v[48:51],v245,s[28:31],s44 offen offset:0 sc1\n"
MF "a[136:139],a[220:223],v[36:39],a[136:139]\n"
DT "a[218:219],v21 offset:23072\n"
MF "a[172:175],a[220:223],v[52:55],a[172:175]\n"
DT "a[220:221],v22 offset:18432\n"
MF "a[140:143],a[224:227],v[36:39],a[140:143]\n"
DT "a[222:223],v22 offset:23040\n"
BL "v[64:67],v246,s[28:31],s44 offen offset:0 sc1\n"
MF "a[176:179],a[224:227],v[52:55],a[176:179]\n"
"s_add_u32 m0,s76,127488\n"
DT "a[224:225],v22 offset:18464\n"
MF "a[144:147],a[228:231],v[36:39],a[144:147]\n"
"s_add_u32 s67,s69,0\n"
DT "a[226:227],v22 offset:23072\n"
MF "a[180:183],a[228:231],v[52:55],a[180:183]\n"
DT "a[228:229],v23 offset:18432\n"
BL "v18,s[24:27],s67 offen offset:0 lds\n"
MF "a[148:151],a[232:235],v[36:39],a[148:151]\n"
DT "a[230:231],v23 offset:23040\n"
MF "a[184:187],a[232:235],v[52:55],a[184:187]\n"
DT "a[232:233],v23 offset:18464\n"
MF "a[152:155],a[236:239],v[36:39],a[152:155]\n"
DT "a[234:235],v23 offset:23072\n"
BL "v19,s[24:27],s67 offen offset:1024 lds\n"
MF "a[188:191],a[236:239],v[52:55],a[188:191]\n"
DT "a[236:237],v24 offset:18432\n"
MF "a[156:159],a[240:243],v[36:39],a[156:159]\n"
DT "a[238:239],v24 offset:23040\n"
MF "a[192:195],a[240:243],v[52:55],a[192:195]\n"
DT "a[240:241],v24 offset:18464\n"
BL "v20,s[24:27],s67 offen offset:2048 lds\n"
MF "a[160:163],a[244:247],v[36:39],a[160:163]\n"
DT "a[242:243],v24 offset:23072\n"
MF "a[196:199],a[244:247],v[52:55],a[196:199]\n"
MF "a[208:211],a[244:247],v[68:71],a[208:211]\n"
DT "a[244:245],v25 offset:18432\n"
DT "a[246:247],v25 offset:23040\n"
MF "v[96:99],a[64:67],a[32:35],v[112:115]\n"
"s_add_u32 m0,s50,s48\n"
DT "a[112:113],v7 offset:128\n"
CV "v120,v88,v89\n"
MF "v[96:99],a[72:75],a[36:39],v[96:99]\n"
DT "a[114:115],v7 offset:640\n"
BL "v8,s[12:15],s78 offen nt lds\n"
MF "v[96:99],a[80:83],a[40:43],v[96:99]\n"
DT "a[116:117],v7 offset:160\n"
CV "v121,v90,v91\n"
MF "v[96:99],a[88:91],a[44:47],v[96:99]\n"
DT "a[118:119],v7 offset:672\n"
CV "v122,v92,v93\n"
MF "v[80:83],a[64:67],a[0:3],v[104:107]\n"
DT "a[120:121],v7 offset:192\n"
CV "v123,v94,v95\n"
MF "v[84:87],a[68:71],a[0:3],v[104:107]\n"
DT "a[122:123],v7 offset:704\n"
BL "v9,s[12:15],s78 offen offset:1056 nt lds\n"
MF "v[80:83],a[72:75],a[4:7],v[80:83]\n"
"s_add_u32 m0,m0,8448\n"
DT "a[124:125],v7 offset:224\n"
MF "v[84:87],a[76:79],a[4:7],v[84:87]\n"
DT "a[126:127],v7 offset:736\n"
MF "v[80:83],a[80:83],a[8:11],v[80:83]\n"
EX "v96,v96\n"
MF "v[84:87],a[84:87],a[8:11],v[84:87]\n"
"s_mov_b32 s50,s42\n"
BL "v10,s[16:19],s79 offen nt lds\n"
EX "v97,v97\n"
MF "v[80:83],a[88:91],a[12:15],v[80:83]\n"
"s_mov_b32 s42,s49\n"
EX "v98,v98\n"
MF "v[84:87],a[92:95],a[12:15],v[84:87]\n"
EX "v99,v99\n"
MF "v[88:91],a[64:67],a[16:19],v[108:111]\n"
CV "v124,v96,v97\n"
CV "v125,v98,v99\n"
MF "v[92:95],a[68:71],a[16:19],v[108:111]\n"
"ds_write_b64 v254,v[124:125]\n"
BL "v11,s[16:19],s79 offen offset:1056 nt lds\n"
"v_xor_b32 v254,0x400,v254\n"
MF "v[88:91],a[72:75],a[20:23],v[88:91]\n"
"s_add_u32 s51,s51,8192\n"
EX "v80,v80\n"
MF "v[92:95],a[76:79],a[20:23],v[92:95]\n"
"s_mov_b32 s79,s78\n"
EX "v81,v81\n"
MF "v[88:91],a[80:83],a[24:27],v[88:91]\n"
"s_xor_b32 s78,s51,s41\n"
EX "v82,v82\n"
MF "v[92:95],a[84:87],a[24:27],v[92:95]\n"
EX "v83,v83\n"
MF "v[88:91],a[88:91],a[28:31],v[88:91]\n"
EX "v84,v84\n"
MF "v[92:95],a[92:95],a[28:31],v[92:95]\n"
WT "vmcnt(25)\n"
WT "lgkmcnt(0)\n"
"s_barrier\n"
"v_add_u32 v6,s49,v4\n"
MF "v[136:139],a[96:99],v[116:119],v[136:139]\n"
DR "a[64:67],v6 offset:0\n"
EX "v85,v85\n"
MF "v[140:143],a[100:103],v[116:119],v[140:143]\n"
DR "a[68:71],v6 offset:512\n"
EX "v86,v86\n"
MF "v[144:147],a[104:107],v[116:119],v[144:147]\n"
DR "a[72:75],v6 offset:64\n"
EX "v87,v87\n"
MF "v[148:151],a[108:111],v[116:119],v[148:151]\n"
DR "a[76:79],v6 offset:576\n"
"v_add_u32 v7,s49,v5\n"
MF "v[152:155],a[112:115],v[116:119],v[152:155]\n"
DR "a[80:83],v6 offset:128\n"
EX "v88,v88\n"
MF "v[156:159],a[116:119],v[116:119],v[156:159]\n"
DR "a[84:87],v6 offset:640\n"
EX "v89,v89\n"
MF "v[160:163],a[120:123],v[116:119],v[160:163]\n"
"s_add_u32 s49,s49,16896\n"
DR "a[88:91],v6 offset:192\n"
EX "v90,v90\n"
MF "v[164:167],a[124:127],v[116:119],v[164:167]\n"
"s_cmp_eq_u32 s49,84480\n"
DR "a[92:95],v6 offset:704\n"
EX "v91,v91\n"
MF "v[168:171],a[48:51],v[116:119],v[168:171]\n"
"s_cselect_b32 s49,0,s49\n"
EX "v92,v92\n"
MF "v[172:175],a[96:99],v[120:123],v[172:175]\n"
EX "v93,v93\n"
MF "v[176:179],a[100:103],v[120:123],v[176:179]\n"
DT "a[96:97],v7 offset:0\n"
CV "v116,v80,v81\n"
MF "v[180:183],a[104:107],v[120:123],v[180:183]\n"
DT "a[98:99],v7 offset:512\n"
CV "v117,v82,v83\n"
MF "v[184:187],a[108:111],v[120:123],v[184:187]\n"
DT "a[100:101],v7 offset:32\n"
CV "v118,v84,v85\n"
MF "v[188:191],a[112:115],v[120:123],v[188:191]\n"
DT "a[102:103],v7 offset:544\n"
CV "v119,v86,v87\n"
MF "v[192:195],a[116:119],v[120:123],v[192:195]\n"
DT "a[104:105],v7 offset:64\n"
EX "v94,v94\n"
MF "v[196:199],a[120:123],v[120:123],v[196:199]\n"
DT "a[106:107],v7 offset:576\n"
EX "v95,v95\n"
MF "v[200:203],a[124:127],v[120:123],v[200:203]\n"
WT "lgkmcnt(6)\n"
DT "a[108:109],v7 offset:96\n"
MF "v[204:207],a[48:51],v[120:123],v[204:207]\n"
DT "a[110:111],v7 offset:608\n"
MF "a[128:131],a[212:215],v[40:43],a[128:131]\n"
MF "a[164:167],a[212:215],v[56:59],a[164:167]\n"
"s_add_u32 s44,s57,0\n"
MF "a[200:203],a[212:215],v[72:75],a[200:203]\n"
DT "a[212:213],v21 offset:27648\n"
BL "v[36:39],v244,s[28:31],s44 offen offset:1024 sc1\n"
MF "a[132:135],a[216:219],v[40:43],a[132:135]\n"
DT "a[214:215],v21 offset:32256\n"
MF "a[168:171],a[216:219],v[56:59],a[168:171]\n"
MF "a[204:207],a[216:219],v[72:75],a[204:207]\n"
DT "a[216:217],v21 offset:27680\n"
MF "a[136:139],a[220:223],v[40:43],a[136:139]\n"
DT "a[218:219],v21 offset:32288\n"
"v_add_u32 v21,36864,v21\n"
MF "a[172:175],a[220:223],v[56:59],a[172:175]\n"
DT "a[220:221],v22 offset:27648\n"
MF "a[140:143],a[224:227],v[40:43],a[140:143]\n"
DT "a[222:223],v22 offset:32256\n"
BL "v[52:55],v245,s[28:31],s44 offen offset:1024 sc1\n"
MF "a[176:179],a[224:227],v[56:59],a[176:179]\n"
DT "a[224:225],v22 offset:27680\n"
MF "a[144:147],a[228:231],v[40:43],a[144:147]\n"
DT "a[226:227],v22 offset:32288\n"
"v_add_u32 v22,36864,v22\n"
MF "a[180:183],a[228:231],v[56:59],a[180:183]\n"
DT "a[228:229],v23 offset:27648\n"
MF "a[148:151],a[232:235],v[40:43],a[148:151]\n"
DT "a[230:231],v23 offset:32256\n"
MF "a[184:187],a[232:235],v[56:59],a[184:187]\n"
DT "a[232:233],v23 offset:27680\n"
MF "a[152:155],a[236:239],v[40:43],a[152:155]\n"
DT "a[234:235],v23 offset:32288\n"
BL "v[68:71],v246,s[28:31],s44 offen offset:1024 sc1\n"
"v_add_u32 v23,36864,v23\n"
MF "a[188:191],a[236:239],v[56:59],a[188:191]\n"
DT "a[236:237],v24 offset:27648\n"
MF "a[156:159],a[240:243],v[40:43],a[156:159]\n"
DT "a[238:239],v24 offset:32256\n"
MF "a[192:195],a[240:243],v[56:59],a[192:195]\n"
DT "a[240:241],v24 offset:27680\n"
MF "a[160:163],a[244:247],v[40:43],a[160:163]\n"
DT "a[242:243],v24 offset:32288\n"
"v_add_u32 v24,36864,v24\n"
MF "a[196:199],a[244:247],v[56:59],a[196:199]\n"
MF "a[208:211],a[244:247],v[72:75],a[208:211]\n"
DT "a[244:245],v25 offset:27648\n"
DT "a[246:247],v25 offset:32256\n"
"v_add_u32 v25,36864,v25\n"
MF "v[96:99],a[64:67],a[32:35],v[112:115]\n"
"s_add_u32 m0,s50,s48\n"
DT "a[112:113],v7 offset:128\n"
CV "v120,v88,v89\n"
MF "v[96:99],a[72:75],a[36:39],v[96:99]\n"
DT "a[114:115],v7 offset:640\n"
BL "v8,s[12:15],s78 offen nt lds\n"
MF "v[96:99],a[80:83],a[40:43],v[96:99]\n"
DT "a[116:117],v7 offset:160\n"
CV "v121,v90,v91\n"
MF "v[96:99],a[88:91],a[44:47],v[96:99]\n"
DT "a[118:119],v7 offset:672\n"
CV "v122,v92,v93\n"
MF "v[80:83],a[64:67],a[0:3],v[104:107]\n"
DT "a[120:121],v7 offset:192\n"
CV "v123,v94,v95\n"
MF "v[84:87],a[68:71],a[0:3],v[104:107]\n"
DT "a[122:123],v7 offset:704\n"
BL "v9,s[12:15],s78 offen offset:1056 nt lds\n"
MF "v[80:83],a[72:75],a[4:7],v[80:83]\n"
"s_add_u32 m0,m0,8448\n"
DT "a[124:125],v7 offset:224\n"
MF "v[84:87],a[76:79],a[4:7],v[84:87]\n"
DT "a[126:127],v7 offset:736\n"
MF "v[80:83],a[80:83],a[8:11],v[80:83]\n"
EX "v96,v96\n"
MF "v[84:87],a[84:87],a[8:11],v[84:87]\n"
"s_mov_b32 s50,s42\n"
BL "v10,s[16:19],s79 offen nt lds\n"
EX "v97,v97\n"
MF "v[80:83],a[88:91],a[12:15],v[80:83]\n"
"s_mov_b32 s42,s49\n"
EX "v98,v98\n"
MF "v[84:87],a[92:95],a[12:15],v[84:87]\n"
EX "v99,v99\n"
MF "v[88:91],a[64:67],a[16:19],v[108:111]\n"
CV "v124,v96,v97\n"
CV "v125,v98,v99\n"
MF "v[92:95],a[68:71],a[16:19],v[108:111]\n"
"ds_write_b64 v254,v[124:125]\n"
BL "v11,s[16:19],s79 offen offset:1056 nt lds\n"
"v_xor_b32 v254,0x400,v254\n"
MF "v[88:91],a[72:75],a[20:23],v[88:91]\n"
"s_add_u32 s51,s51,8192\n"
EX "v80,v80\n"
MF "v[92:95],a[76:79],a[20:23],v[92:95]\n"
"s_mov_b32 s79,s78\n"
EX "v81,v81\n"
MF "v[88:91],a[80:83],a[24:27],v[88:91]\n"
"s_xor_b32 s78,s51,s41\n"
EX "v82,v82\n"
MF "v[92:95],a[84:87],a[24:27],v[92:95]\n"
EX "v83,v83\n"
MF "v[88:91],a[88:91],a[28:31],v[88:91]\n"
EX "v84,v84\n"
MF "v[92:95],a[92:95],a[28:31],v[92:95]\n"
WT "vmcnt(27)\n"
WT "vmcnt(11)\n"
WT "lgkmcnt(0)\n"
"s_barrier\n"
"v_add_u32 v6,s49,v4\n"
MF "v[136:139],a[96:99],v[116:119],v[136:139]\n"
DR "a[64:67],v6 offset:0\n"
EX "v85,v85\n"
MF "v[140:143],a[100:103],v[116:119],v[140:143]\n"
DR "a[68:71],v6 offset:512\n"
EX "v86,v86\n"
MF "v[144:147],a[104:107],v[116:119],v[144:147]\n"
DR "a[72:75],v6 offset:64\n"
EX "v87,v87\n"
MF "v[148:151],a[108:111],v[116:119],v[148:151]\n"
DR "a[76:79],v6 offset:576\n"
"v_add_u32 v7,s49,v5\n"
MF "v[152:155],a[112:115],v[116:119],v[152:155]\n"
DR "a[80:83],v6 offset:128\n"
EX "v88,v88\n"
MF "v[156:159],a[116:119],v[116:119],v[156:159]\n"
DR "a[84:87],v6 offset:640\n"
EX "v89,v89\n"
MF "v[160:163],a[120:123],v[116:119],v[160:163]\n"
"s_add_u32 s49,s49,16896\n"
DR "a[88:91],v6 offset:192\n"
EX "v90,v90\n"
MF "v[164:167],a[124:127],v[116:119],v[164:167]\n"
"s_cmp_eq_u32 s49,84480\n"
DR "a[92:95],v6 offset:704\n"
EX "v91,v91\n"
MF "v[168:171],a[48:51],v[116:119],v[168:171]\n"
"s_cselect_b32 s49,0,s49\n"
EX "v92,v92\n"
MF "v[172:175],a[96:99],v[120:123],v[172:175]\n"
EX "v93,v93\n"
MF "v[176:179],a[100:103],v[120:123],v[176:179]\n"
DT "a[96:97],v7 offset:0\n"
CV "v116,v80,v81\n"
MF "v[180:183],a[104:107],v[120:123],v[180:183]\n"
DT "a[98:99],v7 offset:512\n"
CV "v117,v82,v83\n"
MF "v[184:187],a[108:111],v[120:123],v[184:187]\n"
DT "a[100:101],v7 offset:32\n"
CV "v118,v84,v85\n"
MF "v[188:191],a[112:115],v[120:123],v[188:191]\n"
DT "a[102:103],v7 offset:544\n"
CV "v119,v86,v87\n"
MF "v[192:195],a[116:119],v[120:123],v[192:195]\n"
DT "a[104:105],v7 offset:64\n"
EX "v94,v94\n"
MF "v[196:199],a[120:123],v[120:123],v[196:199]\n"
DT "a[106:107],v7 offset:576\n"
EX "v95,v95\n"
MF "v[200:203],a[124:127],v[120:123],v[200:203]\n"
WT "lgkmcnt(6)\n"
DT "a[108:109],v7 offset:96\n"
MF "v[204:207],a[48:51],v[120:123],v[204:207]\n"
DT "a[110:111],v7 offset:608\n"
MF "a[128:131],a[212:215],v[44:47],a[128:131]\n"
MF "a[164:167],a[212:215],v[60:63],a[164:167]\n"
"s_add_u32 s44,s57,0\n"
MF "a[200:203],a[212:215],v[76:79],a[200:203]\n"
DT "a[212:213],v21 offset:0\n"
BL "v[40:43],v244,s[28:31],s44 offen offset:2048 sc1\n"
MF "a[132:135],a[216:219],v[44:47],a[132:135]\n"
DT "a[214:215],v21 offset:4608\n"
MF "a[168:171],a[216:219],v[60:63],a[168:171]\n"
MF "a[204:207],a[216:219],v[76:79],a[204:207]\n"
DT "a[216:217],v21 offset:32\n"
MF "a[136:139],a[220:223],v[44:47],a[136:139]\n"
DT "a[218:219],v21 offset:4640\n"
MF "a[172:175],a[220:223],v[60:63],a[172:175]\n"
DT "a[220:221],v22 offset:0\n"
MF "a[140:143],a[224:227],v[44:47],a[140:143]\n"
DT "a[222:223],v22 offset:4608\n"
BL "v[56:59],v245,s[28:31],s44 offen offset:2048 sc1\n"
MF "a[176:179],a[224:227],v[60:63],a[176:179]\n"
DT "a[224:225],v22 offset:32\n"
MF "a[144:147],a[228:231],v[44:47],a[144:147]\n"
DT "a[226:227],v22 offset:4640\n"
MF "a[180:183],a[228:231],v[60:63],a[180:183]\n"
DT "a[228:229],v23 offset:0\n"
MF "a[148:151],a[232:235],v[44:47],a[148:151]\n"
DT "a[230:231],v23 offset:4608\n"
MF "a[184:187],a[232:235],v[60:63],a[184:187]\n"
DT "a[232:233],v23 offset:32\n"
MF "a[152:155],a[236:239],v[44:47],a[152:155]\n"
DT "a[234:235],v23 offset:4640\n"
BL "v[72:75],v246,s[28:31],s44 offen offset:2048 sc1\n"
MF "a[188:191],a[236:239],v[60:63],a[188:191]\n"
DT "a[236:237],v24 offset:0\n"
MF "a[156:159],a[240:243],v[44:47],a[156:159]\n"
DT "a[238:239],v24 offset:4608\n"
MF "a[192:195],a[240:243],v[60:63],a[192:195]\n"
DT "a[240:241],v24 offset:32\n"
MF "a[160:163],a[244:247],v[44:47],a[160:163]\n"
DT "a[242:243],v24 offset:4640\n"
MF "a[196:199],a[244:247],v[60:63],a[196:199]\n"
MF "a[208:211],a[244:247],v[76:79],a[208:211]\n"
DT "a[244:245],v25 offset:0\n"
DT "a[246:247],v25 offset:4608\n"
MF "v[96:99],a[64:67],a[32:35],v[112:115]\n"
"s_add_u32 m0,s50,s48\n"
DT "a[112:113],v7 offset:128\n"
CV "v120,v88,v89\n"
MF "v[96:99],a[72:75],a[36:39],v[96:99]\n"
DT "a[114:115],v7 offset:640\n"
BL "v8,s[12:15],s78 offen nt lds\n"
MF "v[96:99],a[80:83],a[40:43],v[96:99]\n"
DT "a[116:117],v7 offset:160\n"
CV "v121,v90,v91\n"
MF "v[96:99],a[88:91],a[44:47],v[96:99]\n"
DT "a[118:119],v7 offset:672\n"
CV "v122,v92,v93\n"
MF "v[80:83],a[64:67],a[0:3],v[104:107]\n"
DT "a[120:121],v7 offset:192\n"
CV "v123,v94,v95\n"
MF "v[84:87],a[68:71],a[0:3],v[104:107]\n"
DT "a[122:123],v7 offset:704\n"
BL "v9,s[12:15],s78 offen offset:1056 nt lds\n"
MF "v[80:83],a[72:75],a[4:7],v[80:83]\n"
"s_add_u32 m0,m0,8448\n"
DT "a[124:125],v7 offset:224\n"
MF "v[84:87],a[76:79],a[4:7],v[84:87]\n"
DT "a[126:127],v7 offset:736\n"
MF "v[80:83],a[80:83],a[8:11],v[80:83]\n"
EX "v96,v96\n"
MF "v[84:87],a[84:87],a[8:11],v[84:87]\n"
"s_mov_b32 s50,s42\n"
BL "v10,s[16:19],s79 offen nt lds\n"
EX "v97,v97\n"
MF "v[80:83],a[88:91],a[12:15],v[80:83]\n"
"s_mov_b32 s42,s49\n"
EX "v98,v98\n"
MF "v[84:87],a[92:95],a[12:15],v[84:87]\n"
EX "v99,v99\n"
MF "v[88:91],a[64:67],a[16:19],v[108:111]\n"
CV "v124,v96,v97\n"
CV "v125,v98,v99\n"
MF "v[92:95],a[68:71],a[16:19],v[108:111]\n"
"ds_write_b64 v254,v[124:125]\n"
BL "v11,s[16:19],s79 offen offset:1056 nt lds\n"
"v_xor_b32 v254,0x400,v254\n"
MF "v[88:91],a[72:75],a[20:23],v[88:91]\n"
"s_add_u32 s51,s51,8192\n"
EX "v80,v80\n"
MF "v[92:95],a[76:79],a[20:23],v[92:95]\n"
"s_mov_b32 s79,s78\n"
EX "v81,v81\n"
MF "v[88:91],a[80:83],a[24:27],v[88:91]\n"
"s_xor_b32 s78,s51,s41\n"
EX "v82,v82\n"
MF "v[92:95],a[84:87],a[24:27],v[92:95]\n"
EX "v83,v83\n"
MF "v[88:91],a[88:91],a[28:31],v[88:91]\n"
EX "v84,v84\n"
MF "v[92:95],a[92:95],a[28:31],v[92:95]\n"
WT "vmcnt(24)\n"
WT "lgkmcnt(0)\n"
"s_barrier\n"
"v_add_u32 v6,s49,v4\n"
MF "v[136:139],a[96:99],v[116:119],v[136:139]\n"
DR "a[64:67],v6 offset:0\n"
EX "v85,v85\n"
MF "v[140:143],a[100:103],v[116:119],v[140:143]\n"
DR "a[68:71],v6 offset:512\n"
EX "v86,v86\n"
MF "v[144:147],a[104:107],v[116:119],v[144:147]\n"
DR "a[72:75],v6 offset:64\n"
EX "v87,v87\n"
MF "v[148:151],a[108:111],v[116:119],v[148:151]\n"
DR "a[76:79],v6 offset:576\n"
"v_add_u32 v7,s49,v5\n"
MF "v[152:155],a[112:115],v[116:119],v[152:155]\n"
DR "a[80:83],v6 offset:128\n"
EX "v88,v88\n"
MF "v[156:159],a[116:119],v[116:119],v[156:159]\n"
DR "a[84:87],v6 offset:640\n"
EX "v89,v89\n"
MF "v[160:163],a[120:123],v[116:119],v[160:163]\n"
"s_add_u32 s49,s49,16896\n"
DR "a[88:91],v6 offset:192\n"
EX "v90,v90\n"
MF "v[164:167],a[124:127],v[116:119],v[164:167]\n"
"s_cmp_eq_u32 s49,84480\n"
DR "a[92:95],v6 offset:704\n"
EX "v91,v91\n"
MF "v[168:171],a[48:51],v[116:119],v[168:171]\n"
"s_cselect_b32 s49,0,s49\n"
EX "v92,v92\n"
MF "v[172:175],a[96:99],v[120:123],v[172:175]\n"
EX "v93,v93\n"
MF "v[176:179],a[100:103],v[120:123],v[176:179]\n"
DT "a[96:97],v7 offset:0\n"
CV "v116,v80,v81\n"
MF "v[180:183],a[104:107],v[120:123],v[180:183]\n"
DT "a[98:99],v7 offset:512\n"
CV "v117,v82,v83\n"
MF "v[184:187],a[108:111],v[120:123],v[184:187]\n"
DT "a[100:101],v7 offset:32\n"
CV "v118,v84,v85\n"
MF "v[188:191],a[112:115],v[120:123],v[188:191]\n"
DT "a[102:103],v7 offset:544\n"
CV "v119,v86,v87\n"
MF "v[192:195],a[116:119],v[120:123],v[192:195]\n"
DT "a[104:105],v7 offset:64\n"
EX "v94,v94\n"
MF "v[196:199],a[120:123],v[120:123],v[196:199]\n"
DT "a[106:107],v7 offset:576\n"
EX "v95,v95\n"
MF "v[200:203],a[124:127],v[120:123],v[200:203]\n"
WT "lgkmcnt(6)\n"
DT "a[108:109],v7 offset:96\n"
MF "v[204:207],a[48:51],v[120:123],v[204:207]\n"
DT "a[110:111],v7 offset:608\n"
WT "vmcnt(21)\n"
MF "a[128:131],a[212:215],v[32:35],a[128:131]\n"
MF "a[164:167],a[212:215],v[48:51],a[164:167]\n"
"s_add_u32 s44,s57,0\n"
MF "a[200:203],a[212:215],v[64:67],a[200:203]\n"
DT "a[212:213],v21 offset:9216\n"
BL "v[44:47],v244,s[28:31],s44 offen offset:3072 sc1\n"
MF "a[132:135],a[216:219],v[32:35],a[132:135]\n"
DT "a[214:215],v21 offset:13824\n"
MF "a[168:171],a[216:219],v[48:51],a[168:171]\n"
BL "v[60:63],v245,s[28:31],s44 offen offset:3072 sc1\n"
MF "a[204:207],a[216:219],v[64:67],a[204:207]\n"
DT "a[216:217],v21 offset:9248\n"
MF "a[136:139],a[220:223],v[32:35],a[136:139]\n"
DT "a[218:219],v21 offset:13856\n"
BL "v[76:79],v246,s[28:31],s44 offen offset:3072 sc1\n"
MF "a[172:175],a[220:223],v[48:51],a[172:175]\n"
"s_add_u32 m0,s76,84480\n"
"s_add_u32 s67,s70,0\n"
DT "a[220:221],v22 offset:9216\n"
MF "a[140:143],a[224:227],v[32:35],a[140:143]\n"
DT "a[222:223],v22 offset:13824\n"
BL "v12,s[24:27],s67 offen offset:0 lds\n"
MF "a[176:179],a[224:227],v[48:51],a[176:179]\n"
DT "a[224:225],v22 offset:9248\n"
MF "a[144:147],a[228:231],v[32:35],a[144:147]\n"
DT "a[226:227],v22 offset:13856\n"
BL "v13,s[24:27],s67 offen offset:1024 lds\n"
MF "a[180:183],a[228:231],v[48:51],a[180:183]\n"
DT "a[228:229],v23 offset:9216\n"
MF "a[148:151],a[232:235],v[32:35],a[148:151]\n"
DT "a[230:231],v23 offset:13824\n"
BL "v14,s[24:27],s67 offen offset:2048 lds\n"
MF "a[184:187],a[232:235],v[48:51],a[184:187]\n"
"s_add_u32 m0,s76,87552\n"
"s_add_u32 s67,s70,0\n"
DT "a[232:233],v23 offset:9248\n"
MF "a[152:155],a[236:239],v[32:35],a[152:155]\n"
DT "a[234:235],v23 offset:13856\n"
BL "v15,s[24:27],s67 offen offset:0 lds\n"
MF "a[188:191],a[236:239],v[48:51],a[188:191]\n"
DT "a[236:237],v24 offset:9216\n"
MF "a[156:159],a[240:243],v[32:35],a[156:159]\n"
DT "a[238:239],v24 offset:13824\n"
BL "v16,s[24:27],s67 offen offset:1024 lds\n"
MF "a[192:195],a[240:243],v[48:51],a[192:195]\n"
DT "a[240:241],v24 offset:9248\n"
MF "a[160:163],a[244:247],v[32:35],a[160:163]\n"
DT "a[242:243],v24 offset:13856\n"
BL "v17,s[24:27],s67 offen offset:2048 lds\n"
MF "a[196:199],a[244:247],v[48:51],a[196:199]\n"
MF "a[208:211],a[244:247],v[64:67],a[208:211]\n"
DT "a[244:245],v25 offset:9216\n"
DT "a[246:247],v25 offset:13824\n"
MF "v[96:99],a[64:67],a[32:35],v[112:115]\n"
"s_add_u32 m0,s50,s48\n"
"s_add_u32 m0,m0,8448\n"
DT "a[112:113],v7 offset:128\n"
CV "v120,v88,v89\n"
MF "v[96:99],a[72:75],a[36:39],v[96:99]\n"
DT "a[114:115],v7 offset:640\n"
BL "v10,s[16:19],s79 offen nt lds\n"
MF "v[96:99],a[80:83],a[40:43],v[96:99]\n"
DT "a[116:117],v7 offset:160\n"
CV "v121,v90,v91\n"
MF "v[96:99],a[88:91],a[44:47],v[96:99]\n"
DT "a[118:119],v7 offset:672\n"
CV "v122,v92,v93\n"
MF "v[80:83],a[64:67],a[0:3],v[104:107]\n"
DT "a[120:121],v7 offset:192\n"
CV "v123,v94,v95\n"
MF "v[84:87],a[68:71],a[0:3],v[104:107]\n"
DT "a[122:123],v7 offset:704\n"
BL "v11,s[16:19],s79 offen offset:1056 nt lds\n"
MF "v[80:83],a[72:75],a[4:7],v[80:83]\n"
"s_add_u32 s51,s51,8192\n"
"s_mov_b32 s79,s78\n"
"s_xor_b32 s78,s51,s41\n"
DT "a[124:125],v7 offset:224\n"
MF "v[84:87],a[76:79],a[4:7],v[84:87]\n"
DT "a[126:127],v7 offset:736\n"
MF "v[80:83],a[80:83],a[8:11],v[80:83]\n"
EX "v96,v96\n"
MF "v[84:87],a[84:87],a[8:11],v[84:87]\n"
"s_mov_b32 s50,s42\n"
EX "v97,v97\n"
MF "v[80:83],a[88:91],a[12:15],v[80:83]\n"
"s_mov_b32 s42,s49\n"
EX "v98,v98\n"
MF "v[84:87],a[92:95],a[12:15],v[84:87]\n"
EX "v99,v99\n"
MF "v[88:91],a[64:67],a[16:19],v[108:111]\n"
CV "v124,v96,v97\n"
CV "v125,v98,v99\n"
MF "v[92:95],a[68:71],a[16:19],v[108:111]\n"
"ds_write_b64 v254,v[124:125]\n"
"v_xor_b32 v254,0x400,v254\n"
MF "v[88:91],a[72:75],a[20:23],v[88:91]\n"
EX "v80,v80\n"
MF "v[92:95],a[76:79],a[20:23],v[92:95]\n"
EX "v81,v81\n"
MF "v[88:91],a[80:83],a[24:27],v[88:91]\n"
EX "v82,v82\n"
MF "v[92:95],a[84:87],a[24:27],v[92:95]\n"
EX "v83,v83\n"
MF "v[88:91],a[88:91],a[28:31],v[88:91]\n"
EX "v84,v84\n"
MF "v[92:95],a[92:95],a[28:31],v[92:95]\n"
WT "vmcnt(25)\n"
WT "lgkmcnt(0)\n"
"s_barrier\n"
"v_add_u32 v6,s49,v4\n"
MF "v[136:139],a[96:99],v[116:119],v[136:139]\n"
DR "a[64:67],v6 offset:0\n"
EX "v85,v85\n"
MF "v[140:143],a[100:103],v[116:119],v[140:143]\n"
DR "a[68:71],v6 offset:512\n"
EX "v86,v86\n"
MF "v[144:147],a[104:107],v[116:119],v[144:147]\n"
DR "a[72:75],v6 offset:64\n"
EX "v87,v87\n"
MF "v[148:151],a[108:111],v[116:119],v[148:151]\n"
DR "a[76:79],v6 offset:576\n"
"v_add_u32 v7,s49,v5\n"
MF "v[152:155],a[112:115],v[116:119],v[152:155]\n"
DR "a[80:83],v6 offset:128\n"
EX "v88,v88\n"
MF "v[156:159],a[116:119],v[116:119],v[156:159]\n"
DR "a[84:87],v6 offset:640\n"
EX "v89,v89\n"
MF "v[160:163],a[120:123],v[116:119],v[160:163]\n"
"s_add_u32 s49,s49,16896\n"
DR "a[88:91],v6 offset:192\n"
EX "v90,v90\n"
MF "v[164:167],a[124:127],v[116:119],v[164:167]\n"
"s_cmp_eq_u32 s49,84480\n"
DR "a[92:95],v6 offset:704\n"
EX "v91,v91\n"
MF "v[168:171],a[48:51],v[116:119],v[168:171]\n"
"s_cselect_b32 s49,0,s49\n"
EX "v92,v92\n"
MF "v[172:175],a[96:99],v[120:123],v[172:175]\n"
EX "v93,v93\n"
MF "v[176:179],a[100:103],v[120:123],v[176:179]\n"
DT "a[96:97],v7 offset:0\n"
CV "v116,v80,v81\n"
MF "v[180:183],a[104:107],v[120:123],v[180:183]\n"
DT "a[98:99],v7 offset:512\n"
CV "v117,v82,v83\n"
MF "v[184:187],a[108:111],v[120:123],v[184:187]\n"
DT "a[100:101],v7 offset:32\n"
CV "v118,v84,v85\n"
MF "v[188:191],a[112:115],v[120:123],v[188:191]\n"
DT "a[102:103],v7 offset:544\n"
CV "v119,v86,v87\n"
MF "v[192:195],a[116:119],v[120:123],v[192:195]\n"
DT "a[104:105],v7 offset:64\n"
EX "v94,v94\n"
MF "v[196:199],a[120:123],v[120:123],v[196:199]\n"
DT "a[106:107],v7 offset:576\n"
EX "v95,v95\n"
MF "v[200:203],a[124:127],v[120:123],v[200:203]\n"
WT "lgkmcnt(6)\n"
DT "a[108:109],v7 offset:96\n"
MF "v[204:207],a[48:51],v[120:123],v[204:207]\n"
DT "a[110:111],v7 offset:608\n"
WT "vmcnt(22)\n"
MF "a[128:131],a[212:215],v[36:39],a[128:131]\n"
MF "a[164:167],a[212:215],v[52:55],a[164:167]\n"
"s_add_u32 s44,s58,0\n"
MF "a[200:203],a[212:215],v[68:71],a[200:203]\n"
DT "a[212:213],v21 offset:18432\n"
BL "v[32:35],v244,s[28:31],s44 offen offset:0 sc1\n"
MF "a[132:135],a[216:219],v[36:39],a[132:135]\n"
DT "a[214:215],v21 offset:23040\n"
MF "a[168:171],a[216:219],v[52:55],a[168:171]\n"
MF "a[204:207],a[216:219],v[68:71],a[204:207]\n"
DT "a[216:217],v21 offset:18464\n"
BL "v[48:51],v245,s[28:31],s44 offen offset:0 sc1\n"
MF "a[136:139],a[220:223],v[36:39],a[136:139]\n"
DT "a[218:219],v21 offset:23072\n"
MF "a[172:175],a[220:223],v[52:55],a[172:175]\n"
DT "a[220:221],v22 offset:18432\n"
MF "a[140:143],a[224:227],v[36:39],a[140:143]\n"
DT "a[222:223],v22 offset:23040\n"
BL "v[64:67],v246,s[28:31],s44 offen offset:0 sc1\n"
MF "a[176:179],a[224:227],v[52:55],a[176:179]\n"
"s_add_u32 m0,s76,90624\n"
DT "a[224:225],v22 offset:18464\n"
MF "a[144:147],a[228:231],v[36:39],a[144:147]\n"
"s_add_u32 s67,s70,0\n"
DT "a[226:227],v22 offset:23072\n"
MF "a[180:183],a[228:231],v[52:55],a[180:183]\n"
DT "a[228:229],v23 offset:18432\n"
BL "v18,s[24:27],s67 offen offset:0 lds\n"
MF "a[148:151],a[232:235],v[36:39],a[148:151]\n"
DT "a[230:231],v23 offset:23040\n"
MF "a[184:187],a[232:235],v[52:55],a[184:187]\n"
DT "a[232:233],v23 offset:18464\n"
MF "a[152:155],a[236:239],v[36:39],a[152:155]\n"
DT "a[234:235],v23 offset:23072\n"
BL "v19,s[24:27],s67 offen offset:1024 lds\n"
MF "a[188:191],a[236:239],v[52:55],a[188:191]\n"
DT "a[236:237],v24 offset:18432\n"
MF "a[156:159],a[240:243],v[36:39],a[156:159]\n"
DT "a[238:239],v24 offset:23040\n"
MF "a[192:195],a[240:243],v[52:55],a[192:195]\n"
DT "a[240:241],v24 offset:18464\n"
BL "v20,s[24:27],s67 offen offset:2048 lds\n"
MF "a[160:163],a[244:247],v[36:39],a[160:163]\n"
DT "a[242:243],v24 offset:23072\n"
MF "a[196:199],a[244:247],v[52:55],a[196:199]\n"
MF "a[208:211],a[244:247],v[68:71],a[208:211]\n"
DT "a[244:245],v25 offset:18432\n"
DT "a[246:247],v25 offset:23040\n"
MF "v[96:99],a[64:67],a[32:35],v[112:115]\n"
DT "a[112:113],v7 offset:128\n"
CV "v120,v88,v89\n"
MF "v[96:99],a[72:75],a[36:39],v[96:99]\n"
DT "a[114:115],v7 offset:640\n"
MF "v[96:99],a[80:83],a[40:43],v[96:99]\n"
DT "a[116:117],v7 offset:160\n"
CV "v121,v90,v91\n"
MF "v[96:99],a[88:91],a[44:47],v[96:99]\n"
DT "a[118:119],v7 offset:672\n"
CV "v122,v92,v93\n"
MF "v[80:83],a[64:67],a[0:3],v[104:107]\n"
DT "a[120:121],v7 offset:192\n"
CV "v123,v94,v95\n"
MF "v[84:87],a[68:71],a[0:3],v[104:107]\n"
DT "a[122:123],v7 offset:704\n"
MF "v[80:83],a[72:75],a[4:7],v[80:83]\n"
DT "a[124:125],v7 offset:224\n"
MF "v[84:87],a[76:79],a[4:7],v[84:87]\n"
DT "a[126:127],v7 offset:736\n"
MF "v[80:83],a[80:83],a[8:11],v[80:83]\n"
EX "v96,v96\n"
MF "v[84:87],a[84:87],a[8:11],v[84:87]\n"
"s_mov_b32 s50,s42\n"
EX "v97,v97\n"
MF "v[80:83],a[88:91],a[12:15],v[80:83]\n"
"s_mov_b32 s42,s49\n"
EX "v98,v98\n"
MF "v[84:87],a[92:95],a[12:15],v[84:87]\n"
EX "v99,v99\n"
MF "v[88:91],a[64:67],a[16:19],v[108:111]\n"
CV "v124,v96,v97\n"
CV "v125,v98,v99\n"
MF "v[92:95],a[68:71],a[16:19],v[108:111]\n"
"ds_write_b64 v254,v[124:125]\n"
"v_xor_b32 v254,0x400,v254\n"
MF "v[88:91],a[72:75],a[20:23],v[88:91]\n"
EX "v80,v80\n"
MF "v[92:95],a[76:79],a[20:23],v[92:95]\n"
EX "v81,v81\n"
MF "v[88:91],a[80:83],a[24:27],v[88:91]\n"
EX "v82,v82\n"
MF "v[92:95],a[84:87],a[24:27],v[92:95]\n"
EX "v83,v83\n"
MF "v[88:91],a[88:91],a[28:31],v[88:91]\n"
EX "v84,v84\n"
MF "v[92:95],a[92:95],a[28:31],v[92:95]\n"
WT "vmcnt(24)\n"
WT "lgkmcnt(0)\n"
"s_barrier\n"
"v_add_u32 v6,s49,v4\n"
MF "v[136:139],a[96:99],v[116:119],v[136:139]\n"
DR "a[64:67],v6 offset:0\n"
EX "v85,v85\n"
MF "v[140:143],a[100:103],v[116:119],v[140:143]\n"
DR "a[68:71],v6 offset:512\n"
EX "v86,v86\n"
MF "v[144:147],a[104:107],v[116:119],v[144:147]\n"
DR "a[72:75],v6 offset:64\n"
EX "v87,v87\n"
MF "v[148:151],a[108:111],v[116:119],v[148:151]\n"
DR "a[76:79],v6 offset:576\n"
"v_add_u32 v7,s49,v5\n"
MF "v[152:155],a[112:115],v[116:119],v[152:155]\n"
DR "a[80:83],v6 offset:128\n"
EX "v88,v88\n"
MF "v[156:159],a[116:119],v[116:119],v[156:159]\n"
DR "a[84:87],v6 offset:640\n"
EX "v89,v89\n"
MF "v[160:163],a[120:123],v[116:119],v[160:163]\n"
"s_add_u32 s49,s49,16896\n"
DR "a[88:91],v6 offset:192\n"
EX "v90,v90\n"
MF "v[164:167],a[124:127],v[116:119],v[164:167]\n"
"s_cmp_eq_u32 s49,84480\n"
DR "a[92:95],v6 offset:704\n"
EX "v91,v91\n"
MF "v[168:171],a[48:51],v[116:119],v[168:171]\n"
"s_cselect_b32 s49,0,s49\n"
EX "v92,v92\n"
MF "v[172:175],a[96:99],v[120:123],v[172:175]\n"
EX "v93,v93\n"
MF "v[176:179],a[100:103],v[120:123],v[176:179]\n"
DT "a[96:97],v7 offset:0\n"
CV "v116,v80,v81\n"
MF "v[180:183],a[104:107],v[120:123],v[180:183]\n"
DT "a[98:99],v7 offset:512\n"
CV "v117,v82,v83\n"
MF "v[184:187],a[108:111],v[120:123],v[184:187]\n"
DT "a[100:101],v7 offset:32\n"
CV "v118,v84,v85\n"
MF "v[188:191],a[112:115],v[120:123],v[188:191]\n"
DT "a[102:103],v7 offset:544\n"
CV "v119,v86,v87\n"
MF "v[192:195],a[116:119],v[120:123],v[192:195]\n"
DT "a[104:105],v7 offset:64\n"
EX "v94,v94\n"
MF "v[196:199],a[120:123],v[120:123],v[196:199]\n"
DT "a[106:107],v7 offset:576\n"
EX "v95,v95\n"
MF "v[200:203],a[124:127],v[120:123],v[200:203]\n"
WT "lgkmcnt(6)\n"
DT "a[108:109],v7 offset:96\n"
MF "v[204:207],a[48:51],v[120:123],v[204:207]\n"
DT "a[110:111],v7 offset:608\n"
WT "vmcnt(21)\n"
MF "a[128:131],a[212:215],v[40:43],a[128:131]\n"
MF "a[164:167],a[212:215],v[56:59],a[164:167]\n"
"s_add_u32 s44,s58,0\n"
MF "a[200:203],a[212:215],v[72:75],a[200:203]\n"
DT "a[212:213],v21 offset:27648\n"
BL "v[36:39],v244,s[28:31],s44 offen offset:1024 sc1\n"
MF "a[132:135],a[216:219],v[40:43],a[132:135]\n"
DT "a[214:215],v21 offset:32256\n"
MF "a[168:171],a[216:219],v[56:59],a[168:171]\n"
MF "a[204:207],a[216:219],v[72:75],a[204:207]\n"
DT "a[216:217],v21 offset:27680\n"
MF "a[136:139],a[220:223],v[40:43],a[136:139]\n"
DT "a[218:219],v21 offset:32288\n"
"v_subrev_u32 v21,36864,v21\n"
MF "a[172:175],a[220:223],v[56:59],a[172:175]\n"
DT "a[220:221],v22 offset:27648\n"
MF "a[140:143],a[224:227],v[40:43],a[140:143]\n"
DT "a[222:223],v22 offset:32256\n"
BL "v[52:55],v245,s[28:31],s44 offen offset:1024 sc1\n"
MF "a[176:179],a[224:227],v[56:59],a[176:179]\n"
DT "a[224:225],v22 offset:27680\n"
MF "a[144:147],a[228:231],v[40:43],a[144:147]\n"
DT "a[226:227],v22 offset:32288\n"
"v_subrev_u32 v22,36864,v22\n"
MF "a[180:183],a[228:231],v[56:59],a[180:183]\n"
DT "a[228:229],v23 offset:27648\n"
MF "a[148:151],a[232:235],v[40:43],a[148:151]\n"
DT "a[230:231],v23 offset:32256\n"
MF "a[184:187],a[232:235],v[56:59],a[184:187]\n"
DT "a[232:233],v23 offset:27680\n"
MF "a[152:155],a[236:239],v[40:43],a[152:155]\n"
DT "a[234:235],v23 offset:32288\n"
BL "v[68:71],v246,s[28:31],s44 offen offset:1024 sc1\n"
"v_subrev_u32 v23,36864,v23\n"
MF "a[188:191],a[236:239],v[56:59],a[188:191]\n"
DT "a[236:237],v24 offset:27648\n"
MF "a[156:159],a[240:243],v[40:43],a[156:159]\n"
DT "a[238:239],v24 offset:32256\n"
MF "a[192:195],a[240:243],v[56:59],a[192:195]\n"
DT "a[240:241],v24 offset:27680\n"
MF "a[160:163],a[244:247],v[40:43],a[160:163]\n"
DT "a[242:243],v24 offset:32288\n"
"v_subrev_u32 v24,36864,v24\n"
MF "a[196:199],a[244:247],v[56:59],a[196:199]\n"
MF "a[208:211],a[244:247],v[72:75],a[208:211]\n"
DT "a[244:245],v25 offset:27648\n"
DT "a[246:247],v25 offset:32256\n"
"v_subrev_u32 v25,36864,v25\n"
MF "v[96:99],a[64:67],a[32:35],v[112:115]\n"
DT "a[112:113],v7 offset:128\n"
CV "v120,v88,v89\n"
MF "v[96:99],a[72:75],a[36:39],v[96:99]\n"
DT "a[114:115],v7 offset:640\n"
MF "v[96:99],a[80:83],a[40:43],v[96:99]\n"
DT "a[116:117],v7 offset:160\n"
CV "v121,v90,v91\n"
MF "v[96:99],a[88:91],a[44:47],v[96:99]\n"
DT "a[118:119],v7 offset:672\n"
CV "v122,v92,v93\n"
MF "v[80:83],a[64:67],a[0:3],v[104:107]\n"
DT "a[120:121],v7 offset:192\n"
CV "v123,v94,v95\n"
MF "v[84:87],a[68:71],a[0:3],v[104:107]\n"
DT "a[122:123],v7 offset:704\n"
MF "v[80:83],a[72:75],a[4:7],v[80:83]\n"
DT "a[124:125],v7 offset:224\n"
MF "v[84:87],a[76:79],a[4:7],v[84:87]\n"
DT "a[126:127],v7 offset:736\n"
MF "v[80:83],a[80:83],a[8:11],v[80:83]\n"
EX "v96,v96\n"
MF "v[84:87],a[84:87],a[8:11],v[84:87]\n"
"s_mov_b32 s50,s42\n"
EX "v97,v97\n"
MF "v[80:83],a[88:91],a[12:15],v[80:83]\n"
"s_mov_b32 s42,s49\n"
EX "v98,v98\n"
MF "v[84:87],a[92:95],a[12:15],v[84:87]\n"
EX "v99,v99\n"
MF "v[88:91],a[64:67],a[16:19],v[108:111]\n"
CV "v124,v96,v97\n"
CV "v125,v98,v99\n"
MF "v[92:95],a[68:71],a[16:19],v[108:111]\n"
"ds_write_b64 v254,v[124:125]\n"
"v_xor_b32 v254,0x400,v254\n"
MF "v[88:91],a[72:75],a[20:23],v[88:91]\n"
EX "v80,v80\n"
MF "v[92:95],a[76:79],a[20:23],v[92:95]\n"
EX "v81,v81\n"
MF "v[88:91],a[80:83],a[24:27],v[88:91]\n"
EX "v82,v82\n"
MF "v[92:95],a[84:87],a[24:27],v[92:95]\n"
EX "v83,v83\n"
MF "v[88:91],a[88:91],a[28:31],v[88:91]\n"
EX "v84,v84\n"
MF "v[92:95],a[92:95],a[28:31],v[92:95]\n"
WT "vmcnt(20)\n"
WT "vmcnt(3)\n"
WT "lgkmcnt(0)\n"
"s_barrier\n"
"v_add_u32 v6,s49,v4\n"
MF "v[136:139],a[96:99],v[116:119],v[136:139]\n"
DR "a[64:67],v6 offset:0\n"
EX "v85,v85\n"
MF "v[140:143],a[100:103],v[116:119],v[140:143]\n"
DR "a[68:71],v6 offset:512\n"
EX "v86,v86\n"
MF "v[144:147],a[104:107],v[116:119],v[144:147]\n"
DR "a[72:75],v6 offset:64\n"
EX "v87,v87\n"
MF "v[148:151],a[108:111],v[116:119],v[148:151]\n"
DR "a[76:79],v6 offset:576\n"
"v_add_u32 v7,s49,v5\n"
MF "v[152:155],a[112:115],v[116:119],v[152:155]\n"
DR "a[80:83],v6 offset:128\n"
EX "v88,v88\n"
MF "v[156:159],a[116:119],v[116:119],v[156:159]\n"
DR "a[84:87],v6 offset:640\n"
EX "v89,v89\n"
MF "v[160:163],a[120:123],v[116:119],v[160:163]\n"
"s_add_u32 s49,s49,16896\n"
DR "a[88:91],v6 offset:192\n"
EX "v90,v90\n"
MF "v[164:167],a[124:127],v[116:119],v[164:167]\n"
"s_cmp_eq_u32 s49,84480\n"
DR "a[92:95],v6 offset:704\n"
EX "v91,v91\n"
MF "v[168:171],a[48:51],v[116:119],v[168:171]\n"
"s_cselect_b32 s49,0,s49\n"
EX "v92,v92\n"
MF "v[172:175],a[96:99],v[120:123],v[172:175]\n"
EX "v93,v93\n"
MF "v[176:179],a[100:103],v[120:123],v[176:179]\n"
DT "a[96:97],v7 offset:0\n"
CV "v116,v80,v81\n"
MF "v[180:183],a[104:107],v[120:123],v[180:183]\n"
DT "a[98:99],v7 offset:512\n"
CV "v117,v82,v83\n"
MF "v[184:187],a[108:111],v[120:123],v[184:187]\n"
DT "a[100:101],v7 offset:32\n"
CV "v118,v84,v85\n"
MF "v[188:191],a[112:115],v[120:123],v[188:191]\n"
DT "a[102:103],v7 offset:544\n"
CV "v119,v86,v87\n"
MF "v[192:195],a[116:119],v[120:123],v[192:195]\n"
DT "a[104:105],v7 offset:64\n"
EX "v94,v94\n"
MF "v[196:199],a[120:123],v[120:123],v[196:199]\n"
DT "a[106:107],v7 offset:576\n"
EX "v95,v95\n"
MF "v[200:203],a[124:127],v[120:123],v[200:203]\n"
WT "lgkmcnt(6)\n"
DT "a[108:109],v7 offset:96\n"
MF "v[204:207],a[48:51],v[120:123],v[204:207]\n"
DT "a[110:111],v7 offset:608\n"
WT "vmcnt(17)\n"
MF "a[128:131],a[212:215],v[44:47],a[128:131]\n"
MF "a[164:167],a[212:215],v[60:63],a[164:167]\n"
"s_add_u32 s44,s58,0\n"
MF "a[200:203],a[212:215],v[76:79],a[200:203]\n"
DT "a[212:213],v21 offset:0\n"
BL "v[40:43],v244,s[28:31],s44 offen offset:2048 sc1\n"
MF "a[132:135],a[216:219],v[44:47],a[132:135]\n"
DT "a[214:215],v21 offset:4608\n"
MF "a[168:171],a[216:219],v[60:63],a[168:171]\n"
MF "a[204:207],a[216:219],v[76:79],a[204:207]\n"
DT "a[216:217],v21 offset:32\n"
MF "a[136:139],a[220:223],v[44:47],a[136:139]\n"
DT "a[218:219],v21 offset:4640\n"
MF "a[172:175],a[220:223],v[60:63],a[172:175]\n"
DT "a[220:221],v22 offset:0\n"
MF "a[140:143],a[224:227],v[44:47],a[140:143]\n"
DT "a[222:223],v22 offset:4608\n"
BL "v[56:59],v245,s[28:31],s44 offen offset:2048 sc1\n"
MF "a[176:179],a[224:227],v[60:63],a[176:179]\n"
DT "a[224:225],v22 offset:32\n"
MF "a[144:147],a[228:231],v[44:47],a[144:147]\n"
DT "a[226:227],v22 offset:4640\n"
MF "a[180:183],a[228:231],v[60:63],a[180:183]\n"
DT "a[228:229],v23 offset:0\n"
MF "a[148:151],a[232:235],v[44:47],a[148:151]\n"
DT "a[230:231],v23 offset:4608\n"
MF "a[184:187],a[232:235],v[60:63],a[184:187]\n"
DT "a[232:233],v23 offset:32\n"
MF "a[152:155],a[236:239],v[44:47],a[152:155]\n"
DT "a[234:235],v23 offset:4640\n"
BL "v[72:75],v246,s[28:31],s44 offen offset:2048 sc1\n"
MF "a[188:191],a[236:239],v[60:63],a[188:191]\n"
DT "a[236:237],v24 offset:0\n"
MF "a[156:159],a[240:243],v[44:47],a[156:159]\n"
DT "a[238:239],v24 offset:4608\n"
MF "a[192:195],a[240:243],v[60:63],a[192:195]\n"
DT "a[240:241],v24 offset:32\n"
MF "a[160:163],a[244:247],v[44:47],a[160:163]\n"
DT "a[242:243],v24 offset:4640\n"
MF "a[196:199],a[244:247],v[60:63],a[196:199]\n"
MF "a[208:211],a[244:247],v[76:79],a[208:211]\n"
DT "a[244:245],v25 offset:0\n"
DT "a[246:247],v25 offset:4608\n"
MF "v[96:99],a[64:67],a[32:35],v[112:115]\n"
"s_add_u32 s44,s58,0\n"
DT "a[112:113],v7 offset:128\n"
CV "v120,v88,v89\n"
MF "v[96:99],a[72:75],a[36:39],v[96:99]\n"
DT "a[114:115],v7 offset:640\n"
BL "v[44:47],v244,s[28:31],s44 offen offset:3072 sc1\n"
MF "v[96:99],a[80:83],a[40:43],v[96:99]\n"
DT "a[116:117],v7 offset:160\n"
CV "v121,v90,v91\n"
MF "v[96:99],a[88:91],a[44:47],v[96:99]\n"
DT "a[118:119],v7 offset:672\n"
CV "v122,v92,v93\n"
MF "v[80:83],a[64:67],a[0:3],v[104:107]\n"
DT "a[120:121],v7 offset:192\n"
BL "v[60:63],v245,s[28:31],s44 offen offset:3072 sc1\n"
MF "v[84:87],a[68:71],a[0:3],v[104:107]\n"
DT "a[122:123],v7 offset:704\n"
CV "v123,v94,v95\n"
MF "v[80:83],a[72:75],a[4:7],v[80:83]\n"
DT "a[124:125],v7 offset:224\n"
MF "v[84:87],a[76:79],a[4:7],v[84:87]\n"
DT "a[126:127],v7 offset:736\n"
BL "v[76:79],v246,s[28:31],s44 offen offset:3072 sc1\n"
MF "v[80:83],a[80:83],a[8:11],v[80:83]\n"
"s_add_u32 m0,s76,121344\n"
"s_add_u32 s67,s68,147456\n"
EX "v96,v96\n"
MF "v[84:87],a[84:87],a[8:11],v[84:87]\n"
"s_mov_b32 s50,s42\n"
EX "v97,v97\n"
MF "v[80:83],a[88:91],a[12:15],v[80:83]\n"
"s_mov_b32 s42,s49\n"
BL "v12,s[24:27],s67 offen offset:0 lds\n"
EX "v98,v98\n"
MF "v[84:87],a[92:95],a[12:15],v[84:87]\n"
EX "v99,v99\n"
MF "v[88:91],a[64:67],a[16:19],v[108:111]\n"
CV "v124,v96,v97\n"
CV "v125,v98,v99\n"
MF "v[92:95],a[68:71],a[16:19],v[108:111]\n"
"ds_write_b64 v254,v[124:125]\n"
BL "v13,s[24:27],s67 offen offset:1024 lds\n"
"v_xor_b32 v254,0x400,v254\n"
MF "v[88:91],a[72:75],a[20:23],v[88:91]\n"
EX "v80,v80\n"
MF "v[92:95],a[76:79],a[20:23],v[92:95]\n"
EX "v81,v81\n"
MF "v[88:91],a[80:83],a[24:27],v[88:91]\n"
BL "v14,s[24:27],s67 offen offset:2048 lds\n"
EX "v82,v82\n"
MF "v[92:95],a[84:87],a[24:27],v[92:95]\n"
"s_add_u32 m0,s76,124416\n"
"s_add_u32 s67,s68,147456\n"
EX "v83,v83\n"
MF "v[88:91],a[88:91],a[28:31],v[88:91]\n"
EX "v84,v84\n"
MF "v[92:95],a[92:95],a[28:31],v[92:95]\n"
WT "vmcnt(18)\n"
WT "lgkmcnt(0)\n"
"s_barrier\n"
BL "v15,s[24:27],s67 offen offset:0 lds\n"
"v_add_u32 v6,s49,v4\n"
MF "v[136:139],a[96:99],v[116:119],v[136:139]\n"
EX "v85,v85\n"
MF "v[140:143],a[100:103],v[116:119],v[140:143]\n"
EX "v86,v86\n"
MF "v[144:147],a[104:107],v[116:119],v[144:147]\n"
BL "v16,s[24:27],s67 offen offset:1024 lds\n"
EX "v87,v87\n"
MF "v[148:151],a[108:111],v[116:119],v[148:151]\n"
"v_add_u32 v7,s49,v5\n"
MF "v[152:155],a[112:115],v[116:119],v[152:155]\n"
EX "v88,v88\n"
MF "v[156:159],a[116:119],v[116:119],v[156:159]\n"
BL "v17,s[24:27],s67 offen offset:2048 lds\n"
EX "v89,v89\n"
MF "v[160:163],a[120:123],v[116:119],v[160:163]\n"
"s_add_u32 m0,s76,127488\n"
"s_add_u32 s67,s68,147456\n"
"s_add_u32 s49,s49,16896\n"
EX "v90,v90\n"
MF "v[164:167],a[124:127],v[116:119],v[164:167]\n"
"s_cmp_eq_u32 s49,84480\n"
EX "v91,v91\n"
MF "v[168:171],a[48:51],v[116:119],v[168:171]\n"
"s_cselect_b32 s49,0,s49\n"
BL "v18,s[24:27],s67 offen offset:0 lds\n"
EX "v92,v92\n"
MF "v[172:175],a[96:99],v[120:123],v[172:175]\n"
DT "a[96:97],v7 offset:0\n"
DT "a[98:99],v7 offset:512\n"
EX "v93,v93\n"
MF "v[176:179],a[100:103],v[120:123],v[176:179]\n"
DT "a[100:101],v7 offset:32\n"
DT "a[102:103],v7 offset:544\n"
CV "v116,v80,v81\n"
MF "v[180:183],a[104:107],v[120:123],v[180:183]\n"
DT "a[104:105],v7 offset:64\n"
DT "a[106:107],v7 offset:576\n"
BL "v19,s[24:27],s67 offen offset:1024 lds\n"
CV "v117,v82,v83\n"
MF "v[184:187],a[108:111],v[120:123],v[184:187]\n"
DT "a[108:109],v7 offset:96\n"
DT "a[110:111],v7 offset:608\n"
CV "v118,v84,v85\n"
MF "v[188:191],a[112:115],v[120:123],v[188:191]\n"
DT "a[112:113],v7 offset:128\n"
DT "a[114:115],v7 offset:640\n"
CV "v119,v86,v87\n"
MF "v[192:195],a[116:119],v[120:123],v[192:195]\n"
DT "a[116:117],v7 offset:160\n"
DT "a[118:119],v7 offset:672\n"
EX "v94,v94\n"
MF "v[196:199],a[120:123],v[120:123],v[196:199]\n"
DT "a[120:121],v7 offset:192\n"
DT "a[122:123],v7 offset:704\n"
BL "v20,s[24:27],s67 offen offset:2048 lds\n"
EX "v95,v95\n"
MF "v[200:203],a[124:127],v[120:123],v[200:203]\n"
DT "a[124:125],v7 offset:224\n"
DT "a[126:127],v7 offset:736\n"
MF "v[204:207],a[48:51],v[120:123],v[204:207]\n"
CV "v120,v88,v89\n"
CV "v121,v90,v91\n"
CV "v122,v92,v93\n"
CV "v123,v94,v95\n"
WT "vmcnt(24)\n"
WT "lgkmcnt(0)\n"
"s_barrier\n"
MF "v[136:139],a[96:99],v[116:119],v[136:139]\n"
MF "v[140:143],a[100:103],v[116:119],v[140:143]\n"
MF "v[144:147],a[104:107],v[116:119],v[144:147]\n"
MF "v[148:151],a[108:111],v[116:119],v[148:151]\n"
MF "v[152:155],a[112:115],v[116:119],v[152:155]\n"
MF "v[156:159],a[116:119],v[116:119],v[156:159]\n"
MF "v[160:163],a[120:123],v[116:119],v[160:163]\n"
MF "v[164:167],a[124:127],v[116:119],v[164:167]\n"
MF "v[168:171],a[48:51],v[116:119],v[168:171]\n"
MF "v[172:175],a[96:99],v[120:123],v[172:175]\n"
MF "v[176:179],a[100:103],v[120:123],v[176:179]\n"
MF "v[180:183],a[104:107],v[120:123],v[180:183]\n"
MF "v[184:187],a[108:111],v[120:123],v[184:187]\n"
MF "v[188:191],a[112:115],v[120:123],v[188:191]\n"
MF "v[192:195],a[116:119],v[120:123],v[192:195]\n"
MF "v[196:199],a[120:123],v[120:123],v[196:199]\n"
MF "v[200:203],a[124:127],v[120:123],v[200:203]\n"
MF "v[204:207],a[48:51],v[120:123],v[204:207]\n"
"s_nop 1\n"
"v_rcp_f32 v130,v168\n"
"s_add_u32 s66,s56,36864\n"
PM "v[136:137],v[136:137],v[130:131] op_sel_hi:[1,0]\n"
PM "v[138:139],v[138:139],v[130:131] op_sel_hi:[1,0]\n"
PM "v[140:141],v[140:141],v[130:131] op_sel_hi:[1,0]\n"
PM "v[142:143],v[142:143],v[130:131] op_sel_hi:[1,0]\n"
CV "v80,v136,v137\n"
CV "v81,v138,v139\n"
CV "v82,v140,v141\n"
CV "v83,v142,v143\n"
"buffer_store_dwordx4 v[80:83],v244,s[28:31],s66 offen offset:0 sc1\n"
PM "v[144:145],v[144:145],v[130:131] op_sel_hi:[1,0]\n"
PM "v[146:147],v[146:147],v[130:131] op_sel_hi:[1,0]\n"
PM "v[148:149],v[148:149],v[130:131] op_sel_hi:[1,0]\n"
PM "v[150:151],v[150:151],v[130:131] op_sel_hi:[1,0]\n"
CV "v84,v144,v145\n"
CV "v85,v146,v147\n"
CV "v86,v148,v149\n"
CV "v87,v150,v151\n"
"buffer_store_dwordx4 v[84:87],v244,s[28:31],s66 offen offset:1024 sc1\n"
PM "v[152:153],v[152:153],v[130:131] op_sel_hi:[1,0]\n"
PM "v[154:155],v[154:155],v[130:131] op_sel_hi:[1,0]\n"
PM "v[156:157],v[156:157],v[130:131] op_sel_hi:[1,0]\n"
PM "v[158:159],v[158:159],v[130:131] op_sel_hi:[1,0]\n"
CV "v88,v152,v153\n"
CV "v89,v154,v155\n"
CV "v90,v156,v157\n"
CV "v91,v158,v159\n"
"buffer_store_dwordx4 v[88:91],v244,s[28:31],s66 offen offset:2048 sc1\n"
PM "v[160:161],v[160:161],v[130:131] op_sel_hi:[1,0]\n"
PM "v[162:163],v[162:163],v[130:131] op_sel_hi:[1,0]\n"
PM "v[164:165],v[164:165],v[130:131] op_sel_hi:[1,0]\n"
PM "v[166:167],v[166:167],v[130:131] op_sel_hi:[1,0]\n"
CV "v92,v160,v161\n"
CV "v93,v162,v163\n"
CV "v94,v164,v165\n"
CV "v95,v166,v167\n"
"buffer_store_dwordx4 v[92:95],v244,s[28:31],s66 offen offset:3072 sc1\n"
"v_rcp_f32 v130,v204\n"
"s_add_u32 s66,s56,36864\n"
PM "v[172:173],v[172:173],v[130:131] op_sel_hi:[1,0]\n"
PM "v[174:175],v[174:175],v[130:131] op_sel_hi:[1,0]\n"
PM "v[176:177],v[176:177],v[130:131] op_sel_hi:[1,0]\n"
PM "v[178:179],v[178:179],v[130:131] op_sel_hi:[1,0]\n"
CV "v96,v172,v173\n"
CV "v97,v174,v175\n"
CV "v98,v176,v177\n"
CV "v99,v178,v179\n"
"buffer_store_dwordx4 v[96:99],v245,s[28:31],s66 offen offset:0 sc1\n"
PM "v[180:181],v[180:181],v[130:131] op_sel_hi:[1,0]\n"
PM "v[182:183],v[182:183],v[130:131] op_sel_hi:[1,0]\n"
PM "v[184:185],v[184:185],v[130:131] op_sel_hi:[1,0]\n"
PM "v[186:187],v[186:187],v[130:131] op_sel_hi:[1,0]\n"
CV "v100,v180,v181\n"
CV "v101,v182,v183\n"
CV "v102,v184,v185\n"
CV "v103,v186,v187\n"
"buffer_store_dwordx4 v[100:103],v245,s[28:31],s66 offen offset:1024 sc1\n"
PM "v[188:189],v[188:189],v[130:131] op_sel_hi:[1,0]\n"
PM "v[190:191],v[190:191],v[130:131] op_sel_hi:[1,0]\n"
PM "v[192:193],v[192:193],v[130:131] op_sel_hi:[1,0]\n"
PM "v[194:195],v[194:195],v[130:131] op_sel_hi:[1,0]\n"
CV "v104,v188,v189\n"
CV "v105,v190,v191\n"
CV "v106,v192,v193\n"
CV "v107,v194,v195\n"
"buffer_store_dwordx4 v[104:107],v245,s[28:31],s66 offen offset:2048 sc1\n"
PM "v[196:197],v[196:197],v[130:131] op_sel_hi:[1,0]\n"
PM "v[198:199],v[198:199],v[130:131] op_sel_hi:[1,0]\n"
PM "v[200:201],v[200:201],v[130:131] op_sel_hi:[1,0]\n"
PM "v[202:203],v[202:203],v[130:131] op_sel_hi:[1,0]\n"
CV "v108,v196,v197\n"
CV "v109,v198,v199\n"
CV "v110,v200,v201\n"
CV "v111,v202,v203\n"
"buffer_store_dwordx4 v[108:111],v245,s[28:31],s66 offen offset:3072 sc1\n"
"v_add_f32 v128,v168,v204\n"
"v_cmp_ngt_f32 vcc,s43,v128\n"
"s_cmp_lg_u64 vcc,0\n"
"s_cselect_b32 s66,1,0\n"
"v_mov_b32 v128,s66\n"
"ds_write_b32 v251,v128 offset:16\n"
"LO14_%=:\n"
"s_lshl_b32 s61,s36,10\n"
WT "vmcnt(17)\n"
WT "lgkmcnt(0)\n"
"v_accvgpr_read_b32 v136,a128\n"
"v_accvgpr_read_b32 v137,a129\n"
"v_accvgpr_read_b32 v138,a130\n"
"v_accvgpr_read_b32 v139,a131\n"
"v_accvgpr_read_b32 v172,a164\n"
"v_accvgpr_read_b32 v173,a165\n"
"v_accvgpr_read_b32 v174,a166\n"
"v_accvgpr_read_b32 v175,a167\n"
MF "v[136:139],a[212:215],v[32:35],v[136:139]\n"
"s_add_u32 m0,s76,0\n"
DT "a[64:65],v21 offset:9216\n"
"v_accvgpr_read_b32 v208,a200\n"
"v_accvgpr_read_b32 v209,a201\n"
"v_accvgpr_read_b32 v210,a202\n"
"v_accvgpr_read_b32 v211,a203\n"
MF "v[172:175],a[212:215],v[48:51],v[172:175]\n"
"s_add_u32 s67,s71,0\n"
DT "a[66:67],v21 offset:13824\n"
"v_accvgpr_read_b32 v140,a132\n"
"v_accvgpr_read_b32 v141,a133\n"
"v_accvgpr_read_b32 v142,a134\n"
"v_accvgpr_read_b32 v143,a135\n"
MF "v[208:211],a[212:215],v[64:67],v[208:211]\n"
DT "a[68:69],v21 offset:9248\n"
BL "v12,s[24:27],s67 offen offset:0 lds\n"
"v_accvgpr_read_b32 v176,a168\n"
"v_accvgpr_read_b32 v177,a169\n"
"v_accvgpr_read_b32 v178,a170\n"
"v_accvgpr_read_b32 v179,a171\n"
MF "v[140:143],a[216:219],v[32:35],v[140:143]\n"
DT "a[70:71],v21 offset:13856\n"
"v_accvgpr_read_b32 v212,a204\n"
"v_accvgpr_read_b32 v213,a205\n"
"v_accvgpr_read_b32 v214,a206\n"
"v_accvgpr_read_b32 v215,a207\n"
MF "v[176:179],a[216:219],v[48:51],v[176:179]\n"
DT "a[72:73],v22 offset:9216\n"
"v_accvgpr_read_b32 v144,a136\n"
"v_accvgpr_read_b32 v145,a137\n"
"v_accvgpr_read_b32 v146,a138\n"
"v_accvgpr_read_b32 v147,a139\n"
MF "v[212:215],a[216:219],v[64:67],v[212:215]\n"
DT "a[74:75],v22 offset:13824\n"
"v_accvgpr_read_b32 v180,a172\n"
"v_accvgpr_read_b32 v181,a173\n"
"v_accvgpr_read_b32 v182,a174\n"
"v_accvgpr_read_b32 v183,a175\n"
MF "v[144:147],a[220:223],v[32:35],v[144:147]\n"
DT "a[76:77],v22 offset:9248\n"
BL "v13,s[24:27],s67 offen offset:1024 lds\n"
"v_accvgpr_read_b32 v148,a140\n"
"v_accvgpr_read_b32 v149,a141\n"
"v_accvgpr_read_b32 v150,a142\n"
"v_accvgpr_read_b32 v151,a143\n"
MF "v[180:183],a[220:223],v[48:51],v[180:183]\n"
DT "a[78:79],v22 offset:13856\n"
"v_accvgpr_read_b32 v184,a176\n"
"v_accvgpr_read_b32 v185,a177\n"
"v_accvgpr_read_b32 v186,a178\n"
"v_accvgpr_read_b32 v187,a179\n"
MF "v[148:151],a[224:227],v[32:35],v[148:151]\n"
DT "a[80:81],v23 offset:9216\n"
"v_accvgpr_read_b32 v152,a144\n"
"v_accvgpr_read_b32 v153,a145\n"
"v_accvgpr_read_b32 v154,a146\n"
"v_accvgpr_read_b32 v155,a147\n"
MF "v[184:187],a[224:227],v[48:51],v[184:187]\n"
DT "a[82:83],v23 offset:13824\n"
"v_accvgpr_read_b32 v188,a180\n"
"v_accvgpr_read_b32 v189,a181\n"
"v_accvgpr_read_b32 v190,a182\n"
"v_accvgpr_read_b32 v191,a183\n"
MF "v[152:155],a[228:231],v[32:35],v[152:155]\n"
DT "a[84:85],v23 offset:9248\n"
BL "v14,s[24:27],s67 offen offset:2048 lds\n"
"v_accvgpr_read_b32 v156,a148\n"
"v_accvgpr_read_b32 v157,a149\n"
"v_accvgpr_read_b32 v158,a150\n"
"v_accvgpr_read_b32 v159,a151\n"
MF "v[188:191],a[228:231],v[48:51],v[188:191]\n"
DT "a[86:87],v23 offset:13856\n"
"v_accvgpr_read_b32 v192,a184\n"
"v_accvgpr_read_b32 v193,a185\n"
"v_accvgpr_read_b32 v194,a186\n"
"v_accvgpr_read_b32 v195,a187\n"
MF "v[156:159],a[232:235],v[32:35],v[156:159]\n"
WT "lgkmcnt(12)\n"
DT "a[88:89],v24 offset:9216\n"
"v_accvgpr_read_b32 v160,a152\n"
"v_accvgpr_read_b32 v161,a153\n"
"v_accvgpr_read_b32 v162,a154\n"
"v_accvgpr_read_b32 v163,a155\n"
MF "v[192:195],a[232:235],v[48:51],v[192:195]\n"
"s_add_u32 m0,s76,3072\n"
DT "a[90:91],v24 offset:13824\n"
"v_accvgpr_read_b32 v196,a188\n"
"v_accvgpr_read_b32 v197,a189\n"
"v_accvgpr_read_b32 v198,a190\n"
"v_accvgpr_read_b32 v199,a191\n"
MF "v[160:163],a[236:239],v[32:35],v[160:163]\n"
"s_add_u32 s67,s71,0\n"
DT "a[92:93],v24 offset:9248\n"
"v_accvgpr_read_b32 v164,a156\n"
"v_accvgpr_read_b32 v165,a157\n"
"v_accvgpr_read_b32 v166,a158\n"
"v_accvgpr_read_b32 v167,a159\n"
MF "v[196:199],a[236:239],v[48:51],v[196:199]\n"
DT "a[94:95],v24 offset:13856\n"
BL "v15,s[24:27],s67 offen offset:0 lds\n"
"v_accvgpr_read_b32 v200,a192\n"
"v_accvgpr_read_b32 v201,a193\n"
"v_accvgpr_read_b32 v202,a194\n"
"v_accvgpr_read_b32 v203,a195\n"
MF "v[164:167],a[240:243],v[32:35],v[164:167]\n"
DT "a[96:97],v25 offset:9216\n"
"v_accvgpr_read_b32 v168,a160\n"
"v_accvgpr_read_b32 v169,a161\n"
"v_accvgpr_read_b32 v170,a162\n"
"v_accvgpr_read_b32 v171,a163\n"
MF "v[200:203],a[240:243],v[48:51],v[200:203]\n"
DT "a[98:99],v25 offset:13824\n"
"v_accvgpr_read_b32 v204,a196\n"
"v_accvgpr_read_b32 v205,a197\n"
"v_accvgpr_read_b32 v206,a198\n"
"v_accvgpr_read_b32 v207,a199\n"
MF "v[168:171],a[244:247],v[32:35],v[168:171]\n"
"v_accvgpr_read_b32 v216,a208\n"
"v_accvgpr_read_b32 v217,a209\n"
"v_accvgpr_read_b32 v218,a210\n"
"v_accvgpr_read_b32 v219,a211\n"
MF "v[204:207],a[244:247],v[48:51],v[204:207]\n"
BL "v16,s[24:27],s67 offen offset:1024 lds\n"
MF "v[216:219],a[244:247],v[64:67],v[216:219]\n"
WT "lgkmcnt(6)\n"
MF "v[136:139],a[64:67],v[36:39],v[136:139]\n"
DT "a[212:213],v21 offset:18432\n"
MF "v[172:175],a[64:67],v[52:55],v[172:175]\n"
DT "a[214:215],v21 offset:23040\n"
MF "v[208:211],a[64:67],v[68:71],v[208:211]\n"
DT "a[216:217],v21 offset:18464\n"
MF "v[140:143],a[68:71],v[36:39],v[140:143]\n"
DT "a[218:219],v21 offset:23072\n"
BL "v17,s[24:27],s67 offen offset:2048 lds\n"
MF "v[176:179],a[68:71],v[52:55],v[176:179]\n"
DT "a[220:221],v22 offset:18432\n"
MF "v[212:215],a[68:71],v[68:71],v[212:215]\n"
"s_add_u32 m0,s76,6144\n"
DT "a[222:223],v22 offset:23040\n"
MF "v[144:147],a[72:75],v[36:39],v[144:147]\n"
"s_add_u32 s67,s71,0\n"
DT "a[224:225],v22 offset:18464\n"
MF "v[180:183],a[72:75],v[52:55],v[180:183]\n"
DT "a[226:227],v22 offset:23072\n"
BL "v18,s[24:27],s67 offen offset:0 lds\n"
MF "v[148:151],a[76:79],v[36:39],v[148:151]\n"
DT "a[228:229],v23 offset:18432\n"
MF "v[184:187],a[76:79],v[52:55],v[184:187]\n"
DT "a[230:231],v23 offset:23040\n"
MF "v[152:155],a[80:83],v[36:39],v[152:155]\n"
DT "a[232:233],v23 offset:18464\n"
MF "v[188:191],a[80:83],v[52:55],v[188:191]\n"
DT "a[234:235],v23 offset:23072\n"
MF "v[156:159],a[84:87],v[36:39],v[156:159]\n"
WT "lgkmcnt(12)\n"
DT "a[236:237],v24 offset:18432\n"
BL "v19,s[24:27],s67 offen offset:1024 lds\n"
MF "v[192:195],a[84:87],v[52:55],v[192:195]\n"
DT "a[238:239],v24 offset:23040\n"
MF "v[160:163],a[88:91],v[36:39],v[160:163]\n"
DT "a[240:241],v24 offset:18464\n"
MF "v[196:199],a[88:91],v[52:55],v[196:199]\n"
DT "a[242:243],v24 offset:23072\n"
MF "v[164:167],a[92:95],v[36:39],v[164:167]\n"
DT "a[244:245],v25 offset:18432\n"
BL "v20,s[24:27],s67 offen offset:2048 lds\n"
MF "v[200:203],a[92:95],v[52:55],v[200:203]\n"
DT "a[246:247],v25 offset:23040\n"
MF "v[168:171],a[96:99],v[36:39],v[168:171]\n"
MF "v[204:207],a[96:99],v[52:55],v[204:207]\n"
"s_add_u32 m0,s61,73728\n"
MF "v[216:219],a[96:99],v[68:71],v[216:219]\n"
"s_add_u32 s45,s59,0\n"
WT "lgkmcnt(6)\n"
MF "v[136:139],a[212:215],v[40:43],v[136:139]\n"
DT "a[64:65],v21 offset:27648\n"
BL "v249,s[28:31],s45 offen sc1 lds\n"
MF "v[172:175],a[212:215],v[56:59],v[172:175]\n"
DT "a[66:67],v21 offset:32256\n"
MF "v[208:211],a[212:215],v[72:75],v[208:211]\n"
DT "a[68:69],v21 offset:27680\n"
MF "v[140:143],a[216:219],v[40:43],v[140:143]\n"
"s_add_u32 s44,s59,0\n"
DT "a[70:71],v21 offset:32288\n"
"v_add_u32 v21,36864,v21\n"
MF "v[176:179],a[216:219],v[56:59],v[176:179]\n"
DT "a[72:73],v22 offset:27648\n"
BL "a[0:3],v244,s[28:31],s44 offen offset:0 sc1\n"
MF "v[212:215],a[216:219],v[72:75],v[212:215]\n"
DT "a[74:75],v22 offset:32256\n"
MF "v[144:147],a[220:223],v[40:43],v[144:147]\n"
DT "a[76:77],v22 offset:27680\n"
MF "v[180:183],a[220:223],v[56:59],v[180:183]\n"
DT "a[78:79],v22 offset:32288\n"
"v_add_u32 v22,36864,v22\n"
MF "v[148:151],a[224:227],v[40:43],v[148:151]\n"
DT "a[80:81],v23 offset:27648\n"
BL "a[16:19],v245,s[28:31],s44 offen offset:0 sc1\n"
MF "v[184:187],a[224:227],v[56:59],v[184:187]\n"
DT "a[82:83],v23 offset:32256\n"
MF "v[152:155],a[228:231],v[40:43],v[152:155]\n"
DT "a[84:85],v23 offset:27680\n"
MF "v[188:191],a[228:231],v[56:59],v[188:191]\n"
DT "a[86:87],v23 offset:32288\n"
"v_add_u32 v23,36864,v23\n"
MF "v[156:159],a[232:235],v[40:43],v[156:159]\n"
WT "lgkmcnt(12)\n"
DT "a[88:89],v24 offset:27648\n"
MF "v[192:195],a[232:235],v[56:59],v[192:195]\n"
DT "a[90:91],v24 offset:32256\n"
BL "a[4:7],v244,s[28:31],s44 offen offset:1024 sc1\n"
MF "v[160:163],a[236:239],v[40:43],v[160:163]\n"
DT "a[92:93],v24 offset:27680\n"
MF "v[196:199],a[236:239],v[56:59],v[196:199]\n"
DT "a[94:95],v24 offset:32288\n"
"v_add_u32 v24,36864,v24\n"
MF "v[164:167],a[240:243],v[40:43],v[164:167]\n"
DT "a[96:97],v25 offset:27648\n"
MF "v[200:203],a[240:243],v[56:59],v[200:203]\n"
DT "a[98:99],v25 offset:32256\n"
BL "a[20:23],v245,s[28:31],s44 offen offset:1024 sc1\n"
"v_add_u32 v25,36864,v25\n"
MF "v[168:171],a[244:247],v[40:43],v[168:171]\n"
MF "v[204:207],a[244:247],v[56:59],v[204:207]\n"
WT "vmcnt(22)\n"
WT "vmcnt(14)\n"
MF "v[216:219],a[244:247],v[72:75],v[216:219]\n"
WT "lgkmcnt(6)\n"
"s_barrier\n"
MF "v[136:139],a[64:67],v[44:47],v[136:139]\n"
DR "v[128:131],v252 offset:16\n"
DR "v[112:115],v247 offset:0\n"
MF "v[172:175],a[64:67],v[60:63],v[172:175]\n"
DT "a[212:213],v21 offset:0\n"
BL "a[8:11],v244,s[28:31],s44 offen offset:2048 sc1\n"
MF "v[208:211],a[64:67],v[76:79],v[208:211]\n"
DT "a[214:215],v21 offset:4608\n"
MF "v[140:143],a[68:71],v[44:47],v[140:143]\n"
DT "a[216:217],v21 offset:32\n"
MF "v[176:179],a[68:71],v[60:63],v[176:179]\n"
DT "a[218:219],v21 offset:4640\n"
MF "v[212:215],a[68:71],v[76:79],v[212:215]\n"
DT "a[220:221],v22 offset:0\n"
BL "a[24:27],v245,s[28:31],s44 offen offset:2048 sc1\n"
MF "v[144:147],a[72:75],v[44:47],v[144:147]\n"
DT "a[222:223],v22 offset:4608\n"
MF "v[180:183],a[72:75],v[60:63],v[180:183]\n"
DT "a[224:225],v22 offset:32\n"
MF "v[148:151],a[76:79],v[44:47],v[148:151]\n"
DT "a[226:227],v22 offset:4640\n"
MF "v[184:187],a[76:79],v[60:63],v[184:187]\n"
DT "a[228:229],v23 offset:0\n"
MF "v[152:155],a[80:83],v[44:47],v[152:155]\n"
DT "a[230:231],v23 offset:4608\n"
BL "a[12:15],v244,s[28:31],s44 offen offset:3072 sc1\n"
MF "v[188:191],a[80:83],v[60:63],v[188:191]\n"
DT "a[232:233],v23 offset:32\n"
MF "v[156:159],a[84:87],v[44:47],v[156:159]\n"
WT "lgkmcnt(12)\n"
DT "a[234:235],v23 offset:4640\n"
MF "v[192:195],a[84:87],v[60:63],v[192:195]\n"
DT "a[236:237],v24 offset:0\n"
MF "v[160:163],a[88:91],v[44:47],v[160:163]\n"
DT "a[238:239],v24 offset:4608\n"
BL "a[28:31],v245,s[28:31],s44 offen offset:3072 sc1\n"
"v_or3_b32 v128,v128,v129,v130\n"
MF "v[196:199],a[88:91],v[60:63],v[196:199]\n"
DT "a[240:241],v24 offset:32\n"
"v_or_b32 v128,v128,v131\n"
MF "v[164:167],a[92:95],v[44:47],v[164:167]\n"
DT "a[242:243],v24 offset:4640\n"
MF "v[200:203],a[92:95],v[60:63],v[200:203]\n"
DT "a[244:245],v25 offset:0\n"
"v_cmp_ne_u32 vcc,0,v128\n"
MF "v[168:171],a[96:99],v[44:47],v[168:171]\n"
DT "a[246:247],v25 offset:4608\n"
"s_cbranch_vccnz Lr22_%=\n"
MF "v[204:207],a[96:99],v[60:63],v[204:207]\n"
MF "v[216:219],a[96:99],v[76:79],v[216:219]\n"
WT "lgkmcnt(6)\n"
"v_add_u32 v3,1,v29\n"
"s_lshl_b64 exec,1,s40\n"
"buffer_store_dword v3,v26,s[28:31],0 offen offset:128 sc1\n"
"s_mov_b64 exec,-1\n"
MF "v[136:139],a[212:215],v[80:83],v[136:139]\n"
"s_add_u32 m0,s76,36864\n"
DR "v[116:119],v247 offset:1024\n"
MF "v[172:175],a[212:215],v[96:99],v[172:175]\n"
"s_add_u32 s67,s69,147456\n"
DT "a[64:65],v21 offset:9216\n"
MF "v[208:211],a[212:215],v[112:115],v[208:211]\n"
DT "a[66:67],v21 offset:13824\n"
BL "v12,s[24:27],s67 offen offset:0 lds\n"
MF "v[140:143],a[216:219],v[80:83],v[140:143]\n"
DT "a[68:69],v21 offset:9248\n"
MF "v[176:179],a[216:219],v[96:99],v[176:179]\n"
DT "a[70:71],v21 offset:13856\n"
MF "v[212:215],a[216:219],v[112:115],v[212:215]\n"
DT "a[72:73],v22 offset:9216\n"
MF "v[144:147],a[220:223],v[80:83],v[144:147]\n"
DT "a[74:75],v22 offset:13824\n"
MF "v[180:183],a[220:223],v[96:99],v[180:183]\n"
DT "a[76:77],v22 offset:9248\n"
MF "v[148:151],a[224:227],v[80:83],v[148:151]\n"
DT "a[78:79],v22 offset:13856\n"
MF "v[184:187],a[224:227],v[96:99],v[184:187]\n"
DT "a[80:81],v23 offset:9216\n"
MF "v[152:155],a[228:231],v[80:83],v[152:155]\n"
DT "a[82:83],v23 offset:13824\n"
BL "v13,s[24:27],s67 offen offset:1024 lds\n"
MF "v[188:191],a[228:231],v[96:99],v[188:191]\n"
DT "a[84:85],v23 offset:9248\n"
MF "v[156:159],a[232:235],v[80:83],v[156:159]\n"
WT "lgkmcnt(12)\n"
DT "a[86:87],v23 offset:13856\n"
MF "v[192:195],a[232:235],v[96:99],v[192:195]\n"
DT "a[88:89],v24 offset:9216\n"
MF "v[160:163],a[236:239],v[80:83],v[160:163]\n"
DT "a[90:91],v24 offset:13824\n"
MF "v[196:199],a[236:239],v[96:99],v[196:199]\n"
DT "a[92:93],v24 offset:9248\n"
MF "v[164:167],a[240:243],v[80:83],v[164:167]\n"
DT "a[94:95],v24 offset:13856\n"
MF "v[200:203],a[240:243],v[96:99],v[200:203]\n"
DT "a[96:97],v25 offset:9216\n"
MF "v[168:171],a[244:247],v[80:83],v[168:171]\n"
DT "a[98:99],v25 offset:13824\n"
MF "v[204:207],a[244:247],v[96:99],v[204:207]\n"
BL "v14,s[24:27],s67 offen offset:2048 lds\n"
MF "v[216:219],a[244:247],v[112:115],v[216:219]\n"
WT "lgkmcnt(6)\n"
MF "v[136:139],a[64:67],v[84:87],v[136:139]\n"
DR "v[120:123],v247 offset:2048\n"
MF "v[172:175],a[64:67],v[100:103],v[172:175]\n"
DT "a[212:213],v21 offset:18432\n"
MF "v[208:211],a[64:67],v[116:119],v[208:211]\n"
DT "a[214:215],v21 offset:23040\n"
MF "v[140:143],a[68:71],v[84:87],v[140:143]\n"
DT "a[216:217],v21 offset:18464\n"
MF "v[176:179],a[68:71],v[100:103],v[176:179]\n"
DT "a[218:219],v21 offset:23072\n"
MF "v[212:215],a[68:71],v[116:119],v[212:215]\n"
"s_add_u32 m0,s76,39936\n"
DT "a[220:221],v22 offset:18432\n"
MF "v[144:147],a[72:75],v[84:87],v[144:147]\n"
"s_add_u32 s67,s69,147456\n"
DT "a[222:223],v22 offset:23040\n"
MF "v[180:183],a[72:75],v[100:103],v[180:183]\n"
DT "a[224:225],v22 offset:18464\n"
BL "v15,s[24:27],s67 offen offset:0 lds\n"
MF "v[148:151],a[76:79],v[84:87],v[148:151]\n"
DT "a[226:227],v22 offset:23072\n"
MF "v[184:187],a[76:79],v[100:103],v[184:187]\n"
DT "a[228:229],v23 offset:18432\n"
MF "v[152:155],a[80:83],v[84:87],v[152:155]\n"
DT "a[230:231],v23 offset:23040\n"
MF "v[188:191],a[80:83],v[100:103],v[188:191]\n"
DT "a[232:233],v23 offset:18464\n"
MF "v[156:159],a[84:87],v[84:87],v[156:159]\n"
WT "lgkmcnt(12)\n"
DT "a[234:235],v23 offset:23072\n"
MF "v[192:195],a[84:87],v[100:103],v[192:195]\n"
DT "a[236:237],v24 offset:18432\n"
MF "v[160:163],a[88:91],v[84:87],v[160:163]\n"
DT "a[238:239],v24 offset:23040\n"
MF "v[196:199],a[88:91],v[100:103],v[196:199]\n"
DT "a[240:241],v24 offset:18464\n"
MF "v[164:167],a[92:95],v[84:87],v[164:167]\n"
DT "a[242:243],v24 offset:23072\n"
BL "v16,s[24:27],s67 offen offset:1024 lds\n"
MF "v[200:203],a[92:95],v[100:103],v[200:203]\n"
DT "a[244:245],v25 offset:18432\n"
MF "v[168:171],a[96:99],v[84:87],v[168:171]\n"
DT "a[246:247],v25 offset:23040\n"
MF "v[204:207],a[96:99],v[100:103],v[204:207]\n"
MF "v[216:219],a[96:99],v[116:119],v[216:219]\n"
WT "lgkmcnt(6)\n"
MF "v[136:139],a[212:215],v[88:91],v[136:139]\n"
DR "v[124:127],v247 offset:3072\n"
MF "v[172:175],a[212:215],v[104:107],v[172:175]\n"
DT "a[64:65],v21 offset:27648\n"
MF "v[208:211],a[212:215],v[120:123],v[208:211]\n"
DT "a[66:67],v21 offset:32256\n"
MF "v[140:143],a[216:219],v[88:91],v[140:143]\n"
DT "a[68:69],v21 offset:27680\n"
MF "v[176:179],a[216:219],v[104:107],v[176:179]\n"
DT "a[70:71],v21 offset:32288\n"
BL "v17,s[24:27],s67 offen offset:2048 lds\n"
"v_subrev_u32 v21,121344,v21\n"
MF "v[212:215],a[216:219],v[120:123],v[212:215]\n"
DT "a[72:73],v22 offset:27648\n"
MF "v[144:147],a[220:223],v[88:91],v[144:147]\n"
DT "a[74:75],v22 offset:32256\n"
MF "v[180:183],a[220:223],v[104:107],v[180:183]\n"
DT "a[76:77],v22 offset:27680\n"
MF "v[148:151],a[224:227],v[88:91],v[148:151]\n"
DT "a[78:79],v22 offset:32288\n"
"v_subrev_u32 v22,121344,v22\n"
MF "v[184:187],a[224:227],v[104:107],v[184:187]\n"
DT "a[80:81],v23 offset:27648\n"
MF "v[152:155],a[228:231],v[88:91],v[152:155]\n"
DT "a[82:83],v23 offset:32256\n"
MF "v[188:191],a[228:231],v[104:107],v[188:191]\n"
"s_add_u32 m0,s76,43008\n"
DT "a[84:85],v23 offset:27680\n"
MF "v[156:159],a[232:235],v[88:91],v[156:159]\n"
WT "lgkmcnt(12)\n"
"s_add_u32 s67,s69,147456\n"
DT "a[86:87],v23 offset:32288\n"
"v_subrev_u32 v23,121344,v23\n"
MF "v[192:195],a[232:235],v[104:107],v[192:195]\n"
DT "a[88:89],v24 offset:27648\n"
BL "v18,s[24:27],s67 offen offset:0 lds\n"
MF "v[160:163],a[236:239],v[88:91],v[160:163]\n"
DT "a[90:91],v24 offset:32256\n"
MF "v[196:199],a[236:239],v[104:107],v[196:199]\n"
DT "a[92:93],v24 offset:27680\n"
MF "v[164:167],a[240:243],v[88:91],v[164:167]\n"
DT "a[94:95],v24 offset:32288\n"
"v_subrev_u32 v24,121344,v24\n"
MF "v[200:203],a[240:243],v[104:107],v[200:203]\n"
DT "a[96:97],v25 offset:27648\n"
MF "v[168:171],a[244:247],v[88:91],v[168:171]\n"
DT "a[98:99],v25 offset:32256\n"
"v_subrev_u32 v25,121344,v25\n"
MF "v[204:207],a[244:247],v[104:107],v[204:207]\n"
WT "vmcnt(16)\n"
MF "v[216:219],a[244:247],v[120:123],v[216:219]\n"
WT "lgkmcnt(6)\n"
"s_barrier\n"
MF "v[136:139],a[64:67],v[92:95],v[136:139]\n"
DR "v[64:67],v248 offset:0\n"
MF "v[172:175],a[64:67],v[108:111],v[172:175]\n"
DT "a[212:213],v21 offset:0\n"
BL "v19,s[24:27],s67 offen offset:1024 lds\n"
"buffer_load_dword v31,v26,s[28:31],0 offen offset:128 sc1\n"
MF "v[208:211],a[64:67],v[124:127],v[208:211]\n"
DT "a[214:215],v21 offset:4608\n"
MF "v[140:143],a[68:71],v[92:95],v[140:143]\n"
DT "a[216:217],v21 offset:32\n"
MF "v[176:179],a[68:71],v[108:111],v[176:179]\n"
DT "a[218:219],v21 offset:4640\n"
MF "v[212:215],a[68:71],v[124:127],v[212:215]\n"
DT "a[220:221],v22 offset:0\n"
MF "v[144:147],a[72:75],v[92:95],v[144:147]\n"
DT "a[222:223],v22 offset:4608\n"
MF "v[180:183],a[72:75],v[108:111],v[180:183]\n"
DT "a[224:225],v22 offset:32\n"
MF "v[148:151],a[76:79],v[92:95],v[148:151]\n"
DT "a[226:227],v22 offset:4640\n"
MF "v[184:187],a[76:79],v[108:111],v[184:187]\n"
DT "a[228:229],v23 offset:0\n"
MF "v[152:155],a[80:83],v[92:95],v[152:155]\n"
DT "a[230:231],v23 offset:4608\n"
BL "v20,s[24:27],s67 offen offset:2048 lds\n"
MF "v[188:191],a[80:83],v[108:111],v[188:191]\n"
DT "a[232:233],v23 offset:32\n"
MF "v[156:159],a[84:87],v[92:95],v[156:159]\n"
WT "lgkmcnt(12)\n"
DT "a[234:235],v23 offset:4640\n"
MF "v[192:195],a[84:87],v[108:111],v[192:195]\n"
DT "a[236:237],v24 offset:0\n"
MF "v[160:163],a[88:91],v[92:95],v[160:163]\n"
DT "a[238:239],v24 offset:4608\n"
MF "v[196:199],a[88:91],v[108:111],v[196:199]\n"
DT "a[240:241],v24 offset:32\n"
MF "v[164:167],a[92:95],v[92:95],v[164:167]\n"
DT "a[242:243],v24 offset:4640\n"
MF "v[200:203],a[92:95],v[108:111],v[200:203]\n"
DT "a[244:245],v25 offset:0\n"
MF "v[168:171],a[96:99],v[92:95],v[168:171]\n"
DT "a[246:247],v25 offset:4608\n"
MF "v[204:207],a[96:99],v[108:111],v[204:207]\n"
WT "vmcnt(11)\n"
MF "v[216:219],a[96:99],v[124:127],v[216:219]\n"
WT "lgkmcnt(6)\n"
WT "vmcnt(1)\n"
"v_cmp_eq_u32 vcc,v31,v29\n"
"s_andn2_b64 vcc,vcc,s[62:63]\n"
"s_cbranch_scc1 Lp23_%=\n"
"Lp24_%=:\n"
MF "v[136:139],a[212:215],a[0:3],v[136:139]\n"
"s_add_u32 m0,s61,77824\n"
DR "v[68:71],v248 offset:1024\n"
MF "v[172:175],a[212:215],a[16:19],v[172:175]\n"
"s_add_u32 s45,s57,36864\n"
DT "a[64:65],v21 offset:9216\n"
MF "v[208:211],a[212:215],v[64:67],v[208:211]\n"
DT "a[66:67],v21 offset:13824\n"
BL "v249,s[28:31],s45 offen sc1 lds\n"
MF "v[140:143],a[216:219],a[0:3],v[140:143]\n"
DT "a[68:69],v21 offset:9248\n"
MF "v[176:179],a[216:219],a[16:19],v[176:179]\n"
DT "a[70:71],v21 offset:13856\n"
MF "v[212:215],a[216:219],v[64:67],v[212:215]\n"
"s_add_u32 s44,s57,36864\n"
DT "a[72:73],v22 offset:9216\n"
MF "v[144:147],a[220:223],a[0:3],v[144:147]\n"
DT "a[74:75],v22 offset:13824\n"
BL "a[32:35],v244,s[28:31],s44 offen offset:0 sc1\n"
MF "v[180:183],a[220:223],a[16:19],v[180:183]\n"
DT "a[76:77],v22 offset:9248\n"
MF "v[148:151],a[224:227],a[0:3],v[148:151]\n"
DT "a[78:79],v22 offset:13856\n"
MF "v[184:187],a[224:227],a[16:19],v[184:187]\n"
DT "a[80:81],v23 offset:9216\n"
MF "v[152:155],a[228:231],a[0:3],v[152:155]\n"
DT "a[82:83],v23 offset:13824\n"
BL "a[48:51],v245,s[28:31],s44 offen offset:0 sc1\n"
MF "v[188:191],a[228:231],a[16:19],v[188:191]\n"
DT "a[84:85],v23 offset:9248\n"
MF "v[156:159],a[232:235],a[0:3],v[156:159]\n"
WT "lgkmcnt(12)\n"
DT "a[86:87],v23 offset:13856\n"
MF "v[192:195],a[232:235],a[16:19],v[192:195]\n"
DT "a[88:89],v24 offset:9216\n"
MF "v[160:163],a[236:239],a[0:3],v[160:163]\n"
DT "a[90:91],v24 offset:13824\n"
MF "v[196:199],a[236:239],a[16:19],v[196:199]\n"
DT "a[92:93],v24 offset:9248\n"
BL "a[36:39],v244,s[28:31],s44 offen offset:1024 sc1\n"
MF "v[164:167],a[240:243],a[0:3],v[164:167]\n"
DT "a[94:95],v24 offset:13856\n"
MF "v[200:203],a[240:243],a[16:19],v[200:203]\n"
DT "a[96:97],v25 offset:9216\n"
MF "v[168:171],a[244:247],a[0:3],v[168:171]\n"
DT "a[98:99],v25 offset:13824\n"
MF "v[204:207],a[244:247],a[16:19],v[204:207]\n"
BL "a[52:55],v245,s[28:31],s44 offen offset:1024 sc1\n"
MF "v[216:219],a[244:247],v[64:67],v[216:219]\n"
WT "lgkmcnt(6)\n"
MF "v[136:139],a[64:67],a[4:7],v[136:139]\n"
DR "v[72:75],v248 offset:2048\n"
MF "v[172:175],a[64:67],a[20:23],v[172:175]\n"
DT "a[212:213],v21 offset:18432\n"
MF "v[208:211],a[64:67],v[68:71],v[208:211]\n"
DT "a[214:215],v21 offset:23040\n"
MF "v[140:143],a[68:71],a[4:7],v[140:143]\n"
DT "a[216:217],v21 offset:18464\n"
BL "a[40:43],v244,s[28:31],s44 offen offset:2048 sc1\n"
MF "v[176:179],a[68:71],a[20:23],v[176:179]\n"
DT "a[218:219],v21 offset:23072\n"
MF "v[212:215],a[68:71],v[68:71],v[212:215]\n"
DT "a[220:221],v22 offset:18432\n"
MF "v[144:147],a[72:75],a[4:7],v[144:147]\n"
DT "a[222:223],v22 offset:23040\n"
MF "v[180:183],a[72:75],a[20:23],v[180:183]\n"
DT "a[224:225],v22 offset:18464\n"
BL "a[56:59],v245,s[28:31],s44 offen offset:2048 sc1\n"
MF "v[148:151],a[76:79],a[4:7],v[148:151]\n"
DT "a[226:227],v22 offset:23072\n"
MF "v[184:187],a[76:79],a[20:23],v[184:187]\n"
DT "a[228:229],v23 offset:18432\n"
MF "v[152:155],a[80:83],a[4:7],v[152:155]\n"
DT "a[230:231],v23 offset:23040\n"
MF "v[188:191],a[80:83],a[20:23],v[188:191]\n"
DT "a[232:233],v23 offset:18464\n"
MF "v[156:159],a[84:87],a[4:7],v[156:159]\n"
WT "lgkmcnt(12)\n"
DT "a[234:235],v23 offset:23072\n"
BL "a[44:47],v244,s[28:31],s44 offen offset:3072 sc1\n"
MF "v[192:195],a[84:87],a[20:23],v[192:195]\n"
DT "a[236:237],v24 offset:18432\n"
MF "v[160:163],a[88:91],a[4:7],v[160:163]\n"
DT "a[238:239],v24 offset:23040\n"
MF "v[196:199],a[88:91],a[20:23],v[196:199]\n"
DT "a[240:241],v24 offset:18464\n"
MF "v[164:167],a[92:95],a[4:7],v[164:167]\n"
DT "a[242:243],v24 offset:23072\n"
BL "a[60:63],v245,s[28:31],s44 offen offset:3072 sc1\n"
MF "v[200:203],a[92:95],a[20:23],v[200:203]\n"
DT "a[244:245],v25 offset:18432\n"
MF "v[168:171],a[96:99],a[4:7],v[168:171]\n"
DT "a[246:247],v25 offset:23040\n"
MF "v[204:207],a[96:99],a[20:23],v[204:207]\n"
"s_add_u32 m0,s76,84480\n"
MF "v[216:219],a[96:99],v[68:71],v[216:219]\n"
"s_add_u32 s67,s70,147456\n"
WT "lgkmcnt(6)\n"
MF "v[136:139],a[212:215],a[8:11],v[136:139]\n"
DR "v[76:79],v248 offset:3072\n"
BL "v12,s[24:27],s67 offen offset:0 lds\n"
MF "v[172:175],a[212:215],a[24:27],v[172:175]\n"
DT "a[64:65],v21 offset:27648\n"
MF "v[208:211],a[212:215],v[72:75],v[208:211]\n"
DT "a[66:67],v21 offset:32256\n"
MF "v[140:143],a[216:219],a[8:11],v[140:143]\n"
DT "a[68:69],v21 offset:27680\n"
MF "v[176:179],a[216:219],a[24:27],v[176:179]\n"
DT "a[70:71],v21 offset:32288\n"
BL "v13,s[24:27],s67 offen offset:1024 lds\n"
"v_add_u32 v21,36864,v21\n"
MF "v[212:215],a[216:219],v[72:75],v[212:215]\n"
DT "a[72:73],v22 offset:27648\n"
MF "v[144:147],a[220:223],a[8:11],v[144:147]\n"
DT "a[74:75],v22 offset:32256\n"
MF "v[180:183],a[220:223],a[24:27],v[180:183]\n"
DT "a[76:77],v22 offset:27680\n"
MF "v[148:151],a[224:227],a[8:11],v[148:151]\n"
DT "a[78:79],v22 offset:32288\n"
BL "v14,s[24:27],s67 offen offset:2048 lds\n"
"v_add_u32 v22,36864,v22\n"
MF "v[184:187],a[224:227],a[24:27],v[184:187]\n"
DT "a[80:81],v23 offset:27648\n"
MF "v[152:155],a[228:231],a[8:11],v[152:155]\n"
DT "a[82:83],v23 offset:32256\n"
MF "v[188:191],a[228:231],a[24:27],v[188:191]\n"
"s_add_u32 m0,s76,87552\n"
DT "a[84:85],v23 offset:27680\n"
MF "v[156:159],a[232:235],a[8:11],v[156:159]\n"
WT "lgkmcnt(12)\n"
"s_add_u32 s67,s70,147456\n"
DT "a[86:87],v23 offset:32288\n"
"v_add_u32 v23,36864,v23\n"
MF "v[192:195],a[232:235],a[24:27],v[192:195]\n"
DT "a[88:89],v24 offset:27648\n"
BL "v15,s[24:27],s67 offen offset:0 lds\n"
MF "v[160:163],a[236:239],a[8:11],v[160:163]\n"
DT "a[90:91],v24 offset:32256\n"
MF "v[196:199],a[236:239],a[24:27],v[196:199]\n"
DT "a[92:93],v24 offset:27680\n"
MF "v[164:167],a[240:243],a[8:11],v[164:167]\n"
DT "a[94:95],v24 offset:32288\n"
"v_add_u32 v24,36864,v24\n"
MF "v[200:203],a[240:243],a[24:27],v[200:203]\n"
DT "a[96:97],v25 offset:27648\n"
BL "v16,s[24:27],s67 offen offset:1024 lds\n"
MF "v[168:171],a[244:247],a[8:11],v[168:171]\n"
DT "a[98:99],v25 offset:32256\n"
"v_add_u32 v25,36864,v25\n"
MF "v[204:207],a[244:247],a[24:27],v[204:207]\n"
WT "vmcnt(13)\n"
MF "v[216:219],a[244:247],v[72:75],v[216:219]\n"
WT "lgkmcnt(6)\n"
"s_barrier\n"
MF "v[136:139],a[64:67],a[12:15],v[136:139]\n"
DR "v[112:115],v248 offset:4096\n"
MF "v[172:175],a[64:67],a[28:31],v[172:175]\n"
DT "a[212:213],v21 offset:0\n"
BL "v17,s[24:27],s67 offen offset:2048 lds\n"
MF "v[208:211],a[64:67],v[76:79],v[208:211]\n"
DT "a[214:215],v21 offset:4608\n"
MF "v[140:143],a[68:71],a[12:15],v[140:143]\n"
"s_add_u32 m0,s76,90624\n"
DT "a[216:217],v21 offset:32\n"
MF "v[176:179],a[68:71],a[28:31],v[176:179]\n"
"s_add_u32 s67,s70,147456\n"
DT "a[218:219],v21 offset:4640\n"
MF "v[212:215],a[68:71],v[76:79],v[212:215]\n"
DT "a[220:221],v22 offset:0\n"
BL "v18,s[24:27],s67 offen offset:0 lds\n"
MF "v[144:147],a[72:75],a[12:15],v[144:147]\n"
DT "a[222:223],v22 offset:4608\n"
MF "v[180:183],a[72:75],a[28:31],v[180:183]\n"
DT "a[224:225],v22 offset:32\n"
MF "v[148:151],a[76:79],a[12:15],v[148:151]\n"
DT "a[226:227],v22 offset:4640\n"
MF "v[184:187],a[76:79],a[28:31],v[184:187]\n"
DT "a[228:229],v23 offset:0\n"
MF "v[152:155],a[80:83],a[12:15],v[152:155]\n"
DT "a[230:231],v23 offset:4608\n"
BL "v19,s[24:27],s67 offen offset:1024 lds\n"
MF "v[188:191],a[80:83],a[28:31],v[188:191]\n"
DT "a[232:233],v23 offset:32\n"
MF "v[156:159],a[84:87],a[12:15],v[156:159]\n"
WT "lgkmcnt(12)\n"
DT "a[234:235],v23 offset:4640\n"
MF "v[192:195],a[84:87],a[28:31],v[192:195]\n"
DT "a[236:237],v24 offset:0\n"
MF "v[160:163],a[88:91],a[12:15],v[160:163]\n"
DT "a[238:239],v24 offset:4608\n"
BL "v20,s[24:27],s67 offen offset:2048 lds\n"
MF "v[196:199],a[88:91],a[28:31],v[196:199]\n"
DT "a[240:241],v24 offset:32\n"
MF "v[164:167],a[92:95],a[12:15],v[164:167]\n"
DT "a[242:243],v24 offset:4640\n"
MF "v[200:203],a[92:95],a[28:31],v[200:203]\n"
DT "a[244:245],v25 offset:0\n"
MF "v[168:171],a[96:99],a[12:15],v[168:171]\n"
DT "a[246:247],v25 offset:4608\n"
MF "v[204:207],a[96:99],a[28:31],v[204:207]\n"
WT "vmcnt(9)\n"
MF "v[216:219],a[96:99],v[76:79],v[216:219]\n"
WT "lgkmcnt(6)\n"
MF "v[136:139],a[212:215],a[32:35],v[136:139]\n"
"s_add_u32 m0,s61,158208\n"
DR "v[116:119],v248 offset:5120\n"
MF "v[172:175],a[212:215],a[48:51],v[172:175]\n"
"s_add_u32 s45,s58,36864\n"
DT "a[64:65],v21 offset:9216\n"
MF "v[208:211],a[212:215],v[112:115],v[208:211]\n"
DT "a[66:67],v21 offset:13824\n"
BL "v249,s[28:31],s45 offen sc1 lds\n"
MF "v[140:143],a[216:219],a[32:35],v[140:143]\n"
DT "a[68:69],v21 offset:9248\n"
MF "v[176:179],a[216:219],a[48:51],v[176:179]\n"
DT "a[70:71],v21 offset:13856\n"
MF "v[212:215],a[216:219],v[112:115],v[212:215]\n"
"s_add_u32 s44,s58,36864\n"
DT "a[72:73],v22 offset:9216\n"
MF "v[144:147],a[220:223],a[32:35],v[144:147]\n"
DT "a[74:75],v22 offset:13824\n"
BL "a[128:131],v244,s[28:31],s44 offen offset:0 sc1\n"
MF "v[180:183],a[220:223],a[48:51],v[180:183]\n"
DT "a[76:77],v22 offset:9248\n"
MF "v[148:151],a[224:227],a[32:35],v[148:151]\n"
DT "a[78:79],v22 offset:13856\n"
MF "v[184:187],a[224:227],a[48:51],v[184:187]\n"
DT "a[80:81],v23 offset:9216\n"
MF "v[152:155],a[228:231],a[32:35],v[152:155]\n"
DT "a[82:83],v23 offset:13824\n"
BL "a[144:147],v245,s[28:31],s44 offen offset:0 sc1\n"
MF "v[188:191],a[228:231],a[48:51],v[188:191]\n"
DT "a[84:85],v23 offset:9248\n"
MF "v[156:159],a[232:235],a[32:35],v[156:159]\n"
WT "lgkmcnt(12)\n"
DT "a[86:87],v23 offset:13856\n"
MF "v[192:195],a[232:235],a[48:51],v[192:195]\n"
DT "a[88:89],v24 offset:9216\n"
MF "v[160:163],a[236:239],a[32:35],v[160:163]\n"
DT "a[90:91],v24 offset:13824\n"
MF "v[196:199],a[236:239],a[48:51],v[196:199]\n"
DT "a[92:93],v24 offset:9248\n"
BL "a[132:135],v244,s[28:31],s44 offen offset:1024 sc1\n"
MF "v[164:167],a[240:243],a[32:35],v[164:167]\n"
DT "a[94:95],v24 offset:13856\n"
MF "v[200:203],a[240:243],a[48:51],v[200:203]\n"
DT "a[96:97],v25 offset:9216\n"
MF "v[168:171],a[244:247],a[32:35],v[168:171]\n"
DT "a[98:99],v25 offset:13824\n"
MF "v[204:207],a[244:247],a[48:51],v[204:207]\n"
BL "a[148:151],v245,s[28:31],s44 offen offset:1024 sc1\n"
MF "v[216:219],a[244:247],v[112:115],v[216:219]\n"
WT "lgkmcnt(6)\n"
MF "v[136:139],a[64:67],a[36:39],v[136:139]\n"
DR "v[120:123],v248 offset:6144\n"
MF "v[172:175],a[64:67],a[52:55],v[172:175]\n"
DT "a[212:213],v21 offset:18432\n"
MF "v[208:211],a[64:67],v[116:119],v[208:211]\n"
DT "a[214:215],v21 offset:23040\n"
MF "v[140:143],a[68:71],a[36:39],v[140:143]\n"
DT "a[216:217],v21 offset:18464\n"
BL "a[136:139],v244,s[28:31],s44 offen offset:2048 sc1\n"
MF "v[176:179],a[68:71],a[52:55],v[176:179]\n"
DT "a[218:219],v21 offset:23072\n"
MF "v[212:215],a[68:71],v[116:119],v[212:215]\n"
DT "a[220:221],v22 offset:18432\n"
MF "v[144:147],a[72:75],a[36:39],v[144:147]\n"
DT "a[222:223],v22 offset:23040\n"
MF "v[180:183],a[72:75],a[52:55],v[180:183]\n"
DT "a[224:225],v22 offset:18464\n"
BL "a[152:155],v245,s[28:31],s44 offen offset:2048 sc1\n"
MF "v[148:151],a[76:79],a[36:39],v[148:151]\n"
DT "a[226:227],v22 offset:23072\n"
MF "v[184:187],a[76:79],a[52:55],v[184:187]\n"
DT "a[228:229],v23 offset:18432\n"
MF "v[152:155],a[80:83],a[36:39],v[152:155]\n"
DT "a[230:231],v23 offset:23040\n"
MF "v[188:191],a[80:83],a[52:55],v[188:191]\n"
DT "a[232:233],v23 offset:18464\n"
MF "v[156:159],a[84:87],a[36:39],v[156:159]\n"
WT "lgkmcnt(12)\n"
DT "a[234:235],v23 offset:23072\n"
BL "a[140:143],v244,s[28:31],s44 offen offset:3072 sc1\n"
MF "v[192:195],a[84:87],a[52:55],v[192:195]\n"
DT "a[236:237],v24 offset:18432\n"
MF "v[160:163],a[88:91],a[36:39],v[160:163]\n"
DT "a[238:239],v24 offset:23040\n"
MF "v[196:199],a[88:91],a[52:55],v[196:199]\n"
DT "a[240:241],v24 offset:18464\n"
MF "v[164:167],a[92:95],a[36:39],v[164:167]\n"
DT "a[242:243],v24 offset:23072\n"
BL "a[156:159],v245,s[28:31],s44 offen offset:3072 sc1\n"
MF "v[200:203],a[92:95],a[52:55],v[200:203]\n"
DT "a[244:245],v25 offset:18432\n"
MF "v[168:171],a[96:99],a[36:39],v[168:171]\n"
DT "a[246:247],v25 offset:23040\n"
MF "v[204:207],a[96:99],a[52:55],v[204:207]\n"
"s_add_u32 m0,s76,121344\n"
MF "v[216:219],a[96:99],v[116:119],v[216:219]\n"
"s_add_u32 s67,s71,147456\n"
WT "lgkmcnt(6)\n"
MF "v[136:139],a[212:215],a[40:43],v[136:139]\n"
DR "v[124:127],v248 offset:7168\n"
BL "v12,s[24:27],s67 offen offset:0 lds\n"
MF "v[172:175],a[212:215],a[56:59],v[172:175]\n"
DT "a[64:65],v21 offset:27648\n"
MF "v[208:211],a[212:215],v[120:123],v[208:211]\n"
DT "a[66:67],v21 offset:32256\n"
MF "v[140:143],a[216:219],a[40:43],v[140:143]\n"
DT "a[68:69],v21 offset:27680\n"
MF "v[176:179],a[216:219],a[56:59],v[176:179]\n"
DT "a[70:71],v21 offset:32288\n"
BL "v13,s[24:27],s67 offen offset:1024 lds\n"
"v_add_u32 v21,47616,v21\n"
MF "v[212:215],a[216:219],v[120:123],v[212:215]\n"
DT "a[72:73],v22 offset:27648\n"
MF "v[144:147],a[220:223],a[40:43],v[144:147]\n"
DT "a[74:75],v22 offset:32256\n"
MF "v[180:183],a[220:223],a[56:59],v[180:183]\n"
DT "a[76:77],v22 offset:27680\n"
MF "v[148:151],a[224:227],a[40:43],v[148:151]\n"
DT "a[78:79],v22 offset:32288\n"
BL "v14,s[24:27],s67 offen offset:2048 lds\n"
"v_add_u32 v22,47616,v22\n"
MF "v[184:187],a[224:227],a[56:59],v[184:187]\n"
DT "a[80:81],v23 offset:27648\n"
MF "v[152:155],a[228:231],a[40:43],v[152:155]\n"
DT "a[82:83],v23 offset:32256\n"
MF "v[188:191],a[228:231],a[56:59],v[188:191]\n"
"s_add_u32 m0,s76,124416\n"
DT "a[84:85],v23 offset:27680\n"
MF "v[156:159],a[232:235],a[40:43],v[156:159]\n"
WT "lgkmcnt(12)\n"
"s_add_u32 s67,s71,147456\n"
DT "a[86:87],v23 offset:32288\n"
"v_add_u32 v23,47616,v23\n"
MF "v[192:195],a[232:235],a[56:59],v[192:195]\n"
DT "a[88:89],v24 offset:27648\n"
BL "v15,s[24:27],s67 offen offset:0 lds\n"
MF "v[160:163],a[236:239],a[40:43],v[160:163]\n"
DT "a[90:91],v24 offset:32256\n"
MF "v[196:199],a[236:239],a[56:59],v[196:199]\n"
DT "a[92:93],v24 offset:27680\n"
MF "v[164:167],a[240:243],a[40:43],v[164:167]\n"
DT "a[94:95],v24 offset:32288\n"
"v_add_u32 v24,47616,v24\n"
MF "v[200:203],a[240:243],a[56:59],v[200:203]\n"
DT "a[96:97],v25 offset:27648\n"
BL "v16,s[24:27],s67 offen offset:1024 lds\n"
MF "v[168:171],a[244:247],a[40:43],v[168:171]\n"
DT "a[98:99],v25 offset:32256\n"
"v_add_u32 v25,47616,v25\n"
MF "v[204:207],a[244:247],a[56:59],v[204:207]\n"
WT "vmcnt(13)\n"
MF "v[216:219],a[244:247],v[120:123],v[216:219]\n"
WT "lgkmcnt(6)\n"
"s_barrier\n"
MF "v[136:139],a[64:67],a[44:47],v[136:139]\n"
DR "v[64:67],v247 offset:0\n"
MF "v[172:175],a[64:67],a[60:63],v[172:175]\n"
DT "a[212:213],v21 offset:0\n"
BL "v17,s[24:27],s67 offen offset:2048 lds\n"
MF "v[208:211],a[64:67],v[124:127],v[208:211]\n"
DT "a[214:215],v21 offset:4608\n"
MF "v[140:143],a[68:71],a[44:47],v[140:143]\n"
"s_add_u32 m0,s76,127488\n"
DT "a[216:217],v21 offset:32\n"
MF "v[176:179],a[68:71],a[60:63],v[176:179]\n"
"s_add_u32 s67,s71,147456\n"
DT "a[218:219],v21 offset:4640\n"
MF "v[212:215],a[68:71],v[124:127],v[212:215]\n"
DT "a[220:221],v22 offset:0\n"
BL "v18,s[24:27],s67 offen offset:0 lds\n"
MF "v[144:147],a[72:75],a[44:47],v[144:147]\n"
DT "a[222:223],v22 offset:4608\n"
MF "v[180:183],a[72:75],a[60:63],v[180:183]\n"
DT "a[224:225],v22 offset:32\n"
MF "v[148:151],a[76:79],a[44:47],v[148:151]\n"
DT "a[226:227],v22 offset:4640\n"
MF "v[184:187],a[76:79],a[60:63],v[184:187]\n"
DT "a[228:229],v23 offset:0\n"
MF "v[152:155],a[80:83],a[44:47],v[152:155]\n"
DT "a[230:231],v23 offset:4608\n"
BL "v19,s[24:27],s67 offen offset:1024 lds\n"
MF "v[188:191],a[80:83],a[60:63],v[188:191]\n"
DT "a[232:233],v23 offset:32\n"
MF "v[156:159],a[84:87],a[44:47],v[156:159]\n"
WT "lgkmcnt(12)\n"
DT "a[234:235],v23 offset:4640\n"
MF "v[192:195],a[84:87],a[60:63],v[192:195]\n"
DT "a[236:237],v24 offset:0\n"
MF "v[160:163],a[88:91],a[44:47],v[160:163]\n"
DT "a[238:239],v24 offset:4608\n"
BL "v20,s[24:27],s67 offen offset:2048 lds\n"
MF "v[196:199],a[88:91],a[60:63],v[196:199]\n"
DT "a[240:241],v24 offset:32\n"
MF "v[164:167],a[92:95],a[44:47],v[164:167]\n"
DT "a[242:243],v24 offset:4640\n"
MF "v[200:203],a[92:95],a[60:63],v[200:203]\n"
DT "a[244:245],v25 offset:0\n"
MF "v[168:171],a[96:99],a[44:47],v[168:171]\n"
DT "a[246:247],v25 offset:4608\n"
MF "v[204:207],a[96:99],a[60:63],v[204:207]\n"
WT "vmcnt(9)\n"
MF "v[216:219],a[96:99],v[124:127],v[216:219]\n"
WT "lgkmcnt(6)\n"
MF "v[136:139],a[212:215],a[128:131],v[136:139]\n"
"s_add_u32 m0,s61,73728\n"
DR "v[68:71],v247 offset:1024\n"
MF "v[172:175],a[212:215],a[144:147],v[172:175]\n"
"s_add_u32 s45,s59,36864\n"
DT "a[64:65],v21 offset:9216\n"
MF "v[208:211],a[212:215],v[64:67],v[208:211]\n"
DT "a[66:67],v21 offset:13824\n"
BL "v249,s[28:31],s45 offen sc1 lds\n"
MF "v[140:143],a[216:219],a[128:131],v[140:143]\n"
DT "a[68:69],v21 offset:9248\n"
MF "v[176:179],a[216:219],a[144:147],v[176:179]\n"
DT "a[70:71],v21 offset:13856\n"
MF "v[212:215],a[216:219],v[64:67],v[212:215]\n"
"s_add_u32 s44,s59,36864\n"
DT "a[72:73],v22 offset:9216\n"
MF "v[144:147],a[220:223],a[128:131],v[144:147]\n"
DT "a[74:75],v22 offset:13824\n"
BL "a[160:163],v244,s[28:31],s44 offen offset:0 sc1\n"
MF "v[180:183],a[220:223],a[144:147],v[180:183]\n"
DT "a[76:77],v22 offset:9248\n"
MF "v[148:151],a[224:227],a[128:131],v[148:151]\n"
DT "a[78:79],v22 offset:13856\n"
MF "v[184:187],a[224:227],a[144:147],v[184:187]\n"
DT "a[80:81],v23 offset:9216\n"
MF "v[152:155],a[228:231],a[128:131],v[152:155]\n"
DT "a[82:83],v23 offset:13824\n"
BL "a[176:179],v245,s[28:31],s44 offen offset:0 sc1\n"
MF "v[188:191],a[228:231],a[144:147],v[188:191]\n"
DT "a[84:85],v23 offset:9248\n"
MF "v[156:159],a[232:235],a[128:131],v[156:159]\n"
WT "lgkmcnt(12)\n"
DT "a[86:87],v23 offset:13856\n"
MF "v[192:195],a[232:235],a[144:147],v[192:195]\n"
DT "a[88:89],v24 offset:9216\n"
MF "v[160:163],a[236:239],a[128:131],v[160:163]\n"
DT "a[90:91],v24 offset:13824\n"
BL "a[164:167],v244,s[28:31],s44 offen offset:1024 sc1\n"
MF "v[196:199],a[236:239],a[144:147],v[196:199]\n"
DT "a[92:93],v24 offset:9248\n"
MF "v[164:167],a[240:243],a[128:131],v[164:167]\n"
DT "a[94:95],v24 offset:13856\n"
MF "v[200:203],a[240:243],a[144:147],v[200:203]\n"
DT "a[96:97],v25 offset:9216\n"
MF "v[168:171],a[244:247],a[128:131],v[168:171]\n"
DT "a[98:99],v25 offset:13824\n"
BL "a[180:183],v245,s[28:31],s44 offen offset:1024 sc1\n"
MF "v[204:207],a[244:247],a[144:147],v[204:207]\n"
MF "v[216:219],a[244:247],v[64:67],v[216:219]\n"
WT "lgkmcnt(6)\n"
MF "v[136:139],a[64:67],a[132:135],v[136:139]\n"
DR "v[72:75],v247 offset:2048\n"
MF "v[172:175],a[64:67],a[148:151],v[172:175]\n"
DT "a[212:213],v21 offset:18432\n"
MF "v[208:211],a[64:67],v[68:71],v[208:211]\n"
DT "a[214:215],v21 offset:23040\n"
BL "a[168:171],v244,s[28:31],s44 offen offset:2048 sc1\n"
MF "v[140:143],a[68:71],a[132:135],v[140:143]\n"
DT "a[216:217],v21 offset:18464\n"
MF "v[176:179],a[68:71],a[148:151],v[176:179]\n"
DT "a[218:219],v21 offset:23072\n"
MF "v[212:215],a[68:71],v[68:71],v[212:215]\n"
DT "a[220:221],v22 offset:18432\n"
MF "v[144:147],a[72:75],a[132:135],v[144:147]\n"
DT "a[222:223],v22 offset:23040\n"
BL "a[184:187],v245,s[28:31],s44 offen offset:2048 sc1\n"
MF "v[180:183],a[72:75],a[148:151],v[180:183]\n"
DT "a[224:225],v22 offset:18464\n"
MF "v[148:151],a[76:79],a[132:135],v[148:151]\n"
DT "a[226:227],v22 offset:23072\n"
MF "v[184:187],a[76:79],a[148:151],v[184:187]\n"
DT "a[228:229],v23 offset:18432\n"
MF "v[152:155],a[80:83],a[132:135],v[152:155]\n"
DT "a[230:231],v23 offset:23040\n"
BL "a[172:175],v244,s[28:31],s44 offen offset:3072 sc1\n"
MF "v[188:191],a[80:83],a[148:151],v[188:191]\n"
DT "a[232:233],v23 offset:18464\n"
MF "v[156:159],a[84:87],a[132:135],v[156:159]\n"
WT "lgkmcnt(12)\n"
DT "a[234:235],v23 offset:23072\n"
MF "v[192:195],a[84:87],a[148:151],v[192:195]\n"
DT "a[236:237],v24 offset:18432\n"
MF "v[160:163],a[88:91],a[132:135],v[160:163]\n"
DT "a[238:239],v24 offset:23040\n"
BL "a[188:191],v245,s[28:31],s44 offen offset:3072 sc1\n"
MF "v[196:199],a[88:91],a[148:151],v[196:199]\n"
DT "a[240:241],v24 offset:18464\n"
MF "v[164:167],a[92:95],a[132:135],v[164:167]\n"
DT "a[242:243],v24 offset:23072\n"
MF "v[200:203],a[92:95],a[148:151],v[200:203]\n"
DT "a[244:245],v25 offset:18432\n"
MF "v[168:171],a[96:99],a[132:135],v[168:171]\n"
DT "a[246:247],v25 offset:23040\n"
MF "v[204:207],a[96:99],a[148:151],v[204:207]\n"
MF "v[216:219],a[96:99],v[68:71],v[216:219]\n"
WT "lgkmcnt(6)\n"
MF "v[136:139],a[212:215],a[136:139],v[136:139]\n"
DR "v[76:79],v247 offset:3072\n"
MF "v[172:175],a[212:215],a[152:155],v[172:175]\n"
DT "a[64:65],v21 offset:27648\n"
MF "v[208:211],a[212:215],v[72:75],v[208:211]\n"
DT "a[66:67],v21 offset:32256\n"
MF "v[140:143],a[216:219],a[136:139],v[140:143]\n"
DT "a[68:69],v21 offset:27680\n"
MF "v[176:179],a[216:219],a[152:155],v[176:179]\n"
DT "a[70:71],v21 offset:32288\n"
"v_add_u32 v21,36864,v21\n"
MF "v[212:215],a[216:219],v[72:75],v[212:215]\n"
DT "a[72:73],v22 offset:27648\n"
MF "v[144:147],a[220:223],a[136:139],v[144:147]\n"
DT "a[74:75],v22 offset:32256\n"
MF "v[180:183],a[220:223],a[152:155],v[180:183]\n"
DT "a[76:77],v22 offset:27680\n"
MF "v[148:151],a[224:227],a[136:139],v[148:151]\n"
DT "a[78:79],v22 offset:32288\n"
"v_add_u32 v22,36864,v22\n"
MF "v[184:187],a[224:227],a[152:155],v[184:187]\n"
DT "a[80:81],v23 offset:27648\n"
MF "v[152:155],a[228:231],a[136:139],v[152:155]\n"
DT "a[82:83],v23 offset:32256\n"
MF "v[188:191],a[228:231],a[152:155],v[188:191]\n"
DT "a[84:85],v23 offset:27680\n"
MF "v[156:159],a[232:235],a[136:139],v[156:159]\n"
WT "lgkmcnt(12)\n"
DT "a[86:87],v23 offset:32288\n"
"v_add_u32 v23,36864,v23\n"
MF "v[192:195],a[232:235],a[152:155],v[192:195]\n"
DT "a[88:89],v24 offset:27648\n"
MF "v[160:163],a[236:239],a[136:139],v[160:163]\n"
DT "a[90:91],v24 offset:32256\n"
MF "v[196:199],a[236:239],a[152:155],v[196:199]\n"
DT "a[92:93],v24 offset:27680\n"
MF "v[164:167],a[240:243],a[136:139],v[164:167]\n"
DT "a[94:95],v24 offset:32288\n"
"v_add_u32 v24,36864,v24\n"
MF "v[200:203],a[240:243],a[152:155],v[200:203]\n"
DT "a[96:97],v25 offset:27648\n"
MF "v[168:171],a[244:247],a[136:139],v[168:171]\n"
DT "a[98:99],v25 offset:32256\n"
"v_add_u32 v25,36864,v25\n"
MF "v[204:207],a[244:247],a[152:155],v[204:207]\n"
WT "vmcnt(8)\n"
MF "v[216:219],a[244:247],v[72:75],v[216:219]\n"
WT "lgkmcnt(6)\n"
"s_barrier\n"
MF "v[136:139],a[64:67],a[140:143],v[136:139]\n"
DR "v[112:115],v248 offset:0\n"
MF "v[172:175],a[64:67],a[156:159],v[172:175]\n"
DT "a[212:213],v21 offset:0\n"
MF "v[208:211],a[64:67],v[76:79],v[208:211]\n"
DT "a[214:215],v21 offset:4608\n"
MF "v[140:143],a[68:71],a[140:143],v[140:143]\n"
DT "a[216:217],v21 offset:32\n"
MF "v[176:179],a[68:71],a[156:159],v[176:179]\n"
DT "a[218:219],v21 offset:4640\n"
MF "v[212:215],a[68:71],v[76:79],v[212:215]\n"
DT "a[220:221],v22 offset:0\n"
MF "v[144:147],a[72:75],a[140:143],v[144:147]\n"
DT "a[222:223],v22 offset:4608\n"
MF "v[180:183],a[72:75],a[156:159],v[180:183]\n"
DT "a[224:225],v22 offset:32\n"
MF "v[148:151],a[76:79],a[140:143],v[148:151]\n"
DT "a[226:227],v22 offset:4640\n"
MF "v[184:187],a[76:79],a[156:159],v[184:187]\n"
DT "a[228:229],v23 offset:0\n"
MF "v[152:155],a[80:83],a[140:143],v[152:155]\n"
DT "a[230:231],v23 offset:4608\n"
MF "v[188:191],a[80:83],a[156:159],v[188:191]\n"
DT "a[232:233],v23 offset:32\n"
MF "v[156:159],a[84:87],a[140:143],v[156:159]\n"
WT "lgkmcnt(12)\n"
DT "a[234:235],v23 offset:4640\n"
MF "v[192:195],a[84:87],a[156:159],v[192:195]\n"
DT "a[236:237],v24 offset:0\n"
MF "v[160:163],a[88:91],a[140:143],v[160:163]\n"
DT "a[238:239],v24 offset:4608\n"
MF "v[196:199],a[88:91],a[156:159],v[196:199]\n"
DT "a[240:241],v24 offset:32\n"
MF "v[164:167],a[92:95],a[140:143],v[164:167]\n"
DT "a[242:243],v24 offset:4640\n"
MF "v[200:203],a[92:95],a[156:159],v[200:203]\n"
DT "a[244:245],v25 offset:0\n"
MF "v[168:171],a[96:99],a[140:143],v[168:171]\n"
DT "a[246:247],v25 offset:4608\n"
MF "v[204:207],a[96:99],a[156:159],v[204:207]\n"
WT "vmcnt(0)\n"
MF "v[216:219],a[96:99],v[76:79],v[216:219]\n"
WT "lgkmcnt(6)\n"
MF "v[136:139],a[212:215],a[160:163],v[136:139]\n"
DR "v[116:119],v248 offset:1024\n"
MF "v[172:175],a[212:215],a[176:179],v[172:175]\n"
DT "a[64:65],v21 offset:9216\n"
MF "v[208:211],a[212:215],v[112:115],v[208:211]\n"
DT "a[66:67],v21 offset:13824\n"
MF "v[140:143],a[216:219],a[160:163],v[140:143]\n"
DT "a[68:69],v21 offset:9248\n"
MF "v[176:179],a[216:219],a[176:179],v[176:179]\n"
DT "a[70:71],v21 offset:13856\n"
MF "v[212:215],a[216:219],v[112:115],v[212:215]\n"
DT "a[72:73],v22 offset:9216\n"
MF "v[144:147],a[220:223],a[160:163],v[144:147]\n"
DT "a[74:75],v22 offset:13824\n"
MF "v[180:183],a[220:223],a[176:179],v[180:183]\n"
DT "a[76:77],v22 offset:9248\n"
MF "v[148:151],a[224:227],a[160:163],v[148:151]\n"
DT "a[78:79],v22 offset:13856\n"
MF "v[184:187],a[224:227],a[176:179],v[184:187]\n"
DT "a[80:81],v23 offset:9216\n"
MF "v[152:155],a[228:231],a[160:163],v[152:155]\n"
DT "a[82:83],v23 offset:13824\n"
MF "v[188:191],a[228:231],a[176:179],v[188:191]\n"
DT "a[84:85],v23 offset:9248\n"
MF "v[156:159],a[232:235],a[160:163],v[156:159]\n"
WT "lgkmcnt(12)\n"
DT "a[86:87],v23 offset:13856\n"
MF "v[192:195],a[232:235],a[176:179],v[192:195]\n"
DT "a[88:89],v24 offset:9216\n"
MF "v[160:163],a[236:239],a[160:163],v[160:163]\n"
DT "a[90:91],v24 offset:13824\n"
MF "v[196:199],a[236:239],a[176:179],v[196:199]\n"
DT "a[92:93],v24 offset:9248\n"
MF "v[164:167],a[240:243],a[160:163],v[164:167]\n"
DT "a[94:95],v24 offset:13856\n"
MF "v[200:203],a[240:243],a[176:179],v[200:203]\n"
DT "a[96:97],v25 offset:9216\n"
MF "v[168:171],a[244:247],a[160:163],v[168:171]\n"
DT "a[98:99],v25 offset:13824\n"
MF "v[204:207],a[244:247],a[176:179],v[204:207]\n"
MF "v[216:219],a[244:247],v[112:115],v[216:219]\n"
WT "lgkmcnt(6)\n"
MF "v[136:139],a[64:67],a[164:167],v[136:139]\n"
DR "v[120:123],v248 offset:2048\n"
MF "v[172:175],a[64:67],a[180:183],v[172:175]\n"
DT "a[212:213],v21 offset:18432\n"
MF "v[208:211],a[64:67],v[116:119],v[208:211]\n"
DT "a[214:215],v21 offset:23040\n"
MF "v[140:143],a[68:71],a[164:167],v[140:143]\n"
DT "a[216:217],v21 offset:18464\n"
MF "v[176:179],a[68:71],a[180:183],v[176:179]\n"
DT "a[218:219],v21 offset:23072\n"
MF "v[212:215],a[68:71],v[116:119],v[212:215]\n"
DT "a[220:221],v22 offset:18432\n"
MF "v[144:147],a[72:75],a[164:167],v[144:147]\n"
DT "a[222:223],v22 offset:23040\n"
MF "v[180:183],a[72:75],a[180:183],v[180:183]\n"
DT "a[224:225],v22 offset:18464\n"
MF "v[148:151],a[76:79],a[164:167],v[148:151]\n"
DT "a[226:227],v22 offset:23072\n"
MF "v[184:187],a[76:79],a[180:183],v[184:187]\n"
DT "a[228:229],v23 offset:18432\n"
MF "v[152:155],a[80:83],a[164:167],v[152:155]\n"
DT "a[230:231],v23 offset:23040\n"
MF "v[188:191],a[80:83],a[180:183],v[188:191]\n"
DT "a[232:233],v23 offset:18464\n"
MF "v[156:159],a[84:87],a[164:167],v[156:159]\n"
WT "lgkmcnt(12)\n"
DT "a[234:235],v23 offset:23072\n"
"v_bfe_u32 v2,v0,2,2\n"
"v_lshrrev_b32 v3,4,v0\n"
MF "v[192:195],a[84:87],a[180:183],v[192:195]\n"
DT "a[236:237],v24 offset:18432\n"
"v_cmp_eq_u32 vcc,v2,v3\n"
"v_and_b32 v2,1,v0\n"
MF "v[160:163],a[88:91],a[164:167],v[160:163]\n"
DT "a[238:239],v24 offset:23040\n"
"v_lshlrev_b32 v2,4,v2\n"
"v_mov_b32 v3,0x3f80\n"
MF "v[196:199],a[88:91],a[180:183],v[196:199]\n"
DT "a[240:241],v24 offset:18464\n"
"v_lshlrev_b32 v2,v2,v3\n"
"v_cndmask_b32 v2,0,v2,vcc\n"
MF "v[164:167],a[92:95],a[164:167],v[164:167]\n"
DT "a[242:243],v24 offset:23072\n"
"v_and_b32 v3,2,v0\n"
"v_cmp_eq_u32 vcc,0,v3\n"
MF "v[200:203],a[92:95],a[180:183],v[200:203]\n"
DT "a[244:245],v25 offset:18432\n"
"v_mov_b32 v220,0\n"
"v_mov_b32 v221,0\n"
MF "v[168:171],a[96:99],a[164:167],v[168:171]\n"
DT "a[246:247],v25 offset:23040\n"
"v_cndmask_b32 v222,0,v2,vcc\n"
"v_cndmask_b32 v223,v2,v220,vcc\n"
MF "v[204:207],a[96:99],a[180:183],v[204:207]\n"
"v_mov_b32 v224,0\n"
"v_mov_b32 v225,0\n"
MF "v[216:219],a[96:99],v[116:119],v[216:219]\n"
WT "lgkmcnt(6)\n"
MF "v[136:139],a[212:215],a[168:171],v[136:139]\n"
DR "v[124:127],v248 offset:3072\n"
MF "v[172:175],a[212:215],a[184:187],v[172:175]\n"
DT "a[64:65],v21 offset:27648\n"
MF "v[208:211],a[212:215],v[120:123],v[208:211]\n"
DT "a[66:67],v21 offset:32256\n"
MF "v[140:143],a[216:219],a[168:171],v[140:143]\n"
DT "a[68:69],v21 offset:27680\n"
MF "v[176:179],a[216:219],a[184:187],v[176:179]\n"
DT "a[70:71],v21 offset:32288\n"
MF "v[212:215],a[216:219],v[120:123],v[212:215]\n"
DT "a[72:73],v22 offset:27648\n"
MF "v[144:147],a[220:223],a[168:171],v[144:147]\n"
DT "a[74:75],v22 offset:32256\n"
MF "v[180:183],a[220:223],a[184:187],v[180:183]\n"
DT "a[76:77],v22 offset:27680\n"
MF "v[148:151],a[224:227],a[168:171],v[148:151]\n"
DT "a[78:79],v22 offset:32288\n"
MF "v[184:187],a[224:227],a[184:187],v[184:187]\n"
DT "a[80:81],v23 offset:27648\n"
MF "v[152:155],a[228:231],a[168:171],v[152:155]\n"
DT "a[82:83],v23 offset:32256\n"
MF "v[188:191],a[228:231],a[184:187],v[188:191]\n"
DT "a[84:85],v23 offset:27680\n"
MF "v[156:159],a[232:235],a[168:171],v[156:159]\n"
WT "lgkmcnt(12)\n"
DT "a[86:87],v23 offset:32288\n"
"s_mul_i32 s66,s36,11264\n"
"v_add_u32 v226,s66,v1\n"
MF "v[192:195],a[232:235],a[184:187],v[192:195]\n"
DT "a[88:89],v24 offset:27648\n"
"v_add_u32 v227,0x1000,v226\n"
"v_add_u32 v228,0x2000,v226\n"
MF "v[160:163],a[236:239],a[168:171],v[160:163]\n"
DT "a[90:91],v24 offset:32256\n"
"s_mul_i32 s66,s39,144\n"
"s_lshl_b32 s67,s36,5\n"
MF "v[196:199],a[236:239],a[184:187],v[196:199]\n"
DT "a[92:93],v24 offset:27680\n"
"s_add_u32 s66,s66,s67\n"
MF "v[164:167],a[240:243],a[168:171],v[164:167]\n"
DT "a[94:95],v24 offset:32288\n"
"v_and_b32 v2,15,v0\n"
"v_add_u32 v2,s66,v2\n"
MF "v[200:203],a[240:243],a[184:187],v[200:203]\n"
DT "a[96:97],v25 offset:27648\n"
"v_mul_u32_u24 v229,2304,v2\n"
"s_mul_i32 s66,s37,1152\n"
MF "v[168:171],a[244:247],a[168:171],v[168:171]\n"
DT "a[98:99],v25 offset:32256\n"
"s_mul_i32 s67,s40,288\n"
"s_add_u32 s66,s66,s67\n"
MF "v[204:207],a[244:247],a[184:187],v[204:207]\n"
"v_add_u32 v229,s66,v229\n"
MF "v[216:219],a[244:247],v[120:123],v[216:219]\n"
WT "lgkmcnt(6)\n"
MF "v[136:139],a[64:67],a[172:175],v[136:139]\n"
"v_lshrrev_b32 v2,4,v0\n"
MF "v[172:175],a[64:67],a[188:191],v[172:175]\n"
"v_lshl_add_u32 v230,v2,3,v229\n"
MF "v[208:211],a[64:67],v[124:127],v[208:211]\n"
"v_lshrrev_b32 v3,1,v2\n"
MF "v[140:143],a[68:71],a[172:175],v[140:143]\n"
"v_and_b32 v2,1,v2\n"
MF "v[176:179],a[68:71],a[188:191],v[176:179]\n"
"v_lshl_or_b32 v2,v2,1,v3\n"
MF "v[212:215],a[68:71],v[124:127],v[212:215]\n"
"v_lshl_add_u32 v229,v2,4,v229\n"
MF "v[144:147],a[72:75],a[172:175],v[144:147]\n"
"s_add_u32 s66,s36,s38\n"
MF "v[180:183],a[72:75],a[188:191],v[180:183]\n"
"s_and_b32 s66,s66,3\n"
MF "v[148:151],a[76:79],a[172:175],v[148:151]\n"
"s_lshl_b32 s66,s66,6\n"
MF "v[184:187],a[76:79],a[188:191],v[184:187]\n"
"v_add_u32 v229,s66,v229\n"
MF "v[152:155],a[80:83],a[172:175],v[152:155]\n"
"s_mov_b32 s42,36864\n"
MF "v[188:191],a[80:83],a[188:191],v[188:191]\n"
"s_mov_b32 s43,294912\n"
MF "v[156:159],a[84:87],a[172:175],v[156:159]\n"
WT "lgkmcnt(0)\n"
MF "v[192:195],a[84:87],a[188:191],v[192:195]\n"
MF "v[160:163],a[88:91],a[172:175],v[160:163]\n"
MF "v[196:199],a[88:91],a[188:191],v[196:199]\n"
MF "v[164:167],a[92:95],a[172:175],v[164:167]\n"
MF "v[200:203],a[92:95],a[188:191],v[200:203]\n"
MF "v[168:171],a[96:99],a[172:175],v[168:171]\n"
MF "v[204:207],a[96:99],a[188:191],v[204:207]\n"
MF "v[216:219],a[96:99],v[124:127],v[216:219]\n"
CV "v36,v144,v145\n"
CV "v38,v148,v149\n"
CV "v37,v146,v147\n"
CV "v39,v150,v151\n"
"buffer_store_dwordx4 v[36:39],v226,s[28:31],s72 offen offset:1024 sc1\n"
CV "v52,v180,v181\n"
CV "v54,v184,v185\n"
CV "v53,v182,v183\n"
CV "v55,v186,v187\n"
"buffer_store_dwordx4 v[52:55],v227,s[28:31],s72 offen offset:1024 sc1\n"
CV "v40,v152,v153\n"
CV "v42,v156,v157\n"
CV "v41,v154,v155\n"
CV "v43,v158,v159\n"
"buffer_store_dwordx4 v[40:43],v226,s[28:31],s72 offen offset:2048 sc1\n"
CV "v56,v188,v189\n"
CV "v58,v192,v193\n"
CV "v57,v190,v191\n"
CV "v59,v194,v195\n"
"buffer_store_dwordx4 v[56:59],v227,s[28:31],s72 offen offset:2048 sc1\n"
CV "v44,v160,v161\n"
CV "v46,v164,v165\n"
CV "v45,v162,v163\n"
CV "v47,v166,v167\n"
"buffer_store_dwordx4 v[44:47],v226,s[28:31],s72 offen offset:3072 sc1\n"
CV "v60,v196,v197\n"
CV "v62,v200,v201\n"
CV "v61,v198,v199\n"
CV "v63,v202,v203\n"
"buffer_store_dwordx4 v[60:63],v227,s[28:31],s72 offen offset:3072 sc1\n"
CV "v68,v168,v169\n"
CV "v70,v204,v205\n"
CV "v69,v170,v171\n"
CV "v71,v206,v207\n"
"buffer_store_dwordx4 v[68:71],v228,s[28:31],s72 offen offset:1024 sc1\n"
CV "v72,v216,v217\n"
CV "v74,v216,v217\n"
CV "v73,v218,v219\n"
CV "v75,v218,v219\n"
"buffer_store_dwordx4 v[72:75],v228,s[28:31],s72 offen offset:2048 sc1\n"
WT "vmcnt(0)\n"
"s_barrier\n"
"v_add_u32 v3,1,v30\n"
"s_lshl_b64 exec,1,s38\n"
"buffer_store_dword v3,v27,s[28:31],0 offen offset:256 sc1\n"
"s_mov_b64 exec,-1\n"
"buffer_load_dword v31,v27,s[28:31],0 offen offset:256 sc1\n"
WT "vmcnt(0)\n"
"v_cmp_eq_u32 vcc,v31,v30\n"
"s_andn2_b64 vcc,vcc,s[64:65]\n"
"s_cbranch_scc1 Lp30_%=\n"
"Lp31_%=:\n"
BL "v[76:79],v226,s[28:31],s73 offen offset:1024 sc1\n"
BL "v[80:83],v226,s[28:31],s74 offen offset:2048 sc1\n"
BL "v[84:87],v226,s[28:31],s75 offen offset:3072 sc1\n"
BL "v[88:91],v227,s[28:31],s73 offen offset:1024 sc1\n"
BL "v[92:95],v227,s[28:31],s74 offen offset:2048 sc1\n"
BL "v[96:99],v227,s[28:31],s75 offen offset:3072 sc1\n"
"s_add_u32 s66,s73,11264\n"
BL "v[100:103],v228,s[28:31],s66 offen offset:0 sc1\n"
"s_add_u32 s66,s74,22528\n"
BL "v[104:107],v228,s[28:31],s66 offen offset:0 sc1\n"
"s_add_u32 s66,s75,33792\n"
BL "v[108:111],v228,s[28:31],s66 offen offset:0 sc1\n"
"s_cmp_lg_u32 s36,s38\n"
"s_cbranch_scc1 Ln26_%=\n"
BL "v[112:115],v228,s[28:31],s73 offen offset:1024 sc1\n"
BL "v[116:119],v228,s[28:31],s74 offen offset:1024 sc1\n"
BL "v[120:123],v228,s[28:31],s75 offen offset:1024 sc1\n"
"Ln26_%=:\n"
"s_cmp_lg_u32 s38,1\n"
"s_cbranch_scc1 Ln27_%=\n"
BL "v[124:127],v228,s[28:31],s73 offen offset:2048 sc1\n"
BL "v[128:131],v228,s[28:31],s74 offen offset:2048 sc1\n"
BL "v[132:135],v228,s[28:31],s75 offen offset:2048 sc1\n"
"Ln27_%=:\n"
"v_add_u32 v3,2,v30\n"
"s_lshl_b64 exec,1,s38\n"
"buffer_store_dword v3,v27,s[28:31],0 offen offset:256 sc1\n"
"s_mov_b64 exec,-1\n"
WT "vmcnt(1)\n"
MF "v[136:139],v[222:225],v[76:79],v[136:139]\n"
MF "v[140:143],v[220:223],v[76:79],v[140:143]\n"
MF "v[136:139],v[222:225],v[80:83],v[136:139]\n"
MF "v[140:143],v[220:223],v[80:83],v[140:143]\n"
MF "v[136:139],v[222:225],v[84:87],v[136:139]\n"
MF "v[140:143],v[220:223],v[84:87],v[140:143]\n"
MF "v[172:175],v[222:225],v[88:91],v[172:175]\n"
MF "v[176:179],v[220:223],v[88:91],v[176:179]\n"
MF "v[172:175],v[222:225],v[92:95],v[172:175]\n"
MF "v[176:179],v[220:223],v[92:95],v[176:179]\n"
MF "v[172:175],v[222:225],v[96:99],v[172:175]\n"
MF "v[176:179],v[220:223],v[96:99],v[176:179]\n"
MF "v[208:211],v[222:225],v[100:103],v[208:211]\n"
MF "v[212:215],v[220:223],v[100:103],v[212:215]\n"
MF "v[208:211],v[222:225],v[104:107],v[208:211]\n"
MF "v[212:215],v[220:223],v[104:107],v[212:215]\n"
MF "v[208:211],v[222:225],v[108:111],v[208:211]\n"
MF "v[212:215],v[220:223],v[108:111],v[212:215]\n"
"s_nop 3\n"
CV "v32,v136,v137\n"
CV "v34,v140,v141\n"
CV "v33,v138,v139\n"
CV "v35,v142,v143\n"
CV "v36,v172,v173\n"
CV "v38,v176,v177\n"
CV "v37,v174,v175\n"
CV "v39,v178,v179\n"
CV "v40,v208,v209\n"
CV "v42,v212,v213\n"
CV "v41,v210,v211\n"
CV "v43,v214,v215\n"
"v_permlane16_swap_b32 v32,v34\n"
"v_permlane16_swap_b32 v33,v35\n"
"buffer_store_dwordx4 v[32:35],v229,s[32:35],0 offen\n"
"v_permlane16_swap_b32 v36,v38\n"
"v_permlane16_swap_b32 v37,v39\n"
"buffer_store_dwordx4 v[36:39],v229,s[32:35],s42 offen\n"
"v_permlane16_swap_b32 v40,v42\n"
"v_permlane16_swap_b32 v41,v43\n"
"buffer_store_dwordx4 v[40:43],v229,s[32:35],s43 offen\n"
"s_cmp_lg_u32 s36,s38\n"
"s_cbranch_scc1 Ln28_%=\n"
MF "v[168:171],v[222:225],v[112:115],v[168:171]\n"
MF "v[204:207],v[220:223],v[112:115],v[204:207]\n"
MF "v[168:171],v[222:225],v[116:119],v[168:171]\n"
MF "v[204:207],v[220:223],v[116:119],v[204:207]\n"
MF "v[168:171],v[222:225],v[120:123],v[168:171]\n"
MF "v[204:207],v[220:223],v[120:123],v[204:207]\n"
"s_nop 7\n"
CV "v48,v168,v169\n"
CV "v50,v204,v205\n"
CV "v49,v170,v171\n"
CV "v51,v206,v207\n"
"buffer_store_dwordx2 v[48:49],v230,s[32:35],0 offen offset:256\n"
"buffer_store_dwordx2 v[50:51],v230,s[32:35],s42 offen offset:256\n"
"Ln28_%=:\n"
"s_cmp_lg_u32 s38,1\n"
"s_cbranch_scc1 Ln29_%=\n"
MF "v[216:219],v[222:225],v[124:127],v[216:219]\n"
MF "v[216:219],v[222:225],v[128:131],v[216:219]\n"
MF "v[216:219],v[222:225],v[132:135],v[216:219]\n"
"s_nop 7\n"
CV "v52,v216,v217\n"
CV "v54,v216,v217\n"
CV "v53,v218,v219\n"
CV "v55,v218,v219\n"
"buffer_store_dwordx2 v[52:53],v230,s[32:35],s43 offen offset:256\n"
"Ln29_%=:\n"
WT "vmcnt(0)\n"
"s_endpgm\n"
"Lw4_%=:\n"
PM "v[112:113],v[112:113],s[54:55]\n"
PM "v[114:115],v[114:115],s[54:55]\n"
PM "v[116:117],v[116:117],s[54:55]\n"
PM "v[118:119],v[118:119],s[54:55]\n"
CV "v40,v112,v113\n"
CV "v42,v116,v117\n"
CV "v41,v114,v115\n"
CV "v43,v118,v119\n"
PM "v[176:177],v[176:177],s[54:55]\n"
PM "v[178:179],v[178:179],s[54:55]\n"
PM "v[180:181],v[180:181],s[54:55]\n"
PM "v[182:183],v[182:183],s[54:55]\n"
CV "v56,v176,v177\n"
CV "v58,v180,v181\n"
CV "v57,v178,v179\n"
CV "v59,v182,v183\n"
"v_permlane16_swap_b32 v40,v42\n"
"v_permlane16_swap_b32 v41,v43\n"
PM "v[120:121],v[120:121],s[54:55]\n"
PM "v[122:123],v[122:123],s[54:55]\n"
PM "v[124:125],v[124:125],s[54:55]\n"
PM "v[126:127],v[126:127],s[54:55]\n"
CV "v44,v120,v121\n"
CV "v46,v124,v125\n"
CV "v45,v122,v123\n"
CV "v47,v126,v127\n"
"v_permlane16_swap_b32 v56,v58\n"
"v_permlane16_swap_b32 v57,v59\n"
PM "v[184:185],v[184:185],s[54:55]\n"
PM "v[186:187],v[186:187],s[54:55]\n"
PM "v[188:189],v[188:189],s[54:55]\n"
PM "v[190:191],v[190:191],s[54:55]\n"
CV "v60,v184,v185\n"
CV "v62,v188,v189\n"
CV "v61,v186,v187\n"
CV "v63,v190,v191\n"
"v_permlane16_swap_b32 v44,v46\n"
"v_permlane16_swap_b32 v45,v47\n"
PM "v[128:129],v[128:129],s[54:55]\n"
PM "v[130:131],v[130:131],s[54:55]\n"
PM "v[132:133],v[132:133],s[54:55]\n"
PM "v[134:135],v[134:135],s[54:55]\n"
CV "v104,v128,v129\n"
CV "v106,v132,v133\n"
CV "v105,v130,v131\n"
CV "v107,v134,v135\n"
"v_permlane16_swap_b32 v60,v62\n"
"v_permlane16_swap_b32 v61,v63\n"
PM "v[192:193],v[192:193],s[54:55]\n"
PM "v[194:195],v[194:195],s[54:55]\n"
PM "v[196:197],v[196:197],s[54:55]\n"
PM "v[198:199],v[198:199],s[54:55]\n"
CV "v108,v192,v193\n"
CV "v110,v196,v197\n"
CV "v109,v194,v195\n"
CV "v111,v198,v199\n"
"v_permlane16_swap_b32 v104,v106\n"
"v_permlane16_swap_b32 v105,v107\n"
"v_accvgpr_write_b32 a0,v104\n"
"v_accvgpr_write_b32 a1,v105\n"
"v_accvgpr_write_b32 a2,v106\n"
"v_accvgpr_write_b32 a3,v107\n"
PM "v[136:137],v[136:137],s[54:55]\n"
PM "v[138:139],v[138:139],s[54:55]\n"
PM "v[140:141],v[140:141],s[54:55]\n"
PM "v[142:143],v[142:143],s[54:55]\n"
CV "v104,v136,v137\n"
CV "v106,v140,v141\n"
CV "v105,v138,v139\n"
CV "v107,v142,v143\n"
"v_permlane16_swap_b32 v108,v110\n"
"v_permlane16_swap_b32 v109,v111\n"
"v_accvgpr_write_b32 a16,v108\n"
"v_accvgpr_write_b32 a17,v109\n"
"v_accvgpr_write_b32 a18,v110\n"
"v_accvgpr_write_b32 a19,v111\n"
PM "v[200:201],v[200:201],s[54:55]\n"
PM "v[202:203],v[202:203],s[54:55]\n"
PM "v[204:205],v[204:205],s[54:55]\n"
PM "v[206:207],v[206:207],s[54:55]\n"
CV "v108,v200,v201\n"
CV "v110,v204,v205\n"
CV "v109,v202,v203\n"
CV "v111,v206,v207\n"
"v_permlane16_swap_b32 v104,v106\n"
"v_permlane16_swap_b32 v105,v107\n"
"v_accvgpr_write_b32 a4,v104\n"
"v_accvgpr_write_b32 a5,v105\n"
"v_accvgpr_write_b32 a6,v106\n"
"v_accvgpr_write_b32 a7,v107\n"
PM "v[144:145],v[144:145],s[54:55]\n"
PM "v[146:147],v[146:147],s[54:55]\n"
PM "v[148:149],v[148:149],s[54:55]\n"
PM "v[150:151],v[150:151],s[54:55]\n"
CV "v104,v144,v145\n"
CV "v106,v148,v149\n"
CV "v105,v146,v147\n"
CV "v107,v150,v151\n"
"v_permlane16_swap_b32 v108,v110\n"
"v_permlane16_swap_b32 v109,v111\n"
"v_accvgpr_write_b32 a20,v108\n"
"v_accvgpr_write_b32 a21,v109\n"
"v_accvgpr_write_b32 a22,v110\n"
"v_accvgpr_write_b32 a23,v111\n"
PM "v[208:209],v[208:209],s[54:55]\n"
PM "v[210:211],v[210:211],s[54:55]\n"
PM "v[212:213],v[212:213],s[54:55]\n"
PM "v[214:215],v[214:215],s[54:55]\n"
CV "v108,v208,v209\n"
CV "v110,v212,v213\n"
CV "v109,v210,v211\n"
CV "v111,v214,v215\n"
"v_permlane16_swap_b32 v104,v106\n"
"v_permlane16_swap_b32 v105,v107\n"
"v_accvgpr_write_b32 a8,v104\n"
"v_accvgpr_write_b32 a9,v105\n"
"v_accvgpr_write_b32 a10,v106\n"
"v_accvgpr_write_b32 a11,v107\n"
PM "v[152:153],v[152:153],s[54:55]\n"
PM "v[154:155],v[154:155],s[54:55]\n"
PM "v[156:157],v[156:157],s[54:55]\n"
PM "v[158:159],v[158:159],s[54:55]\n"
CV "v104,v152,v153\n"
CV "v106,v156,v157\n"
CV "v105,v154,v155\n"
CV "v107,v158,v159\n"
"v_permlane16_swap_b32 v108,v110\n"
"v_permlane16_swap_b32 v109,v111\n"
"v_accvgpr_write_b32 a24,v108\n"
"v_accvgpr_write_b32 a25,v109\n"
"v_accvgpr_write_b32 a26,v110\n"
"v_accvgpr_write_b32 a27,v111\n"
PM "v[216:217],v[216:217],s[54:55]\n"
PM "v[218:219],v[218:219],s[54:55]\n"
PM "v[220:221],v[220:221],s[54:55]\n"
PM "v[222:223],v[222:223],s[54:55]\n"
CV "v108,v216,v217\n"
CV "v110,v220,v221\n"
CV "v109,v218,v219\n"
CV "v111,v222,v223\n"
"v_permlane16_swap_b32 v104,v106\n"
"v_permlane16_swap_b32 v105,v107\n"
"v_accvgpr_write_b32 a12,v104\n"
"v_accvgpr_write_b32 a13,v105\n"
"v_accvgpr_write_b32 a14,v106\n"
"v_accvgpr_write_b32 a15,v107\n"
PM "v[160:161],v[160:161],s[54:55]\n"
PM "v[162:163],v[162:163],s[54:55]\n"
PM "v[164:165],v[164:165],s[54:55]\n"
PM "v[166:167],v[166:167],s[54:55]\n"
CV "v32,v160,v161\n"
CV "v34,v164,v165\n"
CV "v33,v162,v163\n"
CV "v35,v166,v167\n"
"v_permlane16_swap_b32 v108,v110\n"
"v_permlane16_swap_b32 v109,v111\n"
"v_accvgpr_write_b32 a28,v108\n"
"v_accvgpr_write_b32 a29,v109\n"
"v_accvgpr_write_b32 a30,v110\n"
"v_accvgpr_write_b32 a31,v111\n"
PM "v[224:225],v[224:225],s[54:55]\n"
PM "v[226:227],v[226:227],s[54:55]\n"
PM "v[228:229],v[228:229],s[54:55]\n"
PM "v[230:231],v[230:231],s[54:55]\n"
CV "v48,v224,v225\n"
CV "v50,v228,v229\n"
CV "v49,v226,v227\n"
CV "v51,v230,v231\n"
"v_permlane16_swap_b32 v32,v34\n"
"v_permlane16_swap_b32 v33,v35\n"
PM "v[168:169],v[168:169],s[54:55]\n"
PM "v[170:171],v[170:171],s[54:55]\n"
PM "v[172:173],v[172:173],s[54:55]\n"
PM "v[174:175],v[174:175],s[54:55]\n"
CV "v36,v168,v169\n"
CV "v38,v172,v173\n"
CV "v37,v170,v171\n"
CV "v39,v174,v175\n"
"v_permlane16_swap_b32 v48,v50\n"
"v_permlane16_swap_b32 v49,v51\n"
PM "v[232:233],v[232:233],s[54:55]\n"
PM "v[234:235],v[234:235],s[54:55]\n"
PM "v[236:237],v[236:237],s[54:55]\n"
PM "v[238:239],v[238:239],s[54:55]\n"
CV "v52,v232,v233\n"
CV "v54,v236,v237\n"
CV "v53,v234,v235\n"
CV "v55,v238,v239\n"
"v_permlane16_swap_b32 v36,v38\n"
"v_permlane16_swap_b32 v37,v39\n"
"s_nop 1\n"
"v_permlane16_swap_b32 v52,v54\n"
"v_permlane16_swap_b32 v53,v55\n"
"s_lshl_b32 s66,s36,13\n"
"v_add_u32 v244,s66,v1\n"
"v_add_u32 v245,0x1000,v244\n"
"v_add_u32 v246,0x8000,v1\n"
"v_add_u32 v247,158208,v1\n"
"v_add_u32 v253,84480,v1\n"
"v_mov_b32 v252,162304\n"
"v_lshl_add_u32 v251,s36,2,v252\n"
"s_mov_b32 s43,1400897536\n"
"v_add_u32 v248,73728,v1\n"
"v_lshl_add_u32 v249,s36,10,v246\n"
WT "vmcnt(16)\n"
WT "lgkmcnt(0)\n"
"s_barrier\n"
DR "a[64:67],v4 offset:0\n"
DR "a[68:71],v4 offset:512\n"
DR "a[72:75],v4 offset:64\n"
DR "a[76:79],v4 offset:576\n"
DR "a[80:83],v4 offset:128\n"
DR "a[84:87],v4 offset:640\n"
DR "a[88:91],v4 offset:192\n"
DR "a[92:95],v4 offset:704\n"
"s_cmp_lg_u32 s98,0\n"
"s_cbranch_scc1 LS13_%=\n"
DR "v[64:67],v253 offset:0\n"
DR "v[68:71],v253 offset:1024\n"
DR "v[72:75],v253 offset:2048\n"
DR "v[76:79],v253 offset:3072\n"
DR "a[32:35],v253 offset:4096\n"
DR "a[36:39],v253 offset:5120\n"
DR "a[40:43],v253 offset:6144\n"
DR "a[44:47],v253 offset:7168\n"
"v_add_u32 v254,159752,v1\n"
WT "lgkmcnt(0)\n"
MF "v[96:99],a[64:67],v[64:67],0\n"
"v_mov_b64 v[136:137],0\n"
"v_mov_b64 v[138:139],0\n"
"v_mov_b64 v[140:141],0\n"
"v_mov_b64 v[142:143],0\n"
MF "v[100:103],a[68:71],v[64:67],0\n"
"v_mov_b64 v[144:145],0\n"
"v_mov_b64 v[146:147],0\n"
"v_mov_b64 v[148:149],0\n"
MF "v[96:99],a[72:75],v[68:71],v[96:99]\n"
"v_mov_b64 v[150:151],0\n"
"v_mov_b64 v[152:153],0\n"
"v_mov_b64 v[154:155],0\n"
MF "v[100:103],a[76:79],v[68:71],v[100:103]\n"
"v_mov_b64 v[156:157],0\n"
"v_mov_b64 v[158:159],0\n"
"v_mov_b64 v[160:161],0\n"
"v_mov_b64 v[162:163],0\n"
MF "v[96:99],a[80:83],v[72:75],v[96:99]\n"
"v_mov_b64 v[164:165],0\n"
"v_mov_b64 v[166:167],0\n"
"v_mov_b64 v[168:169],0\n"
MF "v[100:103],a[84:87],v[72:75],v[100:103]\n"
"v_mov_b64 v[170:171],0\n"
"v_mov_b64 v[172:173],0\n"
"v_mov_b64 v[174:175],0\n"
MF "v[96:99],a[88:91],v[76:79],v[96:99]\n"
"v_mov_b64 v[176:177],0\n"
"v_mov_b64 v[178:179],0\n"
"v_mov_b64 v[180:181],0\n"
"v_mov_b64 v[182:183],0\n"
MF "v[100:103],a[92:95],v[76:79],v[100:103]\n"
"v_mov_b64 v[184:185],0\n"
"v_mov_b64 v[186:187],0\n"
"v_mov_b64 v[188:189],0\n"
MF "v[80:83],a[64:67],v[32:35],0\n"
"v_mov_b64 v[190:191],0\n"
"v_mov_b64 v[192:193],0\n"
"v_mov_b64 v[194:195],0\n"
MF "v[84:87],a[68:71],v[32:35],0\n"
"v_mov_b64 v[196:197],0\n"
"v_mov_b64 v[198:199],0\n"
"v_mov_b64 v[200:201],0\n"
"v_mov_b64 v[202:203],0\n"
MF "v[80:83],a[72:75],v[36:39],v[80:83]\n"
"v_mov_b64 v[204:205],0\n"
"v_mov_b64 v[206:207],0\n"
"v_add_u32 v6,s49,v4\n"
MF "v[84:87],a[76:79],v[36:39],v[84:87]\n"
"v_add_u32 v7,s49,v5\n"
"s_mov_b32 s50,s42\n"
"s_mov_b32 s42,s49\n"
MF "v[80:83],a[80:83],v[40:43],v[80:83]\n"
"s_add_u32 s49,s49,16896\n"
"s_cmp_eq_u32 s49,84480\n"
"s_cselect_b32 s49,0,s49\n"
"v_max3_f32 v128,v96,v97,v98\n"
MF "v[84:87],a[84:87],v[40:43],v[84:87]\n"
"v_max3_f32 v128,v128,v99,v100\n"
"v_max3_f32 v128,v128,v101,v102\n"
"v_max_f32 v128,v128,v103\n"
MF "v[80:83],a[88:91],v[44:47],v[80:83]\n"
"v_mov_b32 v129,v128\n"
"s_nop 1\n"
"v_permlane32_swap_b32 v129,v128\n"
MF "v[84:87],a[92:95],v[44:47],v[84:87]\n"
"v_max_f32 v128,v129,v128\n"
"v_mov_b32 v129,v128\n"
"s_nop 1\n"
"v_permlane16_swap_b32 v129,v128\n"
MF "v[88:91],a[64:67],v[48:51],0\n"
"v_max_f32 v128,v129,v128\n"
"v_sub_f32 v96,v96,v128\n"
"v_sub_f32 v97,v97,v128\n"
MF "v[92:95],a[68:71],v[48:51],0\n"
"v_sub_f32 v98,v98,v128\n"
"v_sub_f32 v99,v99,v128\n"
"v_sub_f32 v100,v100,v128\n"
MF "v[88:91],a[72:75],v[52:55],v[88:91]\n"
"v_sub_f32 v101,v101,v128\n"
"v_sub_f32 v102,v102,v128\n"
"v_sub_f32 v103,v103,v128\n"
"v_sub_f32 v112,0,v128\n"
MF "v[92:95],a[76:79],v[52:55],v[92:95]\n"
"v_sub_f32 v113,0,v128\n"
"v_sub_f32 v114,0,v128\n"
"v_sub_f32 v115,0,v128\n"
MF "v[88:91],a[80:83],v[56:59],v[88:91]\n"
EX "v100,v100\n"
EX "v101,v101\n"
MF "v[92:95],a[84:87],v[56:59],v[92:95]\n"
EX "v102,v102\n"
EX "v103,v103\n"
MF "v[88:91],a[88:91],v[60:63],v[88:91]\n"
"s_nop 0\n"
CV "v124,v100,v101\n"
MF "v[92:95],a[92:95],v[60:63],v[92:95]\n"
CV "v125,v102,v103\n"
"ds_write_b64 v254,v[124:125]\n"
"v_xor_b32 v254,0x400,v254\n"
WT "vmcnt(12)\n"
WT "lgkmcnt(0)\n"
"s_barrier\n"
DR "a[64:67],v6 offset:0\n"
DR "a[68:71],v6 offset:512\n"
DR "a[72:75],v6 offset:64\n"
DR "a[76:79],v6 offset:576\n"
DR "a[80:83],v6 offset:128\n"
DR "a[84:87],v6 offset:640\n"
DR "a[88:91],v6 offset:192\n"
DR "a[92:95],v6 offset:704\n"
DT "a[96:97],v7 offset:0\n"
DT "a[98:99],v7 offset:512\n"
DT "a[100:101],v7 offset:32\n"
DT "a[102:103],v7 offset:544\n"
DT "a[104:105],v7 offset:64\n"
DT "a[106:107],v7 offset:576\n"
DT "a[108:109],v7 offset:96\n"
DT "a[110:111],v7 offset:608\n"
"v_max3_f32 v128,v80,v81,v82\n"
"v_max3_f32 v128,v128,v83,v84\n"
"v_max3_f32 v128,v128,v85,v86\n"
"v_max_f32 v128,v128,v87\n"
"v_mov_b32 v129,v128\n"
"s_nop 1\n"
"v_permlane32_swap_b32 v129,v128\n"
"v_max_f32 v128,v129,v128\n"
"v_mov_b32 v129,v128\n"
"s_nop 1\n"
"v_permlane16_swap_b32 v129,v128\n"
"v_max_f32 v128,v129,v128\n"
"v_sub_f32 v80,v80,v128\n"
"v_sub_f32 v81,v81,v128\n"
"v_sub_f32 v82,v82,v128\n"
"v_sub_f32 v83,v83,v128\n"
"v_sub_f32 v84,v84,v128\n"
"v_sub_f32 v85,v85,v128\n"
"v_sub_f32 v86,v86,v128\n"
"v_sub_f32 v87,v87,v128\n"
"v_sub_f32 v104,0,v128\n"
"v_sub_f32 v105,0,v128\n"
"v_sub_f32 v106,0,v128\n"
"v_sub_f32 v107,0,v128\n"
EX "v80,v80\n"
EX "v81,v81\n"
EX "v82,v82\n"
EX "v83,v83\n"
EX "v84,v84\n"
EX "v85,v85\n"
EX "v86,v86\n"
EX "v87,v87\n"
"v_max3_f32 v128,v88,v89,v90\n"
"v_max3_f32 v128,v128,v91,v92\n"
"v_max3_f32 v128,v128,v93,v94\n"
"v_max_f32 v128,v128,v95\n"
"v_mov_b32 v129,v128\n"
"s_nop 1\n"
"v_permlane32_swap_b32 v129,v128\n"
"v_max_f32 v128,v129,v128\n"
"v_mov_b32 v129,v128\n"
"s_nop 1\n"
"v_permlane16_swap_b32 v129,v128\n"
"v_max_f32 v128,v129,v128\n"
"v_sub_f32 v88,v88,v128\n"
"v_sub_f32 v89,v89,v128\n"
"v_sub_f32 v90,v90,v128\n"
"v_sub_f32 v91,v91,v128\n"
"v_sub_f32 v92,v92,v128\n"
"v_sub_f32 v93,v93,v128\n"
"v_sub_f32 v94,v94,v128\n"
"v_sub_f32 v95,v95,v128\n"
"v_sub_f32 v108,0,v128\n"
"v_sub_f32 v109,0,v128\n"
"v_sub_f32 v110,0,v128\n"
"v_sub_f32 v111,0,v128\n"
CV "v116,v80,v81\n"
CV "v117,v82,v83\n"
CV "v118,v84,v85\n"
CV "v119,v86,v87\n"
EX "v88,v88\n"
EX "v89,v89\n"
EX "v90,v90\n"
EX "v91,v91\n"
EX "v92,v92\n"
EX "v93,v93\n"
EX "v94,v94\n"
EX "v95,v95\n"
WT "lgkmcnt(8)\n"
"s_mov_b32 s52,163840\n"
"Ll218_%=:\n"
MF "v[100:103],a[68:71],v[64:67],v[112:115]\n"
"s_add_u32 m0,s50,s48\n"
DT "a[112:113],v7 offset:128\n"
CV "v120,v88,v89\n"
MF "v[100:103],a[76:79],v[68:71],v[100:103]\n"
DT "a[114:115],v7 offset:640\n"
BL "v8,s[12:15],s78 offen nt lds\n"
MF "v[100:103],a[84:87],v[72:75],v[100:103]\n"
DT "a[116:117],v7 offset:160\n"
CV "v121,v90,v91\n"
MF "v[100:103],a[92:95],v[76:79],v[100:103]\n"
DT "a[118:119],v7 offset:672\n"
CV "v122,v92,v93\n"
MF "v[80:83],a[64:67],v[32:35],v[104:107]\n"
DT "a[120:121],v7 offset:192\n"
CV "v123,v94,v95\n"
MF "v[84:87],a[68:71],v[32:35],v[104:107]\n"
DT "a[122:123],v7 offset:704\n"
BL "v9,s[12:15],s78 offen offset:1056 nt lds\n"
MF "v[80:83],a[72:75],v[36:39],v[80:83]\n"
"s_add_u32 m0,m0,8448\n"
DT "a[124:125],v7 offset:224\n"
MF "v[84:87],a[76:79],v[36:39],v[84:87]\n"
DT "a[126:127],v7 offset:736\n"
MF "v[80:83],a[80:83],v[40:43],v[80:83]\n"
EX "v100,v100\n"
MF "v[84:87],a[84:87],v[40:43],v[84:87]\n"
"s_mov_b32 s50,s42\n"
BL "v10,s[16:19],s79 offen nt lds\n"
EX "v101,v101\n"
MF "v[80:83],a[88:91],v[44:47],v[80:83]\n"
"s_mov_b32 s42,s49\n"
EX "v102,v102\n"
MF "v[84:87],a[92:95],v[44:47],v[84:87]\n"
EX "v103,v103\n"
MF "v[88:91],a[64:67],v[48:51],v[108:111]\n"
CV "v124,v100,v101\n"
CV "v125,v102,v103\n"
MF "v[92:95],a[68:71],v[48:51],v[108:111]\n"
"ds_write_b64 v254,v[124:125]\n"
BL "v11,s[16:19],s79 offen offset:1056 nt lds\n"
"v_xor_b32 v254,0x400,v254\n"
MF "v[88:91],a[72:75],v[52:55],v[88:91]\n"
"s_add_u32 s51,s51,8192\n"
EX "v80,v80\n"
MF "v[92:95],a[76:79],v[52:55],v[92:95]\n"
"s_mov_b32 s79,s78\n"
EX "v81,v81\n"
MF "v[88:91],a[80:83],v[56:59],v[88:91]\n"
"s_xor_b32 s78,s51,s41\n"
EX "v82,v82\n"
MF "v[92:95],a[84:87],v[56:59],v[92:95]\n"
EX "v83,v83\n"
MF "v[88:91],a[88:91],v[60:63],v[88:91]\n"
EX "v84,v84\n"
MF "v[92:95],a[92:95],v[60:63],v[92:95]\n"
WT "vmcnt(10) lgkmcnt(0)\n"
"s_barrier\n"
"v_add_u32 v6,s49,v4\n"
MF "v[136:139],a[96:99],v[116:119],v[136:139]\n"
DR "a[64:67],v6 offset:0\n"
EX "v85,v85\n"
MF "v[140:143],a[100:103],v[116:119],v[140:143]\n"
DR "a[68:71],v6 offset:512\n"
EX "v86,v86\n"
MF "v[144:147],a[104:107],v[116:119],v[144:147]\n"
DR "a[72:75],v6 offset:64\n"
EX "v87,v87\n"
MF "v[148:151],a[108:111],v[116:119],v[148:151]\n"
DR "a[76:79],v6 offset:576\n"
"v_add_u32 v7,s49,v5\n"
MF "v[152:155],a[112:115],v[116:119],v[152:155]\n"
DR "a[80:83],v6 offset:128\n"
EX "v88,v88\n"
MF "v[156:159],a[116:119],v[116:119],v[156:159]\n"
DR "a[84:87],v6 offset:640\n"
EX "v89,v89\n"
MF "v[160:163],a[120:123],v[116:119],v[160:163]\n"
"s_add_u32 s49,s49,16896\n"
DR "a[88:91],v6 offset:192\n"
EX "v90,v90\n"
MF "v[164:167],a[124:127],v[116:119],v[164:167]\n"
"s_cmp_eq_u32 s49,84480\n"
DR "a[92:95],v6 offset:704\n"
EX "v91,v91\n"
MF "v[168:171],a[48:51],v[116:119],v[168:171]\n"
"s_cselect_b32 s49,0,s49\n"
EX "v92,v92\n"
MF "v[172:175],a[96:99],v[120:123],v[172:175]\n"
EX "v93,v93\n"
MF "v[176:179],a[100:103],v[120:123],v[176:179]\n"
DT "a[96:97],v7 offset:0\n"
CV "v116,v80,v81\n"
MF "v[180:183],a[104:107],v[120:123],v[180:183]\n"
DT "a[98:99],v7 offset:512\n"
CV "v117,v82,v83\n"
MF "v[184:187],a[108:111],v[120:123],v[184:187]\n"
DT "a[100:101],v7 offset:32\n"
CV "v118,v84,v85\n"
MF "v[188:191],a[112:115],v[120:123],v[188:191]\n"
DT "a[102:103],v7 offset:544\n"
CV "v119,v86,v87\n"
MF "v[192:195],a[116:119],v[120:123],v[192:195]\n"
DT "a[104:105],v7 offset:64\n"
EX "v94,v94\n"
MF "v[196:199],a[120:123],v[120:123],v[196:199]\n"
DT "a[106:107],v7 offset:576\n"
EX "v95,v95\n"
MF "v[200:203],a[124:127],v[120:123],v[200:203]\n"
WT "lgkmcnt(6)\n"
DT "a[108:109],v7 offset:96\n"
MF "v[204:207],a[48:51],v[120:123],v[204:207]\n"
"s_cmp_lg_u32 s51,s52\n"
DT "a[110:111],v7 offset:608\n"
"s_cbranch_scc1 Ll218_%=\n"
MF "v[96:99],a[64:67],a[32:35],0\n"
"s_add_u32 m0,s50,s48\n"
DT "a[112:113],v7 offset:128\n"
CV "v120,v88,v89\n"
MF "v[100:103],a[68:71],a[32:35],0\n"
DT "a[114:115],v7 offset:640\n"
BL "v8,s[12:15],s78 offen nt lds\n"
MF "v[96:99],a[72:75],a[36:39],v[96:99]\n"
DT "a[116:117],v7 offset:160\n"
CV "v121,v90,v91\n"
MF "v[100:103],a[76:79],a[36:39],v[100:103]\n"
DT "a[118:119],v7 offset:672\n"
CV "v122,v92,v93\n"
MF "v[96:99],a[80:83],a[40:43],v[96:99]\n"
DT "a[120:121],v7 offset:192\n"
CV "v123,v94,v95\n"
MF "v[100:103],a[84:87],a[40:43],v[100:103]\n"
DT "a[122:123],v7 offset:704\n"
BL "v9,s[12:15],s78 offen offset:1056 nt lds\n"
MF "v[96:99],a[88:91],a[44:47],v[96:99]\n"
"s_add_u32 m0,m0,8448\n"
DT "a[124:125],v7 offset:224\n"
MF "v[100:103],a[92:95],a[44:47],v[100:103]\n"
DT "a[126:127],v7 offset:736\n"
MF "v[80:83],a[64:67],a[0:3],0\n"
MF "v[84:87],a[68:71],a[0:3],0\n"
"s_mov_b32 s50,s42\n"
BL "v10,s[16:19],s79 offen nt lds\n"
MF "v[80:83],a[72:75],a[4:7],v[80:83]\n"
"s_mov_b32 s42,s49\n"
MF "v[84:87],a[76:79],a[4:7],v[84:87]\n"
"v_max3_f32 v128,v96,v97,v98\n"
"v_max3_f32 v128,v128,v99,v100\n"
"v_max3_f32 v128,v128,v101,v102\n"
"v_max_f32 v128,v128,v103\n"
MF "v[80:83],a[80:83],a[8:11],v[80:83]\n"
"v_mov_b32 v129,v128\n"
"s_nop 1\n"
"v_permlane32_swap_b32 v129,v128\n"
MF "v[84:87],a[84:87],a[8:11],v[84:87]\n"
BL "v11,s[16:19],s79 offen offset:1056 nt lds\n"
"v_max_f32 v128,v129,v128\n"
"v_mov_b32 v129,v128\n"
"s_nop 1\n"
MF "v[80:83],a[88:91],a[12:15],v[80:83]\n"
"s_add_u32 s51,s51,8192\n"
"v_permlane16_swap_b32 v129,v128\n"
"v_max_f32 v128,v129,v128\n"
"v_sub_f32 v96,v96,v128\n"
MF "v[84:87],a[92:95],a[12:15],v[84:87]\n"
"s_mov_b32 s79,s78\n"
"v_sub_f32 v97,v97,v128\n"
"v_sub_f32 v98,v98,v128\n"
"v_sub_f32 v99,v99,v128\n"
MF "v[88:91],a[64:67],a[16:19],0\n"
"s_xor_b32 s78,s51,s41\n"
"v_sub_f32 v100,v100,v128\n"
"v_sub_f32 v101,v101,v128\n"
"v_sub_f32 v102,v102,v128\n"
MF "v[92:95],a[68:71],a[16:19],0\n"
"v_sub_f32 v103,v103,v128\n"
"v_sub_f32 v112,0,v128\n"
"v_sub_f32 v113,0,v128\n"
MF "v[88:91],a[72:75],a[20:23],v[88:91]\n"
"v_sub_f32 v114,0,v128\n"
"v_sub_f32 v115,0,v128\n"
EX "v100,v100\n"
MF "v[92:95],a[76:79],a[20:23],v[92:95]\n"
EX "v101,v101\n"
MF "v[88:91],a[80:83],a[24:27],v[88:91]\n"
EX "v102,v102\n"
EX "v103,v103\n"
MF "v[92:95],a[84:87],a[24:27],v[92:95]\n"
"s_nop 0\n"
CV "v124,v100,v101\n"
MF "v[88:91],a[88:91],a[28:31],v[88:91]\n"
CV "v125,v102,v103\n"
"ds_write_b64 v254,v[124:125]\n"
"v_xor_b32 v254,0x400,v254\n"
MF "v[92:95],a[92:95],a[28:31],v[92:95]\n"
WT "vmcnt(12)\n"
WT "lgkmcnt(0)\n"
"s_barrier\n"
"v_add_u32 v6,s49,v4\n"
MF "v[136:139],a[96:99],v[116:119],v[136:139]\n"
DR "a[64:67],v6 offset:0\n"
"v_max3_f32 v128,v80,v81,v82\n"
"v_max3_f32 v128,v128,v83,v84\n"
"v_max3_f32 v128,v128,v85,v86\n"
"v_max_f32 v128,v128,v87\n"
"v_mov_b32 v129,v128\n"
MF "v[140:143],a[100:103],v[116:119],v[140:143]\n"
DR "a[68:71],v6 offset:512\n"
"s_nop 1\n"
"v_permlane32_swap_b32 v129,v128\n"
"v_max_f32 v128,v129,v128\n"
"v_mov_b32 v129,v128\n"
MF "v[144:147],a[104:107],v[116:119],v[144:147]\n"
DR "a[72:75],v6 offset:64\n"
"s_nop 1\n"
"v_permlane16_swap_b32 v129,v128\n"
"v_max_f32 v128,v129,v128\n"
"v_sub_f32 v80,v80,v128\n"
"v_sub_f32 v81,v81,v128\n"
MF "v[148:151],a[108:111],v[116:119],v[148:151]\n"
DR "a[76:79],v6 offset:576\n"
"v_add_u32 v7,s49,v5\n"
"v_sub_f32 v82,v82,v128\n"
"v_sub_f32 v83,v83,v128\n"
"v_sub_f32 v84,v84,v128\n"
"v_sub_f32 v85,v85,v128\n"
MF "v[152:155],a[112:115],v[116:119],v[152:155]\n"
DR "a[80:83],v6 offset:128\n"
"v_sub_f32 v86,v86,v128\n"
"v_sub_f32 v87,v87,v128\n"
"v_sub_f32 v104,0,v128\n"
"v_sub_f32 v105,0,v128\n"
"v_sub_f32 v106,0,v128\n"
MF "v[156:159],a[116:119],v[116:119],v[156:159]\n"
DR "a[84:87],v6 offset:640\n"
"v_sub_f32 v107,0,v128\n"
EX "v80,v80\n"
EX "v81,v81\n"
MF "v[160:163],a[120:123],v[116:119],v[160:163]\n"
"s_add_u32 s49,s49,16896\n"
DR "a[88:91],v6 offset:192\n"
EX "v82,v82\n"
EX "v83,v83\n"
MF "v[164:167],a[124:127],v[116:119],v[164:167]\n"
"s_cmp_eq_u32 s49,84480\n"
DR "a[92:95],v6 offset:704\n"
EX "v84,v84\n"
EX "v85,v85\n"
MF "v[168:171],a[48:51],v[116:119],v[168:171]\n"
"s_cselect_b32 s49,0,s49\n"
EX "v86,v86\n"
EX "v87,v87\n"
MF "v[172:175],a[96:99],v[120:123],v[172:175]\n"
CV "v116,v80,v81\n"
CV "v117,v82,v83\n"
CV "v118,v84,v85\n"
CV "v119,v86,v87\n"
"v_max3_f32 v128,v88,v89,v90\n"
MF "v[176:179],a[100:103],v[120:123],v[176:179]\n"
DT "a[96:97],v7 offset:0\n"
"v_max3_f32 v128,v128,v91,v92\n"
"v_max3_f32 v128,v128,v93,v94\n"
"v_max_f32 v128,v128,v95\n"
"v_mov_b32 v129,v128\n"
"s_nop 1\n"
MF "v[180:183],a[104:107],v[120:123],v[180:183]\n"
DT "a[98:99],v7 offset:512\n"
"v_permlane32_swap_b32 v129,v128\n"
"v_max_f32 v128,v129,v128\n"
"v_mov_b32 v129,v128\n"
"s_nop 1\n"
"v_permlane16_swap_b32 v129,v128\n"
MF "v[184:187],a[108:111],v[120:123],v[184:187]\n"
DT "a[100:101],v7 offset:32\n"
"v_max_f32 v128,v129,v128\n"
"v_sub_f32 v88,v88,v128\n"
"v_sub_f32 v89,v89,v128\n"
"v_sub_f32 v90,v90,v128\n"
"v_sub_f32 v91,v91,v128\n"
MF "v[188:191],a[112:115],v[120:123],v[188:191]\n"
DT "a[102:103],v7 offset:544\n"
"v_sub_f32 v92,v92,v128\n"
"v_sub_f32 v93,v93,v128\n"
"v_sub_f32 v94,v94,v128\n"
"v_sub_f32 v95,v95,v128\n"
"v_sub_f32 v108,0,v128\n"
MF "v[192:195],a[116:119],v[120:123],v[192:195]\n"
DT "a[104:105],v7 offset:64\n"
"v_sub_f32 v109,0,v128\n"
"v_sub_f32 v110,0,v128\n"
"v_sub_f32 v111,0,v128\n"
EX "v88,v88\n"
MF "v[196:199],a[120:123],v[120:123],v[196:199]\n"
DT "a[106:107],v7 offset:576\n"
EX "v89,v89\n"
EX "v90,v90\n"
EX "v91,v91\n"
MF "v[200:203],a[124:127],v[120:123],v[200:203]\n"
WT "lgkmcnt(6)\n"
DT "a[108:109],v7 offset:96\n"
EX "v92,v92\n"
EX "v93,v93\n"
MF "v[204:207],a[48:51],v[120:123],v[204:207]\n"
DT "a[110:111],v7 offset:608\n"
EX "v94,v94\n"
EX "v95,v95\n"
"v_rcp_f32 v130,v168\n"
"s_add_u32 s66,s56,s99\n"
PM "v[136:137],v[136:137],v[130:131] op_sel_hi:[1,0]\n"
PM "v[138:139],v[138:139],v[130:131] op_sel_hi:[1,0]\n"
PM "v[140:141],v[140:141],v[130:131] op_sel_hi:[1,0]\n"
PM "v[142:143],v[142:143],v[130:131] op_sel_hi:[1,0]\n"
CV "v32,v136,v137\n"
CV "v33,v138,v139\n"
CV "v34,v140,v141\n"
CV "v35,v142,v143\n"
"buffer_store_dwordx4 v[32:35],v244,s[28:31],s66 offen offset:0 sc1\n"
PM "v[144:145],v[144:145],v[130:131] op_sel_hi:[1,0]\n"
PM "v[146:147],v[146:147],v[130:131] op_sel_hi:[1,0]\n"
PM "v[148:149],v[148:149],v[130:131] op_sel_hi:[1,0]\n"
PM "v[150:151],v[150:151],v[130:131] op_sel_hi:[1,0]\n"
CV "v36,v144,v145\n"
CV "v37,v146,v147\n"
CV "v38,v148,v149\n"
CV "v39,v150,v151\n"
"buffer_store_dwordx4 v[36:39],v244,s[28:31],s66 offen offset:1024 sc1\n"
PM "v[152:153],v[152:153],v[130:131] op_sel_hi:[1,0]\n"
PM "v[154:155],v[154:155],v[130:131] op_sel_hi:[1,0]\n"
PM "v[156:157],v[156:157],v[130:131] op_sel_hi:[1,0]\n"
PM "v[158:159],v[158:159],v[130:131] op_sel_hi:[1,0]\n"
CV "v40,v152,v153\n"
CV "v41,v154,v155\n"
CV "v42,v156,v157\n"
CV "v43,v158,v159\n"
"buffer_store_dwordx4 v[40:43],v244,s[28:31],s66 offen offset:2048 sc1\n"
PM "v[160:161],v[160:161],v[130:131] op_sel_hi:[1,0]\n"
PM "v[162:163],v[162:163],v[130:131] op_sel_hi:[1,0]\n"
PM "v[164:165],v[164:165],v[130:131] op_sel_hi:[1,0]\n"
PM "v[166:167],v[166:167],v[130:131] op_sel_hi:[1,0]\n"
CV "v44,v160,v161\n"
CV "v45,v162,v163\n"
CV "v46,v164,v165\n"
CV "v47,v166,v167\n"
"buffer_store_dwordx4 v[44:47],v244,s[28:31],s66 offen offset:3072 sc1\n"
"v_add_f32 v128,v168,v204\n"
"v_cmp_ngt_f32 vcc,s43,v128\n"
"s_cmp_lg_u64 vcc,0\n"
"s_cselect_b32 s66,1,0\n"
"v_mov_b32 v128,s66\n"
"ds_write_b32 v251,v128 offset:0\n"
"v_rcp_f32 v130,v204\n"
"s_add_u32 s66,s56,s99\n"
PM "v[172:173],v[172:173],v[130:131] op_sel_hi:[1,0]\n"
PM "v[174:175],v[174:175],v[130:131] op_sel_hi:[1,0]\n"
PM "v[176:177],v[176:177],v[130:131] op_sel_hi:[1,0]\n"
PM "v[178:179],v[178:179],v[130:131] op_sel_hi:[1,0]\n"
CV "v48,v172,v173\n"
CV "v49,v174,v175\n"
CV "v50,v176,v177\n"
CV "v51,v178,v179\n"
"buffer_store_dwordx4 v[48:51],v245,s[28:31],s66 offen offset:0 sc1\n"
PM "v[180:181],v[180:181],v[130:131] op_sel_hi:[1,0]\n"
PM "v[182:183],v[182:183],v[130:131] op_sel_hi:[1,0]\n"
PM "v[184:185],v[184:185],v[130:131] op_sel_hi:[1,0]\n"
PM "v[186:187],v[186:187],v[130:131] op_sel_hi:[1,0]\n"
CV "v52,v180,v181\n"
CV "v53,v182,v183\n"
CV "v54,v184,v185\n"
CV "v55,v186,v187\n"
"buffer_store_dwordx4 v[52:55],v245,s[28:31],s66 offen offset:1024 sc1\n"
PM "v[188:189],v[188:189],v[130:131] op_sel_hi:[1,0]\n"
PM "v[190:191],v[190:191],v[130:131] op_sel_hi:[1,0]\n"
PM "v[192:193],v[192:193],v[130:131] op_sel_hi:[1,0]\n"
PM "v[194:195],v[194:195],v[130:131] op_sel_hi:[1,0]\n"
CV "v56,v188,v189\n"
CV "v57,v190,v191\n"
CV "v58,v192,v193\n"
CV "v59,v194,v195\n"
"buffer_store_dwordx4 v[56:59],v245,s[28:31],s66 offen offset:2048 sc1\n"
PM "v[196:197],v[196:197],v[130:131] op_sel_hi:[1,0]\n"
PM "v[198:199],v[198:199],v[130:131] op_sel_hi:[1,0]\n"
PM "v[200:201],v[200:201],v[130:131] op_sel_hi:[1,0]\n"
PM "v[202:203],v[202:203],v[130:131] op_sel_hi:[1,0]\n"
CV "v60,v196,v197\n"
CV "v61,v198,v199\n"
CV "v62,v200,v201\n"
CV "v63,v202,v203\n"
"buffer_store_dwordx4 v[60:63],v245,s[28:31],s66 offen offset:3072 sc1\n"
"v_mov_b64 v[136:137],0\n"
"v_mov_b64 v[138:139],0\n"
"v_mov_b64 v[140:141],0\n"
"v_mov_b64 v[142:143],0\n"
"v_mov_b64 v[144:145],0\n"
"v_mov_b64 v[146:147],0\n"
"v_mov_b64 v[148:149],0\n"
"v_mov_b64 v[150:151],0\n"
"v_mov_b64 v[152:153],0\n"
"v_mov_b64 v[154:155],0\n"
"v_mov_b64 v[156:157],0\n"
"v_mov_b64 v[158:159],0\n"
"v_mov_b64 v[160:161],0\n"
"v_mov_b64 v[162:163],0\n"
"v_mov_b64 v[164:165],0\n"
"v_mov_b64 v[166:167],0\n"
"v_mov_b64 v[168:169],0\n"
"v_mov_b64 v[170:171],0\n"
"v_mov_b64 v[172:173],0\n"
"v_mov_b64 v[174:175],0\n"
"v_mov_b64 v[176:177],0\n"
"v_mov_b64 v[178:179],0\n"
"v_mov_b64 v[180:181],0\n"
"v_mov_b64 v[182:183],0\n"
"v_mov_b64 v[184:185],0\n"
"v_mov_b64 v[186:187],0\n"
"v_mov_b64 v[188:189],0\n"
"v_mov_b64 v[190:191],0\n"
"v_mov_b64 v[192:193],0\n"
"v_mov_b64 v[194:195],0\n"
"v_mov_b64 v[196:197],0\n"
"v_mov_b64 v[198:199],0\n"
"v_mov_b64 v[200:201],0\n"
"v_mov_b64 v[202:203],0\n"
"v_mov_b64 v[204:205],0\n"
"v_mov_b64 v[206:207],0\n"
"s_mov_b32 s52,204800\n"
"Ll219_%=:\n"
MF "v[100:103],a[68:71],a[32:35],v[112:115]\n"
"s_add_u32 m0,s50,s48\n"
DT "a[112:113],v7 offset:128\n"
CV "v120,v88,v89\n"
MF "v[100:103],a[76:79],a[36:39],v[100:103]\n"
DT "a[114:115],v7 offset:640\n"
BL "v8,s[12:15],s78 offen nt lds\n"
MF "v[100:103],a[84:87],a[40:43],v[100:103]\n"
DT "a[116:117],v7 offset:160\n"
CV "v121,v90,v91\n"
MF "v[100:103],a[92:95],a[44:47],v[100:103]\n"
DT "a[118:119],v7 offset:672\n"
CV "v122,v92,v93\n"
MF "v[80:83],a[64:67],a[0:3],v[104:107]\n"
DT "a[120:121],v7 offset:192\n"
CV "v123,v94,v95\n"
MF "v[84:87],a[68:71],a[0:3],v[104:107]\n"
DT "a[122:123],v7 offset:704\n"
BL "v9,s[12:15],s78 offen offset:1056 nt lds\n"
MF "v[80:83],a[72:75],a[4:7],v[80:83]\n"
"s_add_u32 m0,m0,8448\n"
DT "a[124:125],v7 offset:224\n"
MF "v[84:87],a[76:79],a[4:7],v[84:87]\n"
DT "a[126:127],v7 offset:736\n"
MF "v[80:83],a[80:83],a[8:11],v[80:83]\n"
EX "v100,v100\n"
MF "v[84:87],a[84:87],a[8:11],v[84:87]\n"
"s_mov_b32 s50,s42\n"
BL "v10,s[16:19],s79 offen nt lds\n"
EX "v101,v101\n"
MF "v[80:83],a[88:91],a[12:15],v[80:83]\n"
"s_mov_b32 s42,s49\n"
EX "v102,v102\n"
MF "v[84:87],a[92:95],a[12:15],v[84:87]\n"
EX "v103,v103\n"
MF "v[88:91],a[64:67],a[16:19],v[108:111]\n"
CV "v124,v100,v101\n"
CV "v125,v102,v103\n"
MF "v[92:95],a[68:71],a[16:19],v[108:111]\n"
"ds_write_b64 v254,v[124:125]\n"
BL "v11,s[16:19],s79 offen offset:1056 nt lds\n"
"v_xor_b32 v254,0x400,v254\n"
MF "v[88:91],a[72:75],a[20:23],v[88:91]\n"
"s_add_u32 s51,s51,8192\n"
EX "v80,v80\n"
MF "v[92:95],a[76:79],a[20:23],v[92:95]\n"
"s_mov_b32 s79,s78\n"
EX "v81,v81\n"
MF "v[88:91],a[80:83],a[24:27],v[88:91]\n"
"s_xor_b32 s78,s51,s41\n"
EX "v82,v82\n"
MF "v[92:95],a[84:87],a[24:27],v[92:95]\n"
EX "v83,v83\n"
MF "v[88:91],a[88:91],a[28:31],v[88:91]\n"
EX "v84,v84\n"
MF "v[92:95],a[92:95],a[28:31],v[92:95]\n"
WT "vmcnt(10) lgkmcnt(0)\n"
"s_barrier\n"
"v_add_u32 v6,s49,v4\n"
MF "v[136:139],a[96:99],v[116:119],v[136:139]\n"
DR "a[64:67],v6 offset:0\n"
EX "v85,v85\n"
MF "v[140:143],a[100:103],v[116:119],v[140:143]\n"
DR "a[68:71],v6 offset:512\n"
EX "v86,v86\n"
MF "v[144:147],a[104:107],v[116:119],v[144:147]\n"
DR "a[72:75],v6 offset:64\n"
EX "v87,v87\n"
MF "v[148:151],a[108:111],v[116:119],v[148:151]\n"
DR "a[76:79],v6 offset:576\n"
"v_add_u32 v7,s49,v5\n"
MF "v[152:155],a[112:115],v[116:119],v[152:155]\n"
DR "a[80:83],v6 offset:128\n"
EX "v88,v88\n"
MF "v[156:159],a[116:119],v[116:119],v[156:159]\n"
DR "a[84:87],v6 offset:640\n"
EX "v89,v89\n"
MF "v[160:163],a[120:123],v[116:119],v[160:163]\n"
"s_add_u32 s49,s49,16896\n"
DR "a[88:91],v6 offset:192\n"
EX "v90,v90\n"
MF "v[164:167],a[124:127],v[116:119],v[164:167]\n"
"s_cmp_eq_u32 s49,84480\n"
DR "a[92:95],v6 offset:704\n"
EX "v91,v91\n"
MF "v[168:171],a[48:51],v[116:119],v[168:171]\n"
"s_cselect_b32 s49,0,s49\n"
EX "v92,v92\n"
MF "v[172:175],a[96:99],v[120:123],v[172:175]\n"
EX "v93,v93\n"
MF "v[176:179],a[100:103],v[120:123],v[176:179]\n"
DT "a[96:97],v7 offset:0\n"
CV "v116,v80,v81\n"
MF "v[180:183],a[104:107],v[120:123],v[180:183]\n"
DT "a[98:99],v7 offset:512\n"
CV "v117,v82,v83\n"
MF "v[184:187],a[108:111],v[120:123],v[184:187]\n"
DT "a[100:101],v7 offset:32\n"
CV "v118,v84,v85\n"
MF "v[188:191],a[112:115],v[120:123],v[188:191]\n"
DT "a[102:103],v7 offset:544\n"
CV "v119,v86,v87\n"
MF "v[192:195],a[116:119],v[120:123],v[192:195]\n"
DT "a[104:105],v7 offset:64\n"
EX "v94,v94\n"
MF "v[196:199],a[120:123],v[120:123],v[196:199]\n"
DT "a[106:107],v7 offset:576\n"
EX "v95,v95\n"
MF "v[200:203],a[124:127],v[120:123],v[200:203]\n"
WT "lgkmcnt(6)\n"
DT "a[108:109],v7 offset:96\n"
MF "v[204:207],a[48:51],v[120:123],v[204:207]\n"
"s_cmp_lg_u32 s51,s52\n"
DT "a[110:111],v7 offset:608\n"
"s_cbranch_scc1 Ll219_%=\n"
DR "v[128:131],v252 offset:0\n"
DR "v[64:67],v253 offset:36864\n"
DR "v[68:71],v253 offset:37888\n"
DR "v[72:75],v253 offset:38912\n"
DR "v[76:79],v253 offset:39936\n"
WT "lgkmcnt(0)\n"
"v_or3_b32 v128,v128,v129,v130\n"
"v_or_b32 v128,v128,v131\n"
"v_cmp_ne_u32 vcc,0,v128\n"
"s_cbranch_vccnz Lr220_%=\n"
"s_add_u32 m0,s76,84480\n"
"s_add_u32 s67,s68,0\n"
BL "v12,s[24:27],s67 offen offset:0 lds\n"
BL "v13,s[24:27],s67 offen offset:1024 lds\n"
BL "v14,s[24:27],s67 offen offset:2048 lds\n"
"s_add_u32 m0,s76,87552\n"
"s_add_u32 s67,s68,0\n"
BL "v15,s[24:27],s67 offen offset:0 lds\n"
BL "v16,s[24:27],s67 offen offset:1024 lds\n"
BL "v17,s[24:27],s67 offen offset:2048 lds\n"
"s_add_u32 m0,s76,90624\n"
"s_add_u32 s67,s68,0\n"
BL "v18,s[24:27],s67 offen offset:0 lds\n"
BL "v19,s[24:27],s67 offen offset:1024 lds\n"
BL "v20,s[24:27],s67 offen offset:2048 lds\n"
MF "v[100:103],a[68:71],a[32:35],v[112:115]\n"
"s_add_u32 m0,s50,s48\n"
DT "a[112:113],v7 offset:128\n"
CV "v120,v88,v89\n"
MF "v[100:103],a[76:79],a[36:39],v[100:103]\n"
DT "a[114:115],v7 offset:640\n"
BL "v8,s[12:15],s78 offen nt lds\n"
MF "v[100:103],a[84:87],a[40:43],v[100:103]\n"
DT "a[116:117],v7 offset:160\n"
CV "v121,v90,v91\n"
MF "v[100:103],a[92:95],a[44:47],v[100:103]\n"
DT "a[118:119],v7 offset:672\n"
CV "v122,v92,v93\n"
MF "v[80:83],a[64:67],a[0:3],v[104:107]\n"
DT "a[120:121],v7 offset:192\n"
CV "v123,v94,v95\n"
MF "v[84:87],a[68:71],a[0:3],v[104:107]\n"
DT "a[122:123],v7 offset:704\n"
MF "v[80:83],a[72:75],a[4:7],v[80:83]\n"
DT "a[124:125],v7 offset:224\n"
MF "v[84:87],a[76:79],a[4:7],v[84:87]\n"
DT "a[126:127],v7 offset:736\n"
MF "v[80:83],a[80:83],a[8:11],v[80:83]\n"
EX "v100,v100\n"
MF "v[84:87],a[84:87],a[8:11],v[84:87]\n"
"s_mov_b32 s50,s42\n"
BL "v9,s[12:15],s78 offen offset:1056 nt lds\n"
EX "v101,v101\n"
MF "v[80:83],a[88:91],a[12:15],v[80:83]\n"
"s_add_u32 m0,m0,8448\n"
"s_mov_b32 s42,s49\n"
EX "v102,v102\n"
MF "v[84:87],a[92:95],a[12:15],v[84:87]\n"
EX "v103,v103\n"
MF "v[88:91],a[64:67],a[16:19],v[108:111]\n"
CV "v124,v100,v101\n"
CV "v125,v102,v103\n"
MF "v[92:95],a[68:71],a[16:19],v[108:111]\n"
"ds_write_b64 v254,v[124:125]\n"
"v_xor_b32 v254,0x400,v254\n"
MF "v[88:91],a[72:75],a[20:23],v[88:91]\n"
EX "v80,v80\n"
MF "v[92:95],a[76:79],a[20:23],v[92:95]\n"
EX "v81,v81\n"
MF "v[88:91],a[80:83],a[24:27],v[88:91]\n"
EX "v82,v82\n"
MF "v[92:95],a[84:87],a[24:27],v[92:95]\n"
EX "v83,v83\n"
MF "v[88:91],a[88:91],a[28:31],v[88:91]\n"
BL "v10,s[16:19],s79 offen nt lds\n"
EX "v84,v84\n"
MF "v[92:95],a[92:95],a[28:31],v[92:95]\n"
WT "vmcnt(20)\n"
WT "lgkmcnt(0)\n"
"s_barrier\n"
"v_add_u32 v6,s49,v4\n"
MF "v[136:139],a[96:99],v[116:119],v[136:139]\n"
DR "a[64:67],v6 offset:0\n"
EX "v85,v85\n"
MF "v[140:143],a[100:103],v[116:119],v[140:143]\n"
DR "a[68:71],v6 offset:512\n"
EX "v86,v86\n"
MF "v[144:147],a[104:107],v[116:119],v[144:147]\n"
DR "a[72:75],v6 offset:64\n"
EX "v87,v87\n"
MF "v[148:151],a[108:111],v[116:119],v[148:151]\n"
DR "a[76:79],v6 offset:576\n"
"v_add_u32 v7,s49,v5\n"
MF "v[152:155],a[112:115],v[116:119],v[152:155]\n"
DR "a[80:83],v6 offset:128\n"
EX "v88,v88\n"
MF "v[156:159],a[116:119],v[116:119],v[156:159]\n"
DR "a[84:87],v6 offset:640\n"
EX "v89,v89\n"
MF "v[160:163],a[120:123],v[116:119],v[160:163]\n"
"s_add_u32 s49,s49,16896\n"
DR "a[88:91],v6 offset:192\n"
BL "v11,s[16:19],s79 offen offset:1056 nt lds\n"
EX "v90,v90\n"
MF "v[164:167],a[124:127],v[116:119],v[164:167]\n"
"s_add_u32 s51,s51,8192\n"
"s_mov_b32 s79,s78\n"
"s_xor_b32 s78,s51,s41\n"
"s_cmp_eq_u32 s49,84480\n"
DR "a[92:95],v6 offset:704\n"
EX "v91,v91\n"
MF "v[168:171],a[48:51],v[116:119],v[168:171]\n"
"s_cselect_b32 s49,0,s49\n"
EX "v92,v92\n"
MF "v[172:175],a[96:99],v[120:123],v[172:175]\n"
EX "v93,v93\n"
MF "v[176:179],a[100:103],v[120:123],v[176:179]\n"
DT "a[96:97],v7 offset:0\n"
CV "v116,v80,v81\n"
MF "v[180:183],a[104:107],v[120:123],v[180:183]\n"
DT "a[98:99],v7 offset:512\n"
CV "v117,v82,v83\n"
MF "v[184:187],a[108:111],v[120:123],v[184:187]\n"
DT "a[100:101],v7 offset:32\n"
CV "v118,v84,v85\n"
MF "v[188:191],a[112:115],v[120:123],v[188:191]\n"
DT "a[102:103],v7 offset:544\n"
CV "v119,v86,v87\n"
MF "v[192:195],a[116:119],v[120:123],v[192:195]\n"
DT "a[104:105],v7 offset:64\n"
EX "v94,v94\n"
MF "v[196:199],a[120:123],v[120:123],v[196:199]\n"
DT "a[106:107],v7 offset:576\n"
"buffer_load_dword v31,v26,s[28:31],0 offen offset:0 sc1\n"
EX "v95,v95\n"
MF "v[200:203],a[124:127],v[120:123],v[200:203]\n"
WT "lgkmcnt(6)\n"
DT "a[108:109],v7 offset:96\n"
MF "v[204:207],a[48:51],v[120:123],v[204:207]\n"
DT "a[110:111],v7 offset:608\n"
MF "v[100:103],a[68:71],a[32:35],v[112:115]\n"
"s_add_u32 m0,s50,s48\n"
DT "a[112:113],v7 offset:128\n"
CV "v120,v88,v89\n"
MF "v[100:103],a[76:79],a[36:39],v[100:103]\n"
DT "a[114:115],v7 offset:640\n"
BL "v8,s[12:15],s78 offen nt lds\n"
MF "v[100:103],a[84:87],a[40:43],v[100:103]\n"
DT "a[116:117],v7 offset:160\n"
CV "v121,v90,v91\n"
MF "v[100:103],a[92:95],a[44:47],v[100:103]\n"
DT "a[118:119],v7 offset:672\n"
CV "v122,v92,v93\n"
MF "v[80:83],a[64:67],a[0:3],v[104:107]\n"
DT "a[120:121],v7 offset:192\n"
CV "v123,v94,v95\n"
MF "v[84:87],a[68:71],a[0:3],v[104:107]\n"
DT "a[122:123],v7 offset:704\n"
BL "v9,s[12:15],s78 offen offset:1056 nt lds\n"
MF "v[80:83],a[72:75],a[4:7],v[80:83]\n"
"s_add_u32 m0,m0,8448\n"
DT "a[124:125],v7 offset:224\n"
MF "v[84:87],a[76:79],a[4:7],v[84:87]\n"
DT "a[126:127],v7 offset:736\n"
MF "v[80:83],a[80:83],a[8:11],v[80:83]\n"
EX "v100,v100\n"
MF "v[84:87],a[84:87],a[8:11],v[84:87]\n"
"s_mov_b32 s50,s42\n"
BL "v10,s[16:19],s79 offen nt lds\n"
EX "v101,v101\n"
MF "v[80:83],a[88:91],a[12:15],v[80:83]\n"
"s_mov_b32 s42,s49\n"
EX "v102,v102\n"
MF "v[84:87],a[92:95],a[12:15],v[84:87]\n"
EX "v103,v103\n"
MF "v[88:91],a[64:67],a[16:19],v[108:111]\n"
CV "v124,v100,v101\n"
CV "v125,v102,v103\n"
MF "v[92:95],a[68:71],a[16:19],v[108:111]\n"
"ds_write_b64 v254,v[124:125]\n"
BL "v11,s[16:19],s79 offen offset:1056 nt lds\n"
"v_xor_b32 v254,0x400,v254\n"
MF "v[88:91],a[72:75],a[20:23],v[88:91]\n"
"s_add_u32 s51,s51,8192\n"
EX "v80,v80\n"
MF "v[92:95],a[76:79],a[20:23],v[92:95]\n"
"s_mov_b32 s79,s78\n"
EX "v81,v81\n"
MF "v[88:91],a[80:83],a[24:27],v[88:91]\n"
"s_xor_b32 s78,s51,s41\n"
EX "v82,v82\n"
MF "v[92:95],a[84:87],a[24:27],v[92:95]\n"
EX "v83,v83\n"
MF "v[88:91],a[88:91],a[28:31],v[88:91]\n"
EX "v84,v84\n"
MF "v[92:95],a[92:95],a[28:31],v[92:95]\n"
WT "vmcnt(22)\n"
WT "vmcnt(9)\n"
WT "lgkmcnt(0)\n"
"s_barrier\n"
"v_add_u32 v6,s49,v4\n"
MF "v[136:139],a[96:99],v[116:119],v[136:139]\n"
DR "a[64:67],v6 offset:0\n"
EX "v85,v85\n"
MF "v[140:143],a[100:103],v[116:119],v[140:143]\n"
DR "a[68:71],v6 offset:512\n"
EX "v86,v86\n"
MF "v[144:147],a[104:107],v[116:119],v[144:147]\n"
DR "a[72:75],v6 offset:64\n"
EX "v87,v87\n"
MF "v[148:151],a[108:111],v[116:119],v[148:151]\n"
DR "a[76:79],v6 offset:576\n"
"v_add_u32 v7,s49,v5\n"
MF "v[152:155],a[112:115],v[116:119],v[152:155]\n"
DR "a[80:83],v6 offset:128\n"
EX "v88,v88\n"
MF "v[156:159],a[116:119],v[116:119],v[156:159]\n"
DR "a[84:87],v6 offset:640\n"
EX "v89,v89\n"
MF "v[160:163],a[120:123],v[116:119],v[160:163]\n"
"s_add_u32 s49,s49,16896\n"
DR "a[88:91],v6 offset:192\n"
EX "v90,v90\n"
MF "v[164:167],a[124:127],v[116:119],v[164:167]\n"
"s_cmp_eq_u32 s49,84480\n"
DR "a[92:95],v6 offset:704\n"
EX "v91,v91\n"
MF "v[168:171],a[48:51],v[116:119],v[168:171]\n"
"s_cselect_b32 s49,0,s49\n"
EX "v92,v92\n"
MF "v[172:175],a[96:99],v[120:123],v[172:175]\n"
EX "v93,v93\n"
MF "v[176:179],a[100:103],v[120:123],v[176:179]\n"
DT "a[96:97],v7 offset:0\n"
CV "v116,v80,v81\n"
MF "v[180:183],a[104:107],v[120:123],v[180:183]\n"
DT "a[98:99],v7 offset:512\n"
CV "v117,v82,v83\n"
MF "v[184:187],a[108:111],v[120:123],v[184:187]\n"
DT "a[100:101],v7 offset:32\n"
CV "v118,v84,v85\n"
MF "v[188:191],a[112:115],v[120:123],v[188:191]\n"
DT "a[102:103],v7 offset:544\n"
CV "v119,v86,v87\n"
MF "v[192:195],a[116:119],v[120:123],v[192:195]\n"
DT "a[104:105],v7 offset:64\n"
EX "v94,v94\n"
MF "v[196:199],a[120:123],v[120:123],v[196:199]\n"
DT "a[106:107],v7 offset:576\n"
EX "v95,v95\n"
MF "v[200:203],a[124:127],v[120:123],v[200:203]\n"
WT "lgkmcnt(6)\n"
DT "a[108:109],v7 offset:96\n"
MF "v[204:207],a[48:51],v[120:123],v[204:207]\n"
DT "a[110:111],v7 offset:608\n"
DT "a[212:213],v21 offset:0\n"
DT "a[214:215],v21 offset:4608\n"
DT "a[216:217],v21 offset:32\n"
DT "a[218:219],v21 offset:4640\n"
DT "a[220:221],v22 offset:0\n"
DT "a[222:223],v22 offset:4608\n"
DT "a[224:225],v22 offset:32\n"
DT "a[226:227],v22 offset:4640\n"
DT "a[228:229],v23 offset:0\n"
DT "a[230:231],v23 offset:4608\n"
DT "a[232:233],v23 offset:32\n"
DT "a[234:235],v23 offset:4640\n"
DT "a[236:237],v24 offset:0\n"
DT "a[238:239],v24 offset:4608\n"
DT "a[240:241],v24 offset:32\n"
DT "a[242:243],v24 offset:4640\n"
DT "a[244:245],v25 offset:0\n"
DT "a[246:247],v25 offset:4608\n"
WT "vmcnt(4)\n"
"v_cmp_eq_u32 vcc,v31,v28\n"
"s_andn2_b64 vcc,vcc,s[62:63]\n"
"s_cbranch_scc1 Lp221_%=\n"
"Lp222_%=:\n"
MF "v[100:103],a[68:71],a[32:35],v[112:115]\n"
"s_add_u32 m0,s50,s48\n"
DT "a[112:113],v7 offset:128\n"
CV "v120,v88,v89\n"
MF "v[100:103],a[76:79],a[36:39],v[100:103]\n"
DT "a[114:115],v7 offset:640\n"
BL "v8,s[12:15],s78 offen nt lds\n"
MF "v[100:103],a[84:87],a[40:43],v[100:103]\n"
DT "a[116:117],v7 offset:160\n"
CV "v121,v90,v91\n"
MF "v[100:103],a[92:95],a[44:47],v[100:103]\n"
DT "a[118:119],v7 offset:672\n"
CV "v122,v92,v93\n"
MF "v[80:83],a[64:67],a[0:3],v[104:107]\n"
DT "a[120:121],v7 offset:192\n"
CV "v123,v94,v95\n"
MF "v[84:87],a[68:71],a[0:3],v[104:107]\n"
DT "a[122:123],v7 offset:704\n"
BL "v9,s[12:15],s78 offen offset:1056 nt lds\n"
MF "v[80:83],a[72:75],a[4:7],v[80:83]\n"
"s_add_u32 m0,m0,8448\n"
DT "a[124:125],v7 offset:224\n"
MF "v[84:87],a[76:79],a[4:7],v[84:87]\n"
DT "a[126:127],v7 offset:736\n"
MF "v[80:83],a[80:83],a[8:11],v[80:83]\n"
EX "v100,v100\n"
MF "v[84:87],a[84:87],a[8:11],v[84:87]\n"
"s_mov_b32 s50,s42\n"
BL "v10,s[16:19],s79 offen nt lds\n"
EX "v101,v101\n"
MF "v[80:83],a[88:91],a[12:15],v[80:83]\n"
"s_mov_b32 s42,s49\n"
EX "v102,v102\n"
MF "v[84:87],a[92:95],a[12:15],v[84:87]\n"
EX "v103,v103\n"
MF "v[88:91],a[64:67],a[16:19],v[108:111]\n"
CV "v124,v100,v101\n"
CV "v125,v102,v103\n"
MF "v[92:95],a[68:71],a[16:19],v[108:111]\n"
"ds_write_b64 v254,v[124:125]\n"
BL "v11,s[16:19],s79 offen offset:1056 nt lds\n"
"v_xor_b32 v254,0x400,v254\n"
MF "v[88:91],a[72:75],a[20:23],v[88:91]\n"
"s_add_u32 s51,s51,8192\n"
EX "v80,v80\n"
MF "v[92:95],a[76:79],a[20:23],v[92:95]\n"
"s_mov_b32 s79,s78\n"
EX "v81,v81\n"
MF "v[88:91],a[80:83],a[24:27],v[88:91]\n"
"s_xor_b32 s78,s51,s41\n"
EX "v82,v82\n"
MF "v[92:95],a[84:87],a[24:27],v[92:95]\n"
EX "v83,v83\n"
MF "v[88:91],a[88:91],a[28:31],v[88:91]\n"
EX "v84,v84\n"
MF "v[92:95],a[92:95],a[28:31],v[92:95]\n"
WT "vmcnt(22)\n"
WT "lgkmcnt(0)\n"
"s_barrier\n"
"v_add_u32 v6,s49,v4\n"
MF "v[136:139],a[96:99],v[116:119],v[136:139]\n"
DR "a[64:67],v6 offset:0\n"
EX "v85,v85\n"
MF "v[140:143],a[100:103],v[116:119],v[140:143]\n"
DR "a[68:71],v6 offset:512\n"
EX "v86,v86\n"
MF "v[144:147],a[104:107],v[116:119],v[144:147]\n"
DR "a[72:75],v6 offset:64\n"
EX "v87,v87\n"
MF "v[148:151],a[108:111],v[116:119],v[148:151]\n"
DR "a[76:79],v6 offset:576\n"
"v_add_u32 v7,s49,v5\n"
MF "v[152:155],a[112:115],v[116:119],v[152:155]\n"
DR "a[80:83],v6 offset:128\n"
EX "v88,v88\n"
MF "v[156:159],a[116:119],v[116:119],v[156:159]\n"
DR "a[84:87],v6 offset:640\n"
EX "v89,v89\n"
MF "v[160:163],a[120:123],v[116:119],v[160:163]\n"
"s_add_u32 s49,s49,16896\n"
DR "a[88:91],v6 offset:192\n"
EX "v90,v90\n"
MF "v[164:167],a[124:127],v[116:119],v[164:167]\n"
"s_cmp_eq_u32 s49,84480\n"
DR "a[92:95],v6 offset:704\n"
EX "v91,v91\n"
MF "v[168:171],a[48:51],v[116:119],v[168:171]\n"
"s_cselect_b32 s49,0,s49\n"
EX "v92,v92\n"
MF "v[172:175],a[96:99],v[120:123],v[172:175]\n"
EX "v93,v93\n"
MF "v[176:179],a[100:103],v[120:123],v[176:179]\n"
DT "a[96:97],v7 offset:0\n"
CV "v116,v80,v81\n"
MF "v[180:183],a[104:107],v[120:123],v[180:183]\n"
DT "a[98:99],v7 offset:512\n"
CV "v117,v82,v83\n"
MF "v[184:187],a[108:111],v[120:123],v[184:187]\n"
DT "a[100:101],v7 offset:32\n"
CV "v118,v84,v85\n"
MF "v[188:191],a[112:115],v[120:123],v[188:191]\n"
DT "a[102:103],v7 offset:544\n"
CV "v119,v86,v87\n"
MF "v[192:195],a[116:119],v[120:123],v[192:195]\n"
DT "a[104:105],v7 offset:64\n"
EX "v94,v94\n"
MF "v[196:199],a[120:123],v[120:123],v[196:199]\n"
DT "a[106:107],v7 offset:576\n"
EX "v95,v95\n"
MF "v[200:203],a[124:127],v[120:123],v[200:203]\n"
WT "lgkmcnt(6)\n"
DT "a[108:109],v7 offset:96\n"
MF "v[204:207],a[48:51],v[120:123],v[204:207]\n"
DT "a[110:111],v7 offset:608\n"
MF "a[128:131],a[212:215],v[32:35],0\n"
"s_add_u32 m0,s76,121344\n"
MF "a[164:167],a[212:215],v[48:51],0\n"
"s_add_u32 s67,s69,0\n"
MF "a[200:203],a[212:215],v[64:67],0\n"
DT "a[212:213],v21 offset:9216\n"
BL "v12,s[24:27],s67 offen offset:0 lds\n"
MF "a[132:135],a[216:219],v[32:35],0\n"
DT "a[214:215],v21 offset:13824\n"
MF "a[168:171],a[216:219],v[48:51],0\n"
MF "a[204:207],a[216:219],v[64:67],0\n"
DT "a[216:217],v21 offset:9248\n"
BL "v13,s[24:27],s67 offen offset:1024 lds\n"
MF "a[136:139],a[220:223],v[32:35],0\n"
DT "a[218:219],v21 offset:13856\n"
MF "a[172:175],a[220:223],v[48:51],0\n"
DT "a[220:221],v22 offset:9216\n"
MF "a[140:143],a[224:227],v[32:35],0\n"
DT "a[222:223],v22 offset:13824\n"
BL "v14,s[24:27],s67 offen offset:2048 lds\n"
MF "a[176:179],a[224:227],v[48:51],0\n"
"s_add_u32 m0,s76,124416\n"
DT "a[224:225],v22 offset:9248\n"
MF "a[144:147],a[228:231],v[32:35],0\n"
"s_add_u32 s67,s69,0\n"
DT "a[226:227],v22 offset:13856\n"
MF "a[180:183],a[228:231],v[48:51],0\n"
DT "a[228:229],v23 offset:9216\n"
BL "v15,s[24:27],s67 offen offset:0 lds\n"
MF "a[148:151],a[232:235],v[32:35],0\n"
DT "a[230:231],v23 offset:13824\n"
MF "a[184:187],a[232:235],v[48:51],0\n"
DT "a[232:233],v23 offset:9248\n"
MF "a[152:155],a[236:239],v[32:35],0\n"
DT "a[234:235],v23 offset:13856\n"
BL "v16,s[24:27],s67 offen offset:1024 lds\n"
MF "a[188:191],a[236:239],v[48:51],0\n"
DT "a[236:237],v24 offset:9216\n"
MF "a[156:159],a[240:243],v[32:35],0\n"
DT "a[238:239],v24 offset:13824\n"
MF "a[192:195],a[240:243],v[48:51],0\n"
DT "a[240:241],v24 offset:9248\n"
BL "v17,s[24:27],s67 offen offset:2048 lds\n"
MF "a[160:163],a[244:247],v[32:35],0\n"
DT "a[242:243],v24 offset:13856\n"
MF "a[196:199],a[244:247],v[48:51],0\n"
DT "a[244:245],v25 offset:9216\n"
DT "a[246:247],v25 offset:13824\n"
MF "v[100:103],a[68:71],a[32:35],v[112:115]\n"
"s_add_u32 m0,s50,s48\n"
DT "a[112:113],v7 offset:128\n"
CV "v120,v88,v89\n"
MF "v[100:103],a[76:79],a[36:39],v[100:103]\n"
DT "a[114:115],v7 offset:640\n"
BL "v8,s[12:15],s78 offen nt lds\n"
MF "v[100:103],a[84:87],a[40:43],v[100:103]\n"
DT "a[116:117],v7 offset:160\n"
CV "v121,v90,v91\n"
MF "v[100:103],a[92:95],a[44:47],v[100:103]\n"
DT "a[118:119],v7 offset:672\n"
CV "v122,v92,v93\n"
MF "v[80:83],a[64:67],a[0:3],v[104:107]\n"
DT "a[120:121],v7 offset:192\n"
CV "v123,v94,v95\n"
MF "v[84:87],a[68:71],a[0:3],v[104:107]\n"
DT "a[122:123],v7 offset:704\n"
BL "v9,s[12:15],s78 offen offset:1056 nt lds\n"
MF "v[80:83],a[72:75],a[4:7],v[80:83]\n"
"s_add_u32 m0,m0,8448\n"
DT "a[124:125],v7 offset:224\n"
MF "v[84:87],a[76:79],a[4:7],v[84:87]\n"
DT "a[126:127],v7 offset:736\n"
MF "v[80:83],a[80:83],a[8:11],v[80:83]\n"
EX "v100,v100\n"
MF "v[84:87],a[84:87],a[8:11],v[84:87]\n"
"s_mov_b32 s50,s42\n"
BL "v10,s[16:19],s79 offen nt lds\n"
EX "v101,v101\n"
MF "v[80:83],a[88:91],a[12:15],v[80:83]\n"
"s_mov_b32 s42,s49\n"
EX "v102,v102\n"
MF "v[84:87],a[92:95],a[12:15],v[84:87]\n"
EX "v103,v103\n"
MF "v[88:91],a[64:67],a[16:19],v[108:111]\n"
CV "v124,v100,v101\n"
CV "v125,v102,v103\n"
MF "v[92:95],a[68:71],a[16:19],v[108:111]\n"
"ds_write_b64 v254,v[124:125]\n"
BL "v11,s[16:19],s79 offen offset:1056 nt lds\n"
"v_xor_b32 v254,0x400,v254\n"
MF "v[88:91],a[72:75],a[20:23],v[88:91]\n"
"s_add_u32 s51,s51,8192\n"
EX "v80,v80\n"
MF "v[92:95],a[76:79],a[20:23],v[92:95]\n"
"s_mov_b32 s79,s78\n"
EX "v81,v81\n"
MF "v[88:91],a[80:83],a[24:27],v[88:91]\n"
"s_xor_b32 s78,s51,s41\n"
EX "v82,v82\n"
MF "v[92:95],a[84:87],a[24:27],v[92:95]\n"
EX "v83,v83\n"
MF "v[88:91],a[88:91],a[28:31],v[88:91]\n"
EX "v84,v84\n"
MF "v[92:95],a[92:95],a[28:31],v[92:95]\n"
WT "vmcnt(19)\n"
WT "lgkmcnt(0)\n"
"s_barrier\n"
"v_add_u32 v6,s49,v4\n"
MF "v[136:139],a[96:99],v[116:119],v[136:139]\n"
DR "a[64:67],v6 offset:0\n"
EX "v85,v85\n"
MF "v[140:143],a[100:103],v[116:119],v[140:143]\n"
DR "a[68:71],v6 offset:512\n"
EX "v86,v86\n"
MF "v[144:147],a[104:107],v[116:119],v[144:147]\n"
DR "a[72:75],v6 offset:64\n"
EX "v87,v87\n"
MF "v[148:151],a[108:111],v[116:119],v[148:151]\n"
DR "a[76:79],v6 offset:576\n"
"v_add_u32 v7,s49,v5\n"
MF "v[152:155],a[112:115],v[116:119],v[152:155]\n"
DR "a[80:83],v6 offset:128\n"
EX "v88,v88\n"
MF "v[156:159],a[116:119],v[116:119],v[156:159]\n"
DR "a[84:87],v6 offset:640\n"
EX "v89,v89\n"
MF "v[160:163],a[120:123],v[116:119],v[160:163]\n"
"s_add_u32 s49,s49,16896\n"
DR "a[88:91],v6 offset:192\n"
EX "v90,v90\n"
MF "v[164:167],a[124:127],v[116:119],v[164:167]\n"
"s_cmp_eq_u32 s49,84480\n"
DR "a[92:95],v6 offset:704\n"
EX "v91,v91\n"
MF "v[168:171],a[48:51],v[116:119],v[168:171]\n"
"s_cselect_b32 s49,0,s49\n"
EX "v92,v92\n"
MF "v[172:175],a[96:99],v[120:123],v[172:175]\n"
EX "v93,v93\n"
MF "v[176:179],a[100:103],v[120:123],v[176:179]\n"
DT "a[96:97],v7 offset:0\n"
CV "v116,v80,v81\n"
MF "v[180:183],a[104:107],v[120:123],v[180:183]\n"
DT "a[98:99],v7 offset:512\n"
CV "v117,v82,v83\n"
MF "v[184:187],a[108:111],v[120:123],v[184:187]\n"
DT "a[100:101],v7 offset:32\n"
CV "v118,v84,v85\n"
MF "v[188:191],a[112:115],v[120:123],v[188:191]\n"
DT "a[102:103],v7 offset:544\n"
CV "v119,v86,v87\n"
MF "v[192:195],a[116:119],v[120:123],v[192:195]\n"
DT "a[104:105],v7 offset:64\n"
EX "v94,v94\n"
MF "v[196:199],a[120:123],v[120:123],v[196:199]\n"
DT "a[106:107],v7 offset:576\n"
EX "v95,v95\n"
MF "v[200:203],a[124:127],v[120:123],v[200:203]\n"
WT "lgkmcnt(6)\n"
DT "a[108:109],v7 offset:96\n"
MF "v[204:207],a[48:51],v[120:123],v[204:207]\n"
DT "a[110:111],v7 offset:608\n"
MF "a[128:131],a[212:215],v[36:39],a[128:131]\n"
MF "a[164:167],a[212:215],v[52:55],a[164:167]\n"
"s_add_u32 s44,s57,0\n"
MF "a[200:203],a[212:215],v[68:71],a[200:203]\n"
DT "a[212:213],v21 offset:18432\n"
BL "v[32:35],v244,s[28:31],s44 offen offset:0 sc1\n"
MF "a[132:135],a[216:219],v[36:39],a[132:135]\n"
DT "a[214:215],v21 offset:23040\n"
MF "a[168:171],a[216:219],v[52:55],a[168:171]\n"
MF "a[204:207],a[216:219],v[68:71],a[204:207]\n"
DT "a[216:217],v21 offset:18464\n"
BL "v[48:51],v245,s[28:31],s44 offen offset:0 sc1\n"
MF "a[136:139],a[220:223],v[36:39],a[136:139]\n"
DT "a[218:219],v21 offset:23072\n"
MF "a[172:175],a[220:223],v[52:55],a[172:175]\n"
DT "a[220:221],v22 offset:18432\n"
MF "a[140:143],a[224:227],v[36:39],a[140:143]\n"
DT "a[222:223],v22 offset:23040\n"
BL "v[64:67],v246,s[28:31],s44 offen offset:0 sc1\n"
MF "a[176:179],a[224:227],v[52:55],a[176:179]\n"
"s_add_u32 m0,s76,127488\n"
DT "a[224:225],v22 offset:18464\n"
MF "a[144:147],a[228:231],v[36:39],a[144:147]\n"
"s_add_u32 s67,s69,0\n"
DT "a[226:227],v22 offset:23072\n"
MF "a[180:183],a[228:231],v[52:55],a[180:183]\n"
DT "a[228:229],v23 offset:18432\n"
BL "v18,s[24:27],s67 offen offset:0 lds\n"
MF "a[148:151],a[232:235],v[36:39],a[148:151]\n"
DT "a[230:231],v23 offset:23040\n"
MF "a[184:187],a[232:235],v[52:55],a[184:187]\n"
DT "a[232:233],v23 offset:18464\n"
MF "a[152:155],a[236:239],v[36:39],a[152:155]\n"
DT "a[234:235],v23 offset:23072\n"
BL "v19,s[24:27],s67 offen offset:1024 lds\n"
MF "a[188:191],a[236:239],v[52:55],a[188:191]\n"
DT "a[236:237],v24 offset:18432\n"
MF "a[156:159],a[240:243],v[36:39],a[156:159]\n"
DT "a[238:239],v24 offset:23040\n"
MF "a[192:195],a[240:243],v[52:55],a[192:195]\n"
DT "a[240:241],v24 offset:18464\n"
BL "v20,s[24:27],s67 offen offset:2048 lds\n"
MF "a[160:163],a[244:247],v[36:39],a[160:163]\n"
DT "a[242:243],v24 offset:23072\n"
MF "a[196:199],a[244:247],v[52:55],a[196:199]\n"
DT "a[244:245],v25 offset:18432\n"
DT "a[246:247],v25 offset:23040\n"
MF "v[100:103],a[68:71],a[32:35],v[112:115]\n"
"s_add_u32 m0,s50,s48\n"
DT "a[112:113],v7 offset:128\n"
CV "v120,v88,v89\n"
MF "v[100:103],a[76:79],a[36:39],v[100:103]\n"
DT "a[114:115],v7 offset:640\n"
BL "v8,s[12:15],s78 offen nt lds\n"
MF "v[100:103],a[84:87],a[40:43],v[100:103]\n"
DT "a[116:117],v7 offset:160\n"
CV "v121,v90,v91\n"
MF "v[100:103],a[92:95],a[44:47],v[100:103]\n"
DT "a[118:119],v7 offset:672\n"
CV "v122,v92,v93\n"
MF "v[80:83],a[64:67],a[0:3],v[104:107]\n"
DT "a[120:121],v7 offset:192\n"
CV "v123,v94,v95\n"
MF "v[84:87],a[68:71],a[0:3],v[104:107]\n"
DT "a[122:123],v7 offset:704\n"
BL "v9,s[12:15],s78 offen offset:1056 nt lds\n"
MF "v[80:83],a[72:75],a[4:7],v[80:83]\n"
"s_add_u32 m0,m0,8448\n"
DT "a[124:125],v7 offset:224\n"
MF "v[84:87],a[76:79],a[4:7],v[84:87]\n"
DT "a[126:127],v7 offset:736\n"
MF "v[80:83],a[80:83],a[8:11],v[80:83]\n"
EX "v100,v100\n"
MF "v[84:87],a[84:87],a[8:11],v[84:87]\n"
"s_mov_b32 s50,s42\n"
BL "v10,s[16:19],s79 offen nt lds\n"
EX "v101,v101\n"
MF "v[80:83],a[88:91],a[12:15],v[80:83]\n"
"s_mov_b32 s42,s49\n"
EX "v102,v102\n"
MF "v[84:87],a[92:95],a[12:15],v[84:87]\n"
EX "v103,v103\n"
MF "v[88:91],a[64:67],a[16:19],v[108:111]\n"
CV "v124,v100,v101\n"
CV "v125,v102,v103\n"
MF "v[92:95],a[68:71],a[16:19],v[108:111]\n"
"ds_write_b64 v254,v[124:125]\n"
BL "v11,s[16:19],s79 offen offset:1056 nt lds\n"
"v_xor_b32 v254,0x400,v254\n"
MF "v[88:91],a[72:75],a[20:23],v[88:91]\n"
"s_add_u32 s51,s51,8192\n"
EX "v80,v80\n"
MF "v[92:95],a[76:79],a[20:23],v[92:95]\n"
"s_mov_b32 s79,s78\n"
EX "v81,v81\n"
MF "v[88:91],a[80:83],a[24:27],v[88:91]\n"
"s_xor_b32 s78,s51,s41\n"
EX "v82,v82\n"
MF "v[92:95],a[84:87],a[24:27],v[92:95]\n"
EX "v83,v83\n"
MF "v[88:91],a[88:91],a[28:31],v[88:91]\n"
EX "v84,v84\n"
MF "v[92:95],a[92:95],a[28:31],v[92:95]\n"
WT "vmcnt(24)\n"
WT "lgkmcnt(0)\n"
"s_barrier\n"
"v_add_u32 v6,s49,v4\n"
MF "v[136:139],a[96:99],v[116:119],v[136:139]\n"
DR "a[64:67],v6 offset:0\n"
EX "v85,v85\n"
MF "v[140:143],a[100:103],v[116:119],v[140:143]\n"
DR "a[68:71],v6 offset:512\n"
EX "v86,v86\n"
MF "v[144:147],a[104:107],v[116:119],v[144:147]\n"
DR "a[72:75],v6 offset:64\n"
EX "v87,v87\n"
MF "v[148:151],a[108:111],v[116:119],v[148:151]\n"
DR "a[76:79],v6 offset:576\n"
"v_add_u32 v7,s49,v5\n"
MF "v[152:155],a[112:115],v[116:119],v[152:155]\n"
DR "a[80:83],v6 offset:128\n"
EX "v88,v88\n"
MF "v[156:159],a[116:119],v[116:119],v[156:159]\n"
DR "a[84:87],v6 offset:640\n"
EX "v89,v89\n"
MF "v[160:163],a[120:123],v[116:119],v[160:163]\n"
"s_add_u32 s49,s49,16896\n"
DR "a[88:91],v6 offset:192\n"
EX "v90,v90\n"
MF "v[164:167],a[124:127],v[116:119],v[164:167]\n"
"s_cmp_eq_u32 s49,84480\n"
DR "a[92:95],v6 offset:704\n"
EX "v91,v91\n"
MF "v[168:171],a[48:51],v[116:119],v[168:171]\n"
"s_cselect_b32 s49,0,s49\n"
EX "v92,v92\n"
MF "v[172:175],a[96:99],v[120:123],v[172:175]\n"
EX "v93,v93\n"
MF "v[176:179],a[100:103],v[120:123],v[176:179]\n"
DT "a[96:97],v7 offset:0\n"
CV "v116,v80,v81\n"
MF "v[180:183],a[104:107],v[120:123],v[180:183]\n"
DT "a[98:99],v7 offset:512\n"
CV "v117,v82,v83\n"
MF "v[184:187],a[108:111],v[120:123],v[184:187]\n"
DT "a[100:101],v7 offset:32\n"
CV "v118,v84,v85\n"
MF "v[188:191],a[112:115],v[120:123],v[188:191]\n"
DT "a[102:103],v7 offset:544\n"
CV "v119,v86,v87\n"
MF "v[192:195],a[116:119],v[120:123],v[192:195]\n"
DT "a[104:105],v7 offset:64\n"
EX "v94,v94\n"
MF "v[196:199],a[120:123],v[120:123],v[196:199]\n"
DT "a[106:107],v7 offset:576\n"
EX "v95,v95\n"
MF "v[200:203],a[124:127],v[120:123],v[200:203]\n"
WT "lgkmcnt(6)\n"
DT "a[108:109],v7 offset:96\n"
MF "v[204:207],a[48:51],v[120:123],v[204:207]\n"
DT "a[110:111],v7 offset:608\n"
MF "a[128:131],a[212:215],v[40:43],a[128:131]\n"
MF "a[164:167],a[212:215],v[56:59],a[164:167]\n"
"s_add_u32 s44,s57,0\n"
MF "a[200:203],a[212:215],v[72:75],a[200:203]\n"
DT "a[212:213],v21 offset:27648\n"
BL "v[36:39],v244,s[28:31],s44 offen offset:1024 sc1\n"
MF "a[132:135],a[216:219],v[40:43],a[132:135]\n"
DT "a[214:215],v21 offset:32256\n"
MF "a[168:171],a[216:219],v[56:59],a[168:171]\n"
MF "a[204:207],a[216:219],v[72:75],a[204:207]\n"
DT "a[216:217],v21 offset:27680\n"
MF "a[136:139],a[220:223],v[40:43],a[136:139]\n"
DT "a[218:219],v21 offset:32288\n"
"v_add_u32 v21,36864,v21\n"
MF "a[172:175],a[220:223],v[56:59],a[172:175]\n"
DT "a[220:221],v22 offset:27648\n"
MF "a[140:143],a[224:227],v[40:43],a[140:143]\n"
DT "a[222:223],v22 offset:32256\n"
BL "v[52:55],v245,s[28:31],s44 offen offset:1024 sc1\n"
MF "a[176:179],a[224:227],v[56:59],a[176:179]\n"
DT "a[224:225],v22 offset:27680\n"
MF "a[144:147],a[228:231],v[40:43],a[144:147]\n"
DT "a[226:227],v22 offset:32288\n"
"v_add_u32 v22,36864,v22\n"
MF "a[180:183],a[228:231],v[56:59],a[180:183]\n"
DT "a[228:229],v23 offset:27648\n"
MF "a[148:151],a[232:235],v[40:43],a[148:151]\n"
DT "a[230:231],v23 offset:32256\n"
MF "a[184:187],a[232:235],v[56:59],a[184:187]\n"
DT "a[232:233],v23 offset:27680\n"
MF "a[152:155],a[236:239],v[40:43],a[152:155]\n"
DT "a[234:235],v23 offset:32288\n"
BL "v[68:71],v246,s[28:31],s44 offen offset:1024 sc1\n"
"v_add_u32 v23,36864,v23\n"
MF "a[188:191],a[236:239],v[56:59],a[188:191]\n"
DT "a[236:237],v24 offset:27648\n"
MF "a[156:159],a[240:243],v[40:43],a[156:159]\n"
DT "a[238:239],v24 offset:32256\n"
MF "a[192:195],a[240:243],v[56:59],a[192:195]\n"
DT "a[240:241],v24 offset:27680\n"
MF "a[160:163],a[244:247],v[40:43],a[160:163]\n"
DT "a[242:243],v24 offset:32288\n"
"v_add_u32 v24,36864,v24\n"
MF "a[196:199],a[244:247],v[56:59],a[196:199]\n"
DT "a[244:245],v25 offset:27648\n"
DT "a[246:247],v25 offset:32256\n"
"v_add_u32 v25,36864,v25\n"
MF "v[100:103],a[68:71],a[32:35],v[112:115]\n"
"s_add_u32 m0,s50,s48\n"
DT "a[112:113],v7 offset:128\n"
CV "v120,v88,v89\n"
MF "v[100:103],a[76:79],a[36:39],v[100:103]\n"
DT "a[114:115],v7 offset:640\n"
BL "v8,s[12:15],s78 offen nt lds\n"
MF "v[100:103],a[84:87],a[40:43],v[100:103]\n"
DT "a[116:117],v7 offset:160\n"
CV "v121,v90,v91\n"
MF "v[100:103],a[92:95],a[44:47],v[100:103]\n"
DT "a[118:119],v7 offset:672\n"
CV "v122,v92,v93\n"
MF "v[80:83],a[64:67],a[0:3],v[104:107]\n"
DT "a[120:121],v7 offset:192\n"
CV "v123,v94,v95\n"
MF "v[84:87],a[68:71],a[0:3],v[104:107]\n"
DT "a[122:123],v7 offset:704\n"
BL "v9,s[12:15],s78 offen offset:1056 nt lds\n"
MF "v[80:83],a[72:75],a[4:7],v[80:83]\n"
"s_add_u32 m0,m0,8448\n"
DT "a[124:125],v7 offset:224\n"
MF "v[84:87],a[76:79],a[4:7],v[84:87]\n"
DT "a[126:127],v7 offset:736\n"
MF "v[80:83],a[80:83],a[8:11],v[80:83]\n"
EX "v100,v100\n"
MF "v[84:87],a[84:87],a[8:11],v[84:87]\n"
"s_mov_b32 s50,s42\n"
BL "v10,s[16:19],s79 offen nt lds\n"
EX "v101,v101\n"
MF "v[80:83],a[88:91],a[12:15],v[80:83]\n"
"s_mov_b32 s42,s49\n"
EX "v102,v102\n"
MF "v[84:87],a[92:95],a[12:15],v[84:87]\n"
EX "v103,v103\n"
MF "v[88:91],a[64:67],a[16:19],v[108:111]\n"
CV "v124,v100,v101\n"
CV "v125,v102,v103\n"
MF "v[92:95],a[68:71],a[16:19],v[108:111]\n"
"ds_write_b64 v254,v[124:125]\n"
BL "v11,s[16:19],s79 offen offset:1056 nt lds\n"
"v_xor_b32 v254,0x400,v254\n"
MF "v[88:91],a[72:75],a[20:23],v[88:91]\n"
"s_add_u32 s51,s51,8192\n"
EX "v80,v80\n"
MF "v[92:95],a[76:79],a[20:23],v[92:95]\n"
"s_mov_b32 s79,s78\n"
EX "v81,v81\n"
MF "v[88:91],a[80:83],a[24:27],v[88:91]\n"
"s_xor_b32 s78,s51,s41\n"
EX "v82,v82\n"
MF "v[92:95],a[84:87],a[24:27],v[92:95]\n"
EX "v83,v83\n"
MF "v[88:91],a[88:91],a[28:31],v[88:91]\n"
EX "v84,v84\n"
MF "v[92:95],a[92:95],a[28:31],v[92:95]\n"
WT "vmcnt(27)\n"
WT "vmcnt(11)\n"
WT "lgkmcnt(0)\n"
"s_barrier\n"
"v_add_u32 v6,s49,v4\n"
MF "v[136:139],a[96:99],v[116:119],v[136:139]\n"
DR "a[64:67],v6 offset:0\n"
EX "v85,v85\n"
MF "v[140:143],a[100:103],v[116:119],v[140:143]\n"
DR "a[68:71],v6 offset:512\n"
EX "v86,v86\n"
MF "v[144:147],a[104:107],v[116:119],v[144:147]\n"
DR "a[72:75],v6 offset:64\n"
EX "v87,v87\n"
MF "v[148:151],a[108:111],v[116:119],v[148:151]\n"
DR "a[76:79],v6 offset:576\n"
"v_add_u32 v7,s49,v5\n"
MF "v[152:155],a[112:115],v[116:119],v[152:155]\n"
DR "a[80:83],v6 offset:128\n"
EX "v88,v88\n"
MF "v[156:159],a[116:119],v[116:119],v[156:159]\n"
DR "a[84:87],v6 offset:640\n"
EX "v89,v89\n"
MF "v[160:163],a[120:123],v[116:119],v[160:163]\n"
"s_add_u32 s49,s49,16896\n"
DR "a[88:91],v6 offset:192\n"
EX "v90,v90\n"
MF "v[164:167],a[124:127],v[116:119],v[164:167]\n"
"s_cmp_eq_u32 s49,84480\n"
DR "a[92:95],v6 offset:704\n"
EX "v91,v91\n"
MF "v[168:171],a[48:51],v[116:119],v[168:171]\n"
"s_cselect_b32 s49,0,s49\n"
EX "v92,v92\n"
MF "v[172:175],a[96:99],v[120:123],v[172:175]\n"
EX "v93,v93\n"
MF "v[176:179],a[100:103],v[120:123],v[176:179]\n"
DT "a[96:97],v7 offset:0\n"
CV "v116,v80,v81\n"
MF "v[180:183],a[104:107],v[120:123],v[180:183]\n"
DT "a[98:99],v7 offset:512\n"
CV "v117,v82,v83\n"
MF "v[184:187],a[108:111],v[120:123],v[184:187]\n"
DT "a[100:101],v7 offset:32\n"
CV "v118,v84,v85\n"
MF "v[188:191],a[112:115],v[120:123],v[188:191]\n"
DT "a[102:103],v7 offset:544\n"
CV "v119,v86,v87\n"
MF "v[192:195],a[116:119],v[120:123],v[192:195]\n"
DT "a[104:105],v7 offset:64\n"
EX "v94,v94\n"
MF "v[196:199],a[120:123],v[120:123],v[196:199]\n"
DT "a[106:107],v7 offset:576\n"
EX "v95,v95\n"
MF "v[200:203],a[124:127],v[120:123],v[200:203]\n"
WT "lgkmcnt(6)\n"
DT "a[108:109],v7 offset:96\n"
MF "v[204:207],a[48:51],v[120:123],v[204:207]\n"
DT "a[110:111],v7 offset:608\n"
MF "a[128:131],a[212:215],v[44:47],a[128:131]\n"
MF "a[164:167],a[212:215],v[60:63],a[164:167]\n"
"s_add_u32 s44,s57,0\n"
MF "a[200:203],a[212:215],v[76:79],a[200:203]\n"
DT "a[212:213],v21 offset:0\n"
BL "v[40:43],v244,s[28:31],s44 offen offset:2048 sc1\n"
MF "a[132:135],a[216:219],v[44:47],a[132:135]\n"
DT "a[214:215],v21 offset:4608\n"
MF "a[168:171],a[216:219],v[60:63],a[168:171]\n"
MF "a[204:207],a[216:219],v[76:79],a[204:207]\n"
DT "a[216:217],v21 offset:32\n"
MF "a[136:139],a[220:223],v[44:47],a[136:139]\n"
DT "a[218:219],v21 offset:4640\n"
MF "a[172:175],a[220:223],v[60:63],a[172:175]\n"
DT "a[220:221],v22 offset:0\n"
MF "a[140:143],a[224:227],v[44:47],a[140:143]\n"
DT "a[222:223],v22 offset:4608\n"
BL "v[56:59],v245,s[28:31],s44 offen offset:2048 sc1\n"
MF "a[176:179],a[224:227],v[60:63],a[176:179]\n"
DT "a[224:225],v22 offset:32\n"
MF "a[144:147],a[228:231],v[44:47],a[144:147]\n"
DT "a[226:227],v22 offset:4640\n"
MF "a[180:183],a[228:231],v[60:63],a[180:183]\n"
DT "a[228:229],v23 offset:0\n"
MF "a[148:151],a[232:235],v[44:47],a[148:151]\n"
DT "a[230:231],v23 offset:4608\n"
MF "a[184:187],a[232:235],v[60:63],a[184:187]\n"
DT "a[232:233],v23 offset:32\n"
MF "a[152:155],a[236:239],v[44:47],a[152:155]\n"
DT "a[234:235],v23 offset:4640\n"
BL "v[72:75],v246,s[28:31],s44 offen offset:2048 sc1\n"
MF "a[188:191],a[236:239],v[60:63],a[188:191]\n"
DT "a[236:237],v24 offset:0\n"
MF "a[156:159],a[240:243],v[44:47],a[156:159]\n"
DT "a[238:239],v24 offset:4608\n"
MF "a[192:195],a[240:243],v[60:63],a[192:195]\n"
DT "a[240:241],v24 offset:32\n"
MF "a[160:163],a[244:247],v[44:47],a[160:163]\n"
DT "a[242:243],v24 offset:4640\n"
MF "a[196:199],a[244:247],v[60:63],a[196:199]\n"
DT "a[244:245],v25 offset:0\n"
DT "a[246:247],v25 offset:4608\n"
MF "v[100:103],a[68:71],a[32:35],v[112:115]\n"
"s_add_u32 m0,s50,s48\n"
DT "a[112:113],v7 offset:128\n"
CV "v120,v88,v89\n"
MF "v[100:103],a[76:79],a[36:39],v[100:103]\n"
DT "a[114:115],v7 offset:640\n"
BL "v8,s[12:15],s78 offen nt lds\n"
MF "v[100:103],a[84:87],a[40:43],v[100:103]\n"
DT "a[116:117],v7 offset:160\n"
CV "v121,v90,v91\n"
MF "v[100:103],a[92:95],a[44:47],v[100:103]\n"
DT "a[118:119],v7 offset:672\n"
CV "v122,v92,v93\n"
MF "v[80:83],a[64:67],a[0:3],v[104:107]\n"
DT "a[120:121],v7 offset:192\n"
CV "v123,v94,v95\n"
MF "v[84:87],a[68:71],a[0:3],v[104:107]\n"
DT "a[122:123],v7 offset:704\n"
BL "v9,s[12:15],s78 offen offset:1056 nt lds\n"
MF "v[80:83],a[72:75],a[4:7],v[80:83]\n"
"s_add_u32 m0,m0,8448\n"
DT "a[124:125],v7 offset:224\n"
MF "v[84:87],a[76:79],a[4:7],v[84:87]\n"
DT "a[126:127],v7 offset:736\n"
MF "v[80:83],a[80:83],a[8:11],v[80:83]\n"
EX "v100,v100\n"
MF "v[84:87],a[84:87],a[8:11],v[84:87]\n"
"s_mov_b32 s50,s42\n"
BL "v10,s[16:19],s79 offen nt lds\n"
EX "v101,v101\n"
MF "v[80:83],a[88:91],a[12:15],v[80:83]\n"
"s_mov_b32 s42,s49\n"
EX "v102,v102\n"
MF "v[84:87],a[92:95],a[12:15],v[84:87]\n"
EX "v103,v103\n"
MF "v[88:91],a[64:67],a[16:19],v[108:111]\n"
CV "v124,v100,v101\n"
CV "v125,v102,v103\n"
MF "v[92:95],a[68:71],a[16:19],v[108:111]\n"
"ds_write_b64 v254,v[124:125]\n"
BL "v11,s[16:19],s79 offen offset:1056 nt lds\n"
"v_xor_b32 v254,0x400,v254\n"
MF "v[88:91],a[72:75],a[20:23],v[88:91]\n"
"s_add_u32 s51,s51,8192\n"
EX "v80,v80\n"
MF "v[92:95],a[76:79],a[20:23],v[92:95]\n"
"s_mov_b32 s79,s78\n"
EX "v81,v81\n"
MF "v[88:91],a[80:83],a[24:27],v[88:91]\n"
"s_xor_b32 s78,s51,s41\n"
EX "v82,v82\n"
MF "v[92:95],a[84:87],a[24:27],v[92:95]\n"
EX "v83,v83\n"
MF "v[88:91],a[88:91],a[28:31],v[88:91]\n"
EX "v84,v84\n"
MF "v[92:95],a[92:95],a[28:31],v[92:95]\n"
WT "vmcnt(24)\n"
WT "lgkmcnt(0)\n"
"s_barrier\n"
"v_add_u32 v6,s49,v4\n"
MF "v[136:139],a[96:99],v[116:119],v[136:139]\n"
DR "a[64:67],v6 offset:0\n"
EX "v85,v85\n"
MF "v[140:143],a[100:103],v[116:119],v[140:143]\n"
DR "a[68:71],v6 offset:512\n"
EX "v86,v86\n"
MF "v[144:147],a[104:107],v[116:119],v[144:147]\n"
DR "a[72:75],v6 offset:64\n"
EX "v87,v87\n"
MF "v[148:151],a[108:111],v[116:119],v[148:151]\n"
DR "a[76:79],v6 offset:576\n"
"v_add_u32 v7,s49,v5\n"
MF "v[152:155],a[112:115],v[116:119],v[152:155]\n"
DR "a[80:83],v6 offset:128\n"
EX "v88,v88\n"
MF "v[156:159],a[116:119],v[116:119],v[156:159]\n"
DR "a[84:87],v6 offset:640\n"
EX "v89,v89\n"
MF "v[160:163],a[120:123],v[116:119],v[160:163]\n"
"s_add_u32 s49,s49,16896\n"
DR "a[88:91],v6 offset:192\n"
EX "v90,v90\n"
MF "v[164:167],a[124:127],v[116:119],v[164:167]\n"
"s_cmp_eq_u32 s49,84480\n"
DR "a[92:95],v6 offset:704\n"
EX "v91,v91\n"
MF "v[168:171],a[48:51],v[116:119],v[168:171]\n"
"s_cselect_b32 s49,0,s49\n"
EX "v92,v92\n"
MF "v[172:175],a[96:99],v[120:123],v[172:175]\n"
EX "v93,v93\n"
MF "v[176:179],a[100:103],v[120:123],v[176:179]\n"
DT "a[96:97],v7 offset:0\n"
CV "v116,v80,v81\n"
MF "v[180:183],a[104:107],v[120:123],v[180:183]\n"
DT "a[98:99],v7 offset:512\n"
CV "v117,v82,v83\n"
MF "v[184:187],a[108:111],v[120:123],v[184:187]\n"
DT "a[100:101],v7 offset:32\n"
CV "v118,v84,v85\n"
MF "v[188:191],a[112:115],v[120:123],v[188:191]\n"
DT "a[102:103],v7 offset:544\n"
CV "v119,v86,v87\n"
MF "v[192:195],a[116:119],v[120:123],v[192:195]\n"
DT "a[104:105],v7 offset:64\n"
EX "v94,v94\n"
MF "v[196:199],a[120:123],v[120:123],v[196:199]\n"
DT "a[106:107],v7 offset:576\n"
EX "v95,v95\n"
MF "v[200:203],a[124:127],v[120:123],v[200:203]\n"
WT "lgkmcnt(6)\n"
DT "a[108:109],v7 offset:96\n"
MF "v[204:207],a[48:51],v[120:123],v[204:207]\n"
DT "a[110:111],v7 offset:608\n"
WT "vmcnt(21)\n"
MF "a[128:131],a[212:215],v[32:35],a[128:131]\n"
MF "a[164:167],a[212:215],v[48:51],a[164:167]\n"
"s_add_u32 s44,s57,0\n"
MF "a[200:203],a[212:215],v[64:67],a[200:203]\n"
DT "a[212:213],v21 offset:9216\n"
BL "v[44:47],v244,s[28:31],s44 offen offset:3072 sc1\n"
MF "a[132:135],a[216:219],v[32:35],a[132:135]\n"
DT "a[214:215],v21 offset:13824\n"
MF "a[168:171],a[216:219],v[48:51],a[168:171]\n"
BL "v[60:63],v245,s[28:31],s44 offen offset:3072 sc1\n"
MF "a[204:207],a[216:219],v[64:67],a[204:207]\n"
DT "a[216:217],v21 offset:9248\n"
MF "a[136:139],a[220:223],v[32:35],a[136:139]\n"
DT "a[218:219],v21 offset:13856\n"
BL "v[76:79],v246,s[28:31],s44 offen offset:3072 sc1\n"
MF "a[172:175],a[220:223],v[48:51],a[172:175]\n"
"s_add_u32 m0,s76,84480\n"
"s_add_u32 s67,s70,0\n"
DT "a[220:221],v22 offset:9216\n"
MF "a[140:143],a[224:227],v[32:35],a[140:143]\n"
DT "a[222:223],v22 offset:13824\n"
BL "v12,s[24:27],s67 offen offset:0 lds\n"
MF "a[176:179],a[224:227],v[48:51],a[176:179]\n"
DT "a[224:225],v22 offset:9248\n"
MF "a[144:147],a[228:231],v[32:35],a[144:147]\n"
DT "a[226:227],v22 offset:13856\n"
BL "v13,s[24:27],s67 offen offset:1024 lds\n"
MF "a[180:183],a[228:231],v[48:51],a[180:183]\n"
DT "a[228:229],v23 offset:9216\n"
MF "a[148:151],a[232:235],v[32:35],a[148:151]\n"
DT "a[230:231],v23 offset:13824\n"
BL "v14,s[24:27],s67 offen offset:2048 lds\n"
MF "a[184:187],a[232:235],v[48:51],a[184:187]\n"
"s_add_u32 m0,s76,87552\n"
"s_add_u32 s67,s70,0\n"
DT "a[232:233],v23 offset:9248\n"
MF "a[152:155],a[236:239],v[32:35],a[152:155]\n"
DT "a[234:235],v23 offset:13856\n"
BL "v15,s[24:27],s67 offen offset:0 lds\n"
MF "a[188:191],a[236:239],v[48:51],a[188:191]\n"
DT "a[236:237],v24 offset:9216\n"
MF "a[156:159],a[240:243],v[32:35],a[156:159]\n"
DT "a[238:239],v24 offset:13824\n"
BL "v16,s[24:27],s67 offen offset:1024 lds\n"
MF "a[192:195],a[240:243],v[48:51],a[192:195]\n"
DT "a[240:241],v24 offset:9248\n"
MF "a[160:163],a[244:247],v[32:35],a[160:163]\n"
DT "a[242:243],v24 offset:13856\n"
BL "v17,s[24:27],s67 offen offset:2048 lds\n"
MF "a[196:199],a[244:247],v[48:51],a[196:199]\n"
DT "a[244:245],v25 offset:9216\n"
DT "a[246:247],v25 offset:13824\n"
MF "v[100:103],a[68:71],a[32:35],v[112:115]\n"
"s_add_u32 m0,s50,s48\n"
"s_add_u32 m0,m0,8448\n"
DT "a[112:113],v7 offset:128\n"
CV "v120,v88,v89\n"
MF "v[100:103],a[76:79],a[36:39],v[100:103]\n"
DT "a[114:115],v7 offset:640\n"
BL "v10,s[16:19],s79 offen nt lds\n"
MF "v[100:103],a[84:87],a[40:43],v[100:103]\n"
DT "a[116:117],v7 offset:160\n"
CV "v121,v90,v91\n"
MF "v[100:103],a[92:95],a[44:47],v[100:103]\n"
DT "a[118:119],v7 offset:672\n"
CV "v122,v92,v93\n"
MF "v[80:83],a[64:67],a[0:3],v[104:107]\n"
DT "a[120:121],v7 offset:192\n"
CV "v123,v94,v95\n"
MF "v[84:87],a[68:71],a[0:3],v[104:107]\n"
DT "a[122:123],v7 offset:704\n"
BL "v11,s[16:19],s79 offen offset:1056 nt lds\n"
MF "v[80:83],a[72:75],a[4:7],v[80:83]\n"
"s_add_u32 s51,s51,8192\n"
"s_mov_b32 s79,s78\n"
"s_xor_b32 s78,s51,s41\n"
DT "a[124:125],v7 offset:224\n"
MF "v[84:87],a[76:79],a[4:7],v[84:87]\n"
DT "a[126:127],v7 offset:736\n"
MF "v[80:83],a[80:83],a[8:11],v[80:83]\n"
EX "v100,v100\n"
MF "v[84:87],a[84:87],a[8:11],v[84:87]\n"
"s_mov_b32 s50,s42\n"
EX "v101,v101\n"
MF "v[80:83],a[88:91],a[12:15],v[80:83]\n"
"s_mov_b32 s42,s49\n"
EX "v102,v102\n"
MF "v[84:87],a[92:95],a[12:15],v[84:87]\n"
EX "v103,v103\n"
MF "v[88:91],a[64:67],a[16:19],v[108:111]\n"
CV "v124,v100,v101\n"
CV "v125,v102,v103\n"
MF "v[92:95],a[68:71],a[16:19],v[108:111]\n"
"ds_write_b64 v254,v[124:125]\n"
"v_xor_b32 v254,0x400,v254\n"
MF "v[88:91],a[72:75],a[20:23],v[88:91]\n"
EX "v80,v80\n"
MF "v[92:95],a[76:79],a[20:23],v[92:95]\n"
EX "v81,v81\n"
MF "v[88:91],a[80:83],a[24:27],v[88:91]\n"
EX "v82,v82\n"
MF "v[92:95],a[84:87],a[24:27],v[92:95]\n"
EX "v83,v83\n"
MF "v[88:91],a[88:91],a[28:31],v[88:91]\n"
EX "v84,v84\n"
MF "v[92:95],a[92:95],a[28:31],v[92:95]\n"
WT "vmcnt(25)\n"
WT "lgkmcnt(0)\n"
"s_barrier\n"
"v_add_u32 v6,s49,v4\n"
MF "v[136:139],a[96:99],v[116:119],v[136:139]\n"
DR "a[64:67],v6 offset:0\n"
EX "v85,v85\n"
MF "v[140:143],a[100:103],v[116:119],v[140:143]\n"
DR "a[68:71],v6 offset:512\n"
EX "v86,v86\n"
MF "v[144:147],a[104:107],v[116:119],v[144:147]\n"
DR "a[72:75],v6 offset:64\n"
EX "v87,v87\n"
MF "v[148:151],a[108:111],v[116:119],v[148:151]\n"
DR "a[76:79],v6 offset:576\n"
"v_add_u32 v7,s49,v5\n"
MF "v[152:155],a[112:115],v[116:119],v[152:155]\n"
DR "a[80:83],v6 offset:128\n"
EX "v88,v88\n"
MF "v[156:159],a[116:119],v[116:119],v[156:159]\n"
DR "a[84:87],v6 offset:640\n"
EX "v89,v89\n"
MF "v[160:163],a[120:123],v[116:119],v[160:163]\n"
"s_add_u32 s49,s49,16896\n"
DR "a[88:91],v6 offset:192\n"
EX "v90,v90\n"
MF "v[164:167],a[124:127],v[116:119],v[164:167]\n"
"s_cmp_eq_u32 s49,84480\n"
DR "a[92:95],v6 offset:704\n"
EX "v91,v91\n"
MF "v[168:171],a[48:51],v[116:119],v[168:171]\n"
"s_cselect_b32 s49,0,s49\n"
EX "v92,v92\n"
MF "v[172:175],a[96:99],v[120:123],v[172:175]\n"
EX "v93,v93\n"
MF "v[176:179],a[100:103],v[120:123],v[176:179]\n"
DT "a[96:97],v7 offset:0\n"
CV "v116,v80,v81\n"
MF "v[180:183],a[104:107],v[120:123],v[180:183]\n"
DT "a[98:99],v7 offset:512\n"
CV "v117,v82,v83\n"
MF "v[184:187],a[108:111],v[120:123],v[184:187]\n"
DT "a[100:101],v7 offset:32\n"
CV "v118,v84,v85\n"
MF "v[188:191],a[112:115],v[120:123],v[188:191]\n"
DT "a[102:103],v7 offset:544\n"
CV "v119,v86,v87\n"
MF "v[192:195],a[116:119],v[120:123],v[192:195]\n"
DT "a[104:105],v7 offset:64\n"
EX "v94,v94\n"
MF "v[196:199],a[120:123],v[120:123],v[196:199]\n"
DT "a[106:107],v7 offset:576\n"
EX "v95,v95\n"
MF "v[200:203],a[124:127],v[120:123],v[200:203]\n"
WT "lgkmcnt(6)\n"
DT "a[108:109],v7 offset:96\n"
MF "v[204:207],a[48:51],v[120:123],v[204:207]\n"
DT "a[110:111],v7 offset:608\n"
WT "vmcnt(22)\n"
MF "a[128:131],a[212:215],v[36:39],a[128:131]\n"
MF "a[164:167],a[212:215],v[52:55],a[164:167]\n"
"s_add_u32 s44,s58,0\n"
MF "a[200:203],a[212:215],v[68:71],a[200:203]\n"
DT "a[212:213],v21 offset:18432\n"
BL "v[32:35],v244,s[28:31],s44 offen offset:0 sc1\n"
MF "a[132:135],a[216:219],v[36:39],a[132:135]\n"
DT "a[214:215],v21 offset:23040\n"
MF "a[168:171],a[216:219],v[52:55],a[168:171]\n"
MF "a[204:207],a[216:219],v[68:71],a[204:207]\n"
DT "a[216:217],v21 offset:18464\n"
BL "v[48:51],v245,s[28:31],s44 offen offset:0 sc1\n"
MF "a[136:139],a[220:223],v[36:39],a[136:139]\n"
DT "a[218:219],v21 offset:23072\n"
MF "a[172:175],a[220:223],v[52:55],a[172:175]\n"
DT "a[220:221],v22 offset:18432\n"
MF "a[140:143],a[224:227],v[36:39],a[140:143]\n"
DT "a[222:223],v22 offset:23040\n"
BL "v[64:67],v246,s[28:31],s44 offen offset:0 sc1\n"
MF "a[176:179],a[224:227],v[52:55],a[176:179]\n"
"s_add_u32 m0,s76,90624\n"
DT "a[224:225],v22 offset:18464\n"
MF "a[144:147],a[228:231],v[36:39],a[144:147]\n"
"s_add_u32 s67,s70,0\n"
DT "a[226:227],v22 offset:23072\n"
MF "a[180:183],a[228:231],v[52:55],a[180:183]\n"
DT "a[228:229],v23 offset:18432\n"
BL "v18,s[24:27],s67 offen offset:0 lds\n"
MF "a[148:151],a[232:235],v[36:39],a[148:151]\n"
DT "a[230:231],v23 offset:23040\n"
MF "a[184:187],a[232:235],v[52:55],a[184:187]\n"
DT "a[232:233],v23 offset:18464\n"
MF "a[152:155],a[236:239],v[36:39],a[152:155]\n"
DT "a[234:235],v23 offset:23072\n"
BL "v19,s[24:27],s67 offen offset:1024 lds\n"
MF "a[188:191],a[236:239],v[52:55],a[188:191]\n"
DT "a[236:237],v24 offset:18432\n"
MF "a[156:159],a[240:243],v[36:39],a[156:159]\n"
DT "a[238:239],v24 offset:23040\n"
MF "a[192:195],a[240:243],v[52:55],a[192:195]\n"
DT "a[240:241],v24 offset:18464\n"
BL "v20,s[24:27],s67 offen offset:2048 lds\n"
MF "a[160:163],a[244:247],v[36:39],a[160:163]\n"
DT "a[242:243],v24 offset:23072\n"
MF "a[196:199],a[244:247],v[52:55],a[196:199]\n"
DT "a[244:245],v25 offset:18432\n"
DT "a[246:247],v25 offset:23040\n"
MF "v[100:103],a[68:71],a[32:35],v[112:115]\n"
DT "a[112:113],v7 offset:128\n"
CV "v120,v88,v89\n"
MF "v[100:103],a[76:79],a[36:39],v[100:103]\n"
DT "a[114:115],v7 offset:640\n"
MF "v[100:103],a[84:87],a[40:43],v[100:103]\n"
DT "a[116:117],v7 offset:160\n"
CV "v121,v90,v91\n"
MF "v[100:103],a[92:95],a[44:47],v[100:103]\n"
DT "a[118:119],v7 offset:672\n"
CV "v122,v92,v93\n"
MF "v[80:83],a[64:67],a[0:3],v[104:107]\n"
DT "a[120:121],v7 offset:192\n"
CV "v123,v94,v95\n"
MF "v[84:87],a[68:71],a[0:3],v[104:107]\n"
DT "a[122:123],v7 offset:704\n"
MF "v[80:83],a[72:75],a[4:7],v[80:83]\n"
DT "a[124:125],v7 offset:224\n"
MF "v[84:87],a[76:79],a[4:7],v[84:87]\n"
DT "a[126:127],v7 offset:736\n"
MF "v[80:83],a[80:83],a[8:11],v[80:83]\n"
EX "v100,v100\n"
MF "v[84:87],a[84:87],a[8:11],v[84:87]\n"
"s_mov_b32 s50,s42\n"
EX "v101,v101\n"
MF "v[80:83],a[88:91],a[12:15],v[80:83]\n"
"s_mov_b32 s42,s49\n"
EX "v102,v102\n"
MF "v[84:87],a[92:95],a[12:15],v[84:87]\n"
EX "v103,v103\n"
MF "v[88:91],a[64:67],a[16:19],v[108:111]\n"
CV "v124,v100,v101\n"
CV "v125,v102,v103\n"
MF "v[92:95],a[68:71],a[16:19],v[108:111]\n"
"ds_write_b64 v254,v[124:125]\n"
"v_xor_b32 v254,0x400,v254\n"
MF "v[88:91],a[72:75],a[20:23],v[88:91]\n"
EX "v80,v80\n"
MF "v[92:95],a[76:79],a[20:23],v[92:95]\n"
EX "v81,v81\n"
MF "v[88:91],a[80:83],a[24:27],v[88:91]\n"
EX "v82,v82\n"
MF "v[92:95],a[84:87],a[24:27],v[92:95]\n"
EX "v83,v83\n"
MF "v[88:91],a[88:91],a[28:31],v[88:91]\n"
EX "v84,v84\n"
MF "v[92:95],a[92:95],a[28:31],v[92:95]\n"
WT "vmcnt(24)\n"
WT "lgkmcnt(0)\n"
"s_barrier\n"
"v_add_u32 v6,s49,v4\n"
MF "v[136:139],a[96:99],v[116:119],v[136:139]\n"
DR "a[64:67],v6 offset:0\n"
EX "v85,v85\n"
MF "v[140:143],a[100:103],v[116:119],v[140:143]\n"
DR "a[68:71],v6 offset:512\n"
EX "v86,v86\n"
MF "v[144:147],a[104:107],v[116:119],v[144:147]\n"
DR "a[72:75],v6 offset:64\n"
EX "v87,v87\n"
MF "v[148:151],a[108:111],v[116:119],v[148:151]\n"
DR "a[76:79],v6 offset:576\n"
"v_add_u32 v7,s49,v5\n"
MF "v[152:155],a[112:115],v[116:119],v[152:155]\n"
DR "a[80:83],v6 offset:128\n"
EX "v88,v88\n"
MF "v[156:159],a[116:119],v[116:119],v[156:159]\n"
DR "a[84:87],v6 offset:640\n"
EX "v89,v89\n"
MF "v[160:163],a[120:123],v[116:119],v[160:163]\n"
"s_add_u32 s49,s49,16896\n"
DR "a[88:91],v6 offset:192\n"
EX "v90,v90\n"
MF "v[164:167],a[124:127],v[116:119],v[164:167]\n"
"s_cmp_eq_u32 s49,84480\n"
DR "a[92:95],v6 offset:704\n"
EX "v91,v91\n"
MF "v[168:171],a[48:51],v[116:119],v[168:171]\n"
"s_cselect_b32 s49,0,s49\n"
EX "v92,v92\n"
MF "v[172:175],a[96:99],v[120:123],v[172:175]\n"
EX "v93,v93\n"
MF "v[176:179],a[100:103],v[120:123],v[176:179]\n"
DT "a[96:97],v7 offset:0\n"
CV "v116,v80,v81\n"
MF "v[180:183],a[104:107],v[120:123],v[180:183]\n"
DT "a[98:99],v7 offset:512\n"
CV "v117,v82,v83\n"
MF "v[184:187],a[108:111],v[120:123],v[184:187]\n"
DT "a[100:101],v7 offset:32\n"
CV "v118,v84,v85\n"
MF "v[188:191],a[112:115],v[120:123],v[188:191]\n"
DT "a[102:103],v7 offset:544\n"
CV "v119,v86,v87\n"
MF "v[192:195],a[116:119],v[120:123],v[192:195]\n"
DT "a[104:105],v7 offset:64\n"
EX "v94,v94\n"
MF "v[196:199],a[120:123],v[120:123],v[196:199]\n"
DT "a[106:107],v7 offset:576\n"
EX "v95,v95\n"
MF "v[200:203],a[124:127],v[120:123],v[200:203]\n"
WT "lgkmcnt(6)\n"
DT "a[108:109],v7 offset:96\n"
MF "v[204:207],a[48:51],v[120:123],v[204:207]\n"
DT "a[110:111],v7 offset:608\n"
WT "vmcnt(21)\n"
MF "a[128:131],a[212:215],v[40:43],a[128:131]\n"
MF "a[164:167],a[212:215],v[56:59],a[164:167]\n"
"s_add_u32 s44,s58,0\n"
MF "a[200:203],a[212:215],v[72:75],a[200:203]\n"
DT "a[212:213],v21 offset:27648\n"
BL "v[36:39],v244,s[28:31],s44 offen offset:1024 sc1\n"
MF "a[132:135],a[216:219],v[40:43],a[132:135]\n"
DT "a[214:215],v21 offset:32256\n"
MF "a[168:171],a[216:219],v[56:59],a[168:171]\n"
MF "a[204:207],a[216:219],v[72:75],a[204:207]\n"
DT "a[216:217],v21 offset:27680\n"
MF "a[136:139],a[220:223],v[40:43],a[136:139]\n"
DT "a[218:219],v21 offset:32288\n"
"v_subrev_u32 v21,36864,v21\n"
MF "a[172:175],a[220:223],v[56:59],a[172:175]\n"
DT "a[220:221],v22 offset:27648\n"
MF "a[140:143],a[224:227],v[40:43],a[140:143]\n"
DT "a[222:223],v22 offset:32256\n"
BL "v[52:55],v245,s[28:31],s44 offen offset:1024 sc1\n"
MF "a[176:179],a[224:227],v[56:59],a[176:179]\n"
DT "a[224:225],v22 offset:27680\n"
MF "a[144:147],a[228:231],v[40:43],a[144:147]\n"
DT "a[226:227],v22 offset:32288\n"
"v_subrev_u32 v22,36864,v22\n"
MF "a[180:183],a[228:231],v[56:59],a[180:183]\n"
DT "a[228:229],v23 offset:27648\n"
MF "a[148:151],a[232:235],v[40:43],a[148:151]\n"
DT "a[230:231],v23 offset:32256\n"
MF "a[184:187],a[232:235],v[56:59],a[184:187]\n"
DT "a[232:233],v23 offset:27680\n"
MF "a[152:155],a[236:239],v[40:43],a[152:155]\n"
DT "a[234:235],v23 offset:32288\n"
BL "v[68:71],v246,s[28:31],s44 offen offset:1024 sc1\n"
"v_subrev_u32 v23,36864,v23\n"
MF "a[188:191],a[236:239],v[56:59],a[188:191]\n"
DT "a[236:237],v24 offset:27648\n"
MF "a[156:159],a[240:243],v[40:43],a[156:159]\n"
DT "a[238:239],v24 offset:32256\n"
MF "a[192:195],a[240:243],v[56:59],a[192:195]\n"
DT "a[240:241],v24 offset:27680\n"
MF "a[160:163],a[244:247],v[40:43],a[160:163]\n"
DT "a[242:243],v24 offset:32288\n"
"v_subrev_u32 v24,36864,v24\n"
MF "a[196:199],a[244:247],v[56:59],a[196:199]\n"
DT "a[244:245],v25 offset:27648\n"
DT "a[246:247],v25 offset:32256\n"
"v_subrev_u32 v25,36864,v25\n"
MF "v[100:103],a[68:71],a[32:35],v[112:115]\n"
DT "a[112:113],v7 offset:128\n"
CV "v120,v88,v89\n"
MF "v[100:103],a[76:79],a[36:39],v[100:103]\n"
DT "a[114:115],v7 offset:640\n"
MF "v[100:103],a[84:87],a[40:43],v[100:103]\n"
DT "a[116:117],v7 offset:160\n"
CV "v121,v90,v91\n"
MF "v[100:103],a[92:95],a[44:47],v[100:103]\n"
DT "a[118:119],v7 offset:672\n"
CV "v122,v92,v93\n"
MF "v[80:83],a[64:67],a[0:3],v[104:107]\n"
DT "a[120:121],v7 offset:192\n"
CV "v123,v94,v95\n"
MF "v[84:87],a[68:71],a[0:3],v[104:107]\n"
DT "a[122:123],v7 offset:704\n"
MF "v[80:83],a[72:75],a[4:7],v[80:83]\n"
DT "a[124:125],v7 offset:224\n"
MF "v[84:87],a[76:79],a[4:7],v[84:87]\n"
DT "a[126:127],v7 offset:736\n"
MF "v[80:83],a[80:83],a[8:11],v[80:83]\n"
EX "v100,v100\n"
MF "v[84:87],a[84:87],a[8:11],v[84:87]\n"
"s_mov_b32 s50,s42\n"
EX "v101,v101\n"
MF "v[80:83],a[88:91],a[12:15],v[80:83]\n"
"s_mov_b32 s42,s49\n"
EX "v102,v102\n"
MF "v[84:87],a[92:95],a[12:15],v[84:87]\n"
EX "v103,v103\n"
MF "v[88:91],a[64:67],a[16:19],v[108:111]\n"
CV "v124,v100,v101\n"
CV "v125,v102,v103\n"
MF "v[92:95],a[68:71],a[16:19],v[108:111]\n"
"ds_write_b64 v254,v[124:125]\n"
"v_xor_b32 v254,0x400,v254\n"
MF "v[88:91],a[72:75],a[20:23],v[88:91]\n"
EX "v80,v80\n"
MF "v[92:95],a[76:79],a[20:23],v[92:95]\n"
EX "v81,v81\n"
MF "v[88:91],a[80:83],a[24:27],v[88:91]\n"
EX "v82,v82\n"
MF "v[92:95],a[84:87],a[24:27],v[92:95]\n"
EX "v83,v83\n"
MF "v[88:91],a[88:91],a[28:31],v[88:91]\n"
EX "v84,v84\n"
MF "v[92:95],a[92:95],a[28:31],v[92:95]\n"
WT "vmcnt(20)\n"
WT "vmcnt(3)\n"
WT "lgkmcnt(0)\n"
"s_barrier\n"
"v_add_u32 v6,s49,v4\n"
MF "v[136:139],a[96:99],v[116:119],v[136:139]\n"
DR "a[64:67],v6 offset:0\n"
EX "v85,v85\n"
MF "v[140:143],a[100:103],v[116:119],v[140:143]\n"
DR "a[68:71],v6 offset:512\n"
EX "v86,v86\n"
MF "v[144:147],a[104:107],v[116:119],v[144:147]\n"
DR "a[72:75],v6 offset:64\n"
EX "v87,v87\n"
MF "v[148:151],a[108:111],v[116:119],v[148:151]\n"
DR "a[76:79],v6 offset:576\n"
"v_add_u32 v7,s49,v5\n"
MF "v[152:155],a[112:115],v[116:119],v[152:155]\n"
DR "a[80:83],v6 offset:128\n"
EX "v88,v88\n"
MF "v[156:159],a[116:119],v[116:119],v[156:159]\n"
DR "a[84:87],v6 offset:640\n"
EX "v89,v89\n"
MF "v[160:163],a[120:123],v[116:119],v[160:163]\n"
"s_add_u32 s49,s49,16896\n"
DR "a[88:91],v6 offset:192\n"
EX "v90,v90\n"
MF "v[164:167],a[124:127],v[116:119],v[164:167]\n"
"s_cmp_eq_u32 s49,84480\n"
DR "a[92:95],v6 offset:704\n"
EX "v91,v91\n"
MF "v[168:171],a[48:51],v[116:119],v[168:171]\n"
"s_cselect_b32 s49,0,s49\n"
EX "v92,v92\n"
MF "v[172:175],a[96:99],v[120:123],v[172:175]\n"
EX "v93,v93\n"
MF "v[176:179],a[100:103],v[120:123],v[176:179]\n"
DT "a[96:97],v7 offset:0\n"
CV "v116,v80,v81\n"
MF "v[180:183],a[104:107],v[120:123],v[180:183]\n"
DT "a[98:99],v7 offset:512\n"
CV "v117,v82,v83\n"
MF "v[184:187],a[108:111],v[120:123],v[184:187]\n"
DT "a[100:101],v7 offset:32\n"
CV "v118,v84,v85\n"
MF "v[188:191],a[112:115],v[120:123],v[188:191]\n"
DT "a[102:103],v7 offset:544\n"
CV "v119,v86,v87\n"
MF "v[192:195],a[116:119],v[120:123],v[192:195]\n"
DT "a[104:105],v7 offset:64\n"
EX "v94,v94\n"
MF "v[196:199],a[120:123],v[120:123],v[196:199]\n"
DT "a[106:107],v7 offset:576\n"
EX "v95,v95\n"
MF "v[200:203],a[124:127],v[120:123],v[200:203]\n"
WT "lgkmcnt(6)\n"
DT "a[108:109],v7 offset:96\n"
MF "v[204:207],a[48:51],v[120:123],v[204:207]\n"
DT "a[110:111],v7 offset:608\n"
WT "vmcnt(17)\n"
MF "a[128:131],a[212:215],v[44:47],a[128:131]\n"
MF "a[164:167],a[212:215],v[60:63],a[164:167]\n"
"s_add_u32 s44,s58,0\n"
MF "a[200:203],a[212:215],v[76:79],a[200:203]\n"
DT "a[212:213],v21 offset:0\n"
BL "v[40:43],v244,s[28:31],s44 offen offset:2048 sc1\n"
MF "a[132:135],a[216:219],v[44:47],a[132:135]\n"
DT "a[214:215],v21 offset:4608\n"
MF "a[168:171],a[216:219],v[60:63],a[168:171]\n"
MF "a[204:207],a[216:219],v[76:79],a[204:207]\n"
DT "a[216:217],v21 offset:32\n"
MF "a[136:139],a[220:223],v[44:47],a[136:139]\n"
DT "a[218:219],v21 offset:4640\n"
MF "a[172:175],a[220:223],v[60:63],a[172:175]\n"
DT "a[220:221],v22 offset:0\n"
MF "a[140:143],a[224:227],v[44:47],a[140:143]\n"
DT "a[222:223],v22 offset:4608\n"
BL "v[56:59],v245,s[28:31],s44 offen offset:2048 sc1\n"
MF "a[176:179],a[224:227],v[60:63],a[176:179]\n"
DT "a[224:225],v22 offset:32\n"
MF "a[144:147],a[228:231],v[44:47],a[144:147]\n"
DT "a[226:227],v22 offset:4640\n"
MF "a[180:183],a[228:231],v[60:63],a[180:183]\n"
DT "a[228:229],v23 offset:0\n"
MF "a[148:151],a[232:235],v[44:47],a[148:151]\n"
DT "a[230:231],v23 offset:4608\n"
MF "a[184:187],a[232:235],v[60:63],a[184:187]\n"
DT "a[232:233],v23 offset:32\n"
MF "a[152:155],a[236:239],v[44:47],a[152:155]\n"
DT "a[234:235],v23 offset:4640\n"
BL "v[72:75],v246,s[28:31],s44 offen offset:2048 sc1\n"
MF "a[188:191],a[236:239],v[60:63],a[188:191]\n"
DT "a[236:237],v24 offset:0\n"
MF "a[156:159],a[240:243],v[44:47],a[156:159]\n"
DT "a[238:239],v24 offset:4608\n"
MF "a[192:195],a[240:243],v[60:63],a[192:195]\n"
DT "a[240:241],v24 offset:32\n"
MF "a[160:163],a[244:247],v[44:47],a[160:163]\n"
DT "a[242:243],v24 offset:4640\n"
MF "a[196:199],a[244:247],v[60:63],a[196:199]\n"
DT "a[244:245],v25 offset:0\n"
DT "a[246:247],v25 offset:4608\n"
MF "v[100:103],a[68:71],a[32:35],v[112:115]\n"
"s_add_u32 s44,s58,0\n"
DT "a[112:113],v7 offset:128\n"
CV "v120,v88,v89\n"
MF "v[100:103],a[76:79],a[36:39],v[100:103]\n"
DT "a[114:115],v7 offset:640\n"
BL "v[44:47],v244,s[28:31],s44 offen offset:3072 sc1\n"
MF "v[100:103],a[84:87],a[40:43],v[100:103]\n"
DT "a[116:117],v7 offset:160\n"
CV "v121,v90,v91\n"
MF "v[100:103],a[92:95],a[44:47],v[100:103]\n"
DT "a[118:119],v7 offset:672\n"
CV "v122,v92,v93\n"
MF "v[80:83],a[64:67],a[0:3],v[104:107]\n"
DT "a[120:121],v7 offset:192\n"
BL "v[60:63],v245,s[28:31],s44 offen offset:3072 sc1\n"
MF "v[84:87],a[68:71],a[0:3],v[104:107]\n"
DT "a[122:123],v7 offset:704\n"
CV "v123,v94,v95\n"
MF "v[80:83],a[72:75],a[4:7],v[80:83]\n"
DT "a[124:125],v7 offset:224\n"
MF "v[84:87],a[76:79],a[4:7],v[84:87]\n"
DT "a[126:127],v7 offset:736\n"
BL "v[76:79],v246,s[28:31],s44 offen offset:3072 sc1\n"
MF "v[80:83],a[80:83],a[8:11],v[80:83]\n"
"s_add_u32 m0,s76,121344\n"
"s_add_u32 s67,s68,147456\n"
EX "v100,v100\n"
MF "v[84:87],a[84:87],a[8:11],v[84:87]\n"
"s_mov_b32 s50,s42\n"
EX "v101,v101\n"
MF "v[80:83],a[88:91],a[12:15],v[80:83]\n"
"s_mov_b32 s42,s49\n"
BL "v12,s[24:27],s67 offen offset:0 lds\n"
EX "v102,v102\n"
MF "v[84:87],a[92:95],a[12:15],v[84:87]\n"
EX "v103,v103\n"
MF "v[88:91],a[64:67],a[16:19],v[108:111]\n"
CV "v124,v100,v101\n"
CV "v125,v102,v103\n"
MF "v[92:95],a[68:71],a[16:19],v[108:111]\n"
"ds_write_b64 v254,v[124:125]\n"
BL "v13,s[24:27],s67 offen offset:1024 lds\n"
"v_xor_b32 v254,0x400,v254\n"
MF "v[88:91],a[72:75],a[20:23],v[88:91]\n"
EX "v80,v80\n"
MF "v[92:95],a[76:79],a[20:23],v[92:95]\n"
EX "v81,v81\n"
MF "v[88:91],a[80:83],a[24:27],v[88:91]\n"
BL "v14,s[24:27],s67 offen offset:2048 lds\n"
EX "v82,v82\n"
MF "v[92:95],a[84:87],a[24:27],v[92:95]\n"
"s_add_u32 m0,s76,124416\n"
"s_add_u32 s67,s68,147456\n"
EX "v83,v83\n"
MF "v[88:91],a[88:91],a[28:31],v[88:91]\n"
EX "v84,v84\n"
MF "v[92:95],a[92:95],a[28:31],v[92:95]\n"
WT "vmcnt(18)\n"
WT "lgkmcnt(0)\n"
"s_barrier\n"
BL "v15,s[24:27],s67 offen offset:0 lds\n"
"v_add_u32 v6,s49,v4\n"
MF "v[136:139],a[96:99],v[116:119],v[136:139]\n"
EX "v85,v85\n"
MF "v[140:143],a[100:103],v[116:119],v[140:143]\n"
EX "v86,v86\n"
MF "v[144:147],a[104:107],v[116:119],v[144:147]\n"
BL "v16,s[24:27],s67 offen offset:1024 lds\n"
EX "v87,v87\n"
MF "v[148:151],a[108:111],v[116:119],v[148:151]\n"
"v_add_u32 v7,s49,v5\n"
MF "v[152:155],a[112:115],v[116:119],v[152:155]\n"
EX "v88,v88\n"
MF "v[156:159],a[116:119],v[116:119],v[156:159]\n"
BL "v17,s[24:27],s67 offen offset:2048 lds\n"
EX "v89,v89\n"
MF "v[160:163],a[120:123],v[116:119],v[160:163]\n"
"s_add_u32 m0,s76,127488\n"
"s_add_u32 s67,s68,147456\n"
"s_add_u32 s49,s49,16896\n"
EX "v90,v90\n"
MF "v[164:167],a[124:127],v[116:119],v[164:167]\n"
"s_cmp_eq_u32 s49,84480\n"
EX "v91,v91\n"
MF "v[168:171],a[48:51],v[116:119],v[168:171]\n"
"s_cselect_b32 s49,0,s49\n"
BL "v18,s[24:27],s67 offen offset:0 lds\n"
EX "v92,v92\n"
MF "v[172:175],a[96:99],v[120:123],v[172:175]\n"
DT "a[96:97],v7 offset:0\n"
DT "a[98:99],v7 offset:512\n"
EX "v93,v93\n"
MF "v[176:179],a[100:103],v[120:123],v[176:179]\n"
DT "a[100:101],v7 offset:32\n"
DT "a[102:103],v7 offset:544\n"
CV "v116,v80,v81\n"
MF "v[180:183],a[104:107],v[120:123],v[180:183]\n"
DT "a[104:105],v7 offset:64\n"
DT "a[106:107],v7 offset:576\n"
BL "v19,s[24:27],s67 offen offset:1024 lds\n"
CV "v117,v82,v83\n"
MF "v[184:187],a[108:111],v[120:123],v[184:187]\n"
DT "a[108:109],v7 offset:96\n"
DT "a[110:111],v7 offset:608\n"
CV "v118,v84,v85\n"
MF "v[188:191],a[112:115],v[120:123],v[188:191]\n"
DT "a[112:113],v7 offset:128\n"
DT "a[114:115],v7 offset:640\n"
CV "v119,v86,v87\n"
MF "v[192:195],a[116:119],v[120:123],v[192:195]\n"
DT "a[116:117],v7 offset:160\n"
DT "a[118:119],v7 offset:672\n"
EX "v94,v94\n"
MF "v[196:199],a[120:123],v[120:123],v[196:199]\n"
DT "a[120:121],v7 offset:192\n"
DT "a[122:123],v7 offset:704\n"
BL "v20,s[24:27],s67 offen offset:2048 lds\n"
EX "v95,v95\n"
MF "v[200:203],a[124:127],v[120:123],v[200:203]\n"
DT "a[124:125],v7 offset:224\n"
DT "a[126:127],v7 offset:736\n"
MF "v[204:207],a[48:51],v[120:123],v[204:207]\n"
CV "v120,v88,v89\n"
CV "v121,v90,v91\n"
CV "v122,v92,v93\n"
CV "v123,v94,v95\n"
WT "vmcnt(24)\n"
WT "lgkmcnt(0)\n"
"s_barrier\n"
MF "v[136:139],a[96:99],v[116:119],v[136:139]\n"
MF "v[140:143],a[100:103],v[116:119],v[140:143]\n"
MF "v[144:147],a[104:107],v[116:119],v[144:147]\n"
MF "v[148:151],a[108:111],v[116:119],v[148:151]\n"
MF "v[152:155],a[112:115],v[116:119],v[152:155]\n"
MF "v[156:159],a[116:119],v[116:119],v[156:159]\n"
MF "v[160:163],a[120:123],v[116:119],v[160:163]\n"
MF "v[164:167],a[124:127],v[116:119],v[164:167]\n"
MF "v[168:171],a[48:51],v[116:119],v[168:171]\n"
MF "v[172:175],a[96:99],v[120:123],v[172:175]\n"
MF "v[176:179],a[100:103],v[120:123],v[176:179]\n"
MF "v[180:183],a[104:107],v[120:123],v[180:183]\n"
MF "v[184:187],a[108:111],v[120:123],v[184:187]\n"
MF "v[188:191],a[112:115],v[120:123],v[188:191]\n"
MF "v[192:195],a[116:119],v[120:123],v[192:195]\n"
MF "v[196:199],a[120:123],v[120:123],v[196:199]\n"
MF "v[200:203],a[124:127],v[120:123],v[200:203]\n"
MF "v[204:207],a[48:51],v[120:123],v[204:207]\n"
"s_nop 1\n"
"v_rcp_f32 v130,v168\n"
"s_add_u32 s66,s56,36864\n"
PM "v[136:137],v[136:137],v[130:131] op_sel_hi:[1,0]\n"
PM "v[138:139],v[138:139],v[130:131] op_sel_hi:[1,0]\n"
PM "v[140:141],v[140:141],v[130:131] op_sel_hi:[1,0]\n"
PM "v[142:143],v[142:143],v[130:131] op_sel_hi:[1,0]\n"
CV "v80,v136,v137\n"
CV "v81,v138,v139\n"
CV "v82,v140,v141\n"
CV "v83,v142,v143\n"
"buffer_store_dwordx4 v[80:83],v244,s[28:31],s66 offen offset:0 sc1\n"
PM "v[144:145],v[144:145],v[130:131] op_sel_hi:[1,0]\n"
PM "v[146:147],v[146:147],v[130:131] op_sel_hi:[1,0]\n"
PM "v[148:149],v[148:149],v[130:131] op_sel_hi:[1,0]\n"
PM "v[150:151],v[150:151],v[130:131] op_sel_hi:[1,0]\n"
CV "v84,v144,v145\n"
CV "v85,v146,v147\n"
CV "v86,v148,v149\n"
CV "v87,v150,v151\n"
"buffer_store_dwordx4 v[84:87],v244,s[28:31],s66 offen offset:1024 sc1\n"
PM "v[152:153],v[152:153],v[130:131] op_sel_hi:[1,0]\n"
PM "v[154:155],v[154:155],v[130:131] op_sel_hi:[1,0]\n"
PM "v[156:157],v[156:157],v[130:131] op_sel_hi:[1,0]\n"
PM "v[158:159],v[158:159],v[130:131] op_sel_hi:[1,0]\n"
CV "v88,v152,v153\n"
CV "v89,v154,v155\n"
CV "v90,v156,v157\n"
CV "v91,v158,v159\n"
"buffer_store_dwordx4 v[88:91],v244,s[28:31],s66 offen offset:2048 sc1\n"
PM "v[160:161],v[160:161],v[130:131] op_sel_hi:[1,0]\n"
PM "v[162:163],v[162:163],v[130:131] op_sel_hi:[1,0]\n"
PM "v[164:165],v[164:165],v[130:131] op_sel_hi:[1,0]\n"
PM "v[166:167],v[166:167],v[130:131] op_sel_hi:[1,0]\n"
CV "v92,v160,v161\n"
CV "v93,v162,v163\n"
CV "v94,v164,v165\n"
CV "v95,v166,v167\n"
"buffer_store_dwordx4 v[92:95],v244,s[28:31],s66 offen offset:3072 sc1\n"
"v_rcp_f32 v130,v204\n"
"s_add_u32 s66,s56,36864\n"
PM "v[172:173],v[172:173],v[130:131] op_sel_hi:[1,0]\n"
PM "v[174:175],v[174:175],v[130:131] op_sel_hi:[1,0]\n"
PM "v[176:177],v[176:177],v[130:131] op_sel_hi:[1,0]\n"
PM "v[178:179],v[178:179],v[130:131] op_sel_hi:[1,0]\n"
CV "v96,v172,v173\n"
CV "v97,v174,v175\n"
CV "v98,v176,v177\n"
CV "v99,v178,v179\n"
"buffer_store_dwordx4 v[96:99],v245,s[28:31],s66 offen offset:0 sc1\n"
PM "v[180:181],v[180:181],v[130:131] op_sel_hi:[1,0]\n"
PM "v[182:183],v[182:183],v[130:131] op_sel_hi:[1,0]\n"
PM "v[184:185],v[184:185],v[130:131] op_sel_hi:[1,0]\n"
PM "v[186:187],v[186:187],v[130:131] op_sel_hi:[1,0]\n"
CV "v100,v180,v181\n"
CV "v101,v182,v183\n"
CV "v102,v184,v185\n"
CV "v103,v186,v187\n"
"buffer_store_dwordx4 v[100:103],v245,s[28:31],s66 offen offset:1024 sc1\n"
PM "v[188:189],v[188:189],v[130:131] op_sel_hi:[1,0]\n"
PM "v[190:191],v[190:191],v[130:131] op_sel_hi:[1,0]\n"
PM "v[192:193],v[192:193],v[130:131] op_sel_hi:[1,0]\n"
PM "v[194:195],v[194:195],v[130:131] op_sel_hi:[1,0]\n"
CV "v104,v188,v189\n"
CV "v105,v190,v191\n"
CV "v106,v192,v193\n"
CV "v107,v194,v195\n"
"buffer_store_dwordx4 v[104:107],v245,s[28:31],s66 offen offset:2048 sc1\n"
PM "v[196:197],v[196:197],v[130:131] op_sel_hi:[1,0]\n"
PM "v[198:199],v[198:199],v[130:131] op_sel_hi:[1,0]\n"
PM "v[200:201],v[200:201],v[130:131] op_sel_hi:[1,0]\n"
PM "v[202:203],v[202:203],v[130:131] op_sel_hi:[1,0]\n"
CV "v108,v196,v197\n"
CV "v109,v198,v199\n"
CV "v110,v200,v201\n"
CV "v111,v202,v203\n"
"buffer_store_dwordx4 v[108:111],v245,s[28:31],s66 offen offset:3072 sc1\n"
"v_add_f32 v128,v168,v204\n"
"v_cmp_ngt_f32 vcc,s43,v128\n"
"s_cmp_lg_u64 vcc,0\n"
"s_cselect_b32 s66,1,0\n"
"v_mov_b32 v128,s66\n"
"ds_write_b32 v251,v128 offset:16\n"
"LO15_%=:\n"
"s_lshl_b32 s61,s36,10\n"
WT "vmcnt(17)\n"
WT "lgkmcnt(0)\n"
"v_accvgpr_read_b32 v136,a128\n"
"v_accvgpr_read_b32 v137,a129\n"
"v_accvgpr_read_b32 v138,a130\n"
"v_accvgpr_read_b32 v139,a131\n"
"v_accvgpr_read_b32 v172,a164\n"
"v_accvgpr_read_b32 v173,a165\n"
"v_accvgpr_read_b32 v174,a166\n"
"v_accvgpr_read_b32 v175,a167\n"
MF "v[136:139],a[212:215],v[32:35],v[136:139]\n"
"s_add_u32 m0,s76,0\n"
DT "a[64:65],v21 offset:9216\n"
"v_accvgpr_read_b32 v208,a200\n"
"v_accvgpr_read_b32 v209,a201\n"
"v_accvgpr_read_b32 v210,a202\n"
"v_accvgpr_read_b32 v211,a203\n"
MF "v[172:175],a[212:215],v[48:51],v[172:175]\n"
"s_add_u32 s67,s71,0\n"
DT "a[66:67],v21 offset:13824\n"
"v_accvgpr_read_b32 v140,a132\n"
"v_accvgpr_read_b32 v141,a133\n"
"v_accvgpr_read_b32 v142,a134\n"
"v_accvgpr_read_b32 v143,a135\n"
MF "v[208:211],a[212:215],v[64:67],v[208:211]\n"
DT "a[68:69],v21 offset:9248\n"
BL "v12,s[24:27],s67 offen offset:0 lds\n"
"v_accvgpr_read_b32 v176,a168\n"
"v_accvgpr_read_b32 v177,a169\n"
"v_accvgpr_read_b32 v178,a170\n"
"v_accvgpr_read_b32 v179,a171\n"
MF "v[140:143],a[216:219],v[32:35],v[140:143]\n"
DT "a[70:71],v21 offset:13856\n"
"v_accvgpr_read_b32 v212,a204\n"
"v_accvgpr_read_b32 v213,a205\n"
"v_accvgpr_read_b32 v214,a206\n"
"v_accvgpr_read_b32 v215,a207\n"
MF "v[176:179],a[216:219],v[48:51],v[176:179]\n"
DT "a[72:73],v22 offset:9216\n"
"v_accvgpr_read_b32 v144,a136\n"
"v_accvgpr_read_b32 v145,a137\n"
"v_accvgpr_read_b32 v146,a138\n"
"v_accvgpr_read_b32 v147,a139\n"
MF "v[212:215],a[216:219],v[64:67],v[212:215]\n"
DT "a[74:75],v22 offset:13824\n"
"v_accvgpr_read_b32 v180,a172\n"
"v_accvgpr_read_b32 v181,a173\n"
"v_accvgpr_read_b32 v182,a174\n"
"v_accvgpr_read_b32 v183,a175\n"
MF "v[144:147],a[220:223],v[32:35],v[144:147]\n"
DT "a[76:77],v22 offset:9248\n"
BL "v13,s[24:27],s67 offen offset:1024 lds\n"
"v_accvgpr_read_b32 v148,a140\n"
"v_accvgpr_read_b32 v149,a141\n"
"v_accvgpr_read_b32 v150,a142\n"
"v_accvgpr_read_b32 v151,a143\n"
MF "v[180:183],a[220:223],v[48:51],v[180:183]\n"
DT "a[78:79],v22 offset:13856\n"
"v_accvgpr_read_b32 v184,a176\n"
"v_accvgpr_read_b32 v185,a177\n"
"v_accvgpr_read_b32 v186,a178\n"
"v_accvgpr_read_b32 v187,a179\n"
MF "v[148:151],a[224:227],v[32:35],v[148:151]\n"
DT "a[80:81],v23 offset:9216\n"
"v_accvgpr_read_b32 v152,a144\n"
"v_accvgpr_read_b32 v153,a145\n"
"v_accvgpr_read_b32 v154,a146\n"
"v_accvgpr_read_b32 v155,a147\n"
MF "v[184:187],a[224:227],v[48:51],v[184:187]\n"
DT "a[82:83],v23 offset:13824\n"
"v_accvgpr_read_b32 v188,a180\n"
"v_accvgpr_read_b32 v189,a181\n"
"v_accvgpr_read_b32 v190,a182\n"
"v_accvgpr_read_b32 v191,a183\n"
MF "v[152:155],a[228:231],v[32:35],v[152:155]\n"
DT "a[84:85],v23 offset:9248\n"
BL "v14,s[24:27],s67 offen offset:2048 lds\n"
"v_accvgpr_read_b32 v156,a148\n"
"v_accvgpr_read_b32 v157,a149\n"
"v_accvgpr_read_b32 v158,a150\n"
"v_accvgpr_read_b32 v159,a151\n"
MF "v[188:191],a[228:231],v[48:51],v[188:191]\n"
DT "a[86:87],v23 offset:13856\n"
"v_accvgpr_read_b32 v192,a184\n"
"v_accvgpr_read_b32 v193,a185\n"
"v_accvgpr_read_b32 v194,a186\n"
"v_accvgpr_read_b32 v195,a187\n"
MF "v[156:159],a[232:235],v[32:35],v[156:159]\n"
WT "lgkmcnt(12)\n"
"s_add_u32 m0,s76,3072\n"
DT "a[88:89],v24 offset:9216\n"
"v_accvgpr_read_b32 v160,a152\n"
"v_accvgpr_read_b32 v161,a153\n"
"v_accvgpr_read_b32 v162,a154\n"
"v_accvgpr_read_b32 v163,a155\n"
MF "v[192:195],a[232:235],v[48:51],v[192:195]\n"
"s_add_u32 s67,s71,0\n"
DT "a[90:91],v24 offset:13824\n"
"v_accvgpr_read_b32 v196,a188\n"
"v_accvgpr_read_b32 v197,a189\n"
"v_accvgpr_read_b32 v198,a190\n"
"v_accvgpr_read_b32 v199,a191\n"
MF "v[160:163],a[236:239],v[32:35],v[160:163]\n"
DT "a[92:93],v24 offset:9248\n"
BL "v15,s[24:27],s67 offen offset:0 lds\n"
"v_accvgpr_read_b32 v164,a156\n"
"v_accvgpr_read_b32 v165,a157\n"
"v_accvgpr_read_b32 v166,a158\n"
"v_accvgpr_read_b32 v167,a159\n"
MF "v[196:199],a[236:239],v[48:51],v[196:199]\n"
DT "a[94:95],v24 offset:13856\n"
"v_accvgpr_read_b32 v200,a192\n"
"v_accvgpr_read_b32 v201,a193\n"
"v_accvgpr_read_b32 v202,a194\n"
"v_accvgpr_read_b32 v203,a195\n"
MF "v[164:167],a[240:243],v[32:35],v[164:167]\n"
DT "a[96:97],v25 offset:9216\n"
"v_accvgpr_read_b32 v168,a160\n"
"v_accvgpr_read_b32 v169,a161\n"
"v_accvgpr_read_b32 v170,a162\n"
"v_accvgpr_read_b32 v171,a163\n"
MF "v[200:203],a[240:243],v[48:51],v[200:203]\n"
DT "a[98:99],v25 offset:13824\n"
"v_accvgpr_read_b32 v204,a196\n"
"v_accvgpr_read_b32 v205,a197\n"
"v_accvgpr_read_b32 v206,a198\n"
"v_accvgpr_read_b32 v207,a199\n"
MF "v[168:171],a[244:247],v[32:35],v[168:171]\n"
BL "v16,s[24:27],s67 offen offset:1024 lds\n"
MF "v[204:207],a[244:247],v[48:51],v[204:207]\n"
WT "lgkmcnt(6)\n"
MF "v[136:139],a[64:67],v[36:39],v[136:139]\n"
DT "a[212:213],v21 offset:18432\n"
MF "v[172:175],a[64:67],v[52:55],v[172:175]\n"
DT "a[214:215],v21 offset:23040\n"
MF "v[208:211],a[64:67],v[68:71],v[208:211]\n"
DT "a[216:217],v21 offset:18464\n"
MF "v[140:143],a[68:71],v[36:39],v[140:143]\n"
DT "a[218:219],v21 offset:23072\n"
BL "v17,s[24:27],s67 offen offset:2048 lds\n"
MF "v[176:179],a[68:71],v[52:55],v[176:179]\n"
DT "a[220:221],v22 offset:18432\n"
MF "v[212:215],a[68:71],v[68:71],v[212:215]\n"
"s_add_u32 m0,s76,6144\n"
DT "a[222:223],v22 offset:23040\n"
MF "v[144:147],a[72:75],v[36:39],v[144:147]\n"
"s_add_u32 s67,s71,0\n"
DT "a[224:225],v22 offset:18464\n"
MF "v[180:183],a[72:75],v[52:55],v[180:183]\n"
DT "a[226:227],v22 offset:23072\n"
BL "v18,s[24:27],s67 offen offset:0 lds\n"
MF "v[148:151],a[76:79],v[36:39],v[148:151]\n"
DT "a[228:229],v23 offset:18432\n"
MF "v[184:187],a[76:79],v[52:55],v[184:187]\n"
DT "a[230:231],v23 offset:23040\n"
MF "v[152:155],a[80:83],v[36:39],v[152:155]\n"
DT "a[232:233],v23 offset:18464\n"
MF "v[188:191],a[80:83],v[52:55],v[188:191]\n"
DT "a[234:235],v23 offset:23072\n"
BL "v19,s[24:27],s67 offen offset:1024 lds\n"
MF "v[156:159],a[84:87],v[36:39],v[156:159]\n"
WT "lgkmcnt(12)\n"
DT "a[236:237],v24 offset:18432\n"
MF "v[192:195],a[84:87],v[52:55],v[192:195]\n"
DT "a[238:239],v24 offset:23040\n"
MF "v[160:163],a[88:91],v[36:39],v[160:163]\n"
DT "a[240:241],v24 offset:18464\n"
MF "v[196:199],a[88:91],v[52:55],v[196:199]\n"
DT "a[242:243],v24 offset:23072\n"
BL "v20,s[24:27],s67 offen offset:2048 lds\n"
MF "v[164:167],a[92:95],v[36:39],v[164:167]\n"
DT "a[244:245],v25 offset:18432\n"
MF "v[200:203],a[92:95],v[52:55],v[200:203]\n"
DT "a[246:247],v25 offset:23040\n"
MF "v[168:171],a[96:99],v[36:39],v[168:171]\n"
"s_add_u32 m0,s61,73728\n"
MF "v[204:207],a[96:99],v[52:55],v[204:207]\n"
"s_add_u32 s45,s59,0\n"
WT "lgkmcnt(6)\n"
MF "v[136:139],a[212:215],v[40:43],v[136:139]\n"
DT "a[64:65],v21 offset:27648\n"
BL "v249,s[28:31],s45 offen sc1 lds\n"
MF "v[172:175],a[212:215],v[56:59],v[172:175]\n"
DT "a[66:67],v21 offset:32256\n"
MF "v[208:211],a[212:215],v[72:75],v[208:211]\n"
DT "a[68:69],v21 offset:27680\n"
MF "v[140:143],a[216:219],v[40:43],v[140:143]\n"
"s_add_u32 s44,s59,0\n"
DT "a[70:71],v21 offset:32288\n"
"v_add_u32 v21,36864,v21\n"
MF "v[176:179],a[216:219],v[56:59],v[176:179]\n"
DT "a[72:73],v22 offset:27648\n"
BL "a[0:3],v244,s[28:31],s44 offen offset:0 sc1\n"
MF "v[212:215],a[216:219],v[72:75],v[212:215]\n"
DT "a[74:75],v22 offset:32256\n"
MF "v[144:147],a[220:223],v[40:43],v[144:147]\n"
DT "a[76:77],v22 offset:27680\n"
MF "v[180:183],a[220:223],v[56:59],v[180:183]\n"
DT "a[78:79],v22 offset:32288\n"
"v_add_u32 v22,36864,v22\n"
MF "v[148:151],a[224:227],v[40:43],v[148:151]\n"
DT "a[80:81],v23 offset:27648\n"
BL "a[16:19],v245,s[28:31],s44 offen offset:0 sc1\n"
MF "v[184:187],a[224:227],v[56:59],v[184:187]\n"
DT "a[82:83],v23 offset:32256\n"
MF "v[152:155],a[228:231],v[40:43],v[152:155]\n"
DT "a[84:85],v23 offset:27680\n"
MF "v[188:191],a[228:231],v[56:59],v[188:191]\n"
DT "a[86:87],v23 offset:32288\n"
"v_add_u32 v23,36864,v23\n"
MF "v[156:159],a[232:235],v[40:43],v[156:159]\n"
WT "lgkmcnt(12)\n"
DT "a[88:89],v24 offset:27648\n"
BL "a[4:7],v244,s[28:31],s44 offen offset:1024 sc1\n"
MF "v[192:195],a[232:235],v[56:59],v[192:195]\n"
DT "a[90:91],v24 offset:32256\n"
MF "v[160:163],a[236:239],v[40:43],v[160:163]\n"
DT "a[92:93],v24 offset:27680\n"
MF "v[196:199],a[236:239],v[56:59],v[196:199]\n"
DT "a[94:95],v24 offset:32288\n"
"v_add_u32 v24,36864,v24\n"
MF "v[164:167],a[240:243],v[40:43],v[164:167]\n"
DT "a[96:97],v25 offset:27648\n"
BL "a[20:23],v245,s[28:31],s44 offen offset:1024 sc1\n"
MF "v[200:203],a[240:243],v[56:59],v[200:203]\n"
DT "a[98:99],v25 offset:32256\n"
"v_add_u32 v25,36864,v25\n"
MF "v[168:171],a[244:247],v[40:43],v[168:171]\n"
WT "vmcnt(22)\n"
WT "vmcnt(14)\n"
MF "v[204:207],a[244:247],v[56:59],v[204:207]\n"
WT "lgkmcnt(6)\n"
"s_barrier\n"
MF "v[136:139],a[64:67],v[44:47],v[136:139]\n"
DR "v[128:131],v252 offset:16\n"
DR "v[112:115],v247 offset:0\n"
MF "v[172:175],a[64:67],v[60:63],v[172:175]\n"
DT "a[212:213],v21 offset:0\n"
BL "a[8:11],v244,s[28:31],s44 offen offset:2048 sc1\n"
MF "v[208:211],a[64:67],v[76:79],v[208:211]\n"
DT "a[214:215],v21 offset:4608\n"
MF "v[140:143],a[68:71],v[44:47],v[140:143]\n"
DT "a[216:217],v21 offset:32\n"
MF "v[176:179],a[68:71],v[60:63],v[176:179]\n"
DT "a[218:219],v21 offset:4640\n"
MF "v[212:215],a[68:71],v[76:79],v[212:215]\n"
DT "a[220:221],v22 offset:0\n"
BL "a[24:27],v245,s[28:31],s44 offen offset:2048 sc1\n"
MF "v[144:147],a[72:75],v[44:47],v[144:147]\n"
DT "a[222:223],v22 offset:4608\n"
MF "v[180:183],a[72:75],v[60:63],v[180:183]\n"
DT "a[224:225],v22 offset:32\n"
MF "v[148:151],a[76:79],v[44:47],v[148:151]\n"
DT "a[226:227],v22 offset:4640\n"
MF "v[184:187],a[76:79],v[60:63],v[184:187]\n"
DT "a[228:229],v23 offset:0\n"
BL "a[12:15],v244,s[28:31],s44 offen offset:3072 sc1\n"
MF "v[152:155],a[80:83],v[44:47],v[152:155]\n"
DT "a[230:231],v23 offset:4608\n"
MF "v[188:191],a[80:83],v[60:63],v[188:191]\n"
DT "a[232:233],v23 offset:32\n"
MF "v[156:159],a[84:87],v[44:47],v[156:159]\n"
WT "lgkmcnt(12)\n"
DT "a[234:235],v23 offset:4640\n"
MF "v[192:195],a[84:87],v[60:63],v[192:195]\n"
DT "a[236:237],v24 offset:0\n"
BL "a[28:31],v245,s[28:31],s44 offen offset:3072 sc1\n"
MF "v[160:163],a[88:91],v[44:47],v[160:163]\n"
DT "a[238:239],v24 offset:4608\n"
"v_or3_b32 v128,v128,v129,v130\n"
MF "v[196:199],a[88:91],v[60:63],v[196:199]\n"
DT "a[240:241],v24 offset:32\n"
"v_or_b32 v128,v128,v131\n"
MF "v[164:167],a[92:95],v[44:47],v[164:167]\n"
DT "a[242:243],v24 offset:4640\n"
MF "v[200:203],a[92:95],v[60:63],v[200:203]\n"
DT "a[244:245],v25 offset:0\n"
"v_cmp_ne_u32 vcc,0,v128\n"
MF "v[168:171],a[96:99],v[44:47],v[168:171]\n"
DT "a[246:247],v25 offset:4608\n"
"s_cbranch_vccnz Lr224_%=\n"
MF "v[204:207],a[96:99],v[60:63],v[204:207]\n"
WT "lgkmcnt(6)\n"
MF "v[136:139],a[212:215],v[80:83],v[136:139]\n"
"s_add_u32 m0,s76,36864\n"
DR "v[116:119],v247 offset:1024\n"
MF "v[172:175],a[212:215],v[96:99],v[172:175]\n"
"s_add_u32 s67,s69,147456\n"
DT "a[64:65],v21 offset:9216\n"
MF "v[208:211],a[212:215],v[112:115],v[208:211]\n"
DT "a[66:67],v21 offset:13824\n"
BL "v12,s[24:27],s67 offen offset:0 lds\n"
MF "v[140:143],a[216:219],v[80:83],v[140:143]\n"
DT "a[68:69],v21 offset:9248\n"
MF "v[176:179],a[216:219],v[96:99],v[176:179]\n"
DT "a[70:71],v21 offset:13856\n"
MF "v[212:215],a[216:219],v[112:115],v[212:215]\n"
DT "a[72:73],v22 offset:9216\n"
MF "v[144:147],a[220:223],v[80:83],v[144:147]\n"
DT "a[74:75],v22 offset:13824\n"
MF "v[180:183],a[220:223],v[96:99],v[180:183]\n"
DT "a[76:77],v22 offset:9248\n"
MF "v[148:151],a[224:227],v[80:83],v[148:151]\n"
DT "a[78:79],v22 offset:13856\n"
MF "v[184:187],a[224:227],v[96:99],v[184:187]\n"
DT "a[80:81],v23 offset:9216\n"
MF "v[152:155],a[228:231],v[80:83],v[152:155]\n"
DT "a[82:83],v23 offset:13824\n"
BL "v13,s[24:27],s67 offen offset:1024 lds\n"
MF "v[188:191],a[228:231],v[96:99],v[188:191]\n"
DT "a[84:85],v23 offset:9248\n"
MF "v[156:159],a[232:235],v[80:83],v[156:159]\n"
WT "lgkmcnt(12)\n"
DT "a[86:87],v23 offset:13856\n"
MF "v[192:195],a[232:235],v[96:99],v[192:195]\n"
DT "a[88:89],v24 offset:9216\n"
MF "v[160:163],a[236:239],v[80:83],v[160:163]\n"
DT "a[90:91],v24 offset:13824\n"
MF "v[196:199],a[236:239],v[96:99],v[196:199]\n"
DT "a[92:93],v24 offset:9248\n"
MF "v[164:167],a[240:243],v[80:83],v[164:167]\n"
DT "a[94:95],v24 offset:13856\n"
MF "v[200:203],a[240:243],v[96:99],v[200:203]\n"
DT "a[96:97],v25 offset:9216\n"
MF "v[168:171],a[244:247],v[80:83],v[168:171]\n"
DT "a[98:99],v25 offset:13824\n"
BL "v14,s[24:27],s67 offen offset:2048 lds\n"
MF "v[204:207],a[244:247],v[96:99],v[204:207]\n"
WT "lgkmcnt(6)\n"
MF "v[136:139],a[64:67],v[84:87],v[136:139]\n"
DR "v[120:123],v247 offset:2048\n"
MF "v[172:175],a[64:67],v[100:103],v[172:175]\n"
DT "a[212:213],v21 offset:18432\n"
MF "v[208:211],a[64:67],v[116:119],v[208:211]\n"
DT "a[214:215],v21 offset:23040\n"
MF "v[140:143],a[68:71],v[84:87],v[140:143]\n"
DT "a[216:217],v21 offset:18464\n"
MF "v[176:179],a[68:71],v[100:103],v[176:179]\n"
DT "a[218:219],v21 offset:23072\n"
MF "v[212:215],a[68:71],v[116:119],v[212:215]\n"
"s_add_u32 m0,s76,39936\n"
DT "a[220:221],v22 offset:18432\n"
MF "v[144:147],a[72:75],v[84:87],v[144:147]\n"
"s_add_u32 s67,s69,147456\n"
DT "a[222:223],v22 offset:23040\n"
MF "v[180:183],a[72:75],v[100:103],v[180:183]\n"
DT "a[224:225],v22 offset:18464\n"
BL "v15,s[24:27],s67 offen offset:0 lds\n"
MF "v[148:151],a[76:79],v[84:87],v[148:151]\n"
DT "a[226:227],v22 offset:23072\n"
MF "v[184:187],a[76:79],v[100:103],v[184:187]\n"
DT "a[228:229],v23 offset:18432\n"
MF "v[152:155],a[80:83],v[84:87],v[152:155]\n"
DT "a[230:231],v23 offset:23040\n"
MF "v[188:191],a[80:83],v[100:103],v[188:191]\n"
DT "a[232:233],v23 offset:18464\n"
MF "v[156:159],a[84:87],v[84:87],v[156:159]\n"
WT "lgkmcnt(12)\n"
DT "a[234:235],v23 offset:23072\n"
MF "v[192:195],a[84:87],v[100:103],v[192:195]\n"
DT "a[236:237],v24 offset:18432\n"
MF "v[160:163],a[88:91],v[84:87],v[160:163]\n"
DT "a[238:239],v24 offset:23040\n"
MF "v[196:199],a[88:91],v[100:103],v[196:199]\n"
DT "a[240:241],v24 offset:18464\n"
BL "v16,s[24:27],s67 offen offset:1024 lds\n"
MF "v[164:167],a[92:95],v[84:87],v[164:167]\n"
DT "a[242:243],v24 offset:23072\n"
MF "v[200:203],a[92:95],v[100:103],v[200:203]\n"
DT "a[244:245],v25 offset:18432\n"
MF "v[168:171],a[96:99],v[84:87],v[168:171]\n"
DT "a[246:247],v25 offset:23040\n"
MF "v[204:207],a[96:99],v[100:103],v[204:207]\n"
WT "lgkmcnt(6)\n"
MF "v[136:139],a[212:215],v[88:91],v[136:139]\n"
DR "v[124:127],v247 offset:3072\n"
MF "v[172:175],a[212:215],v[104:107],v[172:175]\n"
DT "a[64:65],v21 offset:27648\n"
MF "v[208:211],a[212:215],v[120:123],v[208:211]\n"
DT "a[66:67],v21 offset:32256\n"
MF "v[140:143],a[216:219],v[88:91],v[140:143]\n"
DT "a[68:69],v21 offset:27680\n"
MF "v[176:179],a[216:219],v[104:107],v[176:179]\n"
DT "a[70:71],v21 offset:32288\n"
BL "v17,s[24:27],s67 offen offset:2048 lds\n"
"v_subrev_u32 v21,121344,v21\n"
MF "v[212:215],a[216:219],v[120:123],v[212:215]\n"
DT "a[72:73],v22 offset:27648\n"
MF "v[144:147],a[220:223],v[88:91],v[144:147]\n"
DT "a[74:75],v22 offset:32256\n"
MF "v[180:183],a[220:223],v[104:107],v[180:183]\n"
DT "a[76:77],v22 offset:27680\n"
MF "v[148:151],a[224:227],v[88:91],v[148:151]\n"
DT "a[78:79],v22 offset:32288\n"
"v_subrev_u32 v22,121344,v22\n"
MF "v[184:187],a[224:227],v[104:107],v[184:187]\n"
DT "a[80:81],v23 offset:27648\n"
MF "v[152:155],a[228:231],v[88:91],v[152:155]\n"
"s_add_u32 m0,s76,43008\n"
DT "a[82:83],v23 offset:32256\n"
MF "v[188:191],a[228:231],v[104:107],v[188:191]\n"
"s_add_u32 s67,s69,147456\n"
DT "a[84:85],v23 offset:27680\n"
MF "v[156:159],a[232:235],v[88:91],v[156:159]\n"
WT "lgkmcnt(12)\n"
DT "a[86:87],v23 offset:32288\n"
BL "v18,s[24:27],s67 offen offset:0 lds\n"
"v_subrev_u32 v23,121344,v23\n"
MF "v[192:195],a[232:235],v[104:107],v[192:195]\n"
DT "a[88:89],v24 offset:27648\n"
MF "v[160:163],a[236:239],v[88:91],v[160:163]\n"
DT "a[90:91],v24 offset:32256\n"
MF "v[196:199],a[236:239],v[104:107],v[196:199]\n"
DT "a[92:93],v24 offset:27680\n"
MF "v[164:167],a[240:243],v[88:91],v[164:167]\n"
DT "a[94:95],v24 offset:32288\n"
"v_subrev_u32 v24,121344,v24\n"
MF "v[200:203],a[240:243],v[104:107],v[200:203]\n"
DT "a[96:97],v25 offset:27648\n"
MF "v[168:171],a[244:247],v[88:91],v[168:171]\n"
WT "vmcnt(15)\n"
DT "a[98:99],v25 offset:32256\n"
"v_subrev_u32 v25,121344,v25\n"
MF "v[204:207],a[244:247],v[104:107],v[204:207]\n"
WT "lgkmcnt(6)\n"
"s_barrier\n"
MF "v[136:139],a[64:67],v[92:95],v[136:139]\n"
DR "v[64:67],v248 offset:0\n"
MF "v[172:175],a[64:67],v[108:111],v[172:175]\n"
DT "a[212:213],v21 offset:0\n"
BL "v19,s[24:27],s67 offen offset:1024 lds\n"
"buffer_load_dword v31,v26,s[28:31],0 offen offset:128 sc1\n"
MF "v[208:211],a[64:67],v[124:127],v[208:211]\n"
DT "a[214:215],v21 offset:4608\n"
MF "v[140:143],a[68:71],v[92:95],v[140:143]\n"
DT "a[216:217],v21 offset:32\n"
MF "v[176:179],a[68:71],v[108:111],v[176:179]\n"
DT "a[218:219],v21 offset:4640\n"
MF "v[212:215],a[68:71],v[124:127],v[212:215]\n"
DT "a[220:221],v22 offset:0\n"
MF "v[144:147],a[72:75],v[92:95],v[144:147]\n"
DT "a[222:223],v22 offset:4608\n"
MF "v[180:183],a[72:75],v[108:111],v[180:183]\n"
DT "a[224:225],v22 offset:32\n"
MF "v[148:151],a[76:79],v[92:95],v[148:151]\n"
DT "a[226:227],v22 offset:4640\n"
MF "v[184:187],a[76:79],v[108:111],v[184:187]\n"
DT "a[228:229],v23 offset:0\n"
BL "v20,s[24:27],s67 offen offset:2048 lds\n"
MF "v[152:155],a[80:83],v[92:95],v[152:155]\n"
DT "a[230:231],v23 offset:4608\n"
MF "v[188:191],a[80:83],v[108:111],v[188:191]\n"
DT "a[232:233],v23 offset:32\n"
MF "v[156:159],a[84:87],v[92:95],v[156:159]\n"
WT "lgkmcnt(12)\n"
DT "a[234:235],v23 offset:4640\n"
MF "v[192:195],a[84:87],v[108:111],v[192:195]\n"
DT "a[236:237],v24 offset:0\n"
MF "v[160:163],a[88:91],v[92:95],v[160:163]\n"
DT "a[238:239],v24 offset:4608\n"
MF "v[196:199],a[88:91],v[108:111],v[196:199]\n"
DT "a[240:241],v24 offset:32\n"
MF "v[164:167],a[92:95],v[92:95],v[164:167]\n"
DT "a[242:243],v24 offset:4640\n"
MF "v[200:203],a[92:95],v[108:111],v[200:203]\n"
DT "a[244:245],v25 offset:0\n"
MF "v[168:171],a[96:99],v[92:95],v[168:171]\n"
WT "vmcnt(10)\n"
DT "a[246:247],v25 offset:4608\n"
MF "v[204:207],a[96:99],v[108:111],v[204:207]\n"
WT "lgkmcnt(6)\n"
WT "vmcnt(1)\n"
"v_cmp_eq_u32 vcc,v31,v29\n"
"s_andn2_b64 vcc,vcc,s[62:63]\n"
"s_cbranch_scc1 Lp225_%=\n"
"Lp226_%=:\n"
MF "v[136:139],a[212:215],a[0:3],v[136:139]\n"
"s_add_u32 m0,s61,77824\n"
DR "v[68:71],v248 offset:1024\n"
MF "v[172:175],a[212:215],a[16:19],v[172:175]\n"
"s_add_u32 s45,s57,36864\n"
DT "a[64:65],v21 offset:9216\n"
MF "v[208:211],a[212:215],v[64:67],v[208:211]\n"
DT "a[66:67],v21 offset:13824\n"
BL "v249,s[28:31],s45 offen sc1 lds\n"
MF "v[140:143],a[216:219],a[0:3],v[140:143]\n"
DT "a[68:69],v21 offset:9248\n"
MF "v[176:179],a[216:219],a[16:19],v[176:179]\n"
DT "a[70:71],v21 offset:13856\n"
MF "v[212:215],a[216:219],v[64:67],v[212:215]\n"
"s_add_u32 s44,s57,36864\n"
DT "a[72:73],v22 offset:9216\n"
MF "v[144:147],a[220:223],a[0:3],v[144:147]\n"
DT "a[74:75],v22 offset:13824\n"
BL "a[32:35],v244,s[28:31],s44 offen offset:0 sc1\n"
MF "v[180:183],a[220:223],a[16:19],v[180:183]\n"
DT "a[76:77],v22 offset:9248\n"
MF "v[148:151],a[224:227],a[0:3],v[148:151]\n"
DT "a[78:79],v22 offset:13856\n"
MF "v[184:187],a[224:227],a[16:19],v[184:187]\n"
DT "a[80:81],v23 offset:9216\n"
MF "v[152:155],a[228:231],a[0:3],v[152:155]\n"
DT "a[82:83],v23 offset:13824\n"
BL "a[48:51],v245,s[28:31],s44 offen offset:0 sc1\n"
MF "v[188:191],a[228:231],a[16:19],v[188:191]\n"
DT "a[84:85],v23 offset:9248\n"
MF "v[156:159],a[232:235],a[0:3],v[156:159]\n"
WT "lgkmcnt(12)\n"
DT "a[86:87],v23 offset:13856\n"
MF "v[192:195],a[232:235],a[16:19],v[192:195]\n"
DT "a[88:89],v24 offset:9216\n"
MF "v[160:163],a[236:239],a[0:3],v[160:163]\n"
DT "a[90:91],v24 offset:13824\n"
BL "a[36:39],v244,s[28:31],s44 offen offset:1024 sc1\n"
MF "v[196:199],a[236:239],a[16:19],v[196:199]\n"
DT "a[92:93],v24 offset:9248\n"
MF "v[164:167],a[240:243],a[0:3],v[164:167]\n"
DT "a[94:95],v24 offset:13856\n"
MF "v[200:203],a[240:243],a[16:19],v[200:203]\n"
DT "a[96:97],v25 offset:9216\n"
MF "v[168:171],a[244:247],a[0:3],v[168:171]\n"
DT "a[98:99],v25 offset:13824\n"
BL "a[52:55],v245,s[28:31],s44 offen offset:1024 sc1\n"
MF "v[204:207],a[244:247],a[16:19],v[204:207]\n"
WT "lgkmcnt(6)\n"
MF "v[136:139],a[64:67],a[4:7],v[136:139]\n"
DR "v[72:75],v248 offset:2048\n"
MF "v[172:175],a[64:67],a[20:23],v[172:175]\n"
DT "a[212:213],v21 offset:18432\n"
MF "v[208:211],a[64:67],v[68:71],v[208:211]\n"
DT "a[214:215],v21 offset:23040\n"
MF "v[140:143],a[68:71],a[4:7],v[140:143]\n"
DT "a[216:217],v21 offset:18464\n"
BL "a[40:43],v244,s[28:31],s44 offen offset:2048 sc1\n"
MF "v[176:179],a[68:71],a[20:23],v[176:179]\n"
DT "a[218:219],v21 offset:23072\n"
MF "v[212:215],a[68:71],v[68:71],v[212:215]\n"
DT "a[220:221],v22 offset:18432\n"
MF "v[144:147],a[72:75],a[4:7],v[144:147]\n"
DT "a[222:223],v22 offset:23040\n"
MF "v[180:183],a[72:75],a[20:23],v[180:183]\n"
DT "a[224:225],v22 offset:18464\n"
BL "a[56:59],v245,s[28:31],s44 offen offset:2048 sc1\n"
MF "v[148:151],a[76:79],a[4:7],v[148:151]\n"
DT "a[226:227],v22 offset:23072\n"
MF "v[184:187],a[76:79],a[20:23],v[184:187]\n"
DT "a[228:229],v23 offset:18432\n"
MF "v[152:155],a[80:83],a[4:7],v[152:155]\n"
DT "a[230:231],v23 offset:23040\n"
MF "v[188:191],a[80:83],a[20:23],v[188:191]\n"
DT "a[232:233],v23 offset:18464\n"
BL "a[44:47],v244,s[28:31],s44 offen offset:3072 sc1\n"
MF "v[156:159],a[84:87],a[4:7],v[156:159]\n"
WT "lgkmcnt(12)\n"
DT "a[234:235],v23 offset:23072\n"
MF "v[192:195],a[84:87],a[20:23],v[192:195]\n"
DT "a[236:237],v24 offset:18432\n"
MF "v[160:163],a[88:91],a[4:7],v[160:163]\n"
DT "a[238:239],v24 offset:23040\n"
MF "v[196:199],a[88:91],a[20:23],v[196:199]\n"
DT "a[240:241],v24 offset:18464\n"
BL "a[60:63],v245,s[28:31],s44 offen offset:3072 sc1\n"
MF "v[164:167],a[92:95],a[4:7],v[164:167]\n"
DT "a[242:243],v24 offset:23072\n"
MF "v[200:203],a[92:95],a[20:23],v[200:203]\n"
DT "a[244:245],v25 offset:18432\n"
MF "v[168:171],a[96:99],a[4:7],v[168:171]\n"
"s_add_u32 m0,s76,84480\n"
DT "a[246:247],v25 offset:23040\n"
MF "v[204:207],a[96:99],a[20:23],v[204:207]\n"
"s_add_u32 s67,s70,147456\n"
WT "lgkmcnt(6)\n"
MF "v[136:139],a[212:215],a[8:11],v[136:139]\n"
DR "v[76:79],v248 offset:3072\n"
BL "v12,s[24:27],s67 offen offset:0 lds\n"
MF "v[172:175],a[212:215],a[24:27],v[172:175]\n"
DT "a[64:65],v21 offset:27648\n"
MF "v[208:211],a[212:215],v[72:75],v[208:211]\n"
DT "a[66:67],v21 offset:32256\n"
MF "v[140:143],a[216:219],a[8:11],v[140:143]\n"
DT "a[68:69],v21 offset:27680\n"
MF "v[176:179],a[216:219],a[24:27],v[176:179]\n"
DT "a[70:71],v21 offset:32288\n"
BL "v13,s[24:27],s67 offen offset:1024 lds\n"
"v_add_u32 v21,36864,v21\n"
MF "v[212:215],a[216:219],v[72:75],v[212:215]\n"
DT "a[72:73],v22 offset:27648\n"
MF "v[144:147],a[220:223],a[8:11],v[144:147]\n"
DT "a[74:75],v22 offset:32256\n"
MF "v[180:183],a[220:223],a[24:27],v[180:183]\n"
DT "a[76:77],v22 offset:27680\n"
MF "v[148:151],a[224:227],a[8:11],v[148:151]\n"
DT "a[78:79],v22 offset:32288\n"
BL "v14,s[24:27],s67 offen offset:2048 lds\n"
"v_add_u32 v22,36864,v22\n"
MF "v[184:187],a[224:227],a[24:27],v[184:187]\n"
DT "a[80:81],v23 offset:27648\n"
MF "v[152:155],a[228:231],a[8:11],v[152:155]\n"
"s_add_u32 m0,s76,87552\n"
DT "a[82:83],v23 offset:32256\n"
MF "v[188:191],a[228:231],a[24:27],v[188:191]\n"
"s_add_u32 s67,s70,147456\n"
DT "a[84:85],v23 offset:27680\n"
MF "v[156:159],a[232:235],a[8:11],v[156:159]\n"
WT "lgkmcnt(12)\n"
DT "a[86:87],v23 offset:32288\n"
BL "v15,s[24:27],s67 offen offset:0 lds\n"
"v_add_u32 v23,36864,v23\n"
MF "v[192:195],a[232:235],a[24:27],v[192:195]\n"
DT "a[88:89],v24 offset:27648\n"
MF "v[160:163],a[236:239],a[8:11],v[160:163]\n"
DT "a[90:91],v24 offset:32256\n"
MF "v[196:199],a[236:239],a[24:27],v[196:199]\n"
DT "a[92:93],v24 offset:27680\n"
MF "v[164:167],a[240:243],a[8:11],v[164:167]\n"
DT "a[94:95],v24 offset:32288\n"
BL "v16,s[24:27],s67 offen offset:1024 lds\n"
"v_add_u32 v24,36864,v24\n"
MF "v[200:203],a[240:243],a[24:27],v[200:203]\n"
DT "a[96:97],v25 offset:27648\n"
MF "v[168:171],a[244:247],a[8:11],v[168:171]\n"
WT "vmcnt(13)\n"
DT "a[98:99],v25 offset:32256\n"
"v_add_u32 v25,36864,v25\n"
MF "v[204:207],a[244:247],a[24:27],v[204:207]\n"
WT "lgkmcnt(6)\n"
"s_barrier\n"
MF "v[136:139],a[64:67],a[12:15],v[136:139]\n"
DR "v[112:115],v248 offset:4096\n"
MF "v[172:175],a[64:67],a[28:31],v[172:175]\n"
DT "a[212:213],v21 offset:0\n"
BL "v17,s[24:27],s67 offen offset:2048 lds\n"
MF "v[208:211],a[64:67],v[76:79],v[208:211]\n"
DT "a[214:215],v21 offset:4608\n"
MF "v[140:143],a[68:71],a[12:15],v[140:143]\n"
"s_add_u32 m0,s76,90624\n"
DT "a[216:217],v21 offset:32\n"
MF "v[176:179],a[68:71],a[28:31],v[176:179]\n"
"s_add_u32 s67,s70,147456\n"
DT "a[218:219],v21 offset:4640\n"
MF "v[212:215],a[68:71],v[76:79],v[212:215]\n"
DT "a[220:221],v22 offset:0\n"
BL "v18,s[24:27],s67 offen offset:0 lds\n"
MF "v[144:147],a[72:75],a[12:15],v[144:147]\n"
DT "a[222:223],v22 offset:4608\n"
MF "v[180:183],a[72:75],a[28:31],v[180:183]\n"
DT "a[224:225],v22 offset:32\n"
MF "v[148:151],a[76:79],a[12:15],v[148:151]\n"
DT "a[226:227],v22 offset:4640\n"
MF "v[184:187],a[76:79],a[28:31],v[184:187]\n"
DT "a[228:229],v23 offset:0\n"
BL "v19,s[24:27],s67 offen offset:1024 lds\n"
MF "v[152:155],a[80:83],a[12:15],v[152:155]\n"
DT "a[230:231],v23 offset:4608\n"
MF "v[188:191],a[80:83],a[28:31],v[188:191]\n"
DT "a[232:233],v23 offset:32\n"
MF "v[156:159],a[84:87],a[12:15],v[156:159]\n"
WT "lgkmcnt(12)\n"
DT "a[234:235],v23 offset:4640\n"
MF "v[192:195],a[84:87],a[28:31],v[192:195]\n"
DT "a[236:237],v24 offset:0\n"
BL "v20,s[24:27],s67 offen offset:2048 lds\n"
MF "v[160:163],a[88:91],a[12:15],v[160:163]\n"
DT "a[238:239],v24 offset:4608\n"
MF "v[196:199],a[88:91],a[28:31],v[196:199]\n"
DT "a[240:241],v24 offset:32\n"
MF "v[164:167],a[92:95],a[12:15],v[164:167]\n"
DT "a[242:243],v24 offset:4640\n"
MF "v[200:203],a[92:95],a[28:31],v[200:203]\n"
DT "a[244:245],v25 offset:0\n"
MF "v[168:171],a[96:99],a[12:15],v[168:171]\n"
WT "vmcnt(9)\n"
DT "a[246:247],v25 offset:4608\n"
MF "v[204:207],a[96:99],a[28:31],v[204:207]\n"
WT "lgkmcnt(6)\n"
MF "v[136:139],a[212:215],a[32:35],v[136:139]\n"
"s_add_u32 m0,s61,158208\n"
DR "v[116:119],v248 offset:5120\n"
MF "v[172:175],a[212:215],a[48:51],v[172:175]\n"
"s_add_u32 s45,s58,36864\n"
DT "a[64:65],v21 offset:9216\n"
MF "v[208:211],a[212:215],v[112:115],v[208:211]\n"
DT "a[66:67],v21 offset:13824\n"
BL "v249,s[28:31],s45 offen sc1 lds\n"
MF "v[140:143],a[216:219],a[32:35],v[140:143]\n"
DT "a[68:69],v21 offset:9248\n"
MF "v[176:179],a[216:219],a[48:51],v[176:179]\n"
DT "a[70:71],v21 offset:13856\n"
MF "v[212:215],a[216:219],v[112:115],v[212:215]\n"
"s_add_u32 s44,s58,36864\n"
DT "a[72:73],v22 offset:9216\n"
MF "v[144:147],a[220:223],a[32:35],v[144:147]\n"
DT "a[74:75],v22 offset:13824\n"
BL "a[128:131],v244,s[28:31],s44 offen offset:0 sc1\n"
MF "v[180:183],a[220:223],a[48:51],v[180:183]\n"
DT "a[76:77],v22 offset:9248\n"
MF "v[148:151],a[224:227],a[32:35],v[148:151]\n"
DT "a[78:79],v22 offset:13856\n"
MF "v[184:187],a[224:227],a[48:51],v[184:187]\n"
DT "a[80:81],v23 offset:9216\n"
MF "v[152:155],a[228:231],a[32:35],v[152:155]\n"
DT "a[82:83],v23 offset:13824\n"
BL "a[144:147],v245,s[28:31],s44 offen offset:0 sc1\n"
MF "v[188:191],a[228:231],a[48:51],v[188:191]\n"
DT "a[84:85],v23 offset:9248\n"
MF "v[156:159],a[232:235],a[32:35],v[156:159]\n"
WT "lgkmcnt(12)\n"
DT "a[86:87],v23 offset:13856\n"
MF "v[192:195],a[232:235],a[48:51],v[192:195]\n"
DT "a[88:89],v24 offset:9216\n"
MF "v[160:163],a[236:239],a[32:35],v[160:163]\n"
DT "a[90:91],v24 offset:13824\n"
BL "a[132:135],v244,s[28:31],s44 offen offset:1024 sc1\n"
MF "v[196:199],a[236:239],a[48:51],v[196:199]\n"
DT "a[92:93],v24 offset:9248\n"
MF "v[164:167],a[240:243],a[32:35],v[164:167]\n"
DT "a[94:95],v24 offset:13856\n"
MF "v[200:203],a[240:243],a[48:51],v[200:203]\n"
DT "a[96:97],v25 offset:9216\n"
MF "v[168:171],a[244:247],a[32:35],v[168:171]\n"
DT "a[98:99],v25 offset:13824\n"
BL "a[148:151],v245,s[28:31],s44 offen offset:1024 sc1\n"
MF "v[204:207],a[244:247],a[48:51],v[204:207]\n"
WT "lgkmcnt(6)\n"
MF "v[136:139],a[64:67],a[36:39],v[136:139]\n"
DR "v[120:123],v248 offset:6144\n"
MF "v[172:175],a[64:67],a[52:55],v[172:175]\n"
DT "a[212:213],v21 offset:18432\n"
MF "v[208:211],a[64:67],v[116:119],v[208:211]\n"
DT "a[214:215],v21 offset:23040\n"
MF "v[140:143],a[68:71],a[36:39],v[140:143]\n"
DT "a[216:217],v21 offset:18464\n"
BL "a[136:139],v244,s[28:31],s44 offen offset:2048 sc1\n"
MF "v[176:179],a[68:71],a[52:55],v[176:179]\n"
DT "a[218:219],v21 offset:23072\n"
MF "v[212:215],a[68:71],v[116:119],v[212:215]\n"
DT "a[220:221],v22 offset:18432\n"
MF "v[144:147],a[72:75],a[36:39],v[144:147]\n"
DT "a[222:223],v22 offset:23040\n"
MF "v[180:183],a[72:75],a[52:55],v[180:183]\n"
DT "a[224:225],v22 offset:18464\n"
BL "a[152:155],v245,s[28:31],s44 offen offset:2048 sc1\n"
MF "v[148:151],a[76:79],a[36:39],v[148:151]\n"
DT "a[226:227],v22 offset:23072\n"
MF "v[184:187],a[76:79],a[52:55],v[184:187]\n"
DT "a[228:229],v23 offset:18432\n"
MF "v[152:155],a[80:83],a[36:39],v[152:155]\n"
DT "a[230:231],v23 offset:23040\n"
MF "v[188:191],a[80:83],a[52:55],v[188:191]\n"
DT "a[232:233],v23 offset:18464\n"
BL "a[140:143],v244,s[28:31],s44 offen offset:3072 sc1\n"
MF "v[156:159],a[84:87],a[36:39],v[156:159]\n"
WT "lgkmcnt(12)\n"
DT "a[234:235],v23 offset:23072\n"
MF "v[192:195],a[84:87],a[52:55],v[192:195]\n"
DT "a[236:237],v24 offset:18432\n"
MF "v[160:163],a[88:91],a[36:39],v[160:163]\n"
DT "a[238:239],v24 offset:23040\n"
MF "v[196:199],a[88:91],a[52:55],v[196:199]\n"
DT "a[240:241],v24 offset:18464\n"
BL "a[156:159],v245,s[28:31],s44 offen offset:3072 sc1\n"
MF "v[164:167],a[92:95],a[36:39],v[164:167]\n"
DT "a[242:243],v24 offset:23072\n"
MF "v[200:203],a[92:95],a[52:55],v[200:203]\n"
DT "a[244:245],v25 offset:18432\n"
MF "v[168:171],a[96:99],a[36:39],v[168:171]\n"
"s_add_u32 m0,s76,121344\n"
DT "a[246:247],v25 offset:23040\n"
MF "v[204:207],a[96:99],a[52:55],v[204:207]\n"
"s_add_u32 s67,s71,147456\n"
WT "lgkmcnt(6)\n"
MF "v[136:139],a[212:215],a[40:43],v[136:139]\n"
DR "v[124:127],v248 offset:7168\n"
BL "v12,s[24:27],s67 offen offset:0 lds\n"
MF "v[172:175],a[212:215],a[56:59],v[172:175]\n"
DT "a[64:65],v21 offset:27648\n"
MF "v[208:211],a[212:215],v[120:123],v[208:211]\n"
DT "a[66:67],v21 offset:32256\n"
MF "v[140:143],a[216:219],a[40:43],v[140:143]\n"
DT "a[68:69],v21 offset:27680\n"
MF "v[176:179],a[216:219],a[56:59],v[176:179]\n"
DT "a[70:71],v21 offset:32288\n"
BL "v13,s[24:27],s67 offen offset:1024 lds\n"
"v_add_u32 v21,47616,v21\n"
MF "v[212:215],a[216:219],v[120:123],v[212:215]\n"
DT "a[72:73],v22 offset:27648\n"
MF "v[144:147],a[220:223],a[40:43],v[144:147]\n"
DT "a[74:75],v22 offset:32256\n"
MF "v[180:183],a[220:223],a[56:59],v[180:183]\n"
DT "a[76:77],v22 offset:27680\n"
MF "v[148:151],a[224:227],a[40:43],v[148:151]\n"
DT "a[78:79],v22 offset:32288\n"
BL "v14,s[24:27],s67 offen offset:2048 lds\n"
"v_add_u32 v22,47616,v22\n"
MF "v[184:187],a[224:227],a[56:59],v[184:187]\n"
DT "a[80:81],v23 offset:27648\n"
MF "v[152:155],a[228:231],a[40:43],v[152:155]\n"
"s_add_u32 m0,s76,124416\n"
DT "a[82:83],v23 offset:32256\n"
MF "v[188:191],a[228:231],a[56:59],v[188:191]\n"
"s_add_u32 s67,s71,147456\n"
DT "a[84:85],v23 offset:27680\n"
MF "v[156:159],a[232:235],a[40:43],v[156:159]\n"
WT "lgkmcnt(12)\n"
DT "a[86:87],v23 offset:32288\n"
BL "v15,s[24:27],s67 offen offset:0 lds\n"
"v_add_u32 v23,47616,v23\n"
MF "v[192:195],a[232:235],a[56:59],v[192:195]\n"
DT "a[88:89],v24 offset:27648\n"
MF "v[160:163],a[236:239],a[40:43],v[160:163]\n"
DT "a[90:91],v24 offset:32256\n"
MF "v[196:199],a[236:239],a[56:59],v[196:199]\n"
DT "a[92:93],v24 offset:27680\n"
MF "v[164:167],a[240:243],a[40:43],v[164:167]\n"
DT "a[94:95],v24 offset:32288\n"
BL "v16,s[24:27],s67 offen offset:1024 lds\n"
"v_add_u32 v24,47616,v24\n"
MF "v[200:203],a[240:243],a[56:59],v[200:203]\n"
DT "a[96:97],v25 offset:27648\n"
MF "v[168:171],a[244:247],a[40:43],v[168:171]\n"
WT "vmcnt(13)\n"
DT "a[98:99],v25 offset:32256\n"
"v_add_u32 v25,47616,v25\n"
MF "v[204:207],a[244:247],a[56:59],v[204:207]\n"
WT "lgkmcnt(6)\n"
"s_barrier\n"
MF "v[136:139],a[64:67],a[44:47],v[136:139]\n"
DR "v[64:67],v247 offset:0\n"
MF "v[172:175],a[64:67],a[60:63],v[172:175]\n"
DT "a[212:213],v21 offset:0\n"
BL "v17,s[24:27],s67 offen offset:2048 lds\n"
MF "v[208:211],a[64:67],v[124:127],v[208:211]\n"
DT "a[214:215],v21 offset:4608\n"
MF "v[140:143],a[68:71],a[44:47],v[140:143]\n"
"s_add_u32 m0,s76,127488\n"
DT "a[216:217],v21 offset:32\n"
MF "v[176:179],a[68:71],a[60:63],v[176:179]\n"
"s_add_u32 s67,s71,147456\n"
DT "a[218:219],v21 offset:4640\n"
MF "v[212:215],a[68:71],v[124:127],v[212:215]\n"
DT "a[220:221],v22 offset:0\n"
BL "v18,s[24:27],s67 offen offset:0 lds\n"
MF "v[144:147],a[72:75],a[44:47],v[144:147]\n"
DT "a[222:223],v22 offset:4608\n"
MF "v[180:183],a[72:75],a[60:63],v[180:183]\n"
DT "a[224:225],v22 offset:32\n"
MF "v[148:151],a[76:79],a[44:47],v[148:151]\n"
DT "a[226:227],v22 offset:4640\n"
MF "v[184:187],a[76:79],a[60:63],v[184:187]\n"
DT "a[228:229],v23 offset:0\n"
BL "v19,s[24:27],s67 offen offset:1024 lds\n"
MF "v[152:155],a[80:83],a[44:47],v[152:155]\n"
DT "a[230:231],v23 offset:4608\n"
MF "v[188:191],a[80:83],a[60:63],v[188:191]\n"
DT "a[232:233],v23 offset:32\n"
MF "v[156:159],a[84:87],a[44:47],v[156:159]\n"
WT "lgkmcnt(12)\n"
DT "a[234:235],v23 offset:4640\n"
MF "v[192:195],a[84:87],a[60:63],v[192:195]\n"
DT "a[236:237],v24 offset:0\n"
BL "v20,s[24:27],s67 offen offset:2048 lds\n"
MF "v[160:163],a[88:91],a[44:47],v[160:163]\n"
DT "a[238:239],v24 offset:4608\n"
MF "v[196:199],a[88:91],a[60:63],v[196:199]\n"
DT "a[240:241],v24 offset:32\n"
MF "v[164:167],a[92:95],a[44:47],v[164:167]\n"
DT "a[242:243],v24 offset:4640\n"
MF "v[200:203],a[92:95],a[60:63],v[200:203]\n"
DT "a[244:245],v25 offset:0\n"
MF "v[168:171],a[96:99],a[44:47],v[168:171]\n"
WT "vmcnt(9)\n"
DT "a[246:247],v25 offset:4608\n"
MF "v[204:207],a[96:99],a[60:63],v[204:207]\n"
WT "lgkmcnt(6)\n"
MF "v[136:139],a[212:215],a[128:131],v[136:139]\n"
"s_add_u32 m0,s61,73728\n"
DR "v[68:71],v247 offset:1024\n"
MF "v[172:175],a[212:215],a[144:147],v[172:175]\n"
"s_add_u32 s45,s59,36864\n"
DT "a[64:65],v21 offset:9216\n"
MF "v[208:211],a[212:215],v[64:67],v[208:211]\n"
DT "a[66:67],v21 offset:13824\n"
BL "v249,s[28:31],s45 offen sc1 lds\n"
MF "v[140:143],a[216:219],a[128:131],v[140:143]\n"
DT "a[68:69],v21 offset:9248\n"
MF "v[176:179],a[216:219],a[144:147],v[176:179]\n"
DT "a[70:71],v21 offset:13856\n"
MF "v[212:215],a[216:219],v[64:67],v[212:215]\n"
"s_add_u32 s44,s59,36864\n"
DT "a[72:73],v22 offset:9216\n"
MF "v[144:147],a[220:223],a[128:131],v[144:147]\n"
DT "a[74:75],v22 offset:13824\n"
BL "a[160:163],v244,s[28:31],s44 offen offset:0 sc1\n"
MF "v[180:183],a[220:223],a[144:147],v[180:183]\n"
DT "a[76:77],v22 offset:9248\n"
MF "v[148:151],a[224:227],a[128:131],v[148:151]\n"
DT "a[78:79],v22 offset:13856\n"
MF "v[184:187],a[224:227],a[144:147],v[184:187]\n"
DT "a[80:81],v23 offset:9216\n"
MF "v[152:155],a[228:231],a[128:131],v[152:155]\n"
DT "a[82:83],v23 offset:13824\n"
BL "a[176:179],v245,s[28:31],s44 offen offset:0 sc1\n"
MF "v[188:191],a[228:231],a[144:147],v[188:191]\n"
DT "a[84:85],v23 offset:9248\n"
MF "v[156:159],a[232:235],a[128:131],v[156:159]\n"
WT "lgkmcnt(12)\n"
DT "a[86:87],v23 offset:13856\n"
MF "v[192:195],a[232:235],a[144:147],v[192:195]\n"
DT "a[88:89],v24 offset:9216\n"
MF "v[160:163],a[236:239],a[128:131],v[160:163]\n"
DT "a[90:91],v24 offset:13824\n"
BL "a[164:167],v244,s[28:31],s44 offen offset:1024 sc1\n"
MF "v[196:199],a[236:239],a[144:147],v[196:199]\n"
DT "a[92:93],v24 offset:9248\n"
MF "v[164:167],a[240:243],a[128:131],v[164:167]\n"
DT "a[94:95],v24 offset:13856\n"
MF "v[200:203],a[240:243],a[144:147],v[200:203]\n"
DT "a[96:97],v25 offset:9216\n"
MF "v[168:171],a[244:247],a[128:131],v[168:171]\n"
DT "a[98:99],v25 offset:13824\n"
BL "a[180:183],v245,s[28:31],s44 offen offset:1024 sc1\n"
MF "v[204:207],a[244:247],a[144:147],v[204:207]\n"
WT "lgkmcnt(6)\n"
MF "v[136:139],a[64:67],a[132:135],v[136:139]\n"
DR "v[72:75],v247 offset:2048\n"
MF "v[172:175],a[64:67],a[148:151],v[172:175]\n"
DT "a[212:213],v21 offset:18432\n"
MF "v[208:211],a[64:67],v[68:71],v[208:211]\n"
DT "a[214:215],v21 offset:23040\n"
BL "a[168:171],v244,s[28:31],s44 offen offset:2048 sc1\n"
MF "v[140:143],a[68:71],a[132:135],v[140:143]\n"
DT "a[216:217],v21 offset:18464\n"
MF "v[176:179],a[68:71],a[148:151],v[176:179]\n"
DT "a[218:219],v21 offset:23072\n"
MF "v[212:215],a[68:71],v[68:71],v[212:215]\n"
DT "a[220:221],v22 offset:18432\n"
MF "v[144:147],a[72:75],a[132:135],v[144:147]\n"
DT "a[222:223],v22 offset:23040\n"
BL "a[184:187],v245,s[28:31],s44 offen offset:2048 sc1\n"
MF "v[180:183],a[72:75],a[148:151],v[180:183]\n"
DT "a[224:225],v22 offset:18464\n"
MF "v[148:151],a[76:79],a[132:135],v[148:151]\n"
DT "a[226:227],v22 offset:23072\n"
MF "v[184:187],a[76:79],a[148:151],v[184:187]\n"
DT "a[228:229],v23 offset:18432\n"
MF "v[152:155],a[80:83],a[132:135],v[152:155]\n"
DT "a[230:231],v23 offset:23040\n"
BL "a[172:175],v244,s[28:31],s44 offen offset:3072 sc1\n"
MF "v[188:191],a[80:83],a[148:151],v[188:191]\n"
DT "a[232:233],v23 offset:18464\n"
MF "v[156:159],a[84:87],a[132:135],v[156:159]\n"
WT "lgkmcnt(12)\n"
DT "a[234:235],v23 offset:23072\n"
MF "v[192:195],a[84:87],a[148:151],v[192:195]\n"
DT "a[236:237],v24 offset:18432\n"
MF "v[160:163],a[88:91],a[132:135],v[160:163]\n"
DT "a[238:239],v24 offset:23040\n"
BL "a[188:191],v245,s[28:31],s44 offen offset:3072 sc1\n"
MF "v[196:199],a[88:91],a[148:151],v[196:199]\n"
DT "a[240:241],v24 offset:18464\n"
MF "v[164:167],a[92:95],a[132:135],v[164:167]\n"
DT "a[242:243],v24 offset:23072\n"
MF "v[200:203],a[92:95],a[148:151],v[200:203]\n"
DT "a[244:245],v25 offset:18432\n"
MF "v[168:171],a[96:99],a[132:135],v[168:171]\n"
DT "a[246:247],v25 offset:23040\n"
MF "v[204:207],a[96:99],a[148:151],v[204:207]\n"
WT "lgkmcnt(6)\n"
MF "v[136:139],a[212:215],a[136:139],v[136:139]\n"
DR "v[76:79],v247 offset:3072\n"
MF "v[172:175],a[212:215],a[152:155],v[172:175]\n"
DT "a[64:65],v21 offset:27648\n"
MF "v[208:211],a[212:215],v[72:75],v[208:211]\n"
DT "a[66:67],v21 offset:32256\n"
MF "v[140:143],a[216:219],a[136:139],v[140:143]\n"
DT "a[68:69],v21 offset:27680\n"
MF "v[176:179],a[216:219],a[152:155],v[176:179]\n"
DT "a[70:71],v21 offset:32288\n"
"v_add_u32 v21,36864,v21\n"
MF "v[212:215],a[216:219],v[72:75],v[212:215]\n"
DT "a[72:73],v22 offset:27648\n"
MF "v[144:147],a[220:223],a[136:139],v[144:147]\n"
DT "a[74:75],v22 offset:32256\n"
MF "v[180:183],a[220:223],a[152:155],v[180:183]\n"
DT "a[76:77],v22 offset:27680\n"
MF "v[148:151],a[224:227],a[136:139],v[148:151]\n"
DT "a[78:79],v22 offset:32288\n"
"v_add_u32 v22,36864,v22\n"
MF "v[184:187],a[224:227],a[152:155],v[184:187]\n"
DT "a[80:81],v23 offset:27648\n"
MF "v[152:155],a[228:231],a[136:139],v[152:155]\n"
DT "a[82:83],v23 offset:32256\n"
MF "v[188:191],a[228:231],a[152:155],v[188:191]\n"
DT "a[84:85],v23 offset:27680\n"
MF "v[156:159],a[232:235],a[136:139],v[156:159]\n"
WT "lgkmcnt(12)\n"
DT "a[86:87],v23 offset:32288\n"
"v_add_u32 v23,36864,v23\n"
MF "v[192:195],a[232:235],a[152:155],v[192:195]\n"
DT "a[88:89],v24 offset:27648\n"
MF "v[160:163],a[236:239],a[136:139],v[160:163]\n"
DT "a[90:91],v24 offset:32256\n"
MF "v[196:199],a[236:239],a[152:155],v[196:199]\n"
DT "a[92:93],v24 offset:27680\n"
MF "v[164:167],a[240:243],a[136:139],v[164:167]\n"
DT "a[94:95],v24 offset:32288\n"
"v_add_u32 v24,36864,v24\n"
MF "v[200:203],a[240:243],a[152:155],v[200:203]\n"
DT "a[96:97],v25 offset:27648\n"
MF "v[168:171],a[244:247],a[136:139],v[168:171]\n"
WT "vmcnt(8)\n"
DT "a[98:99],v25 offset:32256\n"
"v_add_u32 v25,36864,v25\n"
MF "v[204:207],a[244:247],a[152:155],v[204:207]\n"
WT "lgkmcnt(6)\n"
"s_barrier\n"
MF "v[136:139],a[64:67],a[140:143],v[136:139]\n"
DR "v[112:115],v248 offset:0\n"
MF "v[172:175],a[64:67],a[156:159],v[172:175]\n"
DT "a[212:213],v21 offset:0\n"
MF "v[208:211],a[64:67],v[76:79],v[208:211]\n"
DT "a[214:215],v21 offset:4608\n"
MF "v[140:143],a[68:71],a[140:143],v[140:143]\n"
DT "a[216:217],v21 offset:32\n"
MF "v[176:179],a[68:71],a[156:159],v[176:179]\n"
DT "a[218:219],v21 offset:4640\n"
MF "v[212:215],a[68:71],v[76:79],v[212:215]\n"
DT "a[220:221],v22 offset:0\n"
MF "v[144:147],a[72:75],a[140:143],v[144:147]\n"
DT "a[222:223],v22 offset:4608\n"
MF "v[180:183],a[72:75],a[156:159],v[180:183]\n"
DT "a[224:225],v22 offset:32\n"
MF "v[148:151],a[76:79],a[140:143],v[148:151]\n"
DT "a[226:227],v22 offset:4640\n"
MF "v[184:187],a[76:79],a[156:159],v[184:187]\n"
DT "a[228:229],v23 offset:0\n"
MF "v[152:155],a[80:83],a[140:143],v[152:155]\n"
DT "a[230:231],v23 offset:4608\n"
MF "v[188:191],a[80:83],a[156:159],v[188:191]\n"
DT "a[232:233],v23 offset:32\n"
MF "v[156:159],a[84:87],a[140:143],v[156:159]\n"
WT "lgkmcnt(12)\n"
DT "a[234:235],v23 offset:4640\n"
MF "v[192:195],a[84:87],a[156:159],v[192:195]\n"
DT "a[236:237],v24 offset:0\n"
MF "v[160:163],a[88:91],a[140:143],v[160:163]\n"
DT "a[238:239],v24 offset:4608\n"
MF "v[196:199],a[88:91],a[156:159],v[196:199]\n"
DT "a[240:241],v24 offset:32\n"
MF "v[164:167],a[92:95],a[140:143],v[164:167]\n"
DT "a[242:243],v24 offset:4640\n"
MF "v[200:203],a[92:95],a[156:159],v[200:203]\n"
DT "a[244:245],v25 offset:0\n"
MF "v[168:171],a[96:99],a[140:143],v[168:171]\n"
WT "vmcnt(0)\n"
DT "a[246:247],v25 offset:4608\n"
MF "v[204:207],a[96:99],a[156:159],v[204:207]\n"
WT "lgkmcnt(6)\n"
MF "v[136:139],a[212:215],a[160:163],v[136:139]\n"
DR "v[116:119],v248 offset:1024\n"
MF "v[172:175],a[212:215],a[176:179],v[172:175]\n"
DT "a[64:65],v21 offset:9216\n"
MF "v[208:211],a[212:215],v[112:115],v[208:211]\n"
DT "a[66:67],v21 offset:13824\n"
MF "v[140:143],a[216:219],a[160:163],v[140:143]\n"
DT "a[68:69],v21 offset:9248\n"
MF "v[176:179],a[216:219],a[176:179],v[176:179]\n"
DT "a[70:71],v21 offset:13856\n"
MF "v[212:215],a[216:219],v[112:115],v[212:215]\n"
DT "a[72:73],v22 offset:9216\n"
MF "v[144:147],a[220:223],a[160:163],v[144:147]\n"
DT "a[74:75],v22 offset:13824\n"
MF "v[180:183],a[220:223],a[176:179],v[180:183]\n"
DT "a[76:77],v22 offset:9248\n"
MF "v[148:151],a[224:227],a[160:163],v[148:151]\n"
DT "a[78:79],v22 offset:13856\n"
MF "v[184:187],a[224:227],a[176:179],v[184:187]\n"
DT "a[80:81],v23 offset:9216\n"
MF "v[152:155],a[228:231],a[160:163],v[152:155]\n"
DT "a[82:83],v23 offset:13824\n"
MF "v[188:191],a[228:231],a[176:179],v[188:191]\n"
DT "a[84:85],v23 offset:9248\n"
MF "v[156:159],a[232:235],a[160:163],v[156:159]\n"
WT "lgkmcnt(12)\n"
DT "a[86:87],v23 offset:13856\n"
MF "v[192:195],a[232:235],a[176:179],v[192:195]\n"
DT "a[88:89],v24 offset:9216\n"
MF "v[160:163],a[236:239],a[160:163],v[160:163]\n"
DT "a[90:91],v24 offset:13824\n"
MF "v[196:199],a[236:239],a[176:179],v[196:199]\n"
DT "a[92:93],v24 offset:9248\n"
MF "v[164:167],a[240:243],a[160:163],v[164:167]\n"
DT "a[94:95],v24 offset:13856\n"
MF "v[200:203],a[240:243],a[176:179],v[200:203]\n"
DT "a[96:97],v25 offset:9216\n"
MF "v[168:171],a[244:247],a[160:163],v[168:171]\n"
DT "a[98:99],v25 offset:13824\n"
MF "v[204:207],a[244:247],a[176:179],v[204:207]\n"
WT "lgkmcnt(6)\n"
MF "v[136:139],a[64:67],a[164:167],v[136:139]\n"
DR "v[120:123],v248 offset:2048\n"
MF "v[172:175],a[64:67],a[180:183],v[172:175]\n"
DT "a[212:213],v21 offset:18432\n"
MF "v[208:211],a[64:67],v[116:119],v[208:211]\n"
DT "a[214:215],v21 offset:23040\n"
MF "v[140:143],a[68:71],a[164:167],v[140:143]\n"
DT "a[216:217],v21 offset:18464\n"
MF "v[176:179],a[68:71],a[180:183],v[176:179]\n"
DT "a[218:219],v21 offset:23072\n"
MF "v[212:215],a[68:71],v[116:119],v[212:215]\n"
DT "a[220:221],v22 offset:18432\n"
MF "v[144:147],a[72:75],a[164:167],v[144:147]\n"
DT "a[222:223],v22 offset:23040\n"
MF "v[180:183],a[72:75],a[180:183],v[180:183]\n"
DT "a[224:225],v22 offset:18464\n"
MF "v[148:151],a[76:79],a[164:167],v[148:151]\n"
DT "a[226:227],v22 offset:23072\n"
MF "v[184:187],a[76:79],a[180:183],v[184:187]\n"
DT "a[228:229],v23 offset:18432\n"
MF "v[152:155],a[80:83],a[164:167],v[152:155]\n"
DT "a[230:231],v23 offset:23040\n"
MF "v[188:191],a[80:83],a[180:183],v[188:191]\n"
DT "a[232:233],v23 offset:18464\n"
MF "v[156:159],a[84:87],a[164:167],v[156:159]\n"
WT "lgkmcnt(12)\n"
DT "a[234:235],v23 offset:23072\n"
"v_bfe_u32 v2,v0,2,2\n"
"v_lshrrev_b32 v3,4,v0\n"
MF "v[192:195],a[84:87],a[180:183],v[192:195]\n"
DT "a[236:237],v24 offset:18432\n"
"v_cmp_eq_u32 vcc,v2,v3\n"
"v_and_b32 v2,1,v0\n"
MF "v[160:163],a[88:91],a[164:167],v[160:163]\n"
DT "a[238:239],v24 offset:23040\n"
"v_lshlrev_b32 v2,4,v2\n"
"v_mov_b32 v3,0x3f80\n"
MF "v[196:199],a[88:91],a[180:183],v[196:199]\n"
DT "a[240:241],v24 offset:18464\n"
"v_lshlrev_b32 v2,v2,v3\n"
"v_cndmask_b32 v2,0,v2,vcc\n"
MF "v[164:167],a[92:95],a[164:167],v[164:167]\n"
DT "a[242:243],v24 offset:23072\n"
"v_and_b32 v3,2,v0\n"
"v_cmp_eq_u32 vcc,0,v3\n"
MF "v[200:203],a[92:95],a[180:183],v[200:203]\n"
DT "a[244:245],v25 offset:18432\n"
"v_mov_b32 v220,0\n"
"v_mov_b32 v221,0\n"
MF "v[168:171],a[96:99],a[164:167],v[168:171]\n"
DT "a[246:247],v25 offset:23040\n"
"v_cndmask_b32 v222,0,v2,vcc\n"
"v_cndmask_b32 v223,v2,v220,vcc\n"
MF "v[204:207],a[96:99],a[180:183],v[204:207]\n"
WT "lgkmcnt(6)\n"
"v_mov_b32 v224,0\n"
"v_mov_b32 v225,0\n"
MF "v[136:139],a[212:215],a[168:171],v[136:139]\n"
DR "v[124:127],v248 offset:3072\n"
MF "v[172:175],a[212:215],a[184:187],v[172:175]\n"
DT "a[64:65],v21 offset:27648\n"
MF "v[208:211],a[212:215],v[120:123],v[208:211]\n"
DT "a[66:67],v21 offset:32256\n"
MF "v[140:143],a[216:219],a[168:171],v[140:143]\n"
DT "a[68:69],v21 offset:27680\n"
MF "v[176:179],a[216:219],a[184:187],v[176:179]\n"
DT "a[70:71],v21 offset:32288\n"
MF "v[212:215],a[216:219],v[120:123],v[212:215]\n"
DT "a[72:73],v22 offset:27648\n"
MF "v[144:147],a[220:223],a[168:171],v[144:147]\n"
DT "a[74:75],v22 offset:32256\n"
MF "v[180:183],a[220:223],a[184:187],v[180:183]\n"
DT "a[76:77],v22 offset:27680\n"
MF "v[148:151],a[224:227],a[168:171],v[148:151]\n"
DT "a[78:79],v22 offset:32288\n"
MF "v[184:187],a[224:227],a[184:187],v[184:187]\n"
DT "a[80:81],v23 offset:27648\n"
MF "v[152:155],a[228:231],a[168:171],v[152:155]\n"
DT "a[82:83],v23 offset:32256\n"
MF "v[188:191],a[228:231],a[184:187],v[188:191]\n"
DT "a[84:85],v23 offset:27680\n"
MF "v[156:159],a[232:235],a[168:171],v[156:159]\n"
WT "lgkmcnt(12)\n"
DT "a[86:87],v23 offset:32288\n"
"s_mul_i32 s66,s36,11264\n"
"v_add_u32 v226,s66,v1\n"
MF "v[192:195],a[232:235],a[184:187],v[192:195]\n"
DT "a[88:89],v24 offset:27648\n"
"v_add_u32 v227,0x1000,v226\n"
"v_add_u32 v228,0x2000,v226\n"
MF "v[160:163],a[236:239],a[168:171],v[160:163]\n"
DT "a[90:91],v24 offset:32256\n"
"s_mul_i32 s66,s39,144\n"
"s_lshl_b32 s67,s36,5\n"
MF "v[196:199],a[236:239],a[184:187],v[196:199]\n"
DT "a[92:93],v24 offset:27680\n"
"s_add_u32 s66,s66,s67\n"
MF "v[164:167],a[240:243],a[168:171],v[164:167]\n"
DT "a[94:95],v24 offset:32288\n"
"v_and_b32 v2,15,v0\n"
"v_add_u32 v2,s66,v2\n"
MF "v[200:203],a[240:243],a[184:187],v[200:203]\n"
DT "a[96:97],v25 offset:27648\n"
"v_mul_u32_u24 v229,2304,v2\n"
"s_mul_i32 s66,s37,1152\n"
MF "v[168:171],a[244:247],a[168:171],v[168:171]\n"
DT "a[98:99],v25 offset:32256\n"
"s_mul_i32 s67,s40,288\n"
"s_add_u32 s66,s66,s67\n"
MF "v[204:207],a[244:247],a[184:187],v[204:207]\n"
WT "lgkmcnt(6)\n"
"v_add_u32 v229,s66,v229\n"
MF "v[136:139],a[64:67],a[172:175],v[136:139]\n"
"v_lshrrev_b32 v2,4,v0\n"
MF "v[172:175],a[64:67],a[188:191],v[172:175]\n"
"v_lshl_add_u32 v230,v2,3,v229\n"
MF "v[208:211],a[64:67],v[124:127],v[208:211]\n"
"v_lshrrev_b32 v3,1,v2\n"
MF "v[140:143],a[68:71],a[172:175],v[140:143]\n"
"v_and_b32 v2,1,v2\n"
MF "v[176:179],a[68:71],a[188:191],v[176:179]\n"
"v_lshl_or_b32 v2,v2,1,v3\n"
MF "v[212:215],a[68:71],v[124:127],v[212:215]\n"
"v_lshl_add_u32 v229,v2,4,v229\n"
MF "v[144:147],a[72:75],a[172:175],v[144:147]\n"
"s_add_u32 s66,s36,s38\n"
MF "v[180:183],a[72:75],a[188:191],v[180:183]\n"
"s_and_b32 s66,s66,3\n"
MF "v[148:151],a[76:79],a[172:175],v[148:151]\n"
"s_lshl_b32 s66,s66,6\n"
MF "v[184:187],a[76:79],a[188:191],v[184:187]\n"
"v_add_u32 v229,s66,v229\n"
MF "v[152:155],a[80:83],a[172:175],v[152:155]\n"
"s_mov_b32 s42,36864\n"
MF "v[188:191],a[80:83],a[188:191],v[188:191]\n"
"s_mov_b32 s43,294912\n"
MF "v[156:159],a[84:87],a[172:175],v[156:159]\n"
WT "lgkmcnt(0)\n"
MF "v[192:195],a[84:87],a[188:191],v[192:195]\n"
MF "v[160:163],a[88:91],a[172:175],v[160:163]\n"
MF "v[196:199],a[88:91],a[188:191],v[196:199]\n"
MF "v[164:167],a[92:95],a[172:175],v[164:167]\n"
MF "v[200:203],a[92:95],a[188:191],v[200:203]\n"
MF "v[168:171],a[96:99],a[172:175],v[168:171]\n"
MF "v[204:207],a[96:99],a[188:191],v[204:207]\n"
CV "v64,v208,v209\n"
CV "v66,v212,v213\n"
CV "v65,v210,v211\n"
CV "v67,v214,v215\n"
"buffer_store_dwordx4 v[64:67],v228,s[28:31],s72 offen offset:0 sc1\n"
CV "v36,v144,v145\n"
CV "v38,v148,v149\n"
CV "v37,v146,v147\n"
CV "v39,v150,v151\n"
"buffer_store_dwordx4 v[36:39],v226,s[28:31],s72 offen offset:1024 sc1\n"
CV "v52,v180,v181\n"
CV "v54,v184,v185\n"
CV "v53,v182,v183\n"
CV "v55,v186,v187\n"
"buffer_store_dwordx4 v[52:55],v227,s[28:31],s72 offen offset:1024 sc1\n"
CV "v40,v152,v153\n"
CV "v42,v156,v157\n"
CV "v41,v154,v155\n"
CV "v43,v158,v159\n"
"buffer_store_dwordx4 v[40:43],v226,s[28:31],s72 offen offset:2048 sc1\n"
CV "v56,v188,v189\n"
CV "v58,v192,v193\n"
CV "v57,v190,v191\n"
CV "v59,v194,v195\n"
"buffer_store_dwordx4 v[56:59],v227,s[28:31],s72 offen offset:2048 sc1\n"
CV "v44,v160,v161\n"
CV "v46,v164,v165\n"
CV "v45,v162,v163\n"
CV "v47,v166,v167\n"
"buffer_store_dwordx4 v[44:47],v226,s[28:31],s72 offen offset:3072 sc1\n"
CV "v60,v196,v197\n"
CV "v62,v200,v201\n"
CV "v61,v198,v199\n"
CV "v63,v202,v203\n"
"buffer_store_dwordx4 v[60:63],v227,s[28:31],s72 offen offset:3072 sc1\n"
CV "v68,v168,v169\n"
CV "v70,v204,v205\n"
CV "v69,v170,v171\n"
CV "v71,v206,v207\n"
"buffer_store_dwordx4 v[68:71],v228,s[28:31],s72 offen offset:1024 sc1\n"
WT "vmcnt(0)\n"
"s_barrier\n"
"buffer_load_dword v31,v27,s[28:31],0 offen offset:256 sc1\n"
WT "vmcnt(0)\n"
"v_cmp_eq_u32 vcc,v31,v30\n"
"s_andn2_b64 vcc,vcc,s[64:65]\n"
"s_cbranch_scc1 Lp232_%=\n"
"Lp233_%=:\n"
BL "v[76:79],v226,s[28:31],s73 offen offset:1024 sc1\n"
BL "v[80:83],v226,s[28:31],s74 offen offset:2048 sc1\n"
BL "v[84:87],v226,s[28:31],s75 offen offset:3072 sc1\n"
BL "v[88:91],v227,s[28:31],s73 offen offset:1024 sc1\n"
BL "v[92:95],v227,s[28:31],s74 offen offset:2048 sc1\n"
BL "v[96:99],v227,s[28:31],s75 offen offset:3072 sc1\n"
"s_cmp_lg_u32 s36,s38\n"
"s_cbranch_scc1 Ln228_%=\n"
BL "v[112:115],v228,s[28:31],s73 offen offset:1024 sc1\n"
BL "v[116:119],v228,s[28:31],s74 offen offset:1024 sc1\n"
BL "v[120:123],v228,s[28:31],s75 offen offset:1024 sc1\n"
"Ln228_%=:\n"
WT "vmcnt(0)\n"
MF "v[136:139],v[222:225],v[76:79],v[136:139]\n"
MF "v[140:143],v[220:223],v[76:79],v[140:143]\n"
MF "v[136:139],v[222:225],v[80:83],v[136:139]\n"
MF "v[140:143],v[220:223],v[80:83],v[140:143]\n"
MF "v[136:139],v[222:225],v[84:87],v[136:139]\n"
MF "v[140:143],v[220:223],v[84:87],v[140:143]\n"
MF "v[172:175],v[222:225],v[88:91],v[172:175]\n"
MF "v[176:179],v[220:223],v[88:91],v[176:179]\n"
MF "v[172:175],v[222:225],v[92:95],v[172:175]\n"
MF "v[176:179],v[220:223],v[92:95],v[176:179]\n"
MF "v[172:175],v[222:225],v[96:99],v[172:175]\n"
MF "v[176:179],v[220:223],v[96:99],v[176:179]\n"
"s_nop 3\n"
CV "v32,v136,v137\n"
CV "v34,v140,v141\n"
CV "v33,v138,v139\n"
CV "v35,v142,v143\n"
CV "v36,v172,v173\n"
CV "v38,v176,v177\n"
CV "v37,v174,v175\n"
CV "v39,v178,v179\n"
"v_permlane16_swap_b32 v32,v34\n"
"v_permlane16_swap_b32 v33,v35\n"
"buffer_store_dwordx4 v[32:35],v229,s[32:35],0 offen\n"
"v_permlane16_swap_b32 v36,v38\n"
"v_permlane16_swap_b32 v37,v39\n"
"buffer_store_dwordx4 v[36:39],v229,s[32:35],s42 offen\n"
"s_cmp_lg_u32 s36,s38\n"
"s_cbranch_scc1 Ln230_%=\n"
MF "v[168:171],v[222:225],v[112:115],v[168:171]\n"
MF "v[204:207],v[220:223],v[112:115],v[204:207]\n"
MF "v[168:171],v[222:225],v[116:119],v[168:171]\n"
MF "v[204:207],v[220:223],v[116:119],v[204:207]\n"
MF "v[168:171],v[222:225],v[120:123],v[168:171]\n"
MF "v[204:207],v[220:223],v[120:123],v[204:207]\n"
"s_nop 7\n"
CV "v48,v168,v169\n"
CV "v50,v204,v205\n"
CV "v49,v170,v171\n"
CV "v51,v206,v207\n"
"buffer_store_dwordx2 v[48:49],v230,s[32:35],0 offen offset:256\n"
"buffer_store_dwordx2 v[50:51],v230,s[32:35],s42 offen offset:256\n"
"Ln230_%=:\n"
WT "vmcnt(0)\n"
"s_endpgm\n"
"Lw5_%=:\n"
PM "v[112:113],v[112:113],s[54:55]\n"
PM "v[114:115],v[114:115],s[54:55]\n"
PM "v[116:117],v[116:117],s[54:55]\n"
PM "v[118:119],v[118:119],s[54:55]\n"
CV "v104,v112,v113\n"
CV "v106,v116,v117\n"
CV "v105,v114,v115\n"
CV "v107,v118,v119\n"
PM "v[176:177],v[176:177],s[54:55]\n"
PM "v[178:179],v[178:179],s[54:55]\n"
PM "v[180:181],v[180:181],s[54:55]\n"
PM "v[182:183],v[182:183],s[54:55]\n"
CV "v108,v176,v177\n"
CV "v110,v180,v181\n"
CV "v109,v178,v179\n"
CV "v111,v182,v183\n"
"v_permlane16_swap_b32 v104,v106\n"
"v_permlane16_swap_b32 v105,v107\n"
"v_accvgpr_write_b32 a0,v104\n"
"v_accvgpr_write_b32 a1,v105\n"
"v_accvgpr_write_b32 a2,v106\n"
"v_accvgpr_write_b32 a3,v107\n"
PM "v[120:121],v[120:121],s[54:55]\n"
PM "v[122:123],v[122:123],s[54:55]\n"
PM "v[124:125],v[124:125],s[54:55]\n"
PM "v[126:127],v[126:127],s[54:55]\n"
CV "v104,v120,v121\n"
CV "v106,v124,v125\n"
CV "v105,v122,v123\n"
CV "v107,v126,v127\n"
"v_permlane16_swap_b32 v108,v110\n"
"v_permlane16_swap_b32 v109,v111\n"
"v_accvgpr_write_b32 a16,v108\n"
"v_accvgpr_write_b32 a17,v109\n"
"v_accvgpr_write_b32 a18,v110\n"
"v_accvgpr_write_b32 a19,v111\n"
PM "v[184:185],v[184:185],s[54:55]\n"
PM "v[186:187],v[186:187],s[54:55]\n"
PM "v[188:189],v[188:189],s[54:55]\n"
PM "v[190:191],v[190:191],s[54:55]\n"
CV "v108,v184,v185\n"
CV "v110,v188,v189\n"
CV "v109,v186,v187\n"
CV "v111,v190,v191\n"
"v_permlane16_swap_b32 v104,v106\n"
"v_permlane16_swap_b32 v105,v107\n"
"v_accvgpr_write_b32 a4,v104\n"
"v_accvgpr_write_b32 a5,v105\n"
"v_accvgpr_write_b32 a6,v106\n"
"v_accvgpr_write_b32 a7,v107\n"
PM "v[128:129],v[128:129],s[54:55]\n"
PM "v[130:131],v[130:131],s[54:55]\n"
PM "v[132:133],v[132:133],s[54:55]\n"
PM "v[134:135],v[134:135],s[54:55]\n"
CV "v104,v128,v129\n"
CV "v106,v132,v133\n"
CV "v105,v130,v131\n"
CV "v107,v134,v135\n"
"v_permlane16_swap_b32 v108,v110\n"
"v_permlane16_swap_b32 v109,v111\n"
"v_accvgpr_write_b32 a20,v108\n"
"v_accvgpr_write_b32 a21,v109\n"
"v_accvgpr_write_b32 a22,v110\n"
"v_accvgpr_write_b32 a23,v111\n"
PM "v[192:193],v[192:193],s[54:55]\n"
PM "v[194:195],v[194:195],s[54:55]\n"
PM "v[196:197],v[196:197],s[54:55]\n"
PM "v[198:199],v[198:199],s[54:55]\n"
CV "v108,v192,v193\n"
CV "v110,v196,v197\n"
CV "v109,v194,v195\n"
CV "v111,v198,v199\n"
"v_permlane16_swap_b32 v104,v106\n"
"v_permlane16_swap_b32 v105,v107\n"
"v_accvgpr_write_b32 a8,v104\n"
"v_accvgpr_write_b32 a9,v105\n"
"v_accvgpr_write_b32 a10,v106\n"
"v_accvgpr_write_b32 a11,v107\n"
PM "v[136:137],v[136:137],s[54:55]\n"
PM "v[138:139],v[138:139],s[54:55]\n"
PM "v[140:141],v[140:141],s[54:55]\n"
PM "v[142:143],v[142:143],s[54:55]\n"
CV "v104,v136,v137\n"
CV "v106,v140,v141\n"
CV "v105,v138,v139\n"
CV "v107,v142,v143\n"
"v_permlane16_swap_b32 v108,v110\n"
"v_permlane16_swap_b32 v109,v111\n"
"v_accvgpr_write_b32 a24,v108\n"
"v_accvgpr_write_b32 a25,v109\n"
"v_accvgpr_write_b32 a26,v110\n"
"v_accvgpr_write_b32 a27,v111\n"
PM "v[200:201],v[200:201],s[54:55]\n"
PM "v[202:203],v[202:203],s[54:55]\n"
PM "v[204:205],v[204:205],s[54:55]\n"
PM "v[206:207],v[206:207],s[54:55]\n"
CV "v108,v200,v201\n"
CV "v110,v204,v205\n"
CV "v109,v202,v203\n"
CV "v111,v206,v207\n"
"v_permlane16_swap_b32 v104,v106\n"
"v_permlane16_swap_b32 v105,v107\n"
"v_accvgpr_write_b32 a12,v104\n"
"v_accvgpr_write_b32 a13,v105\n"
"v_accvgpr_write_b32 a14,v106\n"
"v_accvgpr_write_b32 a15,v107\n"
PM "v[144:145],v[144:145],s[54:55]\n"
PM "v[146:147],v[146:147],s[54:55]\n"
PM "v[148:149],v[148:149],s[54:55]\n"
PM "v[150:151],v[150:151],s[54:55]\n"
CV "v32,v144,v145\n"
CV "v34,v148,v149\n"
CV "v33,v146,v147\n"
CV "v35,v150,v151\n"
"v_permlane16_swap_b32 v108,v110\n"
"v_permlane16_swap_b32 v109,v111\n"
"v_accvgpr_write_b32 a28,v108\n"
"v_accvgpr_write_b32 a29,v109\n"
"v_accvgpr_write_b32 a30,v110\n"
"v_accvgpr_write_b32 a31,v111\n"
PM "v[208:209],v[208:209],s[54:55]\n"
PM "v[210:211],v[210:211],s[54:55]\n"
PM "v[212:213],v[212:213],s[54:55]\n"
PM "v[214:215],v[214:215],s[54:55]\n"
CV "v48,v208,v209\n"
CV "v50,v212,v213\n"
CV "v49,v210,v211\n"
CV "v51,v214,v215\n"
"v_permlane16_swap_b32 v32,v34\n"
"v_permlane16_swap_b32 v33,v35\n"
PM "v[152:153],v[152:153],s[54:55]\n"
PM "v[154:155],v[154:155],s[54:55]\n"
PM "v[156:157],v[156:157],s[54:55]\n"
PM "v[158:159],v[158:159],s[54:55]\n"
CV "v36,v152,v153\n"
CV "v38,v156,v157\n"
CV "v37,v154,v155\n"
CV "v39,v158,v159\n"
"v_permlane16_swap_b32 v48,v50\n"
"v_permlane16_swap_b32 v49,v51\n"
PM "v[216:217],v[216:217],s[54:55]\n"
PM "v[218:219],v[218:219],s[54:55]\n"
PM "v[220:221],v[220:221],s[54:55]\n"
PM "v[222:223],v[222:223],s[54:55]\n"
CV "v52,v216,v217\n"
CV "v54,v220,v221\n"
CV "v53,v218,v219\n"
CV "v55,v222,v223\n"
"v_permlane16_swap_b32 v36,v38\n"
"v_permlane16_swap_b32 v37,v39\n"
PM "v[160:161],v[160:161],s[54:55]\n"
PM "v[162:163],v[162:163],s[54:55]\n"
PM "v[164:165],v[164:165],s[54:55]\n"
PM "v[166:167],v[166:167],s[54:55]\n"
CV "v40,v160,v161\n"
CV "v42,v164,v165\n"
CV "v41,v162,v163\n"
CV "v43,v166,v167\n"
"v_permlane16_swap_b32 v52,v54\n"
"v_permlane16_swap_b32 v53,v55\n"
PM "v[224:225],v[224:225],s[54:55]\n"
PM "v[226:227],v[226:227],s[54:55]\n"
PM "v[228:229],v[228:229],s[54:55]\n"
PM "v[230:231],v[230:231],s[54:55]\n"
CV "v56,v224,v225\n"
CV "v58,v228,v229\n"
CV "v57,v226,v227\n"
CV "v59,v230,v231\n"
"v_permlane16_swap_b32 v40,v42\n"
"v_permlane16_swap_b32 v41,v43\n"
PM "v[168:169],v[168:169],s[54:55]\n"
PM "v[170:171],v[170:171],s[54:55]\n"
PM "v[172:173],v[172:173],s[54:55]\n"
PM "v[174:175],v[174:175],s[54:55]\n"
CV "v44,v168,v169\n"
CV "v46,v172,v173\n"
CV "v45,v170,v171\n"
CV "v47,v174,v175\n"
"v_permlane16_swap_b32 v56,v58\n"
"v_permlane16_swap_b32 v57,v59\n"
PM "v[232:233],v[232:233],s[54:55]\n"
PM "v[234:235],v[234:235],s[54:55]\n"
PM "v[236:237],v[236:237],s[54:55]\n"
PM "v[238:239],v[238:239],s[54:55]\n"
CV "v60,v232,v233\n"
CV "v62,v236,v237\n"
CV "v61,v234,v235\n"
CV "v63,v238,v239\n"
"v_permlane16_swap_b32 v44,v46\n"
"v_permlane16_swap_b32 v45,v47\n"
"s_nop 1\n"
"v_permlane16_swap_b32 v60,v62\n"
"v_permlane16_swap_b32 v61,v63\n"
"s_lshl_b32 s66,s36,13\n"
"v_add_u32 v244,s66,v1\n"
"v_add_u32 v245,0x1000,v244\n"
"v_add_u32 v246,0x8000,v1\n"
"v_add_u32 v247,158208,v1\n"
"v_add_u32 v253,84480,v1\n"
"v_mov_b32 v252,162304\n"
"v_lshl_add_u32 v251,s36,2,v252\n"
"s_mov_b32 s43,1400897536\n"
"v_add_u32 v248,73728,v1\n"
"v_lshl_add_u32 v249,s36,10,v246\n"
WT "vmcnt(16)\n"
WT "lgkmcnt(0)\n"
"s_barrier\n"
DR "a[64:67],v4 offset:0\n"
DR "a[68:71],v4 offset:512\n"
DR "a[72:75],v4 offset:64\n"
DR "a[76:79],v4 offset:576\n"
DR "a[80:83],v4 offset:128\n"
DR "a[84:87],v4 offset:640\n"
DR "a[88:91],v4 offset:192\n"
DR "a[92:95],v4 offset:704\n"
"s_cmp_lg_u32 s98,0\n"
"s_cbranch_scc1 LS13_%=\n"
"v_add_u32 v254,159744,v1\n"
WT "lgkmcnt(0)\n"
MF "v[80:83],a[64:67],v[32:35],0\n"
"v_mov_b64 v[136:137],0\n"
"v_mov_b64 v[138:139],0\n"
"v_mov_b64 v[140:141],0\n"
"v_mov_b64 v[142:143],0\n"
MF "v[84:87],a[68:71],v[32:35],0\n"
"v_mov_b64 v[144:145],0\n"
"v_mov_b64 v[146:147],0\n"
"v_mov_b64 v[148:149],0\n"
MF "v[80:83],a[72:75],v[36:39],v[80:83]\n"
"v_mov_b64 v[150:151],0\n"
"v_mov_b64 v[152:153],0\n"
"v_mov_b64 v[154:155],0\n"
MF "v[84:87],a[76:79],v[36:39],v[84:87]\n"
"v_mov_b64 v[156:157],0\n"
"v_mov_b64 v[158:159],0\n"
"v_mov_b64 v[160:161],0\n"
"v_mov_b64 v[162:163],0\n"
MF "v[80:83],a[80:83],v[40:43],v[80:83]\n"
"v_mov_b64 v[164:165],0\n"
"v_mov_b64 v[166:167],0\n"
"v_mov_b64 v[168:169],0\n"
MF "v[84:87],a[84:87],v[40:43],v[84:87]\n"
"v_mov_b64 v[170:171],0\n"
"v_mov_b64 v[172:173],0\n"
"v_mov_b64 v[174:175],0\n"
MF "v[80:83],a[88:91],v[44:47],v[80:83]\n"
"v_mov_b64 v[176:177],0\n"
"v_mov_b64 v[178:179],0\n"
"v_mov_b64 v[180:181],0\n"
"v_mov_b64 v[182:183],0\n"
MF "v[84:87],a[92:95],v[44:47],v[84:87]\n"
"v_mov_b64 v[184:185],0\n"
"v_mov_b64 v[186:187],0\n"
"v_mov_b64 v[188:189],0\n"
MF "v[88:91],a[64:67],v[48:51],0\n"
"v_mov_b64 v[190:191],0\n"
"v_mov_b64 v[192:193],0\n"
"v_mov_b64 v[194:195],0\n"
MF "v[92:95],a[68:71],v[48:51],0\n"
"v_mov_b64 v[196:197],0\n"
"v_mov_b64 v[198:199],0\n"
"v_mov_b64 v[200:201],0\n"
"v_mov_b64 v[202:203],0\n"
MF "v[88:91],a[72:75],v[52:55],v[88:91]\n"
"v_mov_b64 v[204:205],0\n"
"v_mov_b64 v[206:207],0\n"
"v_mov_b64 v[208:209],0\n"
MF "v[92:95],a[76:79],v[52:55],v[92:95]\n"
"v_mov_b64 v[210:211],0\n"
"v_mov_b64 v[212:213],0\n"
"v_mov_b64 v[214:215],0\n"
MF "v[88:91],a[80:83],v[56:59],v[88:91]\n"
"v_mov_b64 v[216:217],0\n"
"v_mov_b64 v[218:219],0\n"
"v_mov_b64 v[220:221],0\n"
"v_mov_b64 v[222:223],0\n"
MF "v[92:95],a[84:87],v[56:59],v[92:95]\n"
"v_mov_b64 v[240:241],0\n"
"v_mov_b64 v[242:243],0\n"
"v_add_u32 v6,s49,v4\n"
MF "v[88:91],a[88:91],v[60:63],v[88:91]\n"
"v_add_u32 v7,s49,v5\n"
"s_mov_b32 s50,s42\n"
"s_mov_b32 s42,s49\n"
MF "v[92:95],a[92:95],v[60:63],v[92:95]\n"
"s_add_u32 s49,s49,16896\n"
"s_cmp_eq_u32 s49,84480\n"
"s_cselect_b32 s49,0,s49\n"
WT "vmcnt(12)\n"
WT "lgkmcnt(0)\n"
"s_barrier\n"
DR "a[64:67],v6 offset:0\n"
DR "a[68:71],v6 offset:512\n"
DR "a[72:75],v6 offset:64\n"
DR "a[76:79],v6 offset:576\n"
DR "a[80:83],v6 offset:128\n"
DR "a[84:87],v6 offset:640\n"
DR "a[88:91],v6 offset:192\n"
DR "a[92:95],v6 offset:704\n"
DR "v[124:127],v254\n"
"v_xor_b32 v254,0x400,v254\n"
DT "a[96:97],v7 offset:0\n"
DT "a[98:99],v7 offset:512\n"
DT "a[100:101],v7 offset:32\n"
DT "a[102:103],v7 offset:544\n"
DT "a[104:105],v7 offset:64\n"
DT "a[106:107],v7 offset:576\n"
DT "a[108:109],v7 offset:96\n"
DT "a[110:111],v7 offset:608\n"
"v_max3_f32 v128,v80,v81,v82\n"
"v_max3_f32 v128,v128,v83,v84\n"
"v_max3_f32 v128,v128,v85,v86\n"
"v_max_f32 v128,v128,v87\n"
"v_mov_b32 v129,v128\n"
"s_nop 1\n"
"v_permlane32_swap_b32 v129,v128\n"
"v_max_f32 v128,v129,v128\n"
"v_mov_b32 v129,v128\n"
"s_nop 1\n"
"v_permlane16_swap_b32 v129,v128\n"
"v_max_f32 v128,v129,v128\n"
"v_sub_f32 v80,v80,v128\n"
"v_sub_f32 v81,v81,v128\n"
"v_sub_f32 v82,v82,v128\n"
"v_sub_f32 v83,v83,v128\n"
"v_sub_f32 v84,v84,v128\n"
"v_sub_f32 v85,v85,v128\n"
"v_sub_f32 v86,v86,v128\n"
"v_sub_f32 v87,v87,v128\n"
"v_sub_f32 v104,0,v128\n"
"v_sub_f32 v105,0,v128\n"
"v_sub_f32 v106,0,v128\n"
"v_sub_f32 v107,0,v128\n"
EX "v80,v80\n"
EX "v81,v81\n"
EX "v82,v82\n"
EX "v83,v83\n"
EX "v84,v84\n"
EX "v85,v85\n"
EX "v86,v86\n"
EX "v87,v87\n"
"v_max3_f32 v128,v88,v89,v90\n"
"v_max3_f32 v128,v128,v91,v92\n"
"v_max3_f32 v128,v128,v93,v94\n"
"v_max_f32 v128,v128,v95\n"
"v_mov_b32 v129,v128\n"
"s_nop 1\n"
"v_permlane32_swap_b32 v129,v128\n"
"v_max_f32 v128,v129,v128\n"
"v_mov_b32 v129,v128\n"
"s_nop 1\n"
"v_permlane16_swap_b32 v129,v128\n"
"v_max_f32 v128,v129,v128\n"
"v_sub_f32 v88,v88,v128\n"
"v_sub_f32 v89,v89,v128\n"
"v_sub_f32 v90,v90,v128\n"
"v_sub_f32 v91,v91,v128\n"
"v_sub_f32 v92,v92,v128\n"
"v_sub_f32 v93,v93,v128\n"
"v_sub_f32 v94,v94,v128\n"
"v_sub_f32 v95,v95,v128\n"
"v_sub_f32 v108,0,v128\n"
"v_sub_f32 v109,0,v128\n"
"v_sub_f32 v110,0,v128\n"
"v_sub_f32 v111,0,v128\n"
CV "v116,v80,v81\n"
CV "v117,v82,v83\n"
CV "v118,v84,v85\n"
CV "v119,v86,v87\n"
EX "v88,v88\n"
EX "v89,v89\n"
EX "v90,v90\n"
EX "v91,v91\n"
EX "v92,v92\n"
EX "v93,v93\n"
EX "v94,v94\n"
EX "v95,v95\n"
WT "lgkmcnt(8)\n"
"s_mov_b32 s52,163840\n"
"Ll360_%=:\n"
MF "v[80:83],a[64:67],v[32:35],v[104:107]\n"
"s_add_u32 m0,s50,s48\n"
DT "a[112:113],v7 offset:128\n"
CV "v120,v88,v89\n"
MF "v[84:87],a[68:71],v[32:35],v[104:107]\n"
DT "a[114:115],v7 offset:640\n"
BL "v8,s[12:15],s78 offen nt lds\n"
MF "v[80:83],a[72:75],v[36:39],v[80:83]\n"
DT "a[116:117],v7 offset:160\n"
CV "v121,v90,v91\n"
MF "v[84:87],a[76:79],v[36:39],v[84:87]\n"
DT "a[118:119],v7 offset:672\n"
CV "v122,v92,v93\n"
MF "v[80:83],a[80:83],v[40:43],v[80:83]\n"
DT "a[120:121],v7 offset:192\n"
CV "v123,v94,v95\n"
MF "v[84:87],a[84:87],v[40:43],v[84:87]\n"
DT "a[122:123],v7 offset:704\n"
BL "v9,s[12:15],s78 offen offset:1056 nt lds\n"
MF "v[80:83],a[88:91],v[44:47],v[80:83]\n"
"s_add_u32 m0,m0,8448\n"
DT "a[124:125],v7 offset:224\n"
MF "v[84:87],a[92:95],v[44:47],v[84:87]\n"
DT "a[126:127],v7 offset:736\n"
MF "v[88:91],a[64:67],v[48:51],v[108:111]\n"
"v_add_u32 v6,s49,v4\n"
"v_add_u32 v7,s49,v5\n"
MF "v[92:95],a[68:71],v[48:51],v[108:111]\n"
"s_mov_b32 s50,s42\n"
BL "v10,s[16:19],s79 offen nt lds\n"
EX "v80,v80\n"
MF "v[88:91],a[72:75],v[52:55],v[88:91]\n"
"s_mov_b32 s42,s49\n"
EX "v81,v81\n"
MF "v[92:95],a[76:79],v[52:55],v[92:95]\n"
EX "v82,v82\n"
MF "v[88:91],a[80:83],v[56:59],v[88:91]\n"
EX "v83,v83\n"
MF "v[92:95],a[84:87],v[56:59],v[92:95]\n"
BL "v11,s[16:19],s79 offen offset:1056 nt lds\n"
EX "v84,v84\n"
MF "v[88:91],a[88:91],v[60:63],v[88:91]\n"
"s_add_u32 s51,s51,8192\n"
EX "v85,v85\n"
MF "v[92:95],a[92:95],v[60:63],v[92:95]\n"
"s_mov_b32 s79,s78\n"
WT "vmcnt(10) lgkmcnt(0)\n"
"s_barrier\n"
MF "v[208:211],a[96:99],v[124:127],v[208:211]\n"
"s_xor_b32 s78,s51,s41\n"
DR "a[64:67],v6 offset:0\n"
EX "v86,v86\n"
MF "v[212:215],a[100:103],v[124:127],v[212:215]\n"
DR "a[68:71],v6 offset:512\n"
EX "v87,v87\n"
MF "v[216:219],a[104:107],v[124:127],v[216:219]\n"
DR "a[72:75],v6 offset:64\n"
MF "v[220:223],a[108:111],v[124:127],v[220:223]\n"
DR "a[76:79],v6 offset:576\n"
EX "v88,v88\n"
MF "v[240:243],a[48:51],v[124:127],v[240:243]\n"
DR "a[80:83],v6 offset:128\n"
EX "v89,v89\n"
MF "v[136:139],a[96:99],v[116:119],v[136:139]\n"
DR "a[84:87],v6 offset:640\n"
MF "v[140:143],a[100:103],v[116:119],v[140:143]\n"
DR "a[88:91],v6 offset:192\n"
MF "v[144:147],a[104:107],v[116:119],v[144:147]\n"
DR "a[92:95],v6 offset:704\n"
MF "v[148:151],a[108:111],v[116:119],v[148:151]\n"
DR "v[124:127],v254\n"
"v_xor_b32 v254,0x400,v254\n"
MF "v[152:155],a[112:115],v[116:119],v[152:155]\n"
EX "v90,v90\n"
MF "v[156:159],a[116:119],v[116:119],v[156:159]\n"
EX "v91,v91\n"
MF "v[160:163],a[120:123],v[116:119],v[160:163]\n"
"s_add_u32 s49,s49,16896\n"
EX "v92,v92\n"
MF "v[164:167],a[124:127],v[116:119],v[164:167]\n"
"s_cmp_eq_u32 s49,84480\n"
EX "v93,v93\n"
MF "v[168:171],a[48:51],v[116:119],v[168:171]\n"
"s_cselect_b32 s49,0,s49\n"
EX "v94,v94\n"
MF "v[172:175],a[96:99],v[120:123],v[172:175]\n"
EX "v95,v95\n"
MF "v[176:179],a[100:103],v[120:123],v[176:179]\n"
DT "a[96:97],v7 offset:0\n"
CV "v116,v80,v81\n"
MF "v[180:183],a[104:107],v[120:123],v[180:183]\n"
DT "a[98:99],v7 offset:512\n"
CV "v117,v82,v83\n"
MF "v[184:187],a[108:111],v[120:123],v[184:187]\n"
DT "a[100:101],v7 offset:32\n"
CV "v118,v84,v85\n"
MF "v[188:191],a[112:115],v[120:123],v[188:191]\n"
DT "a[102:103],v7 offset:544\n"
CV "v119,v86,v87\n"
MF "v[192:195],a[116:119],v[120:123],v[192:195]\n"
DT "a[104:105],v7 offset:64\n"
MF "v[196:199],a[120:123],v[120:123],v[196:199]\n"
DT "a[106:107],v7 offset:576\n"
MF "v[200:203],a[124:127],v[120:123],v[200:203]\n"
WT "lgkmcnt(6)\n"
DT "a[108:109],v7 offset:96\n"
MF "v[204:207],a[48:51],v[120:123],v[204:207]\n"
"s_cmp_lg_u32 s51,s52\n"
DT "a[110:111],v7 offset:608\n"
"s_cbranch_scc1 Ll360_%=\n"
MF "v[80:83],a[64:67],a[0:3],0\n"
"s_add_u32 m0,s50,s48\n"
DT "a[112:113],v7 offset:128\n"
CV "v120,v88,v89\n"
MF "v[84:87],a[68:71],a[0:3],0\n"
DT "a[114:115],v7 offset:640\n"
BL "v8,s[12:15],s78 offen nt lds\n"
MF "v[80:83],a[72:75],a[4:7],v[80:83]\n"
DT "a[116:117],v7 offset:160\n"
CV "v121,v90,v91\n"
MF "v[84:87],a[76:79],a[4:7],v[84:87]\n"
DT "a[118:119],v7 offset:672\n"
CV "v122,v92,v93\n"
MF "v[80:83],a[80:83],a[8:11],v[80:83]\n"
DT "a[120:121],v7 offset:192\n"
CV "v123,v94,v95\n"
MF "v[84:87],a[84:87],a[8:11],v[84:87]\n"
DT "a[122:123],v7 offset:704\n"
BL "v9,s[12:15],s78 offen offset:1056 nt lds\n"
MF "v[80:83],a[88:91],a[12:15],v[80:83]\n"
"s_add_u32 m0,m0,8448\n"
DT "a[124:125],v7 offset:224\n"
MF "v[84:87],a[92:95],a[12:15],v[84:87]\n"
DT "a[126:127],v7 offset:736\n"
MF "v[88:91],a[64:67],a[16:19],0\n"
"v_add_u32 v6,s49,v4\n"
"v_add_u32 v7,s49,v5\n"
MF "v[92:95],a[68:71],a[16:19],0\n"
"s_mov_b32 s50,s42\n"
BL "v10,s[16:19],s79 offen nt lds\n"
MF "v[88:91],a[72:75],a[20:23],v[88:91]\n"
"s_mov_b32 s42,s49\n"
MF "v[92:95],a[76:79],a[20:23],v[92:95]\n"
MF "v[88:91],a[80:83],a[24:27],v[88:91]\n"
MF "v[92:95],a[84:87],a[24:27],v[92:95]\n"
BL "v11,s[16:19],s79 offen offset:1056 nt lds\n"
MF "v[88:91],a[88:91],a[28:31],v[88:91]\n"
"s_add_u32 s51,s51,8192\n"
MF "v[92:95],a[92:95],a[28:31],v[92:95]\n"
"s_mov_b32 s79,s78\n"
WT "vmcnt(12)\n"
WT "lgkmcnt(0)\n"
"s_barrier\n"
MF "v[208:211],a[96:99],v[124:127],v[208:211]\n"
"s_xor_b32 s78,s51,s41\n"
DR "a[64:67],v6 offset:0\n"
"v_max3_f32 v128,v80,v81,v82\n"
"v_max3_f32 v128,v128,v83,v84\n"
"v_max3_f32 v128,v128,v85,v86\n"
MF "v[212:215],a[100:103],v[124:127],v[212:215]\n"
DR "a[68:71],v6 offset:512\n"
"v_max_f32 v128,v128,v87\n"
"v_mov_b32 v129,v128\n"
"s_nop 1\n"
MF "v[216:219],a[104:107],v[124:127],v[216:219]\n"
DR "a[72:75],v6 offset:64\n"
"v_permlane32_swap_b32 v129,v128\n"
"v_max_f32 v128,v129,v128\n"
"v_mov_b32 v129,v128\n"
MF "v[220:223],a[108:111],v[124:127],v[220:223]\n"
DR "a[76:79],v6 offset:576\n"
"s_nop 1\n"
"v_permlane16_swap_b32 v129,v128\n"
"v_max_f32 v128,v129,v128\n"
MF "v[240:243],a[48:51],v[124:127],v[240:243]\n"
DR "a[80:83],v6 offset:128\n"
"v_sub_f32 v80,v80,v128\n"
"v_sub_f32 v81,v81,v128\n"
"v_sub_f32 v82,v82,v128\n"
MF "v[136:139],a[96:99],v[116:119],v[136:139]\n"
DR "a[84:87],v6 offset:640\n"
"v_sub_f32 v83,v83,v128\n"
"v_sub_f32 v84,v84,v128\n"
"v_sub_f32 v85,v85,v128\n"
MF "v[140:143],a[100:103],v[116:119],v[140:143]\n"
DR "a[88:91],v6 offset:192\n"
"v_sub_f32 v86,v86,v128\n"
"v_sub_f32 v87,v87,v128\n"
MF "v[144:147],a[104:107],v[116:119],v[144:147]\n"
DR "a[92:95],v6 offset:704\n"
"v_sub_f32 v104,0,v128\n"
"v_sub_f32 v105,0,v128\n"
"v_sub_f32 v106,0,v128\n"
MF "v[148:151],a[108:111],v[116:119],v[148:151]\n"
DR "v[124:127],v254\n"
"v_xor_b32 v254,0x400,v254\n"
"v_sub_f32 v107,0,v128\n"
EX "v80,v80\n"
MF "v[152:155],a[112:115],v[116:119],v[152:155]\n"
EX "v81,v81\n"
EX "v82,v82\n"
MF "v[156:159],a[116:119],v[116:119],v[156:159]\n"
EX "v83,v83\n"
MF "v[160:163],a[120:123],v[116:119],v[160:163]\n"
"s_add_u32 s49,s49,16896\n"
EX "v84,v84\n"
EX "v85,v85\n"
MF "v[164:167],a[124:127],v[116:119],v[164:167]\n"
"s_cmp_eq_u32 s49,84480\n"
EX "v86,v86\n"
MF "v[168:171],a[48:51],v[116:119],v[168:171]\n"
"s_cselect_b32 s49,0,s49\n"
EX "v87,v87\n"
MF "v[172:175],a[96:99],v[120:123],v[172:175]\n"
CV "v116,v80,v81\n"
CV "v117,v82,v83\n"
CV "v118,v84,v85\n"
CV "v119,v86,v87\n"
"v_max3_f32 v128,v88,v89,v90\n"
MF "v[176:179],a[100:103],v[120:123],v[176:179]\n"
DT "a[96:97],v7 offset:0\n"
"v_max3_f32 v128,v128,v91,v92\n"
"v_max3_f32 v128,v128,v93,v94\n"
"v_max_f32 v128,v128,v95\n"
"v_mov_b32 v129,v128\n"
"s_nop 1\n"
MF "v[180:183],a[104:107],v[120:123],v[180:183]\n"
DT "a[98:99],v7 offset:512\n"
"v_permlane32_swap_b32 v129,v128\n"
"v_max_f32 v128,v129,v128\n"
"v_mov_b32 v129,v128\n"
"s_nop 1\n"
"v_permlane16_swap_b32 v129,v128\n"
MF "v[184:187],a[108:111],v[120:123],v[184:187]\n"
DT "a[100:101],v7 offset:32\n"
"v_max_f32 v128,v129,v128\n"
"v_sub_f32 v88,v88,v128\n"
"v_sub_f32 v89,v89,v128\n"
"v_sub_f32 v90,v90,v128\n"
"v_sub_f32 v91,v91,v128\n"
MF "v[188:191],a[112:115],v[120:123],v[188:191]\n"
DT "a[102:103],v7 offset:544\n"
"v_sub_f32 v92,v92,v128\n"
"v_sub_f32 v93,v93,v128\n"
"v_sub_f32 v94,v94,v128\n"
"v_sub_f32 v95,v95,v128\n"
"v_sub_f32 v108,0,v128\n"
MF "v[192:195],a[116:119],v[120:123],v[192:195]\n"
DT "a[104:105],v7 offset:64\n"
"v_sub_f32 v109,0,v128\n"
"v_sub_f32 v110,0,v128\n"
"v_sub_f32 v111,0,v128\n"
EX "v88,v88\n"
MF "v[196:199],a[120:123],v[120:123],v[196:199]\n"
DT "a[106:107],v7 offset:576\n"
EX "v89,v89\n"
EX "v90,v90\n"
EX "v91,v91\n"
MF "v[200:203],a[124:127],v[120:123],v[200:203]\n"
WT "lgkmcnt(6)\n"
DT "a[108:109],v7 offset:96\n"
EX "v92,v92\n"
EX "v93,v93\n"
MF "v[204:207],a[48:51],v[120:123],v[204:207]\n"
DT "a[110:111],v7 offset:608\n"
EX "v94,v94\n"
EX "v95,v95\n"
"v_rcp_f32 v130,v240\n"
"s_add_u32 s66,s56,s99\n"
PM "v[208:209],v[208:209],v[130:131] op_sel_hi:[1,0]\n"
PM "v[210:211],v[210:211],v[130:131] op_sel_hi:[1,0]\n"
PM "v[212:213],v[212:213],v[130:131] op_sel_hi:[1,0]\n"
PM "v[214:215],v[214:215],v[130:131] op_sel_hi:[1,0]\n"
CV "v224,v208,v209\n"
CV "v225,v210,v211\n"
CV "v226,v212,v213\n"
CV "v227,v214,v215\n"
"buffer_store_dwordx4 v[224:227],v246,s[28:31],s66 offen offset:0 sc1\n"
"ds_write_b128 v253,v[224:227] offset:36864\n"
PM "v[216:217],v[216:217],v[130:131] op_sel_hi:[1,0]\n"
PM "v[218:219],v[218:219],v[130:131] op_sel_hi:[1,0]\n"
PM "v[220:221],v[220:221],v[130:131] op_sel_hi:[1,0]\n"
PM "v[222:223],v[222:223],v[130:131] op_sel_hi:[1,0]\n"
CV "v228,v216,v217\n"
CV "v229,v218,v219\n"
CV "v230,v220,v221\n"
CV "v231,v222,v223\n"
"buffer_store_dwordx4 v[228:231],v246,s[28:31],s66 offen offset:1024 sc1\n"
"ds_write_b128 v253,v[228:231] offset:37888\n"
"v_rcp_f32 v130,v168\n"
"s_add_u32 s66,s56,s99\n"
PM "v[136:137],v[136:137],v[130:131] op_sel_hi:[1,0]\n"
PM "v[138:139],v[138:139],v[130:131] op_sel_hi:[1,0]\n"
PM "v[140:141],v[140:141],v[130:131] op_sel_hi:[1,0]\n"
PM "v[142:143],v[142:143],v[130:131] op_sel_hi:[1,0]\n"
CV "v32,v136,v137\n"
CV "v33,v138,v139\n"
CV "v34,v140,v141\n"
CV "v35,v142,v143\n"
"buffer_store_dwordx4 v[32:35],v244,s[28:31],s66 offen offset:0 sc1\n"
PM "v[144:145],v[144:145],v[130:131] op_sel_hi:[1,0]\n"
PM "v[146:147],v[146:147],v[130:131] op_sel_hi:[1,0]\n"
PM "v[148:149],v[148:149],v[130:131] op_sel_hi:[1,0]\n"
PM "v[150:151],v[150:151],v[130:131] op_sel_hi:[1,0]\n"
CV "v36,v144,v145\n"
CV "v37,v146,v147\n"
CV "v38,v148,v149\n"
CV "v39,v150,v151\n"
"buffer_store_dwordx4 v[36:39],v244,s[28:31],s66 offen offset:1024 sc1\n"
PM "v[152:153],v[152:153],v[130:131] op_sel_hi:[1,0]\n"
PM "v[154:155],v[154:155],v[130:131] op_sel_hi:[1,0]\n"
PM "v[156:157],v[156:157],v[130:131] op_sel_hi:[1,0]\n"
PM "v[158:159],v[158:159],v[130:131] op_sel_hi:[1,0]\n"
CV "v40,v152,v153\n"
CV "v41,v154,v155\n"
CV "v42,v156,v157\n"
CV "v43,v158,v159\n"
"buffer_store_dwordx4 v[40:43],v244,s[28:31],s66 offen offset:2048 sc1\n"
PM "v[160:161],v[160:161],v[130:131] op_sel_hi:[1,0]\n"
PM "v[162:163],v[162:163],v[130:131] op_sel_hi:[1,0]\n"
PM "v[164:165],v[164:165],v[130:131] op_sel_hi:[1,0]\n"
PM "v[166:167],v[166:167],v[130:131] op_sel_hi:[1,0]\n"
CV "v44,v160,v161\n"
CV "v45,v162,v163\n"
CV "v46,v164,v165\n"
CV "v47,v166,v167\n"
"buffer_store_dwordx4 v[44:47],v244,s[28:31],s66 offen offset:3072 sc1\n"
"v_add_f32 v128,v168,v204\n"
"v_add_f32 v128,v128,v240\n"
"v_cmp_ngt_f32 vcc,s43,v128\n"
"s_cmp_lg_u64 vcc,0\n"
"s_cselect_b32 s66,1,0\n"
"v_mov_b32 v128,s66\n"
"ds_write_b32 v251,v128 offset:0\n"
"v_rcp_f32 v130,v204\n"
"s_add_u32 s66,s56,s99\n"
PM "v[172:173],v[172:173],v[130:131] op_sel_hi:[1,0]\n"
PM "v[174:175],v[174:175],v[130:131] op_sel_hi:[1,0]\n"
PM "v[176:177],v[176:177],v[130:131] op_sel_hi:[1,0]\n"
PM "v[178:179],v[178:179],v[130:131] op_sel_hi:[1,0]\n"
CV "v48,v172,v173\n"
CV "v49,v174,v175\n"
CV "v50,v176,v177\n"
CV "v51,v178,v179\n"
"buffer_store_dwordx4 v[48:51],v245,s[28:31],s66 offen offset:0 sc1\n"
PM "v[180:181],v[180:181],v[130:131] op_sel_hi:[1,0]\n"
PM "v[182:183],v[182:183],v[130:131] op_sel_hi:[1,0]\n"
PM "v[184:185],v[184:185],v[130:131] op_sel_hi:[1,0]\n"
PM "v[186:187],v[186:187],v[130:131] op_sel_hi:[1,0]\n"
CV "v52,v180,v181\n"
CV "v53,v182,v183\n"
CV "v54,v184,v185\n"
CV "v55,v186,v187\n"
"buffer_store_dwordx4 v[52:55],v245,s[28:31],s66 offen offset:1024 sc1\n"
PM "v[188:189],v[188:189],v[130:131] op_sel_hi:[1,0]\n"
PM "v[190:191],v[190:191],v[130:131] op_sel_hi:[1,0]\n"
PM "v[192:193],v[192:193],v[130:131] op_sel_hi:[1,0]\n"
PM "v[194:195],v[194:195],v[130:131] op_sel_hi:[1,0]\n"
CV "v56,v188,v189\n"
CV "v57,v190,v191\n"
CV "v58,v192,v193\n"
CV "v59,v194,v195\n"
"buffer_store_dwordx4 v[56:59],v245,s[28:31],s66 offen offset:2048 sc1\n"
PM "v[196:197],v[196:197],v[130:131] op_sel_hi:[1,0]\n"
PM "v[198:199],v[198:199],v[130:131] op_sel_hi:[1,0]\n"
PM "v[200:201],v[200:201],v[130:131] op_sel_hi:[1,0]\n"
PM "v[202:203],v[202:203],v[130:131] op_sel_hi:[1,0]\n"
CV "v60,v196,v197\n"
CV "v61,v198,v199\n"
CV "v62,v200,v201\n"
CV "v63,v202,v203\n"
"buffer_store_dwordx4 v[60:63],v245,s[28:31],s66 offen offset:3072 sc1\n"
"v_mov_b64 v[136:137],0\n"
"v_mov_b64 v[138:139],0\n"
"v_mov_b64 v[140:141],0\n"
"v_mov_b64 v[142:143],0\n"
"v_mov_b64 v[144:145],0\n"
"v_mov_b64 v[146:147],0\n"
"v_mov_b64 v[148:149],0\n"
"v_mov_b64 v[150:151],0\n"
"v_mov_b64 v[152:153],0\n"
"v_mov_b64 v[154:155],0\n"
"v_mov_b64 v[156:157],0\n"
"v_mov_b64 v[158:159],0\n"
"v_mov_b64 v[160:161],0\n"
"v_mov_b64 v[162:163],0\n"
"v_mov_b64 v[164:165],0\n"
"v_mov_b64 v[166:167],0\n"
"v_mov_b64 v[168:169],0\n"
"v_mov_b64 v[170:171],0\n"
"v_mov_b64 v[208:209],0\n"
"v_mov_b64 v[210:211],0\n"
"v_mov_b64 v[212:213],0\n"
"v_mov_b64 v[214:215],0\n"
"v_mov_b64 v[216:217],0\n"
"v_mov_b64 v[218:219],0\n"
"v_mov_b64 v[220:221],0\n"
"v_mov_b64 v[222:223],0\n"
"v_mov_b64 v[240:241],0\n"
"v_mov_b64 v[242:243],0\n"
"v_mov_b64 v[172:173],0\n"
"v_mov_b64 v[174:175],0\n"
"v_mov_b64 v[176:177],0\n"
"v_mov_b64 v[178:179],0\n"
"v_mov_b64 v[180:181],0\n"
"v_mov_b64 v[182:183],0\n"
"v_mov_b64 v[184:185],0\n"
"v_mov_b64 v[186:187],0\n"
"v_mov_b64 v[188:189],0\n"
"v_mov_b64 v[190:191],0\n"
"v_mov_b64 v[192:193],0\n"
"v_mov_b64 v[194:195],0\n"
"v_mov_b64 v[196:197],0\n"
"v_mov_b64 v[198:199],0\n"
"v_mov_b64 v[200:201],0\n"
"v_mov_b64 v[202:203],0\n"
"v_mov_b64 v[204:205],0\n"
"v_mov_b64 v[206:207],0\n"
"s_mov_b32 s52,204800\n"
"Ll361_%=:\n"
MF "v[80:83],a[64:67],a[0:3],v[104:107]\n"
"s_add_u32 m0,s50,s48\n"
DT "a[112:113],v7 offset:128\n"
CV "v120,v88,v89\n"
MF "v[84:87],a[68:71],a[0:3],v[104:107]\n"
DT "a[114:115],v7 offset:640\n"
BL "v8,s[12:15],s78 offen nt lds\n"
MF "v[80:83],a[72:75],a[4:7],v[80:83]\n"
DT "a[116:117],v7 offset:160\n"
CV "v121,v90,v91\n"
MF "v[84:87],a[76:79],a[4:7],v[84:87]\n"
DT "a[118:119],v7 offset:672\n"
CV "v122,v92,v93\n"
MF "v[80:83],a[80:83],a[8:11],v[80:83]\n"
DT "a[120:121],v7 offset:192\n"
CV "v123,v94,v95\n"
MF "v[84:87],a[84:87],a[8:11],v[84:87]\n"
DT "a[122:123],v7 offset:704\n"
BL "v9,s[12:15],s78 offen offset:1056 nt lds\n"
MF "v[80:83],a[88:91],a[12:15],v[80:83]\n"
"s_add_u32 m0,m0,8448\n"
DT "a[124:125],v7 offset:224\n"
MF "v[84:87],a[92:95],a[12:15],v[84:87]\n"
DT "a[126:127],v7 offset:736\n"
MF "v[88:91],a[64:67],a[16:19],v[108:111]\n"
"v_add_u32 v6,s49,v4\n"
"v_add_u32 v7,s49,v5\n"
MF "v[92:95],a[68:71],a[16:19],v[108:111]\n"
"s_mov_b32 s50,s42\n"
BL "v10,s[16:19],s79 offen nt lds\n"
EX "v80,v80\n"
MF "v[88:91],a[72:75],a[20:23],v[88:91]\n"
"s_mov_b32 s42,s49\n"
EX "v81,v81\n"
MF "v[92:95],a[76:79],a[20:23],v[92:95]\n"
EX "v82,v82\n"
MF "v[88:91],a[80:83],a[24:27],v[88:91]\n"
EX "v83,v83\n"
MF "v[92:95],a[84:87],a[24:27],v[92:95]\n"
BL "v11,s[16:19],s79 offen offset:1056 nt lds\n"
EX "v84,v84\n"
MF "v[88:91],a[88:91],a[28:31],v[88:91]\n"
"s_add_u32 s51,s51,8192\n"
EX "v85,v85\n"
MF "v[92:95],a[92:95],a[28:31],v[92:95]\n"
"s_mov_b32 s79,s78\n"
WT "vmcnt(10) lgkmcnt(0)\n"
"s_barrier\n"
MF "v[208:211],a[96:99],v[124:127],v[208:211]\n"
"s_xor_b32 s78,s51,s41\n"
DR "a[64:67],v6 offset:0\n"
EX "v86,v86\n"
MF "v[212:215],a[100:103],v[124:127],v[212:215]\n"
DR "a[68:71],v6 offset:512\n"
EX "v87,v87\n"
MF "v[216:219],a[104:107],v[124:127],v[216:219]\n"
DR "a[72:75],v6 offset:64\n"
MF "v[220:223],a[108:111],v[124:127],v[220:223]\n"
DR "a[76:79],v6 offset:576\n"
EX "v88,v88\n"
MF "v[240:243],a[48:51],v[124:127],v[240:243]\n"
DR "a[80:83],v6 offset:128\n"
EX "v89,v89\n"
MF "v[136:139],a[96:99],v[116:119],v[136:139]\n"
DR "a[84:87],v6 offset:640\n"
MF "v[140:143],a[100:103],v[116:119],v[140:143]\n"
DR "a[88:91],v6 offset:192\n"
MF "v[144:147],a[104:107],v[116:119],v[144:147]\n"
DR "a[92:95],v6 offset:704\n"
MF "v[148:151],a[108:111],v[116:119],v[148:151]\n"
DR "v[124:127],v254\n"
"v_xor_b32 v254,0x400,v254\n"
MF "v[152:155],a[112:115],v[116:119],v[152:155]\n"
EX "v90,v90\n"
MF "v[156:159],a[116:119],v[116:119],v[156:159]\n"
EX "v91,v91\n"
MF "v[160:163],a[120:123],v[116:119],v[160:163]\n"
"s_add_u32 s49,s49,16896\n"
EX "v92,v92\n"
MF "v[164:167],a[124:127],v[116:119],v[164:167]\n"
"s_cmp_eq_u32 s49,84480\n"
EX "v93,v93\n"
MF "v[168:171],a[48:51],v[116:119],v[168:171]\n"
"s_cselect_b32 s49,0,s49\n"
EX "v94,v94\n"
MF "v[172:175],a[96:99],v[120:123],v[172:175]\n"
EX "v95,v95\n"
MF "v[176:179],a[100:103],v[120:123],v[176:179]\n"
DT "a[96:97],v7 offset:0\n"
CV "v116,v80,v81\n"
MF "v[180:183],a[104:107],v[120:123],v[180:183]\n"
DT "a[98:99],v7 offset:512\n"
CV "v117,v82,v83\n"
MF "v[184:187],a[108:111],v[120:123],v[184:187]\n"
DT "a[100:101],v7 offset:32\n"
CV "v118,v84,v85\n"
MF "v[188:191],a[112:115],v[120:123],v[188:191]\n"
DT "a[102:103],v7 offset:544\n"
CV "v119,v86,v87\n"
MF "v[192:195],a[116:119],v[120:123],v[192:195]\n"
DT "a[104:105],v7 offset:64\n"
MF "v[196:199],a[120:123],v[120:123],v[196:199]\n"
DT "a[106:107],v7 offset:576\n"
MF "v[200:203],a[124:127],v[120:123],v[200:203]\n"
WT "lgkmcnt(6)\n"
DT "a[108:109],v7 offset:96\n"
MF "v[204:207],a[48:51],v[120:123],v[204:207]\n"
"s_cmp_lg_u32 s51,s52\n"
DT "a[110:111],v7 offset:608\n"
"s_cbranch_scc1 Ll361_%=\n"
DR "v[128:131],v252 offset:0\n"
DR "v[64:67],v253 offset:36864\n"
DR "v[68:71],v253 offset:37888\n"
DR "v[72:75],v253 offset:38912\n"
DR "v[76:79],v253 offset:39936\n"
WT "lgkmcnt(0)\n"
"v_or3_b32 v128,v128,v129,v130\n"
"v_or_b32 v128,v128,v131\n"
"v_cmp_ne_u32 vcc,0,v128\n"
"s_cbranch_vccnz Lr362_%=\n"
"s_add_u32 m0,s76,84480\n"
"s_add_u32 s67,s68,0\n"
BL "v12,s[24:27],s67 offen offset:0 lds\n"
BL "v13,s[24:27],s67 offen offset:1024 lds\n"
BL "v14,s[24:27],s67 offen offset:2048 lds\n"
"s_add_u32 m0,s76,87552\n"
"s_add_u32 s67,s68,0\n"
BL "v15,s[24:27],s67 offen offset:0 lds\n"
BL "v16,s[24:27],s67 offen offset:1024 lds\n"
BL "v17,s[24:27],s67 offen offset:2048 lds\n"
"s_add_u32 m0,s76,90624\n"
"s_add_u32 s67,s68,0\n"
BL "v18,s[24:27],s67 offen offset:0 lds\n"
BL "v19,s[24:27],s67 offen offset:1024 lds\n"
BL "v20,s[24:27],s67 offen offset:2048 lds\n"
MF "v[80:83],a[64:67],a[0:3],v[104:107]\n"
"s_add_u32 m0,s50,s48\n"
DT "a[112:113],v7 offset:128\n"
CV "v120,v88,v89\n"
MF "v[84:87],a[68:71],a[0:3],v[104:107]\n"
DT "a[114:115],v7 offset:640\n"
BL "v8,s[12:15],s78 offen nt lds\n"
MF "v[80:83],a[72:75],a[4:7],v[80:83]\n"
DT "a[116:117],v7 offset:160\n"
CV "v121,v90,v91\n"
MF "v[84:87],a[76:79],a[4:7],v[84:87]\n"
DT "a[118:119],v7 offset:672\n"
CV "v122,v92,v93\n"
MF "v[80:83],a[80:83],a[8:11],v[80:83]\n"
DT "a[120:121],v7 offset:192\n"
CV "v123,v94,v95\n"
MF "v[84:87],a[84:87],a[8:11],v[84:87]\n"
DT "a[122:123],v7 offset:704\n"
MF "v[80:83],a[88:91],a[12:15],v[80:83]\n"
DT "a[124:125],v7 offset:224\n"
MF "v[84:87],a[92:95],a[12:15],v[84:87]\n"
DT "a[126:127],v7 offset:736\n"
MF "v[88:91],a[64:67],a[16:19],v[108:111]\n"
"v_add_u32 v6,s49,v4\n"
"v_add_u32 v7,s49,v5\n"
MF "v[92:95],a[68:71],a[16:19],v[108:111]\n"
"s_mov_b32 s50,s42\n"
BL "v9,s[12:15],s78 offen offset:1056 nt lds\n"
EX "v80,v80\n"
MF "v[88:91],a[72:75],a[20:23],v[88:91]\n"
"s_add_u32 m0,m0,8448\n"
"s_mov_b32 s42,s49\n"
EX "v81,v81\n"
MF "v[92:95],a[76:79],a[20:23],v[92:95]\n"
EX "v82,v82\n"
MF "v[88:91],a[80:83],a[24:27],v[88:91]\n"
EX "v83,v83\n"
MF "v[92:95],a[84:87],a[24:27],v[92:95]\n"
EX "v84,v84\n"
MF "v[88:91],a[88:91],a[28:31],v[88:91]\n"
EX "v85,v85\n"
MF "v[92:95],a[92:95],a[28:31],v[92:95]\n"
WT "vmcnt(19)\n"
WT "lgkmcnt(0)\n"
"s_barrier\n"
MF "v[208:211],a[96:99],v[124:127],v[208:211]\n"
DR "a[64:67],v6 offset:0\n"
EX "v86,v86\n"
MF "v[212:215],a[100:103],v[124:127],v[212:215]\n"
DR "a[68:71],v6 offset:512\n"
EX "v87,v87\n"
MF "v[216:219],a[104:107],v[124:127],v[216:219]\n"
DR "a[72:75],v6 offset:64\n"
BL "v10,s[16:19],s79 offen nt lds\n"
MF "v[220:223],a[108:111],v[124:127],v[220:223]\n"
DR "a[76:79],v6 offset:576\n"
EX "v88,v88\n"
MF "v[240:243],a[48:51],v[124:127],v[240:243]\n"
DR "a[80:83],v6 offset:128\n"
EX "v89,v89\n"
MF "v[136:139],a[96:99],v[116:119],v[136:139]\n"
DR "a[84:87],v6 offset:640\n"
MF "v[140:143],a[100:103],v[116:119],v[140:143]\n"
DR "a[88:91],v6 offset:192\n"
MF "v[144:147],a[104:107],v[116:119],v[144:147]\n"
DR "a[92:95],v6 offset:704\n"
MF "v[148:151],a[108:111],v[116:119],v[148:151]\n"
DR "v[124:127],v254\n"
"v_xor_b32 v254,0x400,v254\n"
MF "v[152:155],a[112:115],v[116:119],v[152:155]\n"
EX "v90,v90\n"
MF "v[156:159],a[116:119],v[116:119],v[156:159]\n"
EX "v91,v91\n"
MF "v[160:163],a[120:123],v[116:119],v[160:163]\n"
"s_add_u32 s49,s49,16896\n"
BL "v11,s[16:19],s79 offen offset:1056 nt lds\n"
EX "v92,v92\n"
MF "v[164:167],a[124:127],v[116:119],v[164:167]\n"
"s_add_u32 s51,s51,8192\n"
"s_mov_b32 s79,s78\n"
"s_xor_b32 s78,s51,s41\n"
"s_cmp_eq_u32 s49,84480\n"
EX "v93,v93\n"
MF "v[168:171],a[48:51],v[116:119],v[168:171]\n"
"s_cselect_b32 s49,0,s49\n"
EX "v94,v94\n"
MF "v[172:175],a[96:99],v[120:123],v[172:175]\n"
EX "v95,v95\n"
MF "v[176:179],a[100:103],v[120:123],v[176:179]\n"
DT "a[96:97],v7 offset:0\n"
CV "v116,v80,v81\n"
MF "v[180:183],a[104:107],v[120:123],v[180:183]\n"
DT "a[98:99],v7 offset:512\n"
CV "v117,v82,v83\n"
MF "v[184:187],a[108:111],v[120:123],v[184:187]\n"
DT "a[100:101],v7 offset:32\n"
CV "v118,v84,v85\n"
MF "v[188:191],a[112:115],v[120:123],v[188:191]\n"
DT "a[102:103],v7 offset:544\n"
CV "v119,v86,v87\n"
MF "v[192:195],a[116:119],v[120:123],v[192:195]\n"
DT "a[104:105],v7 offset:64\n"
MF "v[196:199],a[120:123],v[120:123],v[196:199]\n"
DT "a[106:107],v7 offset:576\n"
"buffer_load_dword v31,v26,s[28:31],0 offen offset:0 sc1\n"
MF "v[200:203],a[124:127],v[120:123],v[200:203]\n"
WT "lgkmcnt(6)\n"
DT "a[108:109],v7 offset:96\n"
MF "v[204:207],a[48:51],v[120:123],v[204:207]\n"
DT "a[110:111],v7 offset:608\n"
MF "v[80:83],a[64:67],a[0:3],v[104:107]\n"
"s_add_u32 m0,s50,s48\n"
DT "a[112:113],v7 offset:128\n"
CV "v120,v88,v89\n"
MF "v[84:87],a[68:71],a[0:3],v[104:107]\n"
DT "a[114:115],v7 offset:640\n"
BL "v8,s[12:15],s78 offen nt lds\n"
MF "v[80:83],a[72:75],a[4:7],v[80:83]\n"
DT "a[116:117],v7 offset:160\n"
CV "v121,v90,v91\n"
MF "v[84:87],a[76:79],a[4:7],v[84:87]\n"
DT "a[118:119],v7 offset:672\n"
CV "v122,v92,v93\n"
MF "v[80:83],a[80:83],a[8:11],v[80:83]\n"
DT "a[120:121],v7 offset:192\n"
CV "v123,v94,v95\n"
MF "v[84:87],a[84:87],a[8:11],v[84:87]\n"
DT "a[122:123],v7 offset:704\n"
BL "v9,s[12:15],s78 offen offset:1056 nt lds\n"
MF "v[80:83],a[88:91],a[12:15],v[80:83]\n"
"s_add_u32 m0,m0,8448\n"
DT "a[124:125],v7 offset:224\n"
MF "v[84:87],a[92:95],a[12:15],v[84:87]\n"
DT "a[126:127],v7 offset:736\n"
MF "v[88:91],a[64:67],a[16:19],v[108:111]\n"
"v_add_u32 v6,s49,v4\n"
"v_add_u32 v7,s49,v5\n"
MF "v[92:95],a[68:71],a[16:19],v[108:111]\n"
"s_mov_b32 s50,s42\n"
BL "v10,s[16:19],s79 offen nt lds\n"
EX "v80,v80\n"
MF "v[88:91],a[72:75],a[20:23],v[88:91]\n"
"s_mov_b32 s42,s49\n"
EX "v81,v81\n"
MF "v[92:95],a[76:79],a[20:23],v[92:95]\n"
EX "v82,v82\n"
MF "v[88:91],a[80:83],a[24:27],v[88:91]\n"
EX "v83,v83\n"
MF "v[92:95],a[84:87],a[24:27],v[92:95]\n"
BL "v11,s[16:19],s79 offen offset:1056 nt lds\n"
EX "v84,v84\n"
MF "v[88:91],a[88:91],a[28:31],v[88:91]\n"
"s_add_u32 s51,s51,8192\n"
EX "v85,v85\n"
MF "v[92:95],a[92:95],a[28:31],v[92:95]\n"
"s_mov_b32 s79,s78\n"
WT "vmcnt(22)\n"
WT "vmcnt(9)\n"
WT "lgkmcnt(0)\n"
"s_barrier\n"
MF "v[208:211],a[96:99],v[124:127],v[208:211]\n"
"s_xor_b32 s78,s51,s41\n"
DR "a[64:67],v6 offset:0\n"
EX "v86,v86\n"
MF "v[212:215],a[100:103],v[124:127],v[212:215]\n"
DR "a[68:71],v6 offset:512\n"
EX "v87,v87\n"
MF "v[216:219],a[104:107],v[124:127],v[216:219]\n"
DR "a[72:75],v6 offset:64\n"
MF "v[220:223],a[108:111],v[124:127],v[220:223]\n"
DR "a[76:79],v6 offset:576\n"
EX "v88,v88\n"
MF "v[240:243],a[48:51],v[124:127],v[240:243]\n"
DR "a[80:83],v6 offset:128\n"
EX "v89,v89\n"
MF "v[136:139],a[96:99],v[116:119],v[136:139]\n"
DR "a[84:87],v6 offset:640\n"
MF "v[140:143],a[100:103],v[116:119],v[140:143]\n"
DR "a[88:91],v6 offset:192\n"
MF "v[144:147],a[104:107],v[116:119],v[144:147]\n"
DR "a[92:95],v6 offset:704\n"
MF "v[148:151],a[108:111],v[116:119],v[148:151]\n"
DR "v[124:127],v254\n"
"v_xor_b32 v254,0x400,v254\n"
MF "v[152:155],a[112:115],v[116:119],v[152:155]\n"
EX "v90,v90\n"
MF "v[156:159],a[116:119],v[116:119],v[156:159]\n"
EX "v91,v91\n"
MF "v[160:163],a[120:123],v[116:119],v[160:163]\n"
"s_add_u32 s49,s49,16896\n"
EX "v92,v92\n"
MF "v[164:167],a[124:127],v[116:119],v[164:167]\n"
"s_cmp_eq_u32 s49,84480\n"
EX "v93,v93\n"
MF "v[168:171],a[48:51],v[116:119],v[168:171]\n"
"s_cselect_b32 s49,0,s49\n"
EX "v94,v94\n"
MF "v[172:175],a[96:99],v[120:123],v[172:175]\n"
EX "v95,v95\n"
MF "v[176:179],a[100:103],v[120:123],v[176:179]\n"
DT "a[96:97],v7 offset:0\n"
CV "v116,v80,v81\n"
MF "v[180:183],a[104:107],v[120:123],v[180:183]\n"
DT "a[98:99],v7 offset:512\n"
CV "v117,v82,v83\n"
MF "v[184:187],a[108:111],v[120:123],v[184:187]\n"
DT "a[100:101],v7 offset:32\n"
CV "v118,v84,v85\n"
MF "v[188:191],a[112:115],v[120:123],v[188:191]\n"
DT "a[102:103],v7 offset:544\n"
CV "v119,v86,v87\n"
MF "v[192:195],a[116:119],v[120:123],v[192:195]\n"
DT "a[104:105],v7 offset:64\n"
MF "v[196:199],a[120:123],v[120:123],v[196:199]\n"
DT "a[106:107],v7 offset:576\n"
MF "v[200:203],a[124:127],v[120:123],v[200:203]\n"
WT "lgkmcnt(6)\n"
DT "a[108:109],v7 offset:96\n"
MF "v[204:207],a[48:51],v[120:123],v[204:207]\n"
DT "a[110:111],v7 offset:608\n"
DT "a[212:213],v21 offset:0\n"
DT "a[214:215],v21 offset:4608\n"
DT "a[216:217],v21 offset:32\n"
DT "a[218:219],v21 offset:4640\n"
DT "a[220:221],v22 offset:0\n"
DT "a[222:223],v22 offset:4608\n"
DT "a[224:225],v22 offset:32\n"
DT "a[226:227],v22 offset:4640\n"
DT "a[228:229],v23 offset:0\n"
DT "a[230:231],v23 offset:4608\n"
DT "a[232:233],v23 offset:32\n"
DT "a[234:235],v23 offset:4640\n"
DT "a[236:237],v24 offset:0\n"
DT "a[238:239],v24 offset:4608\n"
DT "a[240:241],v24 offset:32\n"
DT "a[242:243],v24 offset:4640\n"
DT "a[244:245],v25 offset:0\n"
DT "a[246:247],v25 offset:4608\n"
WT "vmcnt(4)\n"
"v_cmp_eq_u32 vcc,v31,v28\n"
"s_andn2_b64 vcc,vcc,s[62:63]\n"
"s_cbranch_scc1 Lp363_%=\n"
"Lp364_%=:\n"
MF "v[80:83],a[64:67],a[0:3],v[104:107]\n"
"s_add_u32 m0,s50,s48\n"
DT "a[112:113],v7 offset:128\n"
CV "v120,v88,v89\n"
MF "v[84:87],a[68:71],a[0:3],v[104:107]\n"
DT "a[114:115],v7 offset:640\n"
BL "v8,s[12:15],s78 offen nt lds\n"
MF "v[80:83],a[72:75],a[4:7],v[80:83]\n"
DT "a[116:117],v7 offset:160\n"
CV "v121,v90,v91\n"
MF "v[84:87],a[76:79],a[4:7],v[84:87]\n"
DT "a[118:119],v7 offset:672\n"
CV "v122,v92,v93\n"
MF "v[80:83],a[80:83],a[8:11],v[80:83]\n"
DT "a[120:121],v7 offset:192\n"
CV "v123,v94,v95\n"
MF "v[84:87],a[84:87],a[8:11],v[84:87]\n"
DT "a[122:123],v7 offset:704\n"
BL "v9,s[12:15],s78 offen offset:1056 nt lds\n"
MF "v[80:83],a[88:91],a[12:15],v[80:83]\n"
"s_add_u32 m0,m0,8448\n"
DT "a[124:125],v7 offset:224\n"
MF "v[84:87],a[92:95],a[12:15],v[84:87]\n"
DT "a[126:127],v7 offset:736\n"
MF "v[88:91],a[64:67],a[16:19],v[108:111]\n"
"v_add_u32 v6,s49,v4\n"
"v_add_u32 v7,s49,v5\n"
MF "v[92:95],a[68:71],a[16:19],v[108:111]\n"
"s_mov_b32 s50,s42\n"
BL "v10,s[16:19],s79 offen nt lds\n"
EX "v80,v80\n"
MF "v[88:91],a[72:75],a[20:23],v[88:91]\n"
"s_mov_b32 s42,s49\n"
EX "v81,v81\n"
MF "v[92:95],a[76:79],a[20:23],v[92:95]\n"
EX "v82,v82\n"
MF "v[88:91],a[80:83],a[24:27],v[88:91]\n"
EX "v83,v83\n"
MF "v[92:95],a[84:87],a[24:27],v[92:95]\n"
BL "v11,s[16:19],s79 offen offset:1056 nt lds\n"
EX "v84,v84\n"
MF "v[88:91],a[88:91],a[28:31],v[88:91]\n"
"s_add_u32 s51,s51,8192\n"
EX "v85,v85\n"
MF "v[92:95],a[92:95],a[28:31],v[92:95]\n"
"s_mov_b32 s79,s78\n"
WT "vmcnt(22)\n"
WT "lgkmcnt(0)\n"
"s_barrier\n"
MF "v[208:211],a[96:99],v[124:127],v[208:211]\n"
"s_xor_b32 s78,s51,s41\n"
DR "a[64:67],v6 offset:0\n"
EX "v86,v86\n"
MF "v[212:215],a[100:103],v[124:127],v[212:215]\n"
DR "a[68:71],v6 offset:512\n"
EX "v87,v87\n"
MF "v[216:219],a[104:107],v[124:127],v[216:219]\n"
DR "a[72:75],v6 offset:64\n"
MF "v[220:223],a[108:111],v[124:127],v[220:223]\n"
DR "a[76:79],v6 offset:576\n"
EX "v88,v88\n"
MF "v[240:243],a[48:51],v[124:127],v[240:243]\n"
DR "a[80:83],v6 offset:128\n"
EX "v89,v89\n"
MF "v[136:139],a[96:99],v[116:119],v[136:139]\n"
DR "a[84:87],v6 offset:640\n"
MF "v[140:143],a[100:103],v[116:119],v[140:143]\n"
DR "a[88:91],v6 offset:192\n"
MF "v[144:147],a[104:107],v[116:119],v[144:147]\n"
DR "a[92:95],v6 offset:704\n"
MF "v[148:151],a[108:111],v[116:119],v[148:151]\n"
DR "v[124:127],v254\n"
"v_xor_b32 v254,0x400,v254\n"
MF "v[152:155],a[112:115],v[116:119],v[152:155]\n"
EX "v90,v90\n"
MF "v[156:159],a[116:119],v[116:119],v[156:159]\n"
EX "v91,v91\n"
MF "v[160:163],a[120:123],v[116:119],v[160:163]\n"
"s_add_u32 s49,s49,16896\n"
EX "v92,v92\n"
MF "v[164:167],a[124:127],v[116:119],v[164:167]\n"
"s_cmp_eq_u32 s49,84480\n"
EX "v93,v93\n"
MF "v[168:171],a[48:51],v[116:119],v[168:171]\n"
"s_cselect_b32 s49,0,s49\n"
EX "v94,v94\n"
MF "v[172:175],a[96:99],v[120:123],v[172:175]\n"
EX "v95,v95\n"
MF "v[176:179],a[100:103],v[120:123],v[176:179]\n"
DT "a[96:97],v7 offset:0\n"
CV "v116,v80,v81\n"
MF "v[180:183],a[104:107],v[120:123],v[180:183]\n"
DT "a[98:99],v7 offset:512\n"
CV "v117,v82,v83\n"
MF "v[184:187],a[108:111],v[120:123],v[184:187]\n"
DT "a[100:101],v7 offset:32\n"
CV "v118,v84,v85\n"
MF "v[188:191],a[112:115],v[120:123],v[188:191]\n"
DT "a[102:103],v7 offset:544\n"
CV "v119,v86,v87\n"
MF "v[192:195],a[116:119],v[120:123],v[192:195]\n"
DT "a[104:105],v7 offset:64\n"
MF "v[196:199],a[120:123],v[120:123],v[196:199]\n"
DT "a[106:107],v7 offset:576\n"
MF "v[200:203],a[124:127],v[120:123],v[200:203]\n"
WT "lgkmcnt(6)\n"
DT "a[108:109],v7 offset:96\n"
MF "v[204:207],a[48:51],v[120:123],v[204:207]\n"
DT "a[110:111],v7 offset:608\n"
MF "a[128:131],a[212:215],v[32:35],0\n"
"s_add_u32 m0,s76,121344\n"
MF "a[164:167],a[212:215],v[48:51],0\n"
"s_add_u32 s67,s69,0\n"
MF "a[200:203],a[212:215],v[64:67],0\n"
DT "a[212:213],v21 offset:9216\n"
BL "v12,s[24:27],s67 offen offset:0 lds\n"
MF "a[132:135],a[216:219],v[32:35],0\n"
DT "a[214:215],v21 offset:13824\n"
MF "a[168:171],a[216:219],v[48:51],0\n"
MF "a[204:207],a[216:219],v[64:67],0\n"
DT "a[216:217],v21 offset:9248\n"
BL "v13,s[24:27],s67 offen offset:1024 lds\n"
MF "a[136:139],a[220:223],v[32:35],0\n"
DT "a[218:219],v21 offset:13856\n"
MF "a[172:175],a[220:223],v[48:51],0\n"
DT "a[220:221],v22 offset:9216\n"
MF "a[140:143],a[224:227],v[32:35],0\n"
DT "a[222:223],v22 offset:13824\n"
BL "v14,s[24:27],s67 offen offset:2048 lds\n"
MF "a[176:179],a[224:227],v[48:51],0\n"
"s_add_u32 m0,s76,124416\n"
DT "a[224:225],v22 offset:9248\n"
MF "a[144:147],a[228:231],v[32:35],0\n"
"s_add_u32 s67,s69,0\n"
DT "a[226:227],v22 offset:13856\n"
MF "a[180:183],a[228:231],v[48:51],0\n"
DT "a[228:229],v23 offset:9216\n"
BL "v15,s[24:27],s67 offen offset:0 lds\n"
MF "a[148:151],a[232:235],v[32:35],0\n"
DT "a[230:231],v23 offset:13824\n"
MF "a[184:187],a[232:235],v[48:51],0\n"
DT "a[232:233],v23 offset:9248\n"
MF "a[152:155],a[236:239],v[32:35],0\n"
DT "a[234:235],v23 offset:13856\n"
BL "v16,s[24:27],s67 offen offset:1024 lds\n"
MF "a[188:191],a[236:239],v[48:51],0\n"
DT "a[236:237],v24 offset:9216\n"
MF "a[156:159],a[240:243],v[32:35],0\n"
DT "a[238:239],v24 offset:13824\n"
MF "a[192:195],a[240:243],v[48:51],0\n"
DT "a[240:241],v24 offset:9248\n"
BL "v17,s[24:27],s67 offen offset:2048 lds\n"
MF "a[160:163],a[244:247],v[32:35],0\n"
DT "a[242:243],v24 offset:13856\n"
MF "a[196:199],a[244:247],v[48:51],0\n"
DT "a[244:245],v25 offset:9216\n"
DT "a[246:247],v25 offset:13824\n"
MF "v[80:83],a[64:67],a[0:3],v[104:107]\n"
"s_add_u32 m0,s50,s48\n"
DT "a[112:113],v7 offset:128\n"
CV "v120,v88,v89\n"
MF "v[84:87],a[68:71],a[0:3],v[104:107]\n"
DT "a[114:115],v7 offset:640\n"
BL "v8,s[12:15],s78 offen nt lds\n"
MF "v[80:83],a[72:75],a[4:7],v[80:83]\n"
DT "a[116:117],v7 offset:160\n"
CV "v121,v90,v91\n"
MF "v[84:87],a[76:79],a[4:7],v[84:87]\n"
DT "a[118:119],v7 offset:672\n"
CV "v122,v92,v93\n"
MF "v[80:83],a[80:83],a[8:11],v[80:83]\n"
DT "a[120:121],v7 offset:192\n"
CV "v123,v94,v95\n"
MF "v[84:87],a[84:87],a[8:11],v[84:87]\n"
DT "a[122:123],v7 offset:704\n"
BL "v9,s[12:15],s78 offen offset:1056 nt lds\n"
MF "v[80:83],a[88:91],a[12:15],v[80:83]\n"
"s_add_u32 m0,m0,8448\n"
DT "a[124:125],v7 offset:224\n"
MF "v[84:87],a[92:95],a[12:15],v[84:87]\n"
DT "a[126:127],v7 offset:736\n"
MF "v[88:91],a[64:67],a[16:19],v[108:111]\n"
"v_add_u32 v6,s49,v4\n"
"v_add_u32 v7,s49,v5\n"
MF "v[92:95],a[68:71],a[16:19],v[108:111]\n"
"s_mov_b32 s50,s42\n"
BL "v10,s[16:19],s79 offen nt lds\n"
EX "v80,v80\n"
MF "v[88:91],a[72:75],a[20:23],v[88:91]\n"
"s_mov_b32 s42,s49\n"
EX "v81,v81\n"
MF "v[92:95],a[76:79],a[20:23],v[92:95]\n"
EX "v82,v82\n"
MF "v[88:91],a[80:83],a[24:27],v[88:91]\n"
EX "v83,v83\n"
MF "v[92:95],a[84:87],a[24:27],v[92:95]\n"
BL "v11,s[16:19],s79 offen offset:1056 nt lds\n"
EX "v84,v84\n"
MF "v[88:91],a[88:91],a[28:31],v[88:91]\n"
"s_add_u32 s51,s51,8192\n"
EX "v85,v85\n"
MF "v[92:95],a[92:95],a[28:31],v[92:95]\n"
"s_mov_b32 s79,s78\n"
WT "vmcnt(19)\n"
WT "lgkmcnt(0)\n"
"s_barrier\n"
MF "v[208:211],a[96:99],v[124:127],v[208:211]\n"
"s_xor_b32 s78,s51,s41\n"
DR "a[64:67],v6 offset:0\n"
EX "v86,v86\n"
MF "v[212:215],a[100:103],v[124:127],v[212:215]\n"
DR "a[68:71],v6 offset:512\n"
EX "v87,v87\n"
MF "v[216:219],a[104:107],v[124:127],v[216:219]\n"
DR "a[72:75],v6 offset:64\n"
MF "v[220:223],a[108:111],v[124:127],v[220:223]\n"
DR "a[76:79],v6 offset:576\n"
EX "v88,v88\n"
MF "v[240:243],a[48:51],v[124:127],v[240:243]\n"
DR "a[80:83],v6 offset:128\n"
EX "v89,v89\n"
MF "v[136:139],a[96:99],v[116:119],v[136:139]\n"
DR "a[84:87],v6 offset:640\n"
MF "v[140:143],a[100:103],v[116:119],v[140:143]\n"
DR "a[88:91],v6 offset:192\n"
MF "v[144:147],a[104:107],v[116:119],v[144:147]\n"
DR "a[92:95],v6 offset:704\n"
MF "v[148:151],a[108:111],v[116:119],v[148:151]\n"
DR "v[124:127],v254\n"
"v_xor_b32 v254,0x400,v254\n"
MF "v[152:155],a[112:115],v[116:119],v[152:155]\n"
EX "v90,v90\n"
MF "v[156:159],a[116:119],v[116:119],v[156:159]\n"
EX "v91,v91\n"
MF "v[160:163],a[120:123],v[116:119],v[160:163]\n"
"s_add_u32 s49,s49,16896\n"
EX "v92,v92\n"
MF "v[164:167],a[124:127],v[116:119],v[164:167]\n"
"s_cmp_eq_u32 s49,84480\n"
EX "v93,v93\n"
MF "v[168:171],a[48:51],v[116:119],v[168:171]\n"
"s_cselect_b32 s49,0,s49\n"
EX "v94,v94\n"
MF "v[172:175],a[96:99],v[120:123],v[172:175]\n"
EX "v95,v95\n"
MF "v[176:179],a[100:103],v[120:123],v[176:179]\n"
DT "a[96:97],v7 offset:0\n"
CV "v116,v80,v81\n"
MF "v[180:183],a[104:107],v[120:123],v[180:183]\n"
DT "a[98:99],v7 offset:512\n"
CV "v117,v82,v83\n"
MF "v[184:187],a[108:111],v[120:123],v[184:187]\n"
DT "a[100:101],v7 offset:32\n"
CV "v118,v84,v85\n"
MF "v[188:191],a[112:115],v[120:123],v[188:191]\n"
DT "a[102:103],v7 offset:544\n"
CV "v119,v86,v87\n"
MF "v[192:195],a[116:119],v[120:123],v[192:195]\n"
DT "a[104:105],v7 offset:64\n"
MF "v[196:199],a[120:123],v[120:123],v[196:199]\n"
DT "a[106:107],v7 offset:576\n"
MF "v[200:203],a[124:127],v[120:123],v[200:203]\n"
WT "lgkmcnt(6)\n"
DT "a[108:109],v7 offset:96\n"
MF "v[204:207],a[48:51],v[120:123],v[204:207]\n"
DT "a[110:111],v7 offset:608\n"
MF "a[128:131],a[212:215],v[36:39],a[128:131]\n"
MF "a[164:167],a[212:215],v[52:55],a[164:167]\n"
"s_add_u32 s44,s57,0\n"
MF "a[200:203],a[212:215],v[68:71],a[200:203]\n"
DT "a[212:213],v21 offset:18432\n"
BL "v[32:35],v244,s[28:31],s44 offen offset:0 sc1\n"
MF "a[132:135],a[216:219],v[36:39],a[132:135]\n"
DT "a[214:215],v21 offset:23040\n"
MF "a[168:171],a[216:219],v[52:55],a[168:171]\n"
MF "a[204:207],a[216:219],v[68:71],a[204:207]\n"
DT "a[216:217],v21 offset:18464\n"
BL "v[48:51],v245,s[28:31],s44 offen offset:0 sc1\n"
MF "a[136:139],a[220:223],v[36:39],a[136:139]\n"
DT "a[218:219],v21 offset:23072\n"
MF "a[172:175],a[220:223],v[52:55],a[172:175]\n"
DT "a[220:221],v22 offset:18432\n"
MF "a[140:143],a[224:227],v[36:39],a[140:143]\n"
DT "a[222:223],v22 offset:23040\n"
BL "v[64:67],v246,s[28:31],s44 offen offset:0 sc1\n"
MF "a[176:179],a[224:227],v[52:55],a[176:179]\n"
"s_add_u32 m0,s76,127488\n"
DT "a[224:225],v22 offset:18464\n"
MF "a[144:147],a[228:231],v[36:39],a[144:147]\n"
"s_add_u32 s67,s69,0\n"
DT "a[226:227],v22 offset:23072\n"
MF "a[180:183],a[228:231],v[52:55],a[180:183]\n"
DT "a[228:229],v23 offset:18432\n"
BL "v18,s[24:27],s67 offen offset:0 lds\n"
MF "a[148:151],a[232:235],v[36:39],a[148:151]\n"
DT "a[230:231],v23 offset:23040\n"
MF "a[184:187],a[232:235],v[52:55],a[184:187]\n"
DT "a[232:233],v23 offset:18464\n"
MF "a[152:155],a[236:239],v[36:39],a[152:155]\n"
DT "a[234:235],v23 offset:23072\n"
BL "v19,s[24:27],s67 offen offset:1024 lds\n"
MF "a[188:191],a[236:239],v[52:55],a[188:191]\n"
DT "a[236:237],v24 offset:18432\n"
MF "a[156:159],a[240:243],v[36:39],a[156:159]\n"
DT "a[238:239],v24 offset:23040\n"
MF "a[192:195],a[240:243],v[52:55],a[192:195]\n"
DT "a[240:241],v24 offset:18464\n"
BL "v20,s[24:27],s67 offen offset:2048 lds\n"
MF "a[160:163],a[244:247],v[36:39],a[160:163]\n"
DT "a[242:243],v24 offset:23072\n"
MF "a[196:199],a[244:247],v[52:55],a[196:199]\n"
DT "a[244:245],v25 offset:18432\n"
DT "a[246:247],v25 offset:23040\n"
MF "v[80:83],a[64:67],a[0:3],v[104:107]\n"
"s_add_u32 m0,s50,s48\n"
DT "a[112:113],v7 offset:128\n"
CV "v120,v88,v89\n"
MF "v[84:87],a[68:71],a[0:3],v[104:107]\n"
DT "a[114:115],v7 offset:640\n"
BL "v8,s[12:15],s78 offen nt lds\n"
MF "v[80:83],a[72:75],a[4:7],v[80:83]\n"
DT "a[116:117],v7 offset:160\n"
CV "v121,v90,v91\n"
MF "v[84:87],a[76:79],a[4:7],v[84:87]\n"
DT "a[118:119],v7 offset:672\n"
CV "v122,v92,v93\n"
MF "v[80:83],a[80:83],a[8:11],v[80:83]\n"
DT "a[120:121],v7 offset:192\n"
CV "v123,v94,v95\n"
MF "v[84:87],a[84:87],a[8:11],v[84:87]\n"
DT "a[122:123],v7 offset:704\n"
BL "v9,s[12:15],s78 offen offset:1056 nt lds\n"
MF "v[80:83],a[88:91],a[12:15],v[80:83]\n"
"s_add_u32 m0,m0,8448\n"
DT "a[124:125],v7 offset:224\n"
MF "v[84:87],a[92:95],a[12:15],v[84:87]\n"
DT "a[126:127],v7 offset:736\n"
MF "v[88:91],a[64:67],a[16:19],v[108:111]\n"
"v_add_u32 v6,s49,v4\n"
"v_add_u32 v7,s49,v5\n"
MF "v[92:95],a[68:71],a[16:19],v[108:111]\n"
"s_mov_b32 s50,s42\n"
BL "v10,s[16:19],s79 offen nt lds\n"
EX "v80,v80\n"
MF "v[88:91],a[72:75],a[20:23],v[88:91]\n"
"s_mov_b32 s42,s49\n"
EX "v81,v81\n"
MF "v[92:95],a[76:79],a[20:23],v[92:95]\n"
EX "v82,v82\n"
MF "v[88:91],a[80:83],a[24:27],v[88:91]\n"
EX "v83,v83\n"
MF "v[92:95],a[84:87],a[24:27],v[92:95]\n"
BL "v11,s[16:19],s79 offen offset:1056 nt lds\n"
EX "v84,v84\n"
MF "v[88:91],a[88:91],a[28:31],v[88:91]\n"
"s_add_u32 s51,s51,8192\n"
EX "v85,v85\n"
MF "v[92:95],a[92:95],a[28:31],v[92:95]\n"
"s_mov_b32 s79,s78\n"
WT "vmcnt(24)\n"
WT "lgkmcnt(0)\n"
"s_barrier\n"
MF "v[208:211],a[96:99],v[124:127],v[208:211]\n"
"s_xor_b32 s78,s51,s41\n"
DR "a[64:67],v6 offset:0\n"
EX "v86,v86\n"
MF "v[212:215],a[100:103],v[124:127],v[212:215]\n"
DR "a[68:71],v6 offset:512\n"
EX "v87,v87\n"
MF "v[216:219],a[104:107],v[124:127],v[216:219]\n"
DR "a[72:75],v6 offset:64\n"
MF "v[220:223],a[108:111],v[124:127],v[220:223]\n"
DR "a[76:79],v6 offset:576\n"
EX "v88,v88\n"
MF "v[240:243],a[48:51],v[124:127],v[240:243]\n"
DR "a[80:83],v6 offset:128\n"
EX "v89,v89\n"
MF "v[136:139],a[96:99],v[116:119],v[136:139]\n"
DR "a[84:87],v6 offset:640\n"
MF "v[140:143],a[100:103],v[116:119],v[140:143]\n"
DR "a[88:91],v6 offset:192\n"
MF "v[144:147],a[104:107],v[116:119],v[144:147]\n"
DR "a[92:95],v6 offset:704\n"
MF "v[148:151],a[108:111],v[116:119],v[148:151]\n"
DR "v[124:127],v254\n"
"v_xor_b32 v254,0x400,v254\n"
MF "v[152:155],a[112:115],v[116:119],v[152:155]\n"
EX "v90,v90\n"
MF "v[156:159],a[116:119],v[116:119],v[156:159]\n"
EX "v91,v91\n"
MF "v[160:163],a[120:123],v[116:119],v[160:163]\n"
"s_add_u32 s49,s49,16896\n"
EX "v92,v92\n"
MF "v[164:167],a[124:127],v[116:119],v[164:167]\n"
"s_cmp_eq_u32 s49,84480\n"
EX "v93,v93\n"
MF "v[168:171],a[48:51],v[116:119],v[168:171]\n"
"s_cselect_b32 s49,0,s49\n"
EX "v94,v94\n"
MF "v[172:175],a[96:99],v[120:123],v[172:175]\n"
EX "v95,v95\n"
MF "v[176:179],a[100:103],v[120:123],v[176:179]\n"
DT "a[96:97],v7 offset:0\n"
CV "v116,v80,v81\n"
MF "v[180:183],a[104:107],v[120:123],v[180:183]\n"
DT "a[98:99],v7 offset:512\n"
CV "v117,v82,v83\n"
MF "v[184:187],a[108:111],v[120:123],v[184:187]\n"
DT "a[100:101],v7 offset:32\n"
CV "v118,v84,v85\n"
MF "v[188:191],a[112:115],v[120:123],v[188:191]\n"
DT "a[102:103],v7 offset:544\n"
CV "v119,v86,v87\n"
MF "v[192:195],a[116:119],v[120:123],v[192:195]\n"
DT "a[104:105],v7 offset:64\n"
MF "v[196:199],a[120:123],v[120:123],v[196:199]\n"
DT "a[106:107],v7 offset:576\n"
MF "v[200:203],a[124:127],v[120:123],v[200:203]\n"
WT "lgkmcnt(6)\n"
DT "a[108:109],v7 offset:96\n"
MF "v[204:207],a[48:51],v[120:123],v[204:207]\n"
DT "a[110:111],v7 offset:608\n"
MF "a[128:131],a[212:215],v[40:43],a[128:131]\n"
MF "a[164:167],a[212:215],v[56:59],a[164:167]\n"
"s_add_u32 s44,s57,0\n"
MF "a[200:203],a[212:215],v[72:75],a[200:203]\n"
DT "a[212:213],v21 offset:27648\n"
BL "v[36:39],v244,s[28:31],s44 offen offset:1024 sc1\n"
MF "a[132:135],a[216:219],v[40:43],a[132:135]\n"
DT "a[214:215],v21 offset:32256\n"
MF "a[168:171],a[216:219],v[56:59],a[168:171]\n"
MF "a[204:207],a[216:219],v[72:75],a[204:207]\n"
DT "a[216:217],v21 offset:27680\n"
MF "a[136:139],a[220:223],v[40:43],a[136:139]\n"
DT "a[218:219],v21 offset:32288\n"
"v_add_u32 v21,36864,v21\n"
MF "a[172:175],a[220:223],v[56:59],a[172:175]\n"
DT "a[220:221],v22 offset:27648\n"
MF "a[140:143],a[224:227],v[40:43],a[140:143]\n"
DT "a[222:223],v22 offset:32256\n"
BL "v[52:55],v245,s[28:31],s44 offen offset:1024 sc1\n"
MF "a[176:179],a[224:227],v[56:59],a[176:179]\n"
DT "a[224:225],v22 offset:27680\n"
MF "a[144:147],a[228:231],v[40:43],a[144:147]\n"
DT "a[226:227],v22 offset:32288\n"
"v_add_u32 v22,36864,v22\n"
MF "a[180:183],a[228:231],v[56:59],a[180:183]\n"
DT "a[228:229],v23 offset:27648\n"
MF "a[148:151],a[232:235],v[40:43],a[148:151]\n"
DT "a[230:231],v23 offset:32256\n"
MF "a[184:187],a[232:235],v[56:59],a[184:187]\n"
DT "a[232:233],v23 offset:27680\n"
MF "a[152:155],a[236:239],v[40:43],a[152:155]\n"
DT "a[234:235],v23 offset:32288\n"
BL "v[68:71],v246,s[28:31],s44 offen offset:1024 sc1\n"
"v_add_u32 v23,36864,v23\n"
MF "a[188:191],a[236:239],v[56:59],a[188:191]\n"
DT "a[236:237],v24 offset:27648\n"
MF "a[156:159],a[240:243],v[40:43],a[156:159]\n"
DT "a[238:239],v24 offset:32256\n"
MF "a[192:195],a[240:243],v[56:59],a[192:195]\n"
DT "a[240:241],v24 offset:27680\n"
MF "a[160:163],a[244:247],v[40:43],a[160:163]\n"
DT "a[242:243],v24 offset:32288\n"
"v_add_u32 v24,36864,v24\n"
MF "a[196:199],a[244:247],v[56:59],a[196:199]\n"
DT "a[244:245],v25 offset:27648\n"
DT "a[246:247],v25 offset:32256\n"
"v_add_u32 v25,36864,v25\n"
MF "v[80:83],a[64:67],a[0:3],v[104:107]\n"
"s_add_u32 m0,s50,s48\n"
DT "a[112:113],v7 offset:128\n"
CV "v120,v88,v89\n"
MF "v[84:87],a[68:71],a[0:3],v[104:107]\n"
DT "a[114:115],v7 offset:640\n"
BL "v8,s[12:15],s78 offen nt lds\n"
MF "v[80:83],a[72:75],a[4:7],v[80:83]\n"
DT "a[116:117],v7 offset:160\n"
CV "v121,v90,v91\n"
MF "v[84:87],a[76:79],a[4:7],v[84:87]\n"
DT "a[118:119],v7 offset:672\n"
CV "v122,v92,v93\n"
MF "v[80:83],a[80:83],a[8:11],v[80:83]\n"
DT "a[120:121],v7 offset:192\n"
CV "v123,v94,v95\n"
MF "v[84:87],a[84:87],a[8:11],v[84:87]\n"
DT "a[122:123],v7 offset:704\n"
BL "v9,s[12:15],s78 offen offset:1056 nt lds\n"
MF "v[80:83],a[88:91],a[12:15],v[80:83]\n"
"s_add_u32 m0,m0,8448\n"
DT "a[124:125],v7 offset:224\n"
MF "v[84:87],a[92:95],a[12:15],v[84:87]\n"
DT "a[126:127],v7 offset:736\n"
MF "v[88:91],a[64:67],a[16:19],v[108:111]\n"
"v_add_u32 v6,s49,v4\n"
"v_add_u32 v7,s49,v5\n"
MF "v[92:95],a[68:71],a[16:19],v[108:111]\n"
"s_mov_b32 s50,s42\n"
BL "v10,s[16:19],s79 offen nt lds\n"
EX "v80,v80\n"
MF "v[88:91],a[72:75],a[20:23],v[88:91]\n"
"s_mov_b32 s42,s49\n"
EX "v81,v81\n"
MF "v[92:95],a[76:79],a[20:23],v[92:95]\n"
EX "v82,v82\n"
MF "v[88:91],a[80:83],a[24:27],v[88:91]\n"
EX "v83,v83\n"
MF "v[92:95],a[84:87],a[24:27],v[92:95]\n"
BL "v11,s[16:19],s79 offen offset:1056 nt lds\n"
EX "v84,v84\n"
MF "v[88:91],a[88:91],a[28:31],v[88:91]\n"
"s_add_u32 s51,s51,8192\n"
EX "v85,v85\n"
MF "v[92:95],a[92:95],a[28:31],v[92:95]\n"
"s_mov_b32 s79,s78\n"
WT "vmcnt(27)\n"
WT "vmcnt(11)\n"
WT "lgkmcnt(0)\n"
"s_barrier\n"
MF "v[208:211],a[96:99],v[124:127],v[208:211]\n"
"s_xor_b32 s78,s51,s41\n"
DR "a[64:67],v6 offset:0\n"
EX "v86,v86\n"
MF "v[212:215],a[100:103],v[124:127],v[212:215]\n"
DR "a[68:71],v6 offset:512\n"
EX "v87,v87\n"
MF "v[216:219],a[104:107],v[124:127],v[216:219]\n"
DR "a[72:75],v6 offset:64\n"
MF "v[220:223],a[108:111],v[124:127],v[220:223]\n"
DR "a[76:79],v6 offset:576\n"
EX "v88,v88\n"
MF "v[240:243],a[48:51],v[124:127],v[240:243]\n"
DR "a[80:83],v6 offset:128\n"
EX "v89,v89\n"
MF "v[136:139],a[96:99],v[116:119],v[136:139]\n"
DR "a[84:87],v6 offset:640\n"
MF "v[140:143],a[100:103],v[116:119],v[140:143]\n"
DR "a[88:91],v6 offset:192\n"
MF "v[144:147],a[104:107],v[116:119],v[144:147]\n"
DR "a[92:95],v6 offset:704\n"
MF "v[148:151],a[108:111],v[116:119],v[148:151]\n"
DR "v[124:127],v254\n"
"v_xor_b32 v254,0x400,v254\n"
MF "v[152:155],a[112:115],v[116:119],v[152:155]\n"
EX "v90,v90\n"
MF "v[156:159],a[116:119],v[116:119],v[156:159]\n"
EX "v91,v91\n"
MF "v[160:163],a[120:123],v[116:119],v[160:163]\n"
"s_add_u32 s49,s49,16896\n"
EX "v92,v92\n"
MF "v[164:167],a[124:127],v[116:119],v[164:167]\n"
"s_cmp_eq_u32 s49,84480\n"
EX "v93,v93\n"
MF "v[168:171],a[48:51],v[116:119],v[168:171]\n"
"s_cselect_b32 s49,0,s49\n"
EX "v94,v94\n"
MF "v[172:175],a[96:99],v[120:123],v[172:175]\n"
EX "v95,v95\n"
MF "v[176:179],a[100:103],v[120:123],v[176:179]\n"
DT "a[96:97],v7 offset:0\n"
CV "v116,v80,v81\n"
MF "v[180:183],a[104:107],v[120:123],v[180:183]\n"
DT "a[98:99],v7 offset:512\n"
CV "v117,v82,v83\n"
MF "v[184:187],a[108:111],v[120:123],v[184:187]\n"
DT "a[100:101],v7 offset:32\n"
CV "v118,v84,v85\n"
MF "v[188:191],a[112:115],v[120:123],v[188:191]\n"
DT "a[102:103],v7 offset:544\n"
CV "v119,v86,v87\n"
MF "v[192:195],a[116:119],v[120:123],v[192:195]\n"
DT "a[104:105],v7 offset:64\n"
MF "v[196:199],a[120:123],v[120:123],v[196:199]\n"
DT "a[106:107],v7 offset:576\n"
MF "v[200:203],a[124:127],v[120:123],v[200:203]\n"
WT "lgkmcnt(6)\n"
DT "a[108:109],v7 offset:96\n"
MF "v[204:207],a[48:51],v[120:123],v[204:207]\n"
DT "a[110:111],v7 offset:608\n"
MF "a[128:131],a[212:215],v[44:47],a[128:131]\n"
MF "a[164:167],a[212:215],v[60:63],a[164:167]\n"
"s_add_u32 s44,s57,0\n"
MF "a[200:203],a[212:215],v[76:79],a[200:203]\n"
DT "a[212:213],v21 offset:0\n"
BL "v[40:43],v244,s[28:31],s44 offen offset:2048 sc1\n"
MF "a[132:135],a[216:219],v[44:47],a[132:135]\n"
DT "a[214:215],v21 offset:4608\n"
MF "a[168:171],a[216:219],v[60:63],a[168:171]\n"
MF "a[204:207],a[216:219],v[76:79],a[204:207]\n"
DT "a[216:217],v21 offset:32\n"
MF "a[136:139],a[220:223],v[44:47],a[136:139]\n"
DT "a[218:219],v21 offset:4640\n"
MF "a[172:175],a[220:223],v[60:63],a[172:175]\n"
DT "a[220:221],v22 offset:0\n"
MF "a[140:143],a[224:227],v[44:47],a[140:143]\n"
DT "a[222:223],v22 offset:4608\n"
BL "v[56:59],v245,s[28:31],s44 offen offset:2048 sc1\n"
MF "a[176:179],a[224:227],v[60:63],a[176:179]\n"
DT "a[224:225],v22 offset:32\n"
MF "a[144:147],a[228:231],v[44:47],a[144:147]\n"
DT "a[226:227],v22 offset:4640\n"
MF "a[180:183],a[228:231],v[60:63],a[180:183]\n"
DT "a[228:229],v23 offset:0\n"
MF "a[148:151],a[232:235],v[44:47],a[148:151]\n"
DT "a[230:231],v23 offset:4608\n"
MF "a[184:187],a[232:235],v[60:63],a[184:187]\n"
DT "a[232:233],v23 offset:32\n"
MF "a[152:155],a[236:239],v[44:47],a[152:155]\n"
DT "a[234:235],v23 offset:4640\n"
BL "v[72:75],v246,s[28:31],s44 offen offset:2048 sc1\n"
MF "a[188:191],a[236:239],v[60:63],a[188:191]\n"
DT "a[236:237],v24 offset:0\n"
MF "a[156:159],a[240:243],v[44:47],a[156:159]\n"
DT "a[238:239],v24 offset:4608\n"
MF "a[192:195],a[240:243],v[60:63],a[192:195]\n"
DT "a[240:241],v24 offset:32\n"
MF "a[160:163],a[244:247],v[44:47],a[160:163]\n"
DT "a[242:243],v24 offset:4640\n"
MF "a[196:199],a[244:247],v[60:63],a[196:199]\n"
DT "a[244:245],v25 offset:0\n"
DT "a[246:247],v25 offset:4608\n"
MF "v[80:83],a[64:67],a[0:3],v[104:107]\n"
"s_add_u32 m0,s50,s48\n"
DT "a[112:113],v7 offset:128\n"
CV "v120,v88,v89\n"
MF "v[84:87],a[68:71],a[0:3],v[104:107]\n"
DT "a[114:115],v7 offset:640\n"
BL "v8,s[12:15],s78 offen nt lds\n"
MF "v[80:83],a[72:75],a[4:7],v[80:83]\n"
DT "a[116:117],v7 offset:160\n"
CV "v121,v90,v91\n"
MF "v[84:87],a[76:79],a[4:7],v[84:87]\n"
DT "a[118:119],v7 offset:672\n"
CV "v122,v92,v93\n"
MF "v[80:83],a[80:83],a[8:11],v[80:83]\n"
DT "a[120:121],v7 offset:192\n"
CV "v123,v94,v95\n"
MF "v[84:87],a[84:87],a[8:11],v[84:87]\n"
DT "a[122:123],v7 offset:704\n"
BL "v9,s[12:15],s78 offen offset:1056 nt lds\n"
MF "v[80:83],a[88:91],a[12:15],v[80:83]\n"
"s_add_u32 m0,m0,8448\n"
DT "a[124:125],v7 offset:224\n"
MF "v[84:87],a[92:95],a[12:15],v[84:87]\n"
DT "a[126:127],v7 offset:736\n"
MF "v[88:91],a[64:67],a[16:19],v[108:111]\n"
"v_add_u32 v6,s49,v4\n"
"v_add_u32 v7,s49,v5\n"
MF "v[92:95],a[68:71],a[16:19],v[108:111]\n"
"s_mov_b32 s50,s42\n"
BL "v10,s[16:19],s79 offen nt lds\n"
EX "v80,v80\n"
MF "v[88:91],a[72:75],a[20:23],v[88:91]\n"
"s_mov_b32 s42,s49\n"
EX "v81,v81\n"
MF "v[92:95],a[76:79],a[20:23],v[92:95]\n"
EX "v82,v82\n"
MF "v[88:91],a[80:83],a[24:27],v[88:91]\n"
EX "v83,v83\n"
MF "v[92:95],a[84:87],a[24:27],v[92:95]\n"
BL "v11,s[16:19],s79 offen offset:1056 nt lds\n"
EX "v84,v84\n"
MF "v[88:91],a[88:91],a[28:31],v[88:91]\n"
"s_add_u32 s51,s51,8192\n"
EX "v85,v85\n"
MF "v[92:95],a[92:95],a[28:31],v[92:95]\n"
"s_mov_b32 s79,s78\n"
WT "vmcnt(24)\n"
WT "lgkmcnt(0)\n"
"s_barrier\n"
MF "v[208:211],a[96:99],v[124:127],v[208:211]\n"
"s_xor_b32 s78,s51,s41\n"
DR "a[64:67],v6 offset:0\n"
EX "v86,v86\n"
MF "v[212:215],a[100:103],v[124:127],v[212:215]\n"
DR "a[68:71],v6 offset:512\n"
EX "v87,v87\n"
MF "v[216:219],a[104:107],v[124:127],v[216:219]\n"
DR "a[72:75],v6 offset:64\n"
MF "v[220:223],a[108:111],v[124:127],v[220:223]\n"
DR "a[76:79],v6 offset:576\n"
EX "v88,v88\n"
MF "v[240:243],a[48:51],v[124:127],v[240:243]\n"
DR "a[80:83],v6 offset:128\n"
EX "v89,v89\n"
MF "v[136:139],a[96:99],v[116:119],v[136:139]\n"
DR "a[84:87],v6 offset:640\n"
MF "v[140:143],a[100:103],v[116:119],v[140:143]\n"
DR "a[88:91],v6 offset:192\n"
MF "v[144:147],a[104:107],v[116:119],v[144:147]\n"
DR "a[92:95],v6 offset:704\n"
MF "v[148:151],a[108:111],v[116:119],v[148:151]\n"
DR "v[124:127],v254\n"
"v_xor_b32 v254,0x400,v254\n"
MF "v[152:155],a[112:115],v[116:119],v[152:155]\n"
EX "v90,v90\n"
MF "v[156:159],a[116:119],v[116:119],v[156:159]\n"
EX "v91,v91\n"
MF "v[160:163],a[120:123],v[116:119],v[160:163]\n"
"s_add_u32 s49,s49,16896\n"
EX "v92,v92\n"
MF "v[164:167],a[124:127],v[116:119],v[164:167]\n"
"s_cmp_eq_u32 s49,84480\n"
EX "v93,v93\n"
MF "v[168:171],a[48:51],v[116:119],v[168:171]\n"
"s_cselect_b32 s49,0,s49\n"
EX "v94,v94\n"
MF "v[172:175],a[96:99],v[120:123],v[172:175]\n"
EX "v95,v95\n"
MF "v[176:179],a[100:103],v[120:123],v[176:179]\n"
DT "a[96:97],v7 offset:0\n"
CV "v116,v80,v81\n"
MF "v[180:183],a[104:107],v[120:123],v[180:183]\n"
DT "a[98:99],v7 offset:512\n"
CV "v117,v82,v83\n"
MF "v[184:187],a[108:111],v[120:123],v[184:187]\n"
DT "a[100:101],v7 offset:32\n"
CV "v118,v84,v85\n"
MF "v[188:191],a[112:115],v[120:123],v[188:191]\n"
DT "a[102:103],v7 offset:544\n"
CV "v119,v86,v87\n"
MF "v[192:195],a[116:119],v[120:123],v[192:195]\n"
DT "a[104:105],v7 offset:64\n"
MF "v[196:199],a[120:123],v[120:123],v[196:199]\n"
DT "a[106:107],v7 offset:576\n"
MF "v[200:203],a[124:127],v[120:123],v[200:203]\n"
WT "lgkmcnt(6)\n"
DT "a[108:109],v7 offset:96\n"
MF "v[204:207],a[48:51],v[120:123],v[204:207]\n"
DT "a[110:111],v7 offset:608\n"
WT "vmcnt(21)\n"
MF "a[128:131],a[212:215],v[32:35],a[128:131]\n"
MF "a[164:167],a[212:215],v[48:51],a[164:167]\n"
"s_add_u32 s44,s57,0\n"
MF "a[200:203],a[212:215],v[64:67],a[200:203]\n"
DT "a[212:213],v21 offset:9216\n"
BL "v[44:47],v244,s[28:31],s44 offen offset:3072 sc1\n"
MF "a[132:135],a[216:219],v[32:35],a[132:135]\n"
DT "a[214:215],v21 offset:13824\n"
MF "a[168:171],a[216:219],v[48:51],a[168:171]\n"
BL "v[60:63],v245,s[28:31],s44 offen offset:3072 sc1\n"
MF "a[204:207],a[216:219],v[64:67],a[204:207]\n"
DT "a[216:217],v21 offset:9248\n"
MF "a[136:139],a[220:223],v[32:35],a[136:139]\n"
DT "a[218:219],v21 offset:13856\n"
BL "v[76:79],v246,s[28:31],s44 offen offset:3072 sc1\n"
MF "a[172:175],a[220:223],v[48:51],a[172:175]\n"
"s_add_u32 m0,s76,84480\n"
"s_add_u32 s67,s70,0\n"
DT "a[220:221],v22 offset:9216\n"
MF "a[140:143],a[224:227],v[32:35],a[140:143]\n"
DT "a[222:223],v22 offset:13824\n"
BL "v12,s[24:27],s67 offen offset:0 lds\n"
MF "a[176:179],a[224:227],v[48:51],a[176:179]\n"
DT "a[224:225],v22 offset:9248\n"
MF "a[144:147],a[228:231],v[32:35],a[144:147]\n"
DT "a[226:227],v22 offset:13856\n"
BL "v13,s[24:27],s67 offen offset:1024 lds\n"
MF "a[180:183],a[228:231],v[48:51],a[180:183]\n"
DT "a[228:229],v23 offset:9216\n"
MF "a[148:151],a[232:235],v[32:35],a[148:151]\n"
DT "a[230:231],v23 offset:13824\n"
BL "v14,s[24:27],s67 offen offset:2048 lds\n"
MF "a[184:187],a[232:235],v[48:51],a[184:187]\n"
"s_add_u32 m0,s76,87552\n"
"s_add_u32 s67,s70,0\n"
DT "a[232:233],v23 offset:9248\n"
MF "a[152:155],a[236:239],v[32:35],a[152:155]\n"
DT "a[234:235],v23 offset:13856\n"
BL "v15,s[24:27],s67 offen offset:0 lds\n"
MF "a[188:191],a[236:239],v[48:51],a[188:191]\n"
DT "a[236:237],v24 offset:9216\n"
MF "a[156:159],a[240:243],v[32:35],a[156:159]\n"
DT "a[238:239],v24 offset:13824\n"
BL "v16,s[24:27],s67 offen offset:1024 lds\n"
MF "a[192:195],a[240:243],v[48:51],a[192:195]\n"
DT "a[240:241],v24 offset:9248\n"
MF "a[160:163],a[244:247],v[32:35],a[160:163]\n"
DT "a[242:243],v24 offset:13856\n"
BL "v17,s[24:27],s67 offen offset:2048 lds\n"
MF "a[196:199],a[244:247],v[48:51],a[196:199]\n"
DT "a[244:245],v25 offset:9216\n"
DT "a[246:247],v25 offset:13824\n"
MF "v[80:83],a[64:67],a[0:3],v[104:107]\n"
"s_add_u32 m0,s50,s48\n"
"s_add_u32 m0,m0,8448\n"
DT "a[112:113],v7 offset:128\n"
CV "v120,v88,v89\n"
MF "v[84:87],a[68:71],a[0:3],v[104:107]\n"
DT "a[114:115],v7 offset:640\n"
BL "v10,s[16:19],s79 offen nt lds\n"
MF "v[80:83],a[72:75],a[4:7],v[80:83]\n"
DT "a[116:117],v7 offset:160\n"
CV "v121,v90,v91\n"
MF "v[84:87],a[76:79],a[4:7],v[84:87]\n"
DT "a[118:119],v7 offset:672\n"
CV "v122,v92,v93\n"
MF "v[80:83],a[80:83],a[8:11],v[80:83]\n"
DT "a[120:121],v7 offset:192\n"
CV "v123,v94,v95\n"
MF "v[84:87],a[84:87],a[8:11],v[84:87]\n"
DT "a[122:123],v7 offset:704\n"
BL "v11,s[16:19],s79 offen offset:1056 nt lds\n"
MF "v[80:83],a[88:91],a[12:15],v[80:83]\n"
"s_add_u32 s51,s51,8192\n"
"s_mov_b32 s79,s78\n"
"s_xor_b32 s78,s51,s41\n"
DT "a[124:125],v7 offset:224\n"
MF "v[84:87],a[92:95],a[12:15],v[84:87]\n"
DT "a[126:127],v7 offset:736\n"
MF "v[88:91],a[64:67],a[16:19],v[108:111]\n"
"v_add_u32 v6,s49,v4\n"
"v_add_u32 v7,s49,v5\n"
MF "v[92:95],a[68:71],a[16:19],v[108:111]\n"
"s_mov_b32 s50,s42\n"
EX "v80,v80\n"
MF "v[88:91],a[72:75],a[20:23],v[88:91]\n"
"s_mov_b32 s42,s49\n"
EX "v81,v81\n"
MF "v[92:95],a[76:79],a[20:23],v[92:95]\n"
EX "v82,v82\n"
MF "v[88:91],a[80:83],a[24:27],v[88:91]\n"
EX "v83,v83\n"
MF "v[92:95],a[84:87],a[24:27],v[92:95]\n"
EX "v84,v84\n"
MF "v[88:91],a[88:91],a[28:31],v[88:91]\n"
EX "v85,v85\n"
MF "v[92:95],a[92:95],a[28:31],v[92:95]\n"
WT "vmcnt(25)\n"
WT "lgkmcnt(0)\n"
"s_barrier\n"
MF "v[208:211],a[96:99],v[124:127],v[208:211]\n"
DR "a[64:67],v6 offset:0\n"
EX "v86,v86\n"
MF "v[212:215],a[100:103],v[124:127],v[212:215]\n"
DR "a[68:71],v6 offset:512\n"
EX "v87,v87\n"
MF "v[216:219],a[104:107],v[124:127],v[216:219]\n"
DR "a[72:75],v6 offset:64\n"
MF "v[220:223],a[108:111],v[124:127],v[220:223]\n"
DR "a[76:79],v6 offset:576\n"
EX "v88,v88\n"
MF "v[240:243],a[48:51],v[124:127],v[240:243]\n"
DR "a[80:83],v6 offset:128\n"
EX "v89,v89\n"
MF "v[136:139],a[96:99],v[116:119],v[136:139]\n"
DR "a[84:87],v6 offset:640\n"
MF "v[140:143],a[100:103],v[116:119],v[140:143]\n"
DR "a[88:91],v6 offset:192\n"
MF "v[144:147],a[104:107],v[116:119],v[144:147]\n"
DR "a[92:95],v6 offset:704\n"
MF "v[148:151],a[108:111],v[116:119],v[148:151]\n"
DR "v[124:127],v254\n"
"v_xor_b32 v254,0x400,v254\n"
MF "v[152:155],a[112:115],v[116:119],v[152:155]\n"
EX "v90,v90\n"
MF "v[156:159],a[116:119],v[116:119],v[156:159]\n"
EX "v91,v91\n"
MF "v[160:163],a[120:123],v[116:119],v[160:163]\n"
"s_add_u32 s49,s49,16896\n"
EX "v92,v92\n"
MF "v[164:167],a[124:127],v[116:119],v[164:167]\n"
"s_cmp_eq_u32 s49,84480\n"
EX "v93,v93\n"
MF "v[168:171],a[48:51],v[116:119],v[168:171]\n"
"s_cselect_b32 s49,0,s49\n"
EX "v94,v94\n"
MF "v[172:175],a[96:99],v[120:123],v[172:175]\n"
EX "v95,v95\n"
MF "v[176:179],a[100:103],v[120:123],v[176:179]\n"
DT "a[96:97],v7 offset:0\n"
CV "v116,v80,v81\n"
MF "v[180:183],a[104:107],v[120:123],v[180:183]\n"
DT "a[98:99],v7 offset:512\n"
CV "v117,v82,v83\n"
MF "v[184:187],a[108:111],v[120:123],v[184:187]\n"
DT "a[100:101],v7 offset:32\n"
CV "v118,v84,v85\n"
MF "v[188:191],a[112:115],v[120:123],v[188:191]\n"
DT "a[102:103],v7 offset:544\n"
CV "v119,v86,v87\n"
MF "v[192:195],a[116:119],v[120:123],v[192:195]\n"
DT "a[104:105],v7 offset:64\n"
MF "v[196:199],a[120:123],v[120:123],v[196:199]\n"
DT "a[106:107],v7 offset:576\n"
MF "v[200:203],a[124:127],v[120:123],v[200:203]\n"
WT "lgkmcnt(6)\n"
DT "a[108:109],v7 offset:96\n"
MF "v[204:207],a[48:51],v[120:123],v[204:207]\n"
DT "a[110:111],v7 offset:608\n"
WT "vmcnt(22)\n"
MF "a[128:131],a[212:215],v[36:39],a[128:131]\n"
MF "a[164:167],a[212:215],v[52:55],a[164:167]\n"
"s_add_u32 s44,s58,0\n"
MF "a[200:203],a[212:215],v[68:71],a[200:203]\n"
DT "a[212:213],v21 offset:18432\n"
BL "v[32:35],v244,s[28:31],s44 offen offset:0 sc1\n"
MF "a[132:135],a[216:219],v[36:39],a[132:135]\n"
DT "a[214:215],v21 offset:23040\n"
MF "a[168:171],a[216:219],v[52:55],a[168:171]\n"
MF "a[204:207],a[216:219],v[68:71],a[204:207]\n"
DT "a[216:217],v21 offset:18464\n"
BL "v[48:51],v245,s[28:31],s44 offen offset:0 sc1\n"
MF "a[136:139],a[220:223],v[36:39],a[136:139]\n"
DT "a[218:219],v21 offset:23072\n"
MF "a[172:175],a[220:223],v[52:55],a[172:175]\n"
DT "a[220:221],v22 offset:18432\n"
MF "a[140:143],a[224:227],v[36:39],a[140:143]\n"
DT "a[222:223],v22 offset:23040\n"
BL "v[64:67],v246,s[28:31],s44 offen offset:0 sc1\n"
MF "a[176:179],a[224:227],v[52:55],a[176:179]\n"
"s_add_u32 m0,s76,90624\n"
DT "a[224:225],v22 offset:18464\n"
MF "a[144:147],a[228:231],v[36:39],a[144:147]\n"
"s_add_u32 s67,s70,0\n"
DT "a[226:227],v22 offset:23072\n"
MF "a[180:183],a[228:231],v[52:55],a[180:183]\n"
DT "a[228:229],v23 offset:18432\n"
BL "v18,s[24:27],s67 offen offset:0 lds\n"
MF "a[148:151],a[232:235],v[36:39],a[148:151]\n"
DT "a[230:231],v23 offset:23040\n"
MF "a[184:187],a[232:235],v[52:55],a[184:187]\n"
DT "a[232:233],v23 offset:18464\n"
MF "a[152:155],a[236:239],v[36:39],a[152:155]\n"
DT "a[234:235],v23 offset:23072\n"
BL "v19,s[24:27],s67 offen offset:1024 lds\n"
MF "a[188:191],a[236:239],v[52:55],a[188:191]\n"
DT "a[236:237],v24 offset:18432\n"
MF "a[156:159],a[240:243],v[36:39],a[156:159]\n"
DT "a[238:239],v24 offset:23040\n"
MF "a[192:195],a[240:243],v[52:55],a[192:195]\n"
DT "a[240:241],v24 offset:18464\n"
BL "v20,s[24:27],s67 offen offset:2048 lds\n"
MF "a[160:163],a[244:247],v[36:39],a[160:163]\n"
DT "a[242:243],v24 offset:23072\n"
MF "a[196:199],a[244:247],v[52:55],a[196:199]\n"
DT "a[244:245],v25 offset:18432\n"
DT "a[246:247],v25 offset:23040\n"
MF "v[80:83],a[64:67],a[0:3],v[104:107]\n"
DT "a[112:113],v7 offset:128\n"
CV "v120,v88,v89\n"
MF "v[84:87],a[68:71],a[0:3],v[104:107]\n"
DT "a[114:115],v7 offset:640\n"
MF "v[80:83],a[72:75],a[4:7],v[80:83]\n"
DT "a[116:117],v7 offset:160\n"
CV "v121,v90,v91\n"
MF "v[84:87],a[76:79],a[4:7],v[84:87]\n"
DT "a[118:119],v7 offset:672\n"
CV "v122,v92,v93\n"
MF "v[80:83],a[80:83],a[8:11],v[80:83]\n"
DT "a[120:121],v7 offset:192\n"
CV "v123,v94,v95\n"
MF "v[84:87],a[84:87],a[8:11],v[84:87]\n"
DT "a[122:123],v7 offset:704\n"
MF "v[80:83],a[88:91],a[12:15],v[80:83]\n"
DT "a[124:125],v7 offset:224\n"
MF "v[84:87],a[92:95],a[12:15],v[84:87]\n"
DT "a[126:127],v7 offset:736\n"
MF "v[88:91],a[64:67],a[16:19],v[108:111]\n"
"v_add_u32 v6,s49,v4\n"
"v_add_u32 v7,s49,v5\n"
MF "v[92:95],a[68:71],a[16:19],v[108:111]\n"
"s_mov_b32 s50,s42\n"
EX "v80,v80\n"
MF "v[88:91],a[72:75],a[20:23],v[88:91]\n"
"s_mov_b32 s42,s49\n"
EX "v81,v81\n"
MF "v[92:95],a[76:79],a[20:23],v[92:95]\n"
EX "v82,v82\n"
MF "v[88:91],a[80:83],a[24:27],v[88:91]\n"
EX "v83,v83\n"
MF "v[92:95],a[84:87],a[24:27],v[92:95]\n"
EX "v84,v84\n"
MF "v[88:91],a[88:91],a[28:31],v[88:91]\n"
EX "v85,v85\n"
MF "v[92:95],a[92:95],a[28:31],v[92:95]\n"
WT "vmcnt(24)\n"
WT "lgkmcnt(0)\n"
"s_barrier\n"
MF "v[208:211],a[96:99],v[124:127],v[208:211]\n"
DR "a[64:67],v6 offset:0\n"
EX "v86,v86\n"
MF "v[212:215],a[100:103],v[124:127],v[212:215]\n"
DR "a[68:71],v6 offset:512\n"
EX "v87,v87\n"
MF "v[216:219],a[104:107],v[124:127],v[216:219]\n"
DR "a[72:75],v6 offset:64\n"
MF "v[220:223],a[108:111],v[124:127],v[220:223]\n"
DR "a[76:79],v6 offset:576\n"
EX "v88,v88\n"
MF "v[240:243],a[48:51],v[124:127],v[240:243]\n"
DR "a[80:83],v6 offset:128\n"
EX "v89,v89\n"
MF "v[136:139],a[96:99],v[116:119],v[136:139]\n"
DR "a[84:87],v6 offset:640\n"
MF "v[140:143],a[100:103],v[116:119],v[140:143]\n"
DR "a[88:91],v6 offset:192\n"
MF "v[144:147],a[104:107],v[116:119],v[144:147]\n"
DR "a[92:95],v6 offset:704\n"
MF "v[148:151],a[108:111],v[116:119],v[148:151]\n"
DR "v[124:127],v254\n"
"v_xor_b32 v254,0x400,v254\n"
MF "v[152:155],a[112:115],v[116:119],v[152:155]\n"
EX "v90,v90\n"
MF "v[156:159],a[116:119],v[116:119],v[156:159]\n"
EX "v91,v91\n"
MF "v[160:163],a[120:123],v[116:119],v[160:163]\n"
"s_add_u32 s49,s49,16896\n"
EX "v92,v92\n"
MF "v[164:167],a[124:127],v[116:119],v[164:167]\n"
"s_cmp_eq_u32 s49,84480\n"
EX "v93,v93\n"
MF "v[168:171],a[48:51],v[116:119],v[168:171]\n"
"s_cselect_b32 s49,0,s49\n"
EX "v94,v94\n"
MF "v[172:175],a[96:99],v[120:123],v[172:175]\n"
EX "v95,v95\n"
MF "v[176:179],a[100:103],v[120:123],v[176:179]\n"
DT "a[96:97],v7 offset:0\n"
CV "v116,v80,v81\n"
MF "v[180:183],a[104:107],v[120:123],v[180:183]\n"
DT "a[98:99],v7 offset:512\n"
CV "v117,v82,v83\n"
MF "v[184:187],a[108:111],v[120:123],v[184:187]\n"
DT "a[100:101],v7 offset:32\n"
CV "v118,v84,v85\n"
MF "v[188:191],a[112:115],v[120:123],v[188:191]\n"
DT "a[102:103],v7 offset:544\n"
CV "v119,v86,v87\n"
MF "v[192:195],a[116:119],v[120:123],v[192:195]\n"
DT "a[104:105],v7 offset:64\n"
MF "v[196:199],a[120:123],v[120:123],v[196:199]\n"
DT "a[106:107],v7 offset:576\n"
MF "v[200:203],a[124:127],v[120:123],v[200:203]\n"
WT "lgkmcnt(6)\n"
DT "a[108:109],v7 offset:96\n"
MF "v[204:207],a[48:51],v[120:123],v[204:207]\n"
DT "a[110:111],v7 offset:608\n"
WT "vmcnt(21)\n"
MF "a[128:131],a[212:215],v[40:43],a[128:131]\n"
MF "a[164:167],a[212:215],v[56:59],a[164:167]\n"
"s_add_u32 s44,s58,0\n"
MF "a[200:203],a[212:215],v[72:75],a[200:203]\n"
DT "a[212:213],v21 offset:27648\n"
BL "v[36:39],v244,s[28:31],s44 offen offset:1024 sc1\n"
MF "a[132:135],a[216:219],v[40:43],a[132:135]\n"
DT "a[214:215],v21 offset:32256\n"
MF "a[168:171],a[216:219],v[56:59],a[168:171]\n"
MF "a[204:207],a[216:219],v[72:75],a[204:207]\n"
DT "a[216:217],v21 offset:27680\n"
MF "a[136:139],a[220:223],v[40:43],a[136:139]\n"
DT "a[218:219],v21 offset:32288\n"
"v_subrev_u32 v21,36864,v21\n"
MF "a[172:175],a[220:223],v[56:59],a[172:175]\n"
DT "a[220:221],v22 offset:27648\n"
MF "a[140:143],a[224:227],v[40:43],a[140:143]\n"
DT "a[222:223],v22 offset:32256\n"
BL "v[52:55],v245,s[28:31],s44 offen offset:1024 sc1\n"
MF "a[176:179],a[224:227],v[56:59],a[176:179]\n"
DT "a[224:225],v22 offset:27680\n"
MF "a[144:147],a[228:231],v[40:43],a[144:147]\n"
DT "a[226:227],v22 offset:32288\n"
"v_subrev_u32 v22,36864,v22\n"
MF "a[180:183],a[228:231],v[56:59],a[180:183]\n"
DT "a[228:229],v23 offset:27648\n"
MF "a[148:151],a[232:235],v[40:43],a[148:151]\n"
DT "a[230:231],v23 offset:32256\n"
MF "a[184:187],a[232:235],v[56:59],a[184:187]\n"
DT "a[232:233],v23 offset:27680\n"
MF "a[152:155],a[236:239],v[40:43],a[152:155]\n"
DT "a[234:235],v23 offset:32288\n"
BL "v[68:71],v246,s[28:31],s44 offen offset:1024 sc1\n"
"v_subrev_u32 v23,36864,v23\n"
MF "a[188:191],a[236:239],v[56:59],a[188:191]\n"
DT "a[236:237],v24 offset:27648\n"
MF "a[156:159],a[240:243],v[40:43],a[156:159]\n"
DT "a[238:239],v24 offset:32256\n"
MF "a[192:195],a[240:243],v[56:59],a[192:195]\n"
DT "a[240:241],v24 offset:27680\n"
MF "a[160:163],a[244:247],v[40:43],a[160:163]\n"
DT "a[242:243],v24 offset:32288\n"
"v_subrev_u32 v24,36864,v24\n"
MF "a[196:199],a[244:247],v[56:59],a[196:199]\n"
DT "a[244:245],v25 offset:27648\n"
DT "a[246:247],v25 offset:32256\n"
"v_subrev_u32 v25,36864,v25\n"
MF "v[80:83],a[64:67],a[0:3],v[104:107]\n"
DT "a[112:113],v7 offset:128\n"
CV "v120,v88,v89\n"
MF "v[84:87],a[68:71],a[0:3],v[104:107]\n"
DT "a[114:115],v7 offset:640\n"
MF "v[80:83],a[72:75],a[4:7],v[80:83]\n"
DT "a[116:117],v7 offset:160\n"
CV "v121,v90,v91\n"
MF "v[84:87],a[76:79],a[4:7],v[84:87]\n"
DT "a[118:119],v7 offset:672\n"
CV "v122,v92,v93\n"
MF "v[80:83],a[80:83],a[8:11],v[80:83]\n"
DT "a[120:121],v7 offset:192\n"
CV "v123,v94,v95\n"
MF "v[84:87],a[84:87],a[8:11],v[84:87]\n"
DT "a[122:123],v7 offset:704\n"
MF "v[80:83],a[88:91],a[12:15],v[80:83]\n"
DT "a[124:125],v7 offset:224\n"
MF "v[84:87],a[92:95],a[12:15],v[84:87]\n"
DT "a[126:127],v7 offset:736\n"
MF "v[88:91],a[64:67],a[16:19],v[108:111]\n"
"v_add_u32 v6,s49,v4\n"
"v_add_u32 v7,s49,v5\n"
MF "v[92:95],a[68:71],a[16:19],v[108:111]\n"
"s_mov_b32 s50,s42\n"
EX "v80,v80\n"
MF "v[88:91],a[72:75],a[20:23],v[88:91]\n"
"s_mov_b32 s42,s49\n"
EX "v81,v81\n"
MF "v[92:95],a[76:79],a[20:23],v[92:95]\n"
EX "v82,v82\n"
MF "v[88:91],a[80:83],a[24:27],v[88:91]\n"
EX "v83,v83\n"
MF "v[92:95],a[84:87],a[24:27],v[92:95]\n"
EX "v84,v84\n"
MF "v[88:91],a[88:91],a[28:31],v[88:91]\n"
EX "v85,v85\n"
MF "v[92:95],a[92:95],a[28:31],v[92:95]\n"
WT "vmcnt(20)\n"
WT "vmcnt(3)\n"
WT "lgkmcnt(0)\n"
"s_barrier\n"
MF "v[208:211],a[96:99],v[124:127],v[208:211]\n"
DR "a[64:67],v6 offset:0\n"
EX "v86,v86\n"
MF "v[212:215],a[100:103],v[124:127],v[212:215]\n"
DR "a[68:71],v6 offset:512\n"
EX "v87,v87\n"
MF "v[216:219],a[104:107],v[124:127],v[216:219]\n"
DR "a[72:75],v6 offset:64\n"
MF "v[220:223],a[108:111],v[124:127],v[220:223]\n"
DR "a[76:79],v6 offset:576\n"
EX "v88,v88\n"
MF "v[240:243],a[48:51],v[124:127],v[240:243]\n"
DR "a[80:83],v6 offset:128\n"
EX "v89,v89\n"
MF "v[136:139],a[96:99],v[116:119],v[136:139]\n"
DR "a[84:87],v6 offset:640\n"
MF "v[140:143],a[100:103],v[116:119],v[140:143]\n"
DR "a[88:91],v6 offset:192\n"
MF "v[144:147],a[104:107],v[116:119],v[144:147]\n"
DR "a[92:95],v6 offset:704\n"
MF "v[148:151],a[108:111],v[116:119],v[148:151]\n"
DR "v[124:127],v254\n"
"v_xor_b32 v254,0x400,v254\n"
MF "v[152:155],a[112:115],v[116:119],v[152:155]\n"
EX "v90,v90\n"
MF "v[156:159],a[116:119],v[116:119],v[156:159]\n"
EX "v91,v91\n"
MF "v[160:163],a[120:123],v[116:119],v[160:163]\n"
"s_add_u32 s49,s49,16896\n"
EX "v92,v92\n"
MF "v[164:167],a[124:127],v[116:119],v[164:167]\n"
"s_cmp_eq_u32 s49,84480\n"
EX "v93,v93\n"
MF "v[168:171],a[48:51],v[116:119],v[168:171]\n"
"s_cselect_b32 s49,0,s49\n"
EX "v94,v94\n"
MF "v[172:175],a[96:99],v[120:123],v[172:175]\n"
EX "v95,v95\n"
MF "v[176:179],a[100:103],v[120:123],v[176:179]\n"
DT "a[96:97],v7 offset:0\n"
CV "v116,v80,v81\n"
MF "v[180:183],a[104:107],v[120:123],v[180:183]\n"
DT "a[98:99],v7 offset:512\n"
CV "v117,v82,v83\n"
MF "v[184:187],a[108:111],v[120:123],v[184:187]\n"
DT "a[100:101],v7 offset:32\n"
CV "v118,v84,v85\n"
MF "v[188:191],a[112:115],v[120:123],v[188:191]\n"
DT "a[102:103],v7 offset:544\n"
CV "v119,v86,v87\n"
MF "v[192:195],a[116:119],v[120:123],v[192:195]\n"
DT "a[104:105],v7 offset:64\n"
MF "v[196:199],a[120:123],v[120:123],v[196:199]\n"
DT "a[106:107],v7 offset:576\n"
MF "v[200:203],a[124:127],v[120:123],v[200:203]\n"
WT "lgkmcnt(6)\n"
DT "a[108:109],v7 offset:96\n"
MF "v[204:207],a[48:51],v[120:123],v[204:207]\n"
DT "a[110:111],v7 offset:608\n"
WT "vmcnt(17)\n"
MF "a[128:131],a[212:215],v[44:47],a[128:131]\n"
MF "a[164:167],a[212:215],v[60:63],a[164:167]\n"
"s_add_u32 s44,s58,0\n"
MF "a[200:203],a[212:215],v[76:79],a[200:203]\n"
DT "a[212:213],v21 offset:0\n"
BL "v[40:43],v244,s[28:31],s44 offen offset:2048 sc1\n"
MF "a[132:135],a[216:219],v[44:47],a[132:135]\n"
DT "a[214:215],v21 offset:4608\n"
MF "a[168:171],a[216:219],v[60:63],a[168:171]\n"
MF "a[204:207],a[216:219],v[76:79],a[204:207]\n"
DT "a[216:217],v21 offset:32\n"
MF "a[136:139],a[220:223],v[44:47],a[136:139]\n"
DT "a[218:219],v21 offset:4640\n"
MF "a[172:175],a[220:223],v[60:63],a[172:175]\n"
DT "a[220:221],v22 offset:0\n"
MF "a[140:143],a[224:227],v[44:47],a[140:143]\n"
DT "a[222:223],v22 offset:4608\n"
BL "v[56:59],v245,s[28:31],s44 offen offset:2048 sc1\n"
MF "a[176:179],a[224:227],v[60:63],a[176:179]\n"
DT "a[224:225],v22 offset:32\n"
MF "a[144:147],a[228:231],v[44:47],a[144:147]\n"
DT "a[226:227],v22 offset:4640\n"
MF "a[180:183],a[228:231],v[60:63],a[180:183]\n"
DT "a[228:229],v23 offset:0\n"
MF "a[148:151],a[232:235],v[44:47],a[148:151]\n"
DT "a[230:231],v23 offset:4608\n"
MF "a[184:187],a[232:235],v[60:63],a[184:187]\n"
DT "a[232:233],v23 offset:32\n"
MF "a[152:155],a[236:239],v[44:47],a[152:155]\n"
DT "a[234:235],v23 offset:4640\n"
BL "v[72:75],v246,s[28:31],s44 offen offset:2048 sc1\n"
MF "a[188:191],a[236:239],v[60:63],a[188:191]\n"
DT "a[236:237],v24 offset:0\n"
MF "a[156:159],a[240:243],v[44:47],a[156:159]\n"
DT "a[238:239],v24 offset:4608\n"
MF "a[192:195],a[240:243],v[60:63],a[192:195]\n"
DT "a[240:241],v24 offset:32\n"
MF "a[160:163],a[244:247],v[44:47],a[160:163]\n"
DT "a[242:243],v24 offset:4640\n"
MF "a[196:199],a[244:247],v[60:63],a[196:199]\n"
DT "a[244:245],v25 offset:0\n"
DT "a[246:247],v25 offset:4608\n"
MF "v[80:83],a[64:67],a[0:3],v[104:107]\n"
"s_add_u32 s44,s58,0\n"
DT "a[112:113],v7 offset:128\n"
CV "v120,v88,v89\n"
MF "v[84:87],a[68:71],a[0:3],v[104:107]\n"
DT "a[114:115],v7 offset:640\n"
BL "v[44:47],v244,s[28:31],s44 offen offset:3072 sc1\n"
MF "v[80:83],a[72:75],a[4:7],v[80:83]\n"
DT "a[116:117],v7 offset:160\n"
CV "v121,v90,v91\n"
MF "v[84:87],a[76:79],a[4:7],v[84:87]\n"
DT "a[118:119],v7 offset:672\n"
CV "v122,v92,v93\n"
MF "v[80:83],a[80:83],a[8:11],v[80:83]\n"
DT "a[120:121],v7 offset:192\n"
BL "v[60:63],v245,s[28:31],s44 offen offset:3072 sc1\n"
MF "v[84:87],a[84:87],a[8:11],v[84:87]\n"
DT "a[122:123],v7 offset:704\n"
CV "v123,v94,v95\n"
MF "v[80:83],a[88:91],a[12:15],v[80:83]\n"
DT "a[124:125],v7 offset:224\n"
MF "v[84:87],a[92:95],a[12:15],v[84:87]\n"
DT "a[126:127],v7 offset:736\n"
BL "v[76:79],v246,s[28:31],s44 offen offset:3072 sc1\n"
MF "v[88:91],a[64:67],a[16:19],v[108:111]\n"
"s_add_u32 m0,s76,121344\n"
"s_add_u32 s67,s68,147456\n"
"v_add_u32 v6,s49,v4\n"
"v_add_u32 v7,s49,v5\n"
MF "v[92:95],a[68:71],a[16:19],v[108:111]\n"
"s_mov_b32 s50,s42\n"
EX "v80,v80\n"
MF "v[88:91],a[72:75],a[20:23],v[88:91]\n"
"s_mov_b32 s42,s49\n"
BL "v12,s[24:27],s67 offen offset:0 lds\n"
EX "v81,v81\n"
MF "v[92:95],a[76:79],a[20:23],v[92:95]\n"
EX "v82,v82\n"
MF "v[88:91],a[80:83],a[24:27],v[88:91]\n"
EX "v83,v83\n"
MF "v[92:95],a[84:87],a[24:27],v[92:95]\n"
BL "v13,s[24:27],s67 offen offset:1024 lds\n"
EX "v84,v84\n"
MF "v[88:91],a[88:91],a[28:31],v[88:91]\n"
EX "v85,v85\n"
MF "v[92:95],a[92:95],a[28:31],v[92:95]\n"
WT "vmcnt(17)\n"
WT "lgkmcnt(0)\n"
"s_barrier\n"
MF "v[208:211],a[96:99],v[124:127],v[208:211]\n"
BL "v14,s[24:27],s67 offen offset:2048 lds\n"
EX "v86,v86\n"
MF "v[212:215],a[100:103],v[124:127],v[212:215]\n"
"s_add_u32 m0,s76,124416\n"
"s_add_u32 s67,s68,147456\n"
EX "v87,v87\n"
MF "v[216:219],a[104:107],v[124:127],v[216:219]\n"
MF "v[220:223],a[108:111],v[124:127],v[220:223]\n"
EX "v88,v88\n"
MF "v[240:243],a[48:51],v[124:127],v[240:243]\n"
BL "v15,s[24:27],s67 offen offset:0 lds\n"
EX "v89,v89\n"
MF "v[136:139],a[96:99],v[116:119],v[136:139]\n"
MF "v[140:143],a[100:103],v[116:119],v[140:143]\n"
MF "v[144:147],a[104:107],v[116:119],v[144:147]\n"
BL "v16,s[24:27],s67 offen offset:1024 lds\n"
MF "v[148:151],a[108:111],v[116:119],v[148:151]\n"
DR "v[124:127],v254\n"
"v_xor_b32 v254,0x400,v254\n"
MF "v[152:155],a[112:115],v[116:119],v[152:155]\n"
EX "v90,v90\n"
MF "v[156:159],a[116:119],v[116:119],v[156:159]\n"
BL "v17,s[24:27],s67 offen offset:2048 lds\n"
EX "v91,v91\n"
MF "v[160:163],a[120:123],v[116:119],v[160:163]\n"
"s_add_u32 m0,s76,127488\n"
"s_add_u32 s67,s68,147456\n"
"s_add_u32 s49,s49,16896\n"
EX "v92,v92\n"
MF "v[164:167],a[124:127],v[116:119],v[164:167]\n"
"s_cmp_eq_u32 s49,84480\n"
EX "v93,v93\n"
MF "v[168:171],a[48:51],v[116:119],v[168:171]\n"
"s_cselect_b32 s49,0,s49\n"
BL "v18,s[24:27],s67 offen offset:0 lds\n"
EX "v94,v94\n"
MF "v[172:175],a[96:99],v[120:123],v[172:175]\n"
DT "a[96:97],v7 offset:0\n"
DT "a[98:99],v7 offset:512\n"
EX "v95,v95\n"
MF "v[176:179],a[100:103],v[120:123],v[176:179]\n"
DT "a[100:101],v7 offset:32\n"
DT "a[102:103],v7 offset:544\n"
CV "v116,v80,v81\n"
MF "v[180:183],a[104:107],v[120:123],v[180:183]\n"
DT "a[104:105],v7 offset:64\n"
DT "a[106:107],v7 offset:576\n"
BL "v19,s[24:27],s67 offen offset:1024 lds\n"
CV "v117,v82,v83\n"
MF "v[184:187],a[108:111],v[120:123],v[184:187]\n"
DT "a[108:109],v7 offset:96\n"
DT "a[110:111],v7 offset:608\n"
CV "v118,v84,v85\n"
MF "v[188:191],a[112:115],v[120:123],v[188:191]\n"
DT "a[112:113],v7 offset:128\n"
DT "a[114:115],v7 offset:640\n"
CV "v119,v86,v87\n"
MF "v[192:195],a[116:119],v[120:123],v[192:195]\n"
DT "a[116:117],v7 offset:160\n"
DT "a[118:119],v7 offset:672\n"
MF "v[196:199],a[120:123],v[120:123],v[196:199]\n"
DT "a[120:121],v7 offset:192\n"
DT "a[122:123],v7 offset:704\n"
BL "v20,s[24:27],s67 offen offset:2048 lds\n"
MF "v[200:203],a[124:127],v[120:123],v[200:203]\n"
DT "a[124:125],v7 offset:224\n"
DT "a[126:127],v7 offset:736\n"
MF "v[204:207],a[48:51],v[120:123],v[204:207]\n"
CV "v120,v88,v89\n"
CV "v121,v90,v91\n"
CV "v122,v92,v93\n"
CV "v123,v94,v95\n"
WT "vmcnt(24)\n"
WT "lgkmcnt(0)\n"
"s_barrier\n"
MF "v[208:211],a[96:99],v[124:127],v[208:211]\n"
MF "v[212:215],a[100:103],v[124:127],v[212:215]\n"
MF "v[216:219],a[104:107],v[124:127],v[216:219]\n"
MF "v[220:223],a[108:111],v[124:127],v[220:223]\n"
MF "v[240:243],a[48:51],v[124:127],v[240:243]\n"
MF "v[136:139],a[96:99],v[116:119],v[136:139]\n"
MF "v[140:143],a[100:103],v[116:119],v[140:143]\n"
MF "v[144:147],a[104:107],v[116:119],v[144:147]\n"
MF "v[148:151],a[108:111],v[116:119],v[148:151]\n"
MF "v[152:155],a[112:115],v[116:119],v[152:155]\n"
MF "v[156:159],a[116:119],v[116:119],v[156:159]\n"
MF "v[160:163],a[120:123],v[116:119],v[160:163]\n"
MF "v[164:167],a[124:127],v[116:119],v[164:167]\n"
MF "v[168:171],a[48:51],v[116:119],v[168:171]\n"
MF "v[172:175],a[96:99],v[120:123],v[172:175]\n"
MF "v[176:179],a[100:103],v[120:123],v[176:179]\n"
MF "v[180:183],a[104:107],v[120:123],v[180:183]\n"
MF "v[184:187],a[108:111],v[120:123],v[184:187]\n"
MF "v[188:191],a[112:115],v[120:123],v[188:191]\n"
MF "v[192:195],a[116:119],v[120:123],v[192:195]\n"
MF "v[196:199],a[120:123],v[120:123],v[196:199]\n"
MF "v[200:203],a[124:127],v[120:123],v[200:203]\n"
MF "v[204:207],a[48:51],v[120:123],v[204:207]\n"
"s_nop 1\n"
"v_rcp_f32 v130,v240\n"
"s_add_u32 s66,s56,36864\n"
PM "v[208:209],v[208:209],v[130:131] op_sel_hi:[1,0]\n"
PM "v[210:211],v[210:211],v[130:131] op_sel_hi:[1,0]\n"
PM "v[212:213],v[212:213],v[130:131] op_sel_hi:[1,0]\n"
PM "v[214:215],v[214:215],v[130:131] op_sel_hi:[1,0]\n"
CV "v224,v208,v209\n"
CV "v225,v210,v211\n"
CV "v226,v212,v213\n"
CV "v227,v214,v215\n"
"buffer_store_dwordx4 v[224:227],v246,s[28:31],s66 offen offset:0 sc1\n"
"ds_write_b128 v247,v[224:227] offset:0\n"
PM "v[216:217],v[216:217],v[130:131] op_sel_hi:[1,0]\n"
PM "v[218:219],v[218:219],v[130:131] op_sel_hi:[1,0]\n"
PM "v[220:221],v[220:221],v[130:131] op_sel_hi:[1,0]\n"
PM "v[222:223],v[222:223],v[130:131] op_sel_hi:[1,0]\n"
CV "v228,v216,v217\n"
CV "v229,v218,v219\n"
CV "v230,v220,v221\n"
CV "v231,v222,v223\n"
"buffer_store_dwordx4 v[228:231],v246,s[28:31],s66 offen offset:1024 sc1\n"
"ds_write_b128 v247,v[228:231] offset:1024\n"
"v_rcp_f32 v130,v168\n"
"s_add_u32 s66,s56,36864\n"
PM "v[136:137],v[136:137],v[130:131] op_sel_hi:[1,0]\n"
PM "v[138:139],v[138:139],v[130:131] op_sel_hi:[1,0]\n"
PM "v[140:141],v[140:141],v[130:131] op_sel_hi:[1,0]\n"
PM "v[142:143],v[142:143],v[130:131] op_sel_hi:[1,0]\n"
CV "v80,v136,v137\n"
CV "v81,v138,v139\n"
CV "v82,v140,v141\n"
CV "v83,v142,v143\n"
"buffer_store_dwordx4 v[80:83],v244,s[28:31],s66 offen offset:0 sc1\n"
PM "v[144:145],v[144:145],v[130:131] op_sel_hi:[1,0]\n"
PM "v[146:147],v[146:147],v[130:131] op_sel_hi:[1,0]\n"
PM "v[148:149],v[148:149],v[130:131] op_sel_hi:[1,0]\n"
PM "v[150:151],v[150:151],v[130:131] op_sel_hi:[1,0]\n"
CV "v84,v144,v145\n"
CV "v85,v146,v147\n"
CV "v86,v148,v149\n"
CV "v87,v150,v151\n"
"buffer_store_dwordx4 v[84:87],v244,s[28:31],s66 offen offset:1024 sc1\n"
PM "v[152:153],v[152:153],v[130:131] op_sel_hi:[1,0]\n"
PM "v[154:155],v[154:155],v[130:131] op_sel_hi:[1,0]\n"
PM "v[156:157],v[156:157],v[130:131] op_sel_hi:[1,0]\n"
PM "v[158:159],v[158:159],v[130:131] op_sel_hi:[1,0]\n"
CV "v88,v152,v153\n"
CV "v89,v154,v155\n"
CV "v90,v156,v157\n"
CV "v91,v158,v159\n"
"buffer_store_dwordx4 v[88:91],v244,s[28:31],s66 offen offset:2048 sc1\n"
PM "v[160:161],v[160:161],v[130:131] op_sel_hi:[1,0]\n"
PM "v[162:163],v[162:163],v[130:131] op_sel_hi:[1,0]\n"
PM "v[164:165],v[164:165],v[130:131] op_sel_hi:[1,0]\n"
PM "v[166:167],v[166:167],v[130:131] op_sel_hi:[1,0]\n"
CV "v92,v160,v161\n"
CV "v93,v162,v163\n"
CV "v94,v164,v165\n"
CV "v95,v166,v167\n"
"buffer_store_dwordx4 v[92:95],v244,s[28:31],s66 offen offset:3072 sc1\n"
"v_rcp_f32 v130,v204\n"
"s_add_u32 s66,s56,36864\n"
PM "v[172:173],v[172:173],v[130:131] op_sel_hi:[1,0]\n"
PM "v[174:175],v[174:175],v[130:131] op_sel_hi:[1,0]\n"
PM "v[176:177],v[176:177],v[130:131] op_sel_hi:[1,0]\n"
PM "v[178:179],v[178:179],v[130:131] op_sel_hi:[1,0]\n"
CV "v96,v172,v173\n"
CV "v97,v174,v175\n"
CV "v98,v176,v177\n"
CV "v99,v178,v179\n"
"buffer_store_dwordx4 v[96:99],v245,s[28:31],s66 offen offset:0 sc1\n"
PM "v[180:181],v[180:181],v[130:131] op_sel_hi:[1,0]\n"
PM "v[182:183],v[182:183],v[130:131] op_sel_hi:[1,0]\n"
PM "v[184:185],v[184:185],v[130:131] op_sel_hi:[1,0]\n"
PM "v[186:187],v[186:187],v[130:131] op_sel_hi:[1,0]\n"
CV "v100,v180,v181\n"
CV "v101,v182,v183\n"
CV "v102,v184,v185\n"
CV "v103,v186,v187\n"
"buffer_store_dwordx4 v[100:103],v245,s[28:31],s66 offen offset:1024 sc1\n"
PM "v[188:189],v[188:189],v[130:131] op_sel_hi:[1,0]\n"
PM "v[190:191],v[190:191],v[130:131] op_sel_hi:[1,0]\n"
PM "v[192:193],v[192:193],v[130:131] op_sel_hi:[1,0]\n"
PM "v[194:195],v[194:195],v[130:131] op_sel_hi:[1,0]\n"
CV "v104,v188,v189\n"
CV "v105,v190,v191\n"
CV "v106,v192,v193\n"
CV "v107,v194,v195\n"
"buffer_store_dwordx4 v[104:107],v245,s[28:31],s66 offen offset:2048 sc1\n"
PM "v[196:197],v[196:197],v[130:131] op_sel_hi:[1,0]\n"
PM "v[198:199],v[198:199],v[130:131] op_sel_hi:[1,0]\n"
PM "v[200:201],v[200:201],v[130:131] op_sel_hi:[1,0]\n"
PM "v[202:203],v[202:203],v[130:131] op_sel_hi:[1,0]\n"
CV "v108,v196,v197\n"
CV "v109,v198,v199\n"
CV "v110,v200,v201\n"
CV "v111,v202,v203\n"
"buffer_store_dwordx4 v[108:111],v245,s[28:31],s66 offen offset:3072 sc1\n"
"v_add_f32 v128,v168,v204\n"
"v_add_f32 v128,v128,v240\n"
"v_cmp_ngt_f32 vcc,s43,v128\n"
"s_cmp_lg_u64 vcc,0\n"
"s_cselect_b32 s66,1,0\n"
"v_mov_b32 v128,s66\n"
"ds_write_b32 v251,v128 offset:16\n"
"s_branch LO15_%=\n"
"Lw6_%=:\n"
PM "v[112:113],v[112:113],s[54:55]\n"
PM "v[114:115],v[114:115],s[54:55]\n"
PM "v[116:117],v[116:117],s[54:55]\n"
PM "v[118:119],v[118:119],s[54:55]\n"
CV "v104,v112,v113\n"
CV "v106,v116,v117\n"
CV "v105,v114,v115\n"
CV "v107,v118,v119\n"
PM "v[176:177],v[176:177],s[54:55]\n"
PM "v[178:179],v[178:179],s[54:55]\n"
PM "v[180:181],v[180:181],s[54:55]\n"
PM "v[182:183],v[182:183],s[54:55]\n"
CV "v108,v176,v177\n"
CV "v110,v180,v181\n"
CV "v109,v178,v179\n"
CV "v111,v182,v183\n"
"v_permlane16_swap_b32 v104,v106\n"
"v_permlane16_swap_b32 v105,v107\n"
"v_accvgpr_write_b32 a8,v104\n"
"v_accvgpr_write_b32 a9,v105\n"
"v_accvgpr_write_b32 a10,v106\n"
"v_accvgpr_write_b32 a11,v107\n"
PM "v[120:121],v[120:121],s[54:55]\n"
PM "v[122:123],v[122:123],s[54:55]\n"
PM "v[124:125],v[124:125],s[54:55]\n"
PM "v[126:127],v[126:127],s[54:55]\n"
CV "v104,v120,v121\n"
CV "v106,v124,v125\n"
CV "v105,v122,v123\n"
CV "v107,v126,v127\n"
"v_permlane16_swap_b32 v108,v110\n"
"v_permlane16_swap_b32 v109,v111\n"
"v_accvgpr_write_b32 a24,v108\n"
"v_accvgpr_write_b32 a25,v109\n"
"v_accvgpr_write_b32 a26,v110\n"
"v_accvgpr_write_b32 a27,v111\n"
PM "v[184:185],v[184:185],s[54:55]\n"
PM "v[186:187],v[186:187],s[54:55]\n"
PM "v[188:189],v[188:189],s[54:55]\n"
PM "v[190:191],v[190:191],s[54:55]\n"
CV "v108,v184,v185\n"
CV "v110,v188,v189\n"
CV "v109,v186,v187\n"
CV "v111,v190,v191\n"
"v_permlane16_swap_b32 v104,v106\n"
"v_permlane16_swap_b32 v105,v107\n"
"v_accvgpr_write_b32 a12,v104\n"
"v_accvgpr_write_b32 a13,v105\n"
"v_accvgpr_write_b32 a14,v106\n"
"v_accvgpr_write_b32 a15,v107\n"
PM "v[128:129],v[128:129],s[54:55]\n"
PM "v[130:131],v[130:131],s[54:55]\n"
PM "v[132:133],v[132:133],s[54:55]\n"
PM "v[134:135],v[134:135],s[54:55]\n"
CV "v32,v128,v129\n"
CV "v34,v132,v133\n"
CV "v33,v130,v131\n"
CV "v35,v134,v135\n"
"v_permlane16_swap_b32 v108,v110\n"
"v_permlane16_swap_b32 v109,v111\n"
"v_accvgpr_write_b32 a28,v108\n"
"v_accvgpr_write_b32 a29,v109\n"
"v_accvgpr_write_b32 a30,v110\n"
"v_accvgpr_write_b32 a31,v111\n"
PM "v[192:193],v[192:193],s[54:55]\n"
PM "v[194:195],v[194:195],s[54:55]\n"
PM "v[196:197],v[196:197],s[54:55]\n"
PM "v[198:199],v[198:199],s[54:55]\n"
CV "v48,v192,v193\n"
CV "v50,v196,v197\n"
CV "v49,v194,v195\n"
CV "v51,v198,v199\n"
"v_permlane16_swap_b32 v32,v34\n"
"v_permlane16_swap_b32 v33,v35\n"
PM "v[136:137],v[136:137],s[54:55]\n"
PM "v[138:139],v[138:139],s[54:55]\n"
PM "v[140:141],v[140:141],s[54:55]\n"
PM "v[142:143],v[142:143],s[54:55]\n"
CV "v36,v136,v137\n"
CV "v38,v140,v141\n"
CV "v37,v138,v139\n"
CV "v39,v142,v143\n"
"v_permlane16_swap_b32 v48,v50\n"
"v_permlane16_swap_b32 v49,v51\n"
PM "v[200:201],v[200:201],s[54:55]\n"
PM "v[202:203],v[202:203],s[54:55]\n"
PM "v[204:205],v[204:205],s[54:55]\n"
PM "v[206:207],v[206:207],s[54:55]\n"
CV "v52,v200,v201\n"
CV "v54,v204,v205\n"
CV "v53,v202,v203\n"
CV "v55,v206,v207\n"
"v_permlane16_swap_b32 v36,v38\n"
"v_permlane16_swap_b32 v37,v39\n"
PM "v[144:145],v[144:145],s[54:55]\n"
PM "v[146:147],v[146:147],s[54:55]\n"
PM "v[148:149],v[148:149],s[54:55]\n"
PM "v[150:151],v[150:151],s[54:55]\n"
CV "v40,v144,v145\n"
CV "v42,v148,v149\n"
CV "v41,v146,v147\n"
CV "v43,v150,v151\n"
"v_permlane16_swap_b32 v52,v54\n"
"v_permlane16_swap_b32 v53,v55\n"
PM "v[208:209],v[208:209],s[54:55]\n"
PM "v[210:211],v[210:211],s[54:55]\n"
PM "v[212:213],v[212:213],s[54:55]\n"
PM "v[214:215],v[214:215],s[54:55]\n"
CV "v56,v208,v209\n"
CV "v58,v212,v213\n"
CV "v57,v210,v211\n"
CV "v59,v214,v215\n"
"v_permlane16_swap_b32 v40,v42\n"
"v_permlane16_swap_b32 v41,v43\n"
PM "v[152:153],v[152:153],s[54:55]\n"
PM "v[154:155],v[154:155],s[54:55]\n"
PM "v[156:157],v[156:157],s[54:55]\n"
PM "v[158:159],v[158:159],s[54:55]\n"
CV "v44,v152,v153\n"
CV "v46,v156,v157\n"
CV "v45,v154,v155\n"
CV "v47,v158,v159\n"
"v_permlane16_swap_b32 v56,v58\n"
"v_permlane16_swap_b32 v57,v59\n"
PM "v[216:217],v[216:217],s[54:55]\n"
PM "v[218:219],v[218:219],s[54:55]\n"
PM "v[220:221],v[220:221],s[54:55]\n"
PM "v[222:223],v[222:223],s[54:55]\n"
CV "v60,v216,v217\n"
CV "v62,v220,v221\n"
CV "v61,v218,v219\n"
CV "v63,v222,v223\n"
"v_permlane16_swap_b32 v44,v46\n"
"v_permlane16_swap_b32 v45,v47\n"
PM "v[160:161],v[160:161],s[54:55]\n"
PM "v[162:163],v[162:163],s[54:55]\n"
PM "v[164:165],v[164:165],s[54:55]\n"
PM "v[166:167],v[166:167],s[54:55]\n"
CV "v104,v160,v161\n"
CV "v106,v164,v165\n"
CV "v105,v162,v163\n"
CV "v107,v166,v167\n"
"v_permlane16_swap_b32 v60,v62\n"
"v_permlane16_swap_b32 v61,v63\n"
PM "v[224:225],v[224:225],s[54:55]\n"
PM "v[226:227],v[226:227],s[54:55]\n"
PM "v[228:229],v[228:229],s[54:55]\n"
PM "v[230:231],v[230:231],s[54:55]\n"
CV "v108,v224,v225\n"
CV "v110,v228,v229\n"
CV "v109,v226,v227\n"
CV "v111,v230,v231\n"
"v_permlane16_swap_b32 v104,v106\n"
"v_permlane16_swap_b32 v105,v107\n"
"v_accvgpr_write_b32 a0,v104\n"
"v_accvgpr_write_b32 a1,v105\n"
"v_accvgpr_write_b32 a2,v106\n"
"v_accvgpr_write_b32 a3,v107\n"
PM "v[168:169],v[168:169],s[54:55]\n"
PM "v[170:171],v[170:171],s[54:55]\n"
PM "v[172:173],v[172:173],s[54:55]\n"
PM "v[174:175],v[174:175],s[54:55]\n"
CV "v104,v168,v169\n"
CV "v106,v172,v173\n"
CV "v105,v170,v171\n"
CV "v107,v174,v175\n"
"v_permlane16_swap_b32 v108,v110\n"
"v_permlane16_swap_b32 v109,v111\n"
"v_accvgpr_write_b32 a16,v108\n"
"v_accvgpr_write_b32 a17,v109\n"
"v_accvgpr_write_b32 a18,v110\n"
"v_accvgpr_write_b32 a19,v111\n"
PM "v[232:233],v[232:233],s[54:55]\n"
PM "v[234:235],v[234:235],s[54:55]\n"
PM "v[236:237],v[236:237],s[54:55]\n"
PM "v[238:239],v[238:239],s[54:55]\n"
CV "v108,v232,v233\n"
CV "v110,v236,v237\n"
CV "v109,v234,v235\n"
CV "v111,v238,v239\n"
"v_permlane16_swap_b32 v104,v106\n"
"v_permlane16_swap_b32 v105,v107\n"
"v_accvgpr_write_b32 a4,v104\n"
"v_accvgpr_write_b32 a5,v105\n"
"v_accvgpr_write_b32 a6,v106\n"
"v_accvgpr_write_b32 a7,v107\n"
"s_nop 1\n"
"v_permlane16_swap_b32 v108,v110\n"
"v_permlane16_swap_b32 v109,v111\n"
"v_accvgpr_write_b32 a20,v108\n"
"v_accvgpr_write_b32 a21,v109\n"
"v_accvgpr_write_b32 a22,v110\n"
"v_accvgpr_write_b32 a23,v111\n"
"s_lshl_b32 s66,s36,13\n"
"v_add_u32 v244,s66,v1\n"
"v_add_u32 v245,0x1000,v244\n"
"v_add_u32 v246,0x8000,v1\n"
"v_add_u32 v247,158208,v1\n"
"v_add_u32 v253,84480,v1\n"
"v_mov_b32 v252,162304\n"
"v_lshl_add_u32 v251,s36,2,v252\n"
"s_mov_b32 s43,1400897536\n"
"v_add_u32 v248,73728,v1\n"
"v_lshl_add_u32 v249,s36,10,v246\n"
WT "vmcnt(16)\n"
WT "lgkmcnt(0)\n"
"s_barrier\n"
DR "a[64:67],v4 offset:0\n"
DR "a[68:71],v4 offset:512\n"
DR "a[72:75],v4 offset:64\n"
DR "a[76:79],v4 offset:576\n"
DR "a[80:83],v4 offset:128\n"
DR "a[84:87],v4 offset:640\n"
DR "a[88:91],v4 offset:192\n"
DR "a[92:95],v4 offset:704\n"
"s_cmp_lg_u32 s98,0\n"
"s_cbranch_scc1 LS13_%=\n"
"v_add_u32 v254,159744,v1\n"
WT "lgkmcnt(0)\n"
MF "v[80:83],a[64:67],v[32:35],0\n"
"v_mov_b64 v[136:137],0\n"
"v_mov_b64 v[138:139],0\n"
"v_mov_b64 v[140:141],0\n"
"v_mov_b64 v[142:143],0\n"
MF "v[84:87],a[68:71],v[32:35],0\n"
"v_mov_b64 v[144:145],0\n"
"v_mov_b64 v[146:147],0\n"
"v_mov_b64 v[148:149],0\n"
MF "v[80:83],a[72:75],v[36:39],v[80:83]\n"
"v_mov_b64 v[150:151],0\n"
"v_mov_b64 v[152:153],0\n"
"v_mov_b64 v[154:155],0\n"
MF "v[84:87],a[76:79],v[36:39],v[84:87]\n"
"v_mov_b64 v[156:157],0\n"
"v_mov_b64 v[158:159],0\n"
"v_mov_b64 v[160:161],0\n"
"v_mov_b64 v[162:163],0\n"
MF "v[80:83],a[80:83],v[40:43],v[80:83]\n"
"v_mov_b64 v[164:165],0\n"
"v_mov_b64 v[166:167],0\n"
"v_mov_b64 v[168:169],0\n"
MF "v[84:87],a[84:87],v[40:43],v[84:87]\n"
"v_mov_b64 v[170:171],0\n"
"v_mov_b64 v[172:173],0\n"
"v_mov_b64 v[174:175],0\n"
MF "v[80:83],a[88:91],v[44:47],v[80:83]\n"
"v_mov_b64 v[176:177],0\n"
"v_mov_b64 v[178:179],0\n"
"v_mov_b64 v[180:181],0\n"
"v_mov_b64 v[182:183],0\n"
MF "v[84:87],a[92:95],v[44:47],v[84:87]\n"
"v_mov_b64 v[184:185],0\n"
"v_mov_b64 v[186:187],0\n"
"v_mov_b64 v[188:189],0\n"
MF "v[88:91],a[64:67],v[48:51],0\n"
"v_mov_b64 v[190:191],0\n"
"v_mov_b64 v[192:193],0\n"
"v_mov_b64 v[194:195],0\n"
MF "v[92:95],a[68:71],v[48:51],0\n"
"v_mov_b64 v[196:197],0\n"
"v_mov_b64 v[198:199],0\n"
"v_mov_b64 v[200:201],0\n"
"v_mov_b64 v[202:203],0\n"
MF "v[88:91],a[72:75],v[52:55],v[88:91]\n"
"v_mov_b64 v[204:205],0\n"
"v_mov_b64 v[206:207],0\n"
"v_mov_b64 v[208:209],0\n"
MF "v[92:95],a[76:79],v[52:55],v[92:95]\n"
"v_mov_b64 v[210:211],0\n"
"v_mov_b64 v[212:213],0\n"
"v_mov_b64 v[214:215],0\n"
MF "v[88:91],a[80:83],v[56:59],v[88:91]\n"
"v_mov_b64 v[216:217],0\n"
"v_mov_b64 v[218:219],0\n"
"v_mov_b64 v[220:221],0\n"
"v_mov_b64 v[222:223],0\n"
MF "v[92:95],a[84:87],v[56:59],v[92:95]\n"
"v_mov_b64 v[240:241],0\n"
"v_mov_b64 v[242:243],0\n"
"v_add_u32 v6,s49,v4\n"
MF "v[88:91],a[88:91],v[60:63],v[88:91]\n"
"v_add_u32 v7,s49,v5\n"
"s_mov_b32 s50,s42\n"
"s_mov_b32 s42,s49\n"
MF "v[92:95],a[92:95],v[60:63],v[92:95]\n"
"s_add_u32 s49,s49,16896\n"
"s_cmp_eq_u32 s49,84480\n"
"s_cselect_b32 s49,0,s49\n"
WT "vmcnt(12)\n"
WT "lgkmcnt(0)\n"
"s_barrier\n"
DR "a[64:67],v6 offset:0\n"
DR "a[68:71],v6 offset:512\n"
DR "a[72:75],v6 offset:64\n"
DR "a[76:79],v6 offset:576\n"
DR "a[80:83],v6 offset:128\n"
DR "a[84:87],v6 offset:640\n"
DR "a[88:91],v6 offset:192\n"
DR "a[92:95],v6 offset:704\n"
DR "v[124:127],v254\n"
"v_xor_b32 v254,0x400,v254\n"
DT "a[96:97],v7 offset:0\n"
DT "a[98:99],v7 offset:512\n"
DT "a[100:101],v7 offset:32\n"
DT "a[102:103],v7 offset:544\n"
DT "a[104:105],v7 offset:64\n"
DT "a[106:107],v7 offset:576\n"
DT "a[108:109],v7 offset:96\n"
DT "a[110:111],v7 offset:608\n"
"v_max3_f32 v128,v80,v81,v82\n"
"v_max3_f32 v128,v128,v83,v84\n"
"v_max3_f32 v128,v128,v85,v86\n"
"v_max_f32 v128,v128,v87\n"
"v_mov_b32 v129,v128\n"
"s_nop 1\n"
"v_permlane32_swap_b32 v129,v128\n"
"v_max_f32 v128,v129,v128\n"
"v_mov_b32 v129,v128\n"
"s_nop 1\n"
"v_permlane16_swap_b32 v129,v128\n"
"v_max_f32 v128,v129,v128\n"
"v_sub_f32 v80,v80,v128\n"
"v_sub_f32 v81,v81,v128\n"
"v_sub_f32 v82,v82,v128\n"
"v_sub_f32 v83,v83,v128\n"
"v_sub_f32 v84,v84,v128\n"
"v_sub_f32 v85,v85,v128\n"
"v_sub_f32 v86,v86,v128\n"
"v_sub_f32 v87,v87,v128\n"
"v_sub_f32 v104,0,v128\n"
"v_sub_f32 v105,0,v128\n"
"v_sub_f32 v106,0,v128\n"
"v_sub_f32 v107,0,v128\n"
EX "v80,v80\n"
EX "v81,v81\n"
EX "v82,v82\n"
EX "v83,v83\n"
EX "v84,v84\n"
EX "v85,v85\n"
EX "v86,v86\n"
EX "v87,v87\n"
"v_max3_f32 v128,v88,v89,v90\n"
"v_max3_f32 v128,v128,v91,v92\n"
"v_max3_f32 v128,v128,v93,v94\n"
"v_max_f32 v128,v128,v95\n"
"v_mov_b32 v129,v128\n"
"s_nop 1\n"
"v_permlane32_swap_b32 v129,v128\n"
"v_max_f32 v128,v129,v128\n"
"v_mov_b32 v129,v128\n"
"s_nop 1\n"
"v_permlane16_swap_b32 v129,v128\n"
"v_max_f32 v128,v129,v128\n"
"v_sub_f32 v88,v88,v128\n"
"v_sub_f32 v89,v89,v128\n"
"v_sub_f32 v90,v90,v128\n"
"v_sub_f32 v91,v91,v128\n"
"v_sub_f32 v92,v92,v128\n"
"v_sub_f32 v93,v93,v128\n"
"v_sub_f32 v94,v94,v128\n"
"v_sub_f32 v95,v95,v128\n"
"v_sub_f32 v108,0,v128\n"
"v_sub_f32 v109,0,v128\n"
"v_sub_f32 v110,0,v128\n"
"v_sub_f32 v111,0,v128\n"
CV "v116,v80,v81\n"
CV "v117,v82,v83\n"
CV "v118,v84,v85\n"
CV "v119,v86,v87\n"
EX "v88,v88\n"
EX "v89,v89\n"
EX "v90,v90\n"
EX "v91,v91\n"
EX "v92,v92\n"
EX "v93,v93\n"
EX "v94,v94\n"
EX "v95,v95\n"
WT "lgkmcnt(8)\n"
"s_mov_b32 s52,163840\n"
"Ll366_%=:\n"
MF "v[80:83],a[64:67],v[32:35],v[104:107]\n"
"s_add_u32 m0,s50,s48\n"
DT "a[112:113],v7 offset:128\n"
CV "v120,v88,v89\n"
MF "v[84:87],a[68:71],v[32:35],v[104:107]\n"
DT "a[114:115],v7 offset:640\n"
BL "v8,s[12:15],s78 offen nt lds\n"
MF "v[80:83],a[72:75],v[36:39],v[80:83]\n"
DT "a[116:117],v7 offset:160\n"
CV "v121,v90,v91\n"
MF "v[84:87],a[76:79],v[36:39],v[84:87]\n"
DT "a[118:119],v7 offset:672\n"
CV "v122,v92,v93\n"
MF "v[80:83],a[80:83],v[40:43],v[80:83]\n"
DT "a[120:121],v7 offset:192\n"
CV "v123,v94,v95\n"
MF "v[84:87],a[84:87],v[40:43],v[84:87]\n"
DT "a[122:123],v7 offset:704\n"
BL "v9,s[12:15],s78 offen offset:1056 nt lds\n"
MF "v[80:83],a[88:91],v[44:47],v[80:83]\n"
"s_add_u32 m0,m0,8448\n"
DT "a[124:125],v7 offset:224\n"
MF "v[84:87],a[92:95],v[44:47],v[84:87]\n"
DT "a[126:127],v7 offset:736\n"
MF "v[88:91],a[64:67],v[48:51],v[108:111]\n"
"v_add_u32 v6,s49,v4\n"
"v_add_u32 v7,s49,v5\n"
MF "v[92:95],a[68:71],v[48:51],v[108:111]\n"
"s_mov_b32 s50,s42\n"
BL "v10,s[16:19],s79 offen nt lds\n"
EX "v80,v80\n"
MF "v[88:91],a[72:75],v[52:55],v[88:91]\n"
"s_mov_b32 s42,s49\n"
EX "v81,v81\n"
MF "v[92:95],a[76:79],v[52:55],v[92:95]\n"
EX "v82,v82\n"
MF "v[88:91],a[80:83],v[56:59],v[88:91]\n"
EX "v83,v83\n"
MF "v[92:95],a[84:87],v[56:59],v[92:95]\n"
BL "v11,s[16:19],s79 offen offset:1056 nt lds\n"
EX "v84,v84\n"
MF "v[88:91],a[88:91],v[60:63],v[88:91]\n"
"s_add_u32 s51,s51,8192\n"
EX "v85,v85\n"
MF "v[92:95],a[92:95],v[60:63],v[92:95]\n"
"s_mov_b32 s79,s78\n"
WT "vmcnt(10) lgkmcnt(0)\n"
"s_barrier\n"
MF "v[208:211],a[112:115],v[124:127],v[208:211]\n"
"s_xor_b32 s78,s51,s41\n"
DR "a[64:67],v6 offset:0\n"
EX "v86,v86\n"
MF "v[212:215],a[116:119],v[124:127],v[212:215]\n"
DR "a[68:71],v6 offset:512\n"
EX "v87,v87\n"
MF "v[216:219],a[120:123],v[124:127],v[216:219]\n"
DR "a[72:75],v6 offset:64\n"
MF "v[220:223],a[124:127],v[124:127],v[220:223]\n"
DR "a[76:79],v6 offset:576\n"
EX "v88,v88\n"
MF "v[240:243],a[48:51],v[124:127],v[240:243]\n"
DR "a[80:83],v6 offset:128\n"
EX "v89,v89\n"
MF "v[136:139],a[96:99],v[116:119],v[136:139]\n"
DR "a[84:87],v6 offset:640\n"
MF "v[140:143],a[100:103],v[116:119],v[140:143]\n"
DR "a[88:91],v6 offset:192\n"
MF "v[144:147],a[104:107],v[116:119],v[144:147]\n"
DR "a[92:95],v6 offset:704\n"
MF "v[148:151],a[108:111],v[116:119],v[148:151]\n"
DR "v[124:127],v254\n"
"v_xor_b32 v254,0x400,v254\n"
MF "v[152:155],a[112:115],v[116:119],v[152:155]\n"
EX "v90,v90\n"
MF "v[156:159],a[116:119],v[116:119],v[156:159]\n"
EX "v91,v91\n"
MF "v[160:163],a[120:123],v[116:119],v[160:163]\n"
"s_add_u32 s49,s49,16896\n"
EX "v92,v92\n"
MF "v[164:167],a[124:127],v[116:119],v[164:167]\n"
"s_cmp_eq_u32 s49,84480\n"
EX "v93,v93\n"
MF "v[168:171],a[48:51],v[116:119],v[168:171]\n"
"s_cselect_b32 s49,0,s49\n"
EX "v94,v94\n"
MF "v[172:175],a[96:99],v[120:123],v[172:175]\n"
EX "v95,v95\n"
MF "v[176:179],a[100:103],v[120:123],v[176:179]\n"
DT "a[96:97],v7 offset:0\n"
CV "v116,v80,v81\n"
MF "v[180:183],a[104:107],v[120:123],v[180:183]\n"
DT "a[98:99],v7 offset:512\n"
CV "v117,v82,v83\n"
MF "v[184:187],a[108:111],v[120:123],v[184:187]\n"
DT "a[100:101],v7 offset:32\n"
CV "v118,v84,v85\n"
MF "v[188:191],a[112:115],v[120:123],v[188:191]\n"
DT "a[102:103],v7 offset:544\n"
CV "v119,v86,v87\n"
MF "v[192:195],a[116:119],v[120:123],v[192:195]\n"
DT "a[104:105],v7 offset:64\n"
MF "v[196:199],a[120:123],v[120:123],v[196:199]\n"
DT "a[106:107],v7 offset:576\n"
MF "v[200:203],a[124:127],v[120:123],v[200:203]\n"
WT "lgkmcnt(6)\n"
DT "a[108:109],v7 offset:96\n"
MF "v[204:207],a[48:51],v[120:123],v[204:207]\n"
"s_cmp_lg_u32 s51,s52\n"
DT "a[110:111],v7 offset:608\n"
"s_cbranch_scc1 Ll366_%=\n"
MF "v[80:83],a[64:67],a[0:3],0\n"
"s_add_u32 m0,s50,s48\n"
DT "a[112:113],v7 offset:128\n"
CV "v120,v88,v89\n"
MF "v[84:87],a[68:71],a[0:3],0\n"
DT "a[114:115],v7 offset:640\n"
BL "v8,s[12:15],s78 offen nt lds\n"
MF "v[80:83],a[72:75],a[4:7],v[80:83]\n"
DT "a[116:117],v7 offset:160\n"
CV "v121,v90,v91\n"
MF "v[84:87],a[76:79],a[4:7],v[84:87]\n"
DT "a[118:119],v7 offset:672\n"
CV "v122,v92,v93\n"
MF "v[80:83],a[80:83],a[8:11],v[80:83]\n"
DT "a[120:121],v7 offset:192\n"
CV "v123,v94,v95\n"
MF "v[84:87],a[84:87],a[8:11],v[84:87]\n"
DT "a[122:123],v7 offset:704\n"
BL "v9,s[12:15],s78 offen offset:1056 nt lds\n"
MF "v[80:83],a[88:91],a[12:15],v[80:83]\n"
"s_add_u32 m0,m0,8448\n"
DT "a[124:125],v7 offset:224\n"
MF "v[84:87],a[92:95],a[12:15],v[84:87]\n"
DT "a[126:127],v7 offset:736\n"
MF "v[88:91],a[64:67],a[16:19],0\n"
"v_add_u32 v6,s49,v4\n"
"v_add_u32 v7,s49,v5\n"
MF "v[92:95],a[68:71],a[16:19],0\n"
"s_mov_b32 s50,s42\n"
BL "v10,s[16:19],s79 offen nt lds\n"
MF "v[88:91],a[72:75],a[20:23],v[88:91]\n"
"s_mov_b32 s42,s49\n"
MF "v[92:95],a[76:79],a[20:23],v[92:95]\n"
MF "v[88:91],a[80:83],a[24:27],v[88:91]\n"
MF "v[92:95],a[84:87],a[24:27],v[92:95]\n"
BL "v11,s[16:19],s79 offen offset:1056 nt lds\n"
MF "v[88:91],a[88:91],a[28:31],v[88:91]\n"
"s_add_u32 s51,s51,8192\n"
MF "v[92:95],a[92:95],a[28:31],v[92:95]\n"
"s_mov_b32 s79,s78\n"
WT "vmcnt(12)\n"
WT "lgkmcnt(0)\n"
"s_barrier\n"
MF "v[208:211],a[112:115],v[124:127],v[208:211]\n"
"s_xor_b32 s78,s51,s41\n"
DR "a[64:67],v6 offset:0\n"
"v_max3_f32 v128,v80,v81,v82\n"
"v_max3_f32 v128,v128,v83,v84\n"
"v_max3_f32 v128,v128,v85,v86\n"
MF "v[212:215],a[116:119],v[124:127],v[212:215]\n"
DR "a[68:71],v6 offset:512\n"
"v_max_f32 v128,v128,v87\n"
"v_mov_b32 v129,v128\n"
"s_nop 1\n"
MF "v[216:219],a[120:123],v[124:127],v[216:219]\n"
DR "a[72:75],v6 offset:64\n"
"v_permlane32_swap_b32 v129,v128\n"
"v_max_f32 v128,v129,v128\n"
"v_mov_b32 v129,v128\n"
MF "v[220:223],a[124:127],v[124:127],v[220:223]\n"
DR "a[76:79],v6 offset:576\n"
"s_nop 1\n"
"v_permlane16_swap_b32 v129,v128\n"
"v_max_f32 v128,v129,v128\n"
MF "v[240:243],a[48:51],v[124:127],v[240:243]\n"
DR "a[80:83],v6 offset:128\n"
"v_sub_f32 v80,v80,v128\n"
"v_sub_f32 v81,v81,v128\n"
"v_sub_f32 v82,v82,v128\n"
MF "v[136:139],a[96:99],v[116:119],v[136:139]\n"
DR "a[84:87],v6 offset:640\n"
"v_sub_f32 v83,v83,v128\n"
"v_sub_f32 v84,v84,v128\n"
"v_sub_f32 v85,v85,v128\n"
MF "v[140:143],a[100:103],v[116:119],v[140:143]\n"
DR "a[88:91],v6 offset:192\n"
"v_sub_f32 v86,v86,v128\n"
"v_sub_f32 v87,v87,v128\n"
MF "v[144:147],a[104:107],v[116:119],v[144:147]\n"
DR "a[92:95],v6 offset:704\n"
"v_sub_f32 v104,0,v128\n"
"v_sub_f32 v105,0,v128\n"
"v_sub_f32 v106,0,v128\n"
MF "v[148:151],a[108:111],v[116:119],v[148:151]\n"
DR "v[124:127],v254\n"
"v_xor_b32 v254,0x400,v254\n"
"v_sub_f32 v107,0,v128\n"
EX "v80,v80\n"
MF "v[152:155],a[112:115],v[116:119],v[152:155]\n"
EX "v81,v81\n"
EX "v82,v82\n"
MF "v[156:159],a[116:119],v[116:119],v[156:159]\n"
EX "v83,v83\n"
MF "v[160:163],a[120:123],v[116:119],v[160:163]\n"
"s_add_u32 s49,s49,16896\n"
EX "v84,v84\n"
EX "v85,v85\n"
MF "v[164:167],a[124:127],v[116:119],v[164:167]\n"
"s_cmp_eq_u32 s49,84480\n"
EX "v86,v86\n"
MF "v[168:171],a[48:51],v[116:119],v[168:171]\n"
"s_cselect_b32 s49,0,s49\n"
EX "v87,v87\n"
MF "v[172:175],a[96:99],v[120:123],v[172:175]\n"
CV "v116,v80,v81\n"
CV "v117,v82,v83\n"
CV "v118,v84,v85\n"
CV "v119,v86,v87\n"
"v_max3_f32 v128,v88,v89,v90\n"
MF "v[176:179],a[100:103],v[120:123],v[176:179]\n"
DT "a[96:97],v7 offset:0\n"
"v_max3_f32 v128,v128,v91,v92\n"
"v_max3_f32 v128,v128,v93,v94\n"
"v_max_f32 v128,v128,v95\n"
"v_mov_b32 v129,v128\n"
"s_nop 1\n"
MF "v[180:183],a[104:107],v[120:123],v[180:183]\n"
DT "a[98:99],v7 offset:512\n"
"v_permlane32_swap_b32 v129,v128\n"
"v_max_f32 v128,v129,v128\n"
"v_mov_b32 v129,v128\n"
"s_nop 1\n"
"v_permlane16_swap_b32 v129,v128\n"
MF "v[184:187],a[108:111],v[120:123],v[184:187]\n"
DT "a[100:101],v7 offset:32\n"
"v_max_f32 v128,v129,v128\n"
"v_sub_f32 v88,v88,v128\n"
"v_sub_f32 v89,v89,v128\n"
"v_sub_f32 v90,v90,v128\n"
"v_sub_f32 v91,v91,v128\n"
MF "v[188:191],a[112:115],v[120:123],v[188:191]\n"
DT "a[102:103],v7 offset:544\n"
"v_sub_f32 v92,v92,v128\n"
"v_sub_f32 v93,v93,v128\n"
"v_sub_f32 v94,v94,v128\n"
"v_sub_f32 v95,v95,v128\n"
"v_sub_f32 v108,0,v128\n"
MF "v[192:195],a[116:119],v[120:123],v[192:195]\n"
DT "a[104:105],v7 offset:64\n"
"v_sub_f32 v109,0,v128\n"
"v_sub_f32 v110,0,v128\n"
"v_sub_f32 v111,0,v128\n"
EX "v88,v88\n"
MF "v[196:199],a[120:123],v[120:123],v[196:199]\n"
DT "a[106:107],v7 offset:576\n"
EX "v89,v89\n"
EX "v90,v90\n"
EX "v91,v91\n"
MF "v[200:203],a[124:127],v[120:123],v[200:203]\n"
WT "lgkmcnt(6)\n"
DT "a[108:109],v7 offset:96\n"
EX "v92,v92\n"
EX "v93,v93\n"
MF "v[204:207],a[48:51],v[120:123],v[204:207]\n"
DT "a[110:111],v7 offset:608\n"
EX "v94,v94\n"
EX "v95,v95\n"
"v_rcp_f32 v130,v240\n"
"s_add_u32 s66,s56,s99\n"
PM "v[208:209],v[208:209],v[130:131] op_sel_hi:[1,0]\n"
PM "v[210:211],v[210:211],v[130:131] op_sel_hi:[1,0]\n"
PM "v[212:213],v[212:213],v[130:131] op_sel_hi:[1,0]\n"
PM "v[214:215],v[214:215],v[130:131] op_sel_hi:[1,0]\n"
CV "v224,v208,v209\n"
CV "v225,v210,v211\n"
CV "v226,v212,v213\n"
CV "v227,v214,v215\n"
"buffer_store_dwordx4 v[224:227],v246,s[28:31],s66 offen offset:2048 sc1\n"
"ds_write_b128 v253,v[224:227] offset:38912\n"
PM "v[216:217],v[216:217],v[130:131] op_sel_hi:[1,0]\n"
PM "v[218:219],v[218:219],v[130:131] op_sel_hi:[1,0]\n"
PM "v[220:221],v[220:221],v[130:131] op_sel_hi:[1,0]\n"
PM "v[222:223],v[222:223],v[130:131] op_sel_hi:[1,0]\n"
CV "v228,v216,v217\n"
CV "v229,v218,v219\n"
CV "v230,v220,v221\n"
CV "v231,v222,v223\n"
"buffer_store_dwordx4 v[228:231],v246,s[28:31],s66 offen offset:3072 sc1\n"
"ds_write_b128 v253,v[228:231] offset:39936\n"
"v_rcp_f32 v130,v168\n"
"s_add_u32 s66,s56,s99\n"
PM "v[136:137],v[136:137],v[130:131] op_sel_hi:[1,0]\n"
PM "v[138:139],v[138:139],v[130:131] op_sel_hi:[1,0]\n"
PM "v[140:141],v[140:141],v[130:131] op_sel_hi:[1,0]\n"
PM "v[142:143],v[142:143],v[130:131] op_sel_hi:[1,0]\n"
CV "v32,v136,v137\n"
CV "v33,v138,v139\n"
CV "v34,v140,v141\n"
CV "v35,v142,v143\n"
"buffer_store_dwordx4 v[32:35],v244,s[28:31],s66 offen offset:0 sc1\n"
PM "v[144:145],v[144:145],v[130:131] op_sel_hi:[1,0]\n"
PM "v[146:147],v[146:147],v[130:131] op_sel_hi:[1,0]\n"
PM "v[148:149],v[148:149],v[130:131] op_sel_hi:[1,0]\n"
PM "v[150:151],v[150:151],v[130:131] op_sel_hi:[1,0]\n"
CV "v36,v144,v145\n"
CV "v37,v146,v147\n"
CV "v38,v148,v149\n"
CV "v39,v150,v151\n"
"buffer_store_dwordx4 v[36:39],v244,s[28:31],s66 offen offset:1024 sc1\n"
PM "v[152:153],v[152:153],v[130:131] op_sel_hi:[1,0]\n"
PM "v[154:155],v[154:155],v[130:131] op_sel_hi:[1,0]\n"
PM "v[156:157],v[156:157],v[130:131] op_sel_hi:[1,0]\n"
PM "v[158:159],v[158:159],v[130:131] op_sel_hi:[1,0]\n"
CV "v40,v152,v153\n"
CV "v41,v154,v155\n"
CV "v42,v156,v157\n"
CV "v43,v158,v159\n"
"buffer_store_dwordx4 v[40:43],v244,s[28:31],s66 offen offset:2048 sc1\n"
PM "v[160:161],v[160:161],v[130:131] op_sel_hi:[1,0]\n"
PM "v[162:163],v[162:163],v[130:131] op_sel_hi:[1,0]\n"
PM "v[164:165],v[164:165],v[130:131] op_sel_hi:[1,0]\n"
PM "v[166:167],v[166:167],v[130:131] op_sel_hi:[1,0]\n"
CV "v44,v160,v161\n"
CV "v45,v162,v163\n"
CV "v46,v164,v165\n"
CV "v47,v166,v167\n"
"buffer_store_dwordx4 v[44:47],v244,s[28:31],s66 offen offset:3072 sc1\n"
"v_add_f32 v128,v168,v204\n"
"v_add_f32 v128,v128,v240\n"
"v_cmp_ngt_f32 vcc,s43,v128\n"
"s_cmp_lg_u64 vcc,0\n"
"s_cselect_b32 s66,1,0\n"
"v_mov_b32 v128,s66\n"
"ds_write_b32 v251,v128 offset:0\n"
"v_rcp_f32 v130,v204\n"
"s_add_u32 s66,s56,s99\n"
PM "v[172:173],v[172:173],v[130:131] op_sel_hi:[1,0]\n"
PM "v[174:175],v[174:175],v[130:131] op_sel_hi:[1,0]\n"
PM "v[176:177],v[176:177],v[130:131] op_sel_hi:[1,0]\n"
PM "v[178:179],v[178:179],v[130:131] op_sel_hi:[1,0]\n"
CV "v48,v172,v173\n"
CV "v49,v174,v175\n"
CV "v50,v176,v177\n"
CV "v51,v178,v179\n"
"buffer_store_dwordx4 v[48:51],v245,s[28:31],s66 offen offset:0 sc1\n"
PM "v[180:181],v[180:181],v[130:131] op_sel_hi:[1,0]\n"
PM "v[182:183],v[182:183],v[130:131] op_sel_hi:[1,0]\n"
PM "v[184:185],v[184:185],v[130:131] op_sel_hi:[1,0]\n"
PM "v[186:187],v[186:187],v[130:131] op_sel_hi:[1,0]\n"
CV "v52,v180,v181\n"
CV "v53,v182,v183\n"
CV "v54,v184,v185\n"
CV "v55,v186,v187\n"
"buffer_store_dwordx4 v[52:55],v245,s[28:31],s66 offen offset:1024 sc1\n"
PM "v[188:189],v[188:189],v[130:131] op_sel_hi:[1,0]\n"
PM "v[190:191],v[190:191],v[130:131] op_sel_hi:[1,0]\n"
PM "v[192:193],v[192:193],v[130:131] op_sel_hi:[1,0]\n"
PM "v[194:195],v[194:195],v[130:131] op_sel_hi:[1,0]\n"
CV "v56,v188,v189\n"
CV "v57,v190,v191\n"
CV "v58,v192,v193\n"
CV "v59,v194,v195\n"
"buffer_store_dwordx4 v[56:59],v245,s[28:31],s66 offen offset:2048 sc1\n"
PM "v[196:197],v[196:197],v[130:131] op_sel_hi:[1,0]\n"
PM "v[198:199],v[198:199],v[130:131] op_sel_hi:[1,0]\n"
PM "v[200:201],v[200:201],v[130:131] op_sel_hi:[1,0]\n"
PM "v[202:203],v[202:203],v[130:131] op_sel_hi:[1,0]\n"
CV "v60,v196,v197\n"
CV "v61,v198,v199\n"
CV "v62,v200,v201\n"
CV "v63,v202,v203\n"
"buffer_store_dwordx4 v[60:63],v245,s[28:31],s66 offen offset:3072 sc1\n"
"v_mov_b64 v[136:137],0\n"
"v_mov_b64 v[138:139],0\n"
"v_mov_b64 v[140:141],0\n"
"v_mov_b64 v[142:143],0\n"
"v_mov_b64 v[144:145],0\n"
"v_mov_b64 v[146:147],0\n"
"v_mov_b64 v[148:149],0\n"
"v_mov_b64 v[150:151],0\n"
"v_mov_b64 v[152:153],0\n"
"v_mov_b64 v[154:155],0\n"
"v_mov_b64 v[156:157],0\n"
"v_mov_b64 v[158:159],0\n"
"v_mov_b64 v[160:161],0\n"
"v_mov_b64 v[162:163],0\n"
"v_mov_b64 v[164:165],0\n"
"v_mov_b64 v[166:167],0\n"
"v_mov_b64 v[168:169],0\n"
"v_mov_b64 v[170:171],0\n"
"v_mov_b64 v[208:209],0\n"
"v_mov_b64 v[210:211],0\n"
"v_mov_b64 v[212:213],0\n"
"v_mov_b64 v[214:215],0\n"
"v_mov_b64 v[216:217],0\n"
"v_mov_b64 v[218:219],0\n"
"v_mov_b64 v[220:221],0\n"
"v_mov_b64 v[222:223],0\n"
"v_mov_b64 v[240:241],0\n"
"v_mov_b64 v[242:243],0\n"
"v_mov_b64 v[172:173],0\n"
"v_mov_b64 v[174:175],0\n"
"v_mov_b64 v[176:177],0\n"
"v_mov_b64 v[178:179],0\n"
"v_mov_b64 v[180:181],0\n"
"v_mov_b64 v[182:183],0\n"
"v_mov_b64 v[184:185],0\n"
"v_mov_b64 v[186:187],0\n"
"v_mov_b64 v[188:189],0\n"
"v_mov_b64 v[190:191],0\n"
"v_mov_b64 v[192:193],0\n"
"v_mov_b64 v[194:195],0\n"
"v_mov_b64 v[196:197],0\n"
"v_mov_b64 v[198:199],0\n"
"v_mov_b64 v[200:201],0\n"
"v_mov_b64 v[202:203],0\n"
"v_mov_b64 v[204:205],0\n"
"v_mov_b64 v[206:207],0\n"
"s_mov_b32 s52,204800\n"
"Ll367_%=:\n"
MF "v[80:83],a[64:67],a[0:3],v[104:107]\n"
"s_add_u32 m0,s50,s48\n"
DT "a[112:113],v7 offset:128\n"
CV "v120,v88,v89\n"
MF "v[84:87],a[68:71],a[0:3],v[104:107]\n"
DT "a[114:115],v7 offset:640\n"
BL "v8,s[12:15],s78 offen nt lds\n"
MF "v[80:83],a[72:75],a[4:7],v[80:83]\n"
DT "a[116:117],v7 offset:160\n"
CV "v121,v90,v91\n"
MF "v[84:87],a[76:79],a[4:7],v[84:87]\n"
DT "a[118:119],v7 offset:672\n"
CV "v122,v92,v93\n"
MF "v[80:83],a[80:83],a[8:11],v[80:83]\n"
DT "a[120:121],v7 offset:192\n"
CV "v123,v94,v95\n"
MF "v[84:87],a[84:87],a[8:11],v[84:87]\n"
DT "a[122:123],v7 offset:704\n"
BL "v9,s[12:15],s78 offen offset:1056 nt lds\n"
MF "v[80:83],a[88:91],a[12:15],v[80:83]\n"
"s_add_u32 m0,m0,8448\n"
DT "a[124:125],v7 offset:224\n"
MF "v[84:87],a[92:95],a[12:15],v[84:87]\n"
DT "a[126:127],v7 offset:736\n"
MF "v[88:91],a[64:67],a[16:19],v[108:111]\n"
"v_add_u32 v6,s49,v4\n"
"v_add_u32 v7,s49,v5\n"
MF "v[92:95],a[68:71],a[16:19],v[108:111]\n"
"s_mov_b32 s50,s42\n"
BL "v10,s[16:19],s79 offen nt lds\n"
EX "v80,v80\n"
MF "v[88:91],a[72:75],a[20:23],v[88:91]\n"
"s_mov_b32 s42,s49\n"
EX "v81,v81\n"
MF "v[92:95],a[76:79],a[20:23],v[92:95]\n"
EX "v82,v82\n"
MF "v[88:91],a[80:83],a[24:27],v[88:91]\n"
EX "v83,v83\n"
MF "v[92:95],a[84:87],a[24:27],v[92:95]\n"
BL "v11,s[16:19],s79 offen offset:1056 nt lds\n"
EX "v84,v84\n"
MF "v[88:91],a[88:91],a[28:31],v[88:91]\n"
"s_add_u32 s51,s51,8192\n"
EX "v85,v85\n"
MF "v[92:95],a[92:95],a[28:31],v[92:95]\n"
"s_mov_b32 s79,s78\n"
WT "vmcnt(10) lgkmcnt(0)\n"
"s_barrier\n"
MF "v[208:211],a[112:115],v[124:127],v[208:211]\n"
"s_xor_b32 s78,s51,s41\n"
DR "a[64:67],v6 offset:0\n"
EX "v86,v86\n"
MF "v[212:215],a[116:119],v[124:127],v[212:215]\n"
DR "a[68:71],v6 offset:512\n"
EX "v87,v87\n"
MF "v[216:219],a[120:123],v[124:127],v[216:219]\n"
DR "a[72:75],v6 offset:64\n"
MF "v[220:223],a[124:127],v[124:127],v[220:223]\n"
DR "a[76:79],v6 offset:576\n"
EX "v88,v88\n"
MF "v[240:243],a[48:51],v[124:127],v[240:243]\n"
DR "a[80:83],v6 offset:128\n"
EX "v89,v89\n"
MF "v[136:139],a[96:99],v[116:119],v[136:139]\n"
DR "a[84:87],v6 offset:640\n"
MF "v[140:143],a[100:103],v[116:119],v[140:143]\n"
DR "a[88:91],v6 offset:192\n"
MF "v[144:147],a[104:107],v[116:119],v[144:147]\n"
DR "a[92:95],v6 offset:704\n"
MF "v[148:151],a[108:111],v[116:119],v[148:151]\n"
DR "v[124:127],v254\n"
"v_xor_b32 v254,0x400,v254\n"
MF "v[152:155],a[112:115],v[116:119],v[152:155]\n"
EX "v90,v90\n"
MF "v[156:159],a[116:119],v[116:119],v[156:159]\n"
EX "v91,v91\n"
MF "v[160:163],a[120:123],v[116:119],v[160:163]\n"
"s_add_u32 s49,s49,16896\n"
EX "v92,v92\n"
MF "v[164:167],a[124:127],v[116:119],v[164:167]\n"
"s_cmp_eq_u32 s49,84480\n"
EX "v93,v93\n"
MF "v[168:171],a[48:51],v[116:119],v[168:171]\n"
"s_cselect_b32 s49,0,s49\n"
EX "v94,v94\n"
MF "v[172:175],a[96:99],v[120:123],v[172:175]\n"
EX "v95,v95\n"
MF "v[176:179],a[100:103],v[120:123],v[176:179]\n"
DT "a[96:97],v7 offset:0\n"
CV "v116,v80,v81\n"
MF "v[180:183],a[104:107],v[120:123],v[180:183]\n"
DT "a[98:99],v7 offset:512\n"
CV "v117,v82,v83\n"
MF "v[184:187],a[108:111],v[120:123],v[184:187]\n"
DT "a[100:101],v7 offset:32\n"
CV "v118,v84,v85\n"
MF "v[188:191],a[112:115],v[120:123],v[188:191]\n"
DT "a[102:103],v7 offset:544\n"
CV "v119,v86,v87\n"
MF "v[192:195],a[116:119],v[120:123],v[192:195]\n"
DT "a[104:105],v7 offset:64\n"
MF "v[196:199],a[120:123],v[120:123],v[196:199]\n"
DT "a[106:107],v7 offset:576\n"
MF "v[200:203],a[124:127],v[120:123],v[200:203]\n"
WT "lgkmcnt(6)\n"
DT "a[108:109],v7 offset:96\n"
MF "v[204:207],a[48:51],v[120:123],v[204:207]\n"
"s_cmp_lg_u32 s51,s52\n"
DT "a[110:111],v7 offset:608\n"
"s_cbranch_scc1 Ll367_%=\n"
DR "v[128:131],v252 offset:0\n"
DR "v[64:67],v253 offset:36864\n"
DR "v[68:71],v253 offset:37888\n"
DR "v[72:75],v253 offset:38912\n"
DR "v[76:79],v253 offset:39936\n"
WT "lgkmcnt(0)\n"
"v_or3_b32 v128,v128,v129,v130\n"
"v_or_b32 v128,v128,v131\n"
"v_cmp_ne_u32 vcc,0,v128\n"
"s_cbranch_vccnz Lr368_%=\n"
"s_add_u32 m0,s76,84480\n"
"s_add_u32 s67,s68,0\n"
BL "v12,s[24:27],s67 offen offset:0 lds\n"
BL "v13,s[24:27],s67 offen offset:1024 lds\n"
BL "v14,s[24:27],s67 offen offset:2048 lds\n"
"s_add_u32 m0,s76,87552\n"
"s_add_u32 s67,s68,0\n"
BL "v15,s[24:27],s67 offen offset:0 lds\n"
BL "v16,s[24:27],s67 offen offset:1024 lds\n"
BL "v17,s[24:27],s67 offen offset:2048 lds\n"
"s_add_u32 m0,s76,90624\n"
"s_add_u32 s67,s68,0\n"
BL "v18,s[24:27],s67 offen offset:0 lds\n"
BL "v19,s[24:27],s67 offen offset:1024 lds\n"
BL "v20,s[24:27],s67 offen offset:2048 lds\n"
MF "v[80:83],a[64:67],a[0:3],v[104:107]\n"
"s_add_u32 m0,s50,s48\n"
DT "a[112:113],v7 offset:128\n"
CV "v120,v88,v89\n"
MF "v[84:87],a[68:71],a[0:3],v[104:107]\n"
DT "a[114:115],v7 offset:640\n"
BL "v8,s[12:15],s78 offen nt lds\n"
MF "v[80:83],a[72:75],a[4:7],v[80:83]\n"
DT "a[116:117],v7 offset:160\n"
CV "v121,v90,v91\n"
MF "v[84:87],a[76:79],a[4:7],v[84:87]\n"
DT "a[118:119],v7 offset:672\n"
CV "v122,v92,v93\n"
MF "v[80:83],a[80:83],a[8:11],v[80:83]\n"
DT "a[120:121],v7 offset:192\n"
CV "v123,v94,v95\n"
MF "v[84:87],a[84:87],a[8:11],v[84:87]\n"
DT "a[122:123],v7 offset:704\n"
MF "v[80:83],a[88:91],a[12:15],v[80:83]\n"
DT "a[124:125],v7 offset:224\n"
MF "v[84:87],a[92:95],a[12:15],v[84:87]\n"
DT "a[126:127],v7 offset:736\n"
MF "v[88:91],a[64:67],a[16:19],v[108:111]\n"
"v_add_u32 v6,s49,v4\n"
"v_add_u32 v7,s49,v5\n"
MF "v[92:95],a[68:71],a[16:19],v[108:111]\n"
"s_mov_b32 s50,s42\n"
BL "v9,s[12:15],s78 offen offset:1056 nt lds\n"
EX "v80,v80\n"
MF "v[88:91],a[72:75],a[20:23],v[88:91]\n"
"s_add_u32 m0,m0,8448\n"
"s_mov_b32 s42,s49\n"
EX "v81,v81\n"
MF "v[92:95],a[76:79],a[20:23],v[92:95]\n"
EX "v82,v82\n"
MF "v[88:91],a[80:83],a[24:27],v[88:91]\n"
EX "v83,v83\n"
MF "v[92:95],a[84:87],a[24:27],v[92:95]\n"
EX "v84,v84\n"
MF "v[88:91],a[88:91],a[28:31],v[88:91]\n"
EX "v85,v85\n"
MF "v[92:95],a[92:95],a[28:31],v[92:95]\n"
WT "vmcnt(19)\n"
WT "lgkmcnt(0)\n"
"s_barrier\n"
MF "v[208:211],a[112:115],v[124:127],v[208:211]\n"
DR "a[64:67],v6 offset:0\n"
EX "v86,v86\n"
MF "v[212:215],a[116:119],v[124:127],v[212:215]\n"
DR "a[68:71],v6 offset:512\n"
EX "v87,v87\n"
MF "v[216:219],a[120:123],v[124:127],v[216:219]\n"
DR "a[72:75],v6 offset:64\n"
BL "v10,s[16:19],s79 offen nt lds\n"
MF "v[220:223],a[124:127],v[124:127],v[220:223]\n"
DR "a[76:79],v6 offset:576\n"
EX "v88,v88\n"
MF "v[240:243],a[48:51],v[124:127],v[240:243]\n"
DR "a[80:83],v6 offset:128\n"
EX "v89,v89\n"
MF "v[136:139],a[96:99],v[116:119],v[136:139]\n"
DR "a[84:87],v6 offset:640\n"
MF "v[140:143],a[100:103],v[116:119],v[140:143]\n"
DR "a[88:91],v6 offset:192\n"
MF "v[144:147],a[104:107],v[116:119],v[144:147]\n"
DR "a[92:95],v6 offset:704\n"
MF "v[148:151],a[108:111],v[116:119],v[148:151]\n"
DR "v[124:127],v254\n"
"v_xor_b32 v254,0x400,v254\n"
MF "v[152:155],a[112:115],v[116:119],v[152:155]\n"
EX "v90,v90\n"
MF "v[156:159],a[116:119],v[116:119],v[156:159]\n"
EX "v91,v91\n"
MF "v[160:163],a[120:123],v[116:119],v[160:163]\n"
"s_add_u32 s49,s49,16896\n"
BL "v11,s[16:19],s79 offen offset:1056 nt lds\n"
EX "v92,v92\n"
MF "v[164:167],a[124:127],v[116:119],v[164:167]\n"
"s_add_u32 s51,s51,8192\n"
"s_mov_b32 s79,s78\n"
"s_xor_b32 s78,s51,s41\n"
"s_cmp_eq_u32 s49,84480\n"
EX "v93,v93\n"
MF "v[168:171],a[48:51],v[116:119],v[168:171]\n"
"s_cselect_b32 s49,0,s49\n"
EX "v94,v94\n"
MF "v[172:175],a[96:99],v[120:123],v[172:175]\n"
EX "v95,v95\n"
MF "v[176:179],a[100:103],v[120:123],v[176:179]\n"
DT "a[96:97],v7 offset:0\n"
CV "v116,v80,v81\n"
MF "v[180:183],a[104:107],v[120:123],v[180:183]\n"
DT "a[98:99],v7 offset:512\n"
CV "v117,v82,v83\n"
MF "v[184:187],a[108:111],v[120:123],v[184:187]\n"
DT "a[100:101],v7 offset:32\n"
CV "v118,v84,v85\n"
MF "v[188:191],a[112:115],v[120:123],v[188:191]\n"
DT "a[102:103],v7 offset:544\n"
CV "v119,v86,v87\n"
MF "v[192:195],a[116:119],v[120:123],v[192:195]\n"
DT "a[104:105],v7 offset:64\n"
MF "v[196:199],a[120:123],v[120:123],v[196:199]\n"
DT "a[106:107],v7 offset:576\n"
"buffer_load_dword v31,v26,s[28:31],0 offen offset:0 sc1\n"
MF "v[200:203],a[124:127],v[120:123],v[200:203]\n"
WT "lgkmcnt(6)\n"
DT "a[108:109],v7 offset:96\n"
MF "v[204:207],a[48:51],v[120:123],v[204:207]\n"
DT "a[110:111],v7 offset:608\n"
MF "v[80:83],a[64:67],a[0:3],v[104:107]\n"
"s_add_u32 m0,s50,s48\n"
DT "a[112:113],v7 offset:128\n"
CV "v120,v88,v89\n"
MF "v[84:87],a[68:71],a[0:3],v[104:107]\n"
DT "a[114:115],v7 offset:640\n"
BL "v8,s[12:15],s78 offen nt lds\n"
MF "v[80:83],a[72:75],a[4:7],v[80:83]\n"
DT "a[116:117],v7 offset:160\n"
CV "v121,v90,v91\n"
MF "v[84:87],a[76:79],a[4:7],v[84:87]\n"
DT "a[118:119],v7 offset:672\n"
CV "v122,v92,v93\n"
MF "v[80:83],a[80:83],a[8:11],v[80:83]\n"
DT "a[120:121],v7 offset:192\n"
CV "v123,v94,v95\n"
MF "v[84:87],a[84:87],a[8:11],v[84:87]\n"
DT "a[122:123],v7 offset:704\n"
BL "v9,s[12:15],s78 offen offset:1056 nt lds\n"
MF "v[80:83],a[88:91],a[12:15],v[80:83]\n"
"s_add_u32 m0,m0,8448\n"
DT "a[124:125],v7 offset:224\n"
MF "v[84:87],a[92:95],a[12:15],v[84:87]\n"
DT "a[126:127],v7 offset:736\n"
MF "v[88:91],a[64:67],a[16:19],v[108:111]\n"
"v_add_u32 v6,s49,v4\n"
"v_add_u32 v7,s49,v5\n"
MF "v[92:95],a[68:71],a[16:19],v[108:111]\n"
"s_mov_b32 s50,s42\n"
BL "v10,s[16:19],s79 offen nt lds\n"
EX "v80,v80\n"
MF "v[88:91],a[72:75],a[20:23],v[88:91]\n"
"s_mov_b32 s42,s49\n"
EX "v81,v81\n"
MF "v[92:95],a[76:79],a[20:23],v[92:95]\n"
EX "v82,v82\n"
MF "v[88:91],a[80:83],a[24:27],v[88:91]\n"
EX "v83,v83\n"
MF "v[92:95],a[84:87],a[24:27],v[92:95]\n"
BL "v11,s[16:19],s79 offen offset:1056 nt lds\n"
EX "v84,v84\n"
MF "v[88:91],a[88:91],a[28:31],v[88:91]\n"
"s_add_u32 s51,s51,8192\n"
EX "v85,v85\n"
MF "v[92:95],a[92:95],a[28:31],v[92:95]\n"
"s_mov_b32 s79,s78\n"
WT "vmcnt(22)\n"
WT "vmcnt(9)\n"
WT "lgkmcnt(0)\n"
"s_barrier\n"
MF "v[208:211],a[112:115],v[124:127],v[208:211]\n"
"s_xor_b32 s78,s51,s41\n"
DR "a[64:67],v6 offset:0\n"
EX "v86,v86\n"
MF "v[212:215],a[116:119],v[124:127],v[212:215]\n"
DR "a[68:71],v6 offset:512\n"
EX "v87,v87\n"
MF "v[216:219],a[120:123],v[124:127],v[216:219]\n"
DR "a[72:75],v6 offset:64\n"
MF "v[220:223],a[124:127],v[124:127],v[220:223]\n"
DR "a[76:79],v6 offset:576\n"
EX "v88,v88\n"
MF "v[240:243],a[48:51],v[124:127],v[240:243]\n"
DR "a[80:83],v6 offset:128\n"
EX "v89,v89\n"
MF "v[136:139],a[96:99],v[116:119],v[136:139]\n"
DR "a[84:87],v6 offset:640\n"
MF "v[140:143],a[100:103],v[116:119],v[140:143]\n"
DR "a[88:91],v6 offset:192\n"
MF "v[144:147],a[104:107],v[116:119],v[144:147]\n"
DR "a[92:95],v6 offset:704\n"
MF "v[148:151],a[108:111],v[116:119],v[148:151]\n"
DR "v[124:127],v254\n"
"v_xor_b32 v254,0x400,v254\n"
MF "v[152:155],a[112:115],v[116:119],v[152:155]\n"
EX "v90,v90\n"
MF "v[156:159],a[116:119],v[116:119],v[156:159]\n"
EX "v91,v91\n"
MF "v[160:163],a[120:123],v[116:119],v[160:163]\n"
"s_add_u32 s49,s49,16896\n"
EX "v92,v92\n"
MF "v[164:167],a[124:127],v[116:119],v[164:167]\n"
"s_cmp_eq_u32 s49,84480\n"
EX "v93,v93\n"
MF "v[168:171],a[48:51],v[116:119],v[168:171]\n"
"s_cselect_b32 s49,0,s49\n"
EX "v94,v94\n"
MF "v[172:175],a[96:99],v[120:123],v[172:175]\n"
EX "v95,v95\n"
MF "v[176:179],a[100:103],v[120:123],v[176:179]\n"
DT "a[96:97],v7 offset:0\n"
CV "v116,v80,v81\n"
MF "v[180:183],a[104:107],v[120:123],v[180:183]\n"
DT "a[98:99],v7 offset:512\n"
CV "v117,v82,v83\n"
MF "v[184:187],a[108:111],v[120:123],v[184:187]\n"
DT "a[100:101],v7 offset:32\n"
CV "v118,v84,v85\n"
MF "v[188:191],a[112:115],v[120:123],v[188:191]\n"
DT "a[102:103],v7 offset:544\n"
CV "v119,v86,v87\n"
MF "v[192:195],a[116:119],v[120:123],v[192:195]\n"
DT "a[104:105],v7 offset:64\n"
MF "v[196:199],a[120:123],v[120:123],v[196:199]\n"
DT "a[106:107],v7 offset:576\n"
MF "v[200:203],a[124:127],v[120:123],v[200:203]\n"
WT "lgkmcnt(6)\n"
DT "a[108:109],v7 offset:96\n"
MF "v[204:207],a[48:51],v[120:123],v[204:207]\n"
DT "a[110:111],v7 offset:608\n"
DT "a[212:213],v21 offset:0\n"
DT "a[214:215],v21 offset:4608\n"
DT "a[216:217],v21 offset:32\n"
DT "a[218:219],v21 offset:4640\n"
DT "a[220:221],v22 offset:0\n"
DT "a[222:223],v22 offset:4608\n"
DT "a[224:225],v22 offset:32\n"
DT "a[226:227],v22 offset:4640\n"
DT "a[228:229],v23 offset:0\n"
DT "a[230:231],v23 offset:4608\n"
DT "a[232:233],v23 offset:32\n"
DT "a[234:235],v23 offset:4640\n"
DT "a[236:237],v24 offset:0\n"
DT "a[238:239],v24 offset:4608\n"
DT "a[240:241],v24 offset:32\n"
DT "a[242:243],v24 offset:4640\n"
DT "a[244:245],v25 offset:0\n"
DT "a[246:247],v25 offset:4608\n"
WT "vmcnt(4)\n"
"v_cmp_eq_u32 vcc,v31,v28\n"
"s_andn2_b64 vcc,vcc,s[62:63]\n"
"s_cbranch_scc1 Lp369_%=\n"
"Lp370_%=:\n"
MF "v[80:83],a[64:67],a[0:3],v[104:107]\n"
"s_add_u32 m0,s50,s48\n"
DT "a[112:113],v7 offset:128\n"
CV "v120,v88,v89\n"
MF "v[84:87],a[68:71],a[0:3],v[104:107]\n"
DT "a[114:115],v7 offset:640\n"
BL "v8,s[12:15],s78 offen nt lds\n"
MF "v[80:83],a[72:75],a[4:7],v[80:83]\n"
DT "a[116:117],v7 offset:160\n"
CV "v121,v90,v91\n"
MF "v[84:87],a[76:79],a[4:7],v[84:87]\n"
DT "a[118:119],v7 offset:672\n"
CV "v122,v92,v93\n"
MF "v[80:83],a[80:83],a[8:11],v[80:83]\n"
DT "a[120:121],v7 offset:192\n"
CV "v123,v94,v95\n"
MF "v[84:87],a[84:87],a[8:11],v[84:87]\n"
DT "a[122:123],v7 offset:704\n"
BL "v9,s[12:15],s78 offen offset:1056 nt lds\n"
MF "v[80:83],a[88:91],a[12:15],v[80:83]\n"
"s_add_u32 m0,m0,8448\n"
DT "a[124:125],v7 offset:224\n"
MF "v[84:87],a[92:95],a[12:15],v[84:87]\n"
DT "a[126:127],v7 offset:736\n"
MF "v[88:91],a[64:67],a[16:19],v[108:111]\n"
"v_add_u32 v6,s49,v4\n"
"v_add_u32 v7,s49,v5\n"
MF "v[92:95],a[68:71],a[16:19],v[108:111]\n"
"s_mov_b32 s50,s42\n"
BL "v10,s[16:19],s79 offen nt lds\n"
EX "v80,v80\n"
MF "v[88:91],a[72:75],a[20:23],v[88:91]\n"
"s_mov_b32 s42,s49\n"
EX "v81,v81\n"
MF "v[92:95],a[76:79],a[20:23],v[92:95]\n"
EX "v82,v82\n"
MF "v[88:91],a[80:83],a[24:27],v[88:91]\n"
EX "v83,v83\n"
MF "v[92:95],a[84:87],a[24:27],v[92:95]\n"
BL "v11,s[16:19],s79 offen offset:1056 nt lds\n"
EX "v84,v84\n"
MF "v[88:91],a[88:91],a[28:31],v[88:91]\n"
"s_add_u32 s51,s51,8192\n"
EX "v85,v85\n"
MF "v[92:95],a[92:95],a[28:31],v[92:95]\n"
"s_mov_b32 s79,s78\n"
WT "vmcnt(22)\n"
WT "lgkmcnt(0)\n"
"s_barrier\n"
MF "v[208:211],a[112:115],v[124:127],v[208:211]\n"
"s_xor_b32 s78,s51,s41\n"
DR "a[64:67],v6 offset:0\n"
EX "v86,v86\n"
MF "v[212:215],a[116:119],v[124:127],v[212:215]\n"
DR "a[68:71],v6 offset:512\n"
EX "v87,v87\n"
MF "v[216:219],a[120:123],v[124:127],v[216:219]\n"
DR "a[72:75],v6 offset:64\n"
MF "v[220:223],a[124:127],v[124:127],v[220:223]\n"
DR "a[76:79],v6 offset:576\n"
EX "v88,v88\n"
MF "v[240:243],a[48:51],v[124:127],v[240:243]\n"
DR "a[80:83],v6 offset:128\n"
EX "v89,v89\n"
MF "v[136:139],a[96:99],v[116:119],v[136:139]\n"
DR "a[84:87],v6 offset:640\n"
MF "v[140:143],a[100:103],v[116:119],v[140:143]\n"
DR "a[88:91],v6 offset:192\n"
MF "v[144:147],a[104:107],v[116:119],v[144:147]\n"
DR "a[92:95],v6 offset:704\n"
MF "v[148:151],a[108:111],v[116:119],v[148:151]\n"
DR "v[124:127],v254\n"
"v_xor_b32 v254,0x400,v254\n"
MF "v[152:155],a[112:115],v[116:119],v[152:155]\n"
EX "v90,v90\n"
MF "v[156:159],a[116:119],v[116:119],v[156:159]\n"
EX "v91,v91\n"
MF "v[160:163],a[120:123],v[116:119],v[160:163]\n"
"s_add_u32 s49,s49,16896\n"
EX "v92,v92\n"
MF "v[164:167],a[124:127],v[116:119],v[164:167]\n"
"s_cmp_eq_u32 s49,84480\n"
EX "v93,v93\n"
MF "v[168:171],a[48:51],v[116:119],v[168:171]\n"
"s_cselect_b32 s49,0,s49\n"
EX "v94,v94\n"
MF "v[172:175],a[96:99],v[120:123],v[172:175]\n"
EX "v95,v95\n"
MF "v[176:179],a[100:103],v[120:123],v[176:179]\n"
DT "a[96:97],v7 offset:0\n"
CV "v116,v80,v81\n"
MF "v[180:183],a[104:107],v[120:123],v[180:183]\n"
DT "a[98:99],v7 offset:512\n"
CV "v117,v82,v83\n"
MF "v[184:187],a[108:111],v[120:123],v[184:187]\n"
DT "a[100:101],v7 offset:32\n"
CV "v118,v84,v85\n"
MF "v[188:191],a[112:115],v[120:123],v[188:191]\n"
DT "a[102:103],v7 offset:544\n"
CV "v119,v86,v87\n"
MF "v[192:195],a[116:119],v[120:123],v[192:195]\n"
DT "a[104:105],v7 offset:64\n"
MF "v[196:199],a[120:123],v[120:123],v[196:199]\n"
DT "a[106:107],v7 offset:576\n"
MF "v[200:203],a[124:127],v[120:123],v[200:203]\n"
WT "lgkmcnt(6)\n"
DT "a[108:109],v7 offset:96\n"
MF "v[204:207],a[48:51],v[120:123],v[204:207]\n"
DT "a[110:111],v7 offset:608\n"
MF "a[128:131],a[212:215],v[32:35],0\n"
"s_add_u32 m0,s76,121344\n"
MF "a[164:167],a[212:215],v[48:51],0\n"
"s_add_u32 s67,s69,0\n"
MF "a[200:203],a[212:215],v[64:67],0\n"
DT "a[212:213],v21 offset:9216\n"
BL "v12,s[24:27],s67 offen offset:0 lds\n"
MF "a[132:135],a[216:219],v[32:35],0\n"
DT "a[214:215],v21 offset:13824\n"
MF "a[168:171],a[216:219],v[48:51],0\n"
MF "a[204:207],a[216:219],v[64:67],0\n"
DT "a[216:217],v21 offset:9248\n"
BL "v13,s[24:27],s67 offen offset:1024 lds\n"
MF "a[136:139],a[220:223],v[32:35],0\n"
DT "a[218:219],v21 offset:13856\n"
MF "a[172:175],a[220:223],v[48:51],0\n"
DT "a[220:221],v22 offset:9216\n"
MF "a[140:143],a[224:227],v[32:35],0\n"
DT "a[222:223],v22 offset:13824\n"
BL "v14,s[24:27],s67 offen offset:2048 lds\n"
MF "a[176:179],a[224:227],v[48:51],0\n"
"s_add_u32 m0,s76,124416\n"
DT "a[224:225],v22 offset:9248\n"
MF "a[144:147],a[228:231],v[32:35],0\n"
"s_add_u32 s67,s69,0\n"
DT "a[226:227],v22 offset:13856\n"
MF "a[180:183],a[228:231],v[48:51],0\n"
DT "a[228:229],v23 offset:9216\n"
BL "v15,s[24:27],s67 offen offset:0 lds\n"
MF "a[148:151],a[232:235],v[32:35],0\n"
DT "a[230:231],v23 offset:13824\n"
MF "a[184:187],a[232:235],v[48:51],0\n"
DT "a[232:233],v23 offset:9248\n"
MF "a[152:155],a[236:239],v[32:35],0\n"
DT "a[234:235],v23 offset:13856\n"
BL "v16,s[24:27],s67 offen offset:1024 lds\n"
MF "a[188:191],a[236:239],v[48:51],0\n"
DT "a[236:237],v24 offset:9216\n"
MF "a[156:159],a[240:243],v[32:35],0\n"
DT "a[238:239],v24 offset:13824\n"
MF "a[192:195],a[240:243],v[48:51],0\n"
DT "a[240:241],v24 offset:9248\n"
BL "v17,s[24:27],s67 offen offset:2048 lds\n"
MF "a[160:163],a[244:247],v[32:35],0\n"
DT "a[242:243],v24 offset:13856\n"
MF "a[196:199],a[244:247],v[48:51],0\n"
DT "a[244:245],v25 offset:9216\n"
DT "a[246:247],v25 offset:13824\n"
MF "v[80:83],a[64:67],a[0:3],v[104:107]\n"
"s_add_u32 m0,s50,s48\n"
DT "a[112:113],v7 offset:128\n"
CV "v120,v88,v89\n"
MF "v[84:87],a[68:71],a[0:3],v[104:107]\n"
DT "a[114:115],v7 offset:640\n"
BL "v8,s[12:15],s78 offen nt lds\n"
MF "v[80:83],a[72:75],a[4:7],v[80:83]\n"
DT "a[116:117],v7 offset:160\n"
CV "v121,v90,v91\n"
MF "v[84:87],a[76:79],a[4:7],v[84:87]\n"
DT "a[118:119],v7 offset:672\n"
CV "v122,v92,v93\n"
MF "v[80:83],a[80:83],a[8:11],v[80:83]\n"
DT "a[120:121],v7 offset:192\n"
CV "v123,v94,v95\n"
MF "v[84:87],a[84:87],a[8:11],v[84:87]\n"
DT "a[122:123],v7 offset:704\n"
BL "v9,s[12:15],s78 offen offset:1056 nt lds\n"
MF "v[80:83],a[88:91],a[12:15],v[80:83]\n"
"s_add_u32 m0,m0,8448\n"
DT "a[124:125],v7 offset:224\n"
MF "v[84:87],a[92:95],a[12:15],v[84:87]\n"
DT "a[126:127],v7 offset:736\n"
MF "v[88:91],a[64:67],a[16:19],v[108:111]\n"
"v_add_u32 v6,s49,v4\n"
"v_add_u32 v7,s49,v5\n"
MF "v[92:95],a[68:71],a[16:19],v[108:111]\n"
"s_mov_b32 s50,s42\n"
BL "v10,s[16:19],s79 offen nt lds\n"
EX "v80,v80\n"
MF "v[88:91],a[72:75],a[20:23],v[88:91]\n"
"s_mov_b32 s42,s49\n"
EX "v81,v81\n"
MF "v[92:95],a[76:79],a[20:23],v[92:95]\n"
EX "v82,v82\n"
MF "v[88:91],a[80:83],a[24:27],v[88:91]\n"
EX "v83,v83\n"
MF "v[92:95],a[84:87],a[24:27],v[92:95]\n"
BL "v11,s[16:19],s79 offen offset:1056 nt lds\n"
EX "v84,v84\n"
MF "v[88:91],a[88:91],a[28:31],v[88:91]\n"
"s_add_u32 s51,s51,8192\n"
EX "v85,v85\n"
MF "v[92:95],a[92:95],a[28:31],v[92:95]\n"
"s_mov_b32 s79,s78\n"
WT "vmcnt(19)\n"
WT "lgkmcnt(0)\n"
"s_barrier\n"
MF "v[208:211],a[112:115],v[124:127],v[208:211]\n"
"s_xor_b32 s78,s51,s41\n"
DR "a[64:67],v6 offset:0\n"
EX "v86,v86\n"
MF "v[212:215],a[116:119],v[124:127],v[212:215]\n"
DR "a[68:71],v6 offset:512\n"
EX "v87,v87\n"
MF "v[216:219],a[120:123],v[124:127],v[216:219]\n"
DR "a[72:75],v6 offset:64\n"
MF "v[220:223],a[124:127],v[124:127],v[220:223]\n"
DR "a[76:79],v6 offset:576\n"
EX "v88,v88\n"
MF "v[240:243],a[48:51],v[124:127],v[240:243]\n"
DR "a[80:83],v6 offset:128\n"
EX "v89,v89\n"
MF "v[136:139],a[96:99],v[116:119],v[136:139]\n"
DR "a[84:87],v6 offset:640\n"
MF "v[140:143],a[100:103],v[116:119],v[140:143]\n"
DR "a[88:91],v6 offset:192\n"
MF "v[144:147],a[104:107],v[116:119],v[144:147]\n"
DR "a[92:95],v6 offset:704\n"
MF "v[148:151],a[108:111],v[116:119],v[148:151]\n"
DR "v[124:127],v254\n"
"v_xor_b32 v254,0x400,v254\n"
MF "v[152:155],a[112:115],v[116:119],v[152:155]\n"
EX "v90,v90\n"
MF "v[156:159],a[116:119],v[116:119],v[156:159]\n"
EX "v91,v91\n"
MF "v[160:163],a[120:123],v[116:119],v[160:163]\n"
"s_add_u32 s49,s49,16896\n"
EX "v92,v92\n"
MF "v[164:167],a[124:127],v[116:119],v[164:167]\n"
"s_cmp_eq_u32 s49,84480\n"
EX "v93,v93\n"
MF "v[168:171],a[48:51],v[116:119],v[168:171]\n"
"s_cselect_b32 s49,0,s49\n"
EX "v94,v94\n"
MF "v[172:175],a[96:99],v[120:123],v[172:175]\n"
EX "v95,v95\n"
MF "v[176:179],a[100:103],v[120:123],v[176:179]\n"
DT "a[96:97],v7 offset:0\n"
CV "v116,v80,v81\n"
MF "v[180:183],a[104:107],v[120:123],v[180:183]\n"
DT "a[98:99],v7 offset:512\n"
CV "v117,v82,v83\n"
MF "v[184:187],a[108:111],v[120:123],v[184:187]\n"
DT "a[100:101],v7 offset:32\n"
CV "v118,v84,v85\n"
MF "v[188:191],a[112:115],v[120:123],v[188:191]\n"
DT "a[102:103],v7 offset:544\n"
CV "v119,v86,v87\n"
MF "v[192:195],a[116:119],v[120:123],v[192:195]\n"
DT "a[104:105],v7 offset:64\n"
MF "v[196:199],a[120:123],v[120:123],v[196:199]\n"
DT "a[106:107],v7 offset:576\n"
MF "v[200:203],a[124:127],v[120:123],v[200:203]\n"
WT "lgkmcnt(6)\n"
DT "a[108:109],v7 offset:96\n"
MF "v[204:207],a[48:51],v[120:123],v[204:207]\n"
DT "a[110:111],v7 offset:608\n"
MF "a[128:131],a[212:215],v[36:39],a[128:131]\n"
MF "a[164:167],a[212:215],v[52:55],a[164:167]\n"
"s_add_u32 s44,s57,0\n"
MF "a[200:203],a[212:215],v[68:71],a[200:203]\n"
DT "a[212:213],v21 offset:18432\n"
BL "v[32:35],v244,s[28:31],s44 offen offset:0 sc1\n"
MF "a[132:135],a[216:219],v[36:39],a[132:135]\n"
DT "a[214:215],v21 offset:23040\n"
MF "a[168:171],a[216:219],v[52:55],a[168:171]\n"
MF "a[204:207],a[216:219],v[68:71],a[204:207]\n"
DT "a[216:217],v21 offset:18464\n"
BL "v[48:51],v245,s[28:31],s44 offen offset:0 sc1\n"
MF "a[136:139],a[220:223],v[36:39],a[136:139]\n"
DT "a[218:219],v21 offset:23072\n"
MF "a[172:175],a[220:223],v[52:55],a[172:175]\n"
DT "a[220:221],v22 offset:18432\n"
MF "a[140:143],a[224:227],v[36:39],a[140:143]\n"
DT "a[222:223],v22 offset:23040\n"
BL "v[64:67],v246,s[28:31],s44 offen offset:0 sc1\n"
MF "a[176:179],a[224:227],v[52:55],a[176:179]\n"
"s_add_u32 m0,s76,127488\n"
DT "a[224:225],v22 offset:18464\n"
MF "a[144:147],a[228:231],v[36:39],a[144:147]\n"
"s_add_u32 s67,s69,0\n"
DT "a[226:227],v22 offset:23072\n"
MF "a[180:183],a[228:231],v[52:55],a[180:183]\n"
DT "a[228:229],v23 offset:18432\n"
BL "v18,s[24:27],s67 offen offset:0 lds\n"
MF "a[148:151],a[232:235],v[36:39],a[148:151]\n"
DT "a[230:231],v23 offset:23040\n"
MF "a[184:187],a[232:235],v[52:55],a[184:187]\n"
DT "a[232:233],v23 offset:18464\n"
MF "a[152:155],a[236:239],v[36:39],a[152:155]\n"
DT "a[234:235],v23 offset:23072\n"
BL "v19,s[24:27],s67 offen offset:1024 lds\n"
MF "a[188:191],a[236:239],v[52:55],a[188:191]\n"
DT "a[236:237],v24 offset:18432\n"
MF "a[156:159],a[240:243],v[36:39],a[156:159]\n"
DT "a[238:239],v24 offset:23040\n"
MF "a[192:195],a[240:243],v[52:55],a[192:195]\n"
DT "a[240:241],v24 offset:18464\n"
BL "v20,s[24:27],s67 offen offset:2048 lds\n"
MF "a[160:163],a[244:247],v[36:39],a[160:163]\n"
DT "a[242:243],v24 offset:23072\n"
MF "a[196:199],a[244:247],v[52:55],a[196:199]\n"
DT "a[244:245],v25 offset:18432\n"
DT "a[246:247],v25 offset:23040\n"
MF "v[80:83],a[64:67],a[0:3],v[104:107]\n"
"s_add_u32 m0,s50,s48\n"
DT "a[112:113],v7 offset:128\n"
CV "v120,v88,v89\n"
MF "v[84:87],a[68:71],a[0:3],v[104:107]\n"
DT "a[114:115],v7 offset:640\n"
BL "v8,s[12:15],s78 offen nt lds\n"
MF "v[80:83],a[72:75],a[4:7],v[80:83]\n"
DT "a[116:117],v7 offset:160\n"
CV "v121,v90,v91\n"
MF "v[84:87],a[76:79],a[4:7],v[84:87]\n"
DT "a[118:119],v7 offset:672\n"
CV "v122,v92,v93\n"
MF "v[80:83],a[80:83],a[8:11],v[80:83]\n"
DT "a[120:121],v7 offset:192\n"
CV "v123,v94,v95\n"
MF "v[84:87],a[84:87],a[8:11],v[84:87]\n"
DT "a[122:123],v7 offset:704\n"
BL "v9,s[12:15],s78 offen offset:1056 nt lds\n"
MF "v[80:83],a[88:91],a[12:15],v[80:83]\n"
"s_add_u32 m0,m0,8448\n"
DT "a[124:125],v7 offset:224\n"
MF "v[84:87],a[92:95],a[12:15],v[84:87]\n"
DT "a[126:127],v7 offset:736\n"
MF "v[88:91],a[64:67],a[16:19],v[108:111]\n"
"v_add_u32 v6,s49,v4\n"
"v_add_u32 v7,s49,v5\n"
MF "v[92:95],a[68:71],a[16:19],v[108:111]\n"
"s_mov_b32 s50,s42\n"
BL "v10,s[16:19],s79 offen nt lds\n"
EX "v80,v80\n"
MF "v[88:91],a[72:75],a[20:23],v[88:91]\n"
"s_mov_b32 s42,s49\n"
EX "v81,v81\n"
MF "v[92:95],a[76:79],a[20:23],v[92:95]\n"
EX "v82,v82\n"
MF "v[88:91],a[80:83],a[24:27],v[88:91]\n"
EX "v83,v83\n"
MF "v[92:95],a[84:87],a[24:27],v[92:95]\n"
BL "v11,s[16:19],s79 offen offset:1056 nt lds\n"
EX "v84,v84\n"
MF "v[88:91],a[88:91],a[28:31],v[88:91]\n"
"s_add_u32 s51,s51,8192\n"
EX "v85,v85\n"
MF "v[92:95],a[92:95],a[28:31],v[92:95]\n"
"s_mov_b32 s79,s78\n"
WT "vmcnt(24)\n"
WT "lgkmcnt(0)\n"
"s_barrier\n"
MF "v[208:211],a[112:115],v[124:127],v[208:211]\n"
"s_xor_b32 s78,s51,s41\n"
DR "a[64:67],v6 offset:0\n"
EX "v86,v86\n"
MF "v[212:215],a[116:119],v[124:127],v[212:215]\n"
DR "a[68:71],v6 offset:512\n"
EX "v87,v87\n"
MF "v[216:219],a[120:123],v[124:127],v[216:219]\n"
DR "a[72:75],v6 offset:64\n"
MF "v[220:223],a[124:127],v[124:127],v[220:223]\n"
DR "a[76:79],v6 offset:576\n"
EX "v88,v88\n"
MF "v[240:243],a[48:51],v[124:127],v[240:243]\n"
DR "a[80:83],v6 offset:128\n"
EX "v89,v89\n"
MF "v[136:139],a[96:99],v[116:119],v[136:139]\n"
DR "a[84:87],v6 offset:640\n"
MF "v[140:143],a[100:103],v[116:119],v[140:143]\n"
DR "a[88:91],v6 offset:192\n"
MF "v[144:147],a[104:107],v[116:119],v[144:147]\n"
DR "a[92:95],v6 offset:704\n"
MF "v[148:151],a[108:111],v[116:119],v[148:151]\n"
DR "v[124:127],v254\n"
"v_xor_b32 v254,0x400,v254\n"
MF "v[152:155],a[112:115],v[116:119],v[152:155]\n"
EX "v90,v90\n"
MF "v[156:159],a[116:119],v[116:119],v[156:159]\n"
EX "v91,v91\n"
MF "v[160:163],a[120:123],v[116:119],v[160:163]\n"
"s_add_u32 s49,s49,16896\n"
EX "v92,v92\n"
MF "v[164:167],a[124:127],v[116:119],v[164:167]\n"
"s_cmp_eq_u32 s49,84480\n"
EX "v93,v93\n"
MF "v[168:171],a[48:51],v[116:119],v[168:171]\n"
"s_cselect_b32 s49,0,s49\n"
EX "v94,v94\n"
MF "v[172:175],a[96:99],v[120:123],v[172:175]\n"
EX "v95,v95\n"
MF "v[176:179],a[100:103],v[120:123],v[176:179]\n"
DT "a[96:97],v7 offset:0\n"
CV "v116,v80,v81\n"
MF "v[180:183],a[104:107],v[120:123],v[180:183]\n"
DT "a[98:99],v7 offset:512\n"
CV "v117,v82,v83\n"
MF "v[184:187],a[108:111],v[120:123],v[184:187]\n"
DT "a[100:101],v7 offset:32\n"
CV "v118,v84,v85\n"
MF "v[188:191],a[112:115],v[120:123],v[188:191]\n"
DT "a[102:103],v7 offset:544\n"
CV "v119,v86,v87\n"
MF "v[192:195],a[116:119],v[120:123],v[192:195]\n"
DT "a[104:105],v7 offset:64\n"
MF "v[196:199],a[120:123],v[120:123],v[196:199]\n"
DT "a[106:107],v7 offset:576\n"
MF "v[200:203],a[124:127],v[120:123],v[200:203]\n"
WT "lgkmcnt(6)\n"
DT "a[108:109],v7 offset:96\n"
MF "v[204:207],a[48:51],v[120:123],v[204:207]\n"
DT "a[110:111],v7 offset:608\n"
MF "a[128:131],a[212:215],v[40:43],a[128:131]\n"
MF "a[164:167],a[212:215],v[56:59],a[164:167]\n"
"s_add_u32 s44,s57,0\n"
MF "a[200:203],a[212:215],v[72:75],a[200:203]\n"
DT "a[212:213],v21 offset:27648\n"
BL "v[36:39],v244,s[28:31],s44 offen offset:1024 sc1\n"
MF "a[132:135],a[216:219],v[40:43],a[132:135]\n"
DT "a[214:215],v21 offset:32256\n"
MF "a[168:171],a[216:219],v[56:59],a[168:171]\n"
MF "a[204:207],a[216:219],v[72:75],a[204:207]\n"
DT "a[216:217],v21 offset:27680\n"
MF "a[136:139],a[220:223],v[40:43],a[136:139]\n"
DT "a[218:219],v21 offset:32288\n"
"v_add_u32 v21,36864,v21\n"
MF "a[172:175],a[220:223],v[56:59],a[172:175]\n"
DT "a[220:221],v22 offset:27648\n"
MF "a[140:143],a[224:227],v[40:43],a[140:143]\n"
DT "a[222:223],v22 offset:32256\n"
BL "v[52:55],v245,s[28:31],s44 offen offset:1024 sc1\n"
MF "a[176:179],a[224:227],v[56:59],a[176:179]\n"
DT "a[224:225],v22 offset:27680\n"
MF "a[144:147],a[228:231],v[40:43],a[144:147]\n"
DT "a[226:227],v22 offset:32288\n"
"v_add_u32 v22,36864,v22\n"
MF "a[180:183],a[228:231],v[56:59],a[180:183]\n"
DT "a[228:229],v23 offset:27648\n"
MF "a[148:151],a[232:235],v[40:43],a[148:151]\n"
DT "a[230:231],v23 offset:32256\n"
MF "a[184:187],a[232:235],v[56:59],a[184:187]\n"
DT "a[232:233],v23 offset:27680\n"
MF "a[152:155],a[236:239],v[40:43],a[152:155]\n"
DT "a[234:235],v23 offset:32288\n"
BL "v[68:71],v246,s[28:31],s44 offen offset:1024 sc1\n"
"v_add_u32 v23,36864,v23\n"
MF "a[188:191],a[236:239],v[56:59],a[188:191]\n"
DT "a[236:237],v24 offset:27648\n"
MF "a[156:159],a[240:243],v[40:43],a[156:159]\n"
DT "a[238:239],v24 offset:32256\n"
MF "a[192:195],a[240:243],v[56:59],a[192:195]\n"
DT "a[240:241],v24 offset:27680\n"
MF "a[160:163],a[244:247],v[40:43],a[160:163]\n"
DT "a[242:243],v24 offset:32288\n"
"v_add_u32 v24,36864,v24\n"
MF "a[196:199],a[244:247],v[56:59],a[196:199]\n"
DT "a[244:245],v25 offset:27648\n"
DT "a[246:247],v25 offset:32256\n"
"v_add_u32 v25,36864,v25\n"
MF "v[80:83],a[64:67],a[0:3],v[104:107]\n"
"s_add_u32 m0,s50,s48\n"
DT "a[112:113],v7 offset:128\n"
CV "v120,v88,v89\n"
MF "v[84:87],a[68:71],a[0:3],v[104:107]\n"
DT "a[114:115],v7 offset:640\n"
BL "v8,s[12:15],s78 offen nt lds\n"
MF "v[80:83],a[72:75],a[4:7],v[80:83]\n"
DT "a[116:117],v7 offset:160\n"
CV "v121,v90,v91\n"
MF "v[84:87],a[76:79],a[4:7],v[84:87]\n"
DT "a[118:119],v7 offset:672\n"
CV "v122,v92,v93\n"
MF "v[80:83],a[80:83],a[8:11],v[80:83]\n"
DT "a[120:121],v7 offset:192\n"
CV "v123,v94,v95\n"
MF "v[84:87],a[84:87],a[8:11],v[84:87]\n"
DT "a[122:123],v7 offset:704\n"
BL "v9,s[12:15],s78 offen offset:1056 nt lds\n"
MF "v[80:83],a[88:91],a[12:15],v[80:83]\n"
"s_add_u32 m0,m0,8448\n"
DT "a[124:125],v7 offset:224\n"
MF "v[84:87],a[92:95],a[12:15],v[84:87]\n"
DT "a[126:127],v7 offset:736\n"
MF "v[88:91],a[64:67],a[16:19],v[108:111]\n"
"v_add_u32 v6,s49,v4\n"
"v_add_u32 v7,s49,v5\n"
MF "v[92:95],a[68:71],a[16:19],v[108:111]\n"
"s_mov_b32 s50,s42\n"
BL "v10,s[16:19],s79 offen nt lds\n"
EX "v80,v80\n"
MF "v[88:91],a[72:75],a[20:23],v[88:91]\n"
"s_mov_b32 s42,s49\n"
EX "v81,v81\n"
MF "v[92:95],a[76:79],a[20:23],v[92:95]\n"
EX "v82,v82\n"
MF "v[88:91],a[80:83],a[24:27],v[88:91]\n"
EX "v83,v83\n"
MF "v[92:95],a[84:87],a[24:27],v[92:95]\n"
BL "v11,s[16:19],s79 offen offset:1056 nt lds\n"
EX "v84,v84\n"
MF "v[88:91],a[88:91],a[28:31],v[88:91]\n"
"s_add_u32 s51,s51,8192\n"
EX "v85,v85\n"
MF "v[92:95],a[92:95],a[28:31],v[92:95]\n"
"s_mov_b32 s79,s78\n"
WT "vmcnt(27)\n"
WT "vmcnt(11)\n"
WT "lgkmcnt(0)\n"
"s_barrier\n"
MF "v[208:211],a[112:115],v[124:127],v[208:211]\n"
"s_xor_b32 s78,s51,s41\n"
DR "a[64:67],v6 offset:0\n"
EX "v86,v86\n"
MF "v[212:215],a[116:119],v[124:127],v[212:215]\n"
DR "a[68:71],v6 offset:512\n"
EX "v87,v87\n"
MF "v[216:219],a[120:123],v[124:127],v[216:219]\n"
DR "a[72:75],v6 offset:64\n"
MF "v[220:223],a[124:127],v[124:127],v[220:223]\n"
DR "a[76:79],v6 offset:576\n"
EX "v88,v88\n"
MF "v[240:243],a[48:51],v[124:127],v[240:243]\n"
DR "a[80:83],v6 offset:128\n"
EX "v89,v89\n"
MF "v[136:139],a[96:99],v[116:119],v[136:139]\n"
DR "a[84:87],v6 offset:640\n"
MF "v[140:143],a[100:103],v[116:119],v[140:143]\n"
DR "a[88:91],v6 offset:192\n"
MF "v[144:147],a[104:107],v[116:119],v[144:147]\n"
DR "a[92:95],v6 offset:704\n"
MF "v[148:151],a[108:111],v[116:119],v[148:151]\n"
DR "v[124:127],v254\n"
"v_xor_b32 v254,0x400,v254\n"
MF "v[152:155],a[112:115],v[116:119],v[152:155]\n"
EX "v90,v90\n"
MF "v[156:159],a[116:119],v[116:119],v[156:159]\n"
EX "v91,v91\n"
MF "v[160:163],a[120:123],v[116:119],v[160:163]\n"
"s_add_u32 s49,s49,16896\n"
EX "v92,v92\n"
MF "v[164:167],a[124:127],v[116:119],v[164:167]\n"
"s_cmp_eq_u32 s49,84480\n"
EX "v93,v93\n"
MF "v[168:171],a[48:51],v[116:119],v[168:171]\n"
"s_cselect_b32 s49,0,s49\n"
EX "v94,v94\n"
MF "v[172:175],a[96:99],v[120:123],v[172:175]\n"
EX "v95,v95\n"
MF "v[176:179],a[100:103],v[120:123],v[176:179]\n"
DT "a[96:97],v7 offset:0\n"
CV "v116,v80,v81\n"
MF "v[180:183],a[104:107],v[120:123],v[180:183]\n"
DT "a[98:99],v7 offset:512\n"
CV "v117,v82,v83\n"
MF "v[184:187],a[108:111],v[120:123],v[184:187]\n"
DT "a[100:101],v7 offset:32\n"
CV "v118,v84,v85\n"
MF "v[188:191],a[112:115],v[120:123],v[188:191]\n"
DT "a[102:103],v7 offset:544\n"
CV "v119,v86,v87\n"
MF "v[192:195],a[116:119],v[120:123],v[192:195]\n"
DT "a[104:105],v7 offset:64\n"
MF "v[196:199],a[120:123],v[120:123],v[196:199]\n"
DT "a[106:107],v7 offset:576\n"
MF "v[200:203],a[124:127],v[120:123],v[200:203]\n"
WT "lgkmcnt(6)\n"
DT "a[108:109],v7 offset:96\n"
MF "v[204:207],a[48:51],v[120:123],v[204:207]\n"
DT "a[110:111],v7 offset:608\n"
MF "a[128:131],a[212:215],v[44:47],a[128:131]\n"
MF "a[164:167],a[212:215],v[60:63],a[164:167]\n"
"s_add_u32 s44,s57,0\n"
MF "a[200:203],a[212:215],v[76:79],a[200:203]\n"
DT "a[212:213],v21 offset:0\n"
BL "v[40:43],v244,s[28:31],s44 offen offset:2048 sc1\n"
MF "a[132:135],a[216:219],v[44:47],a[132:135]\n"
DT "a[214:215],v21 offset:4608\n"
MF "a[168:171],a[216:219],v[60:63],a[168:171]\n"
MF "a[204:207],a[216:219],v[76:79],a[204:207]\n"
DT "a[216:217],v21 offset:32\n"
MF "a[136:139],a[220:223],v[44:47],a[136:139]\n"
DT "a[218:219],v21 offset:4640\n"
MF "a[172:175],a[220:223],v[60:63],a[172:175]\n"
DT "a[220:221],v22 offset:0\n"
MF "a[140:143],a[224:227],v[44:47],a[140:143]\n"
DT "a[222:223],v22 offset:4608\n"
BL "v[56:59],v245,s[28:31],s44 offen offset:2048 sc1\n"
MF "a[176:179],a[224:227],v[60:63],a[176:179]\n"
DT "a[224:225],v22 offset:32\n"
MF "a[144:147],a[228:231],v[44:47],a[144:147]\n"
DT "a[226:227],v22 offset:4640\n"
MF "a[180:183],a[228:231],v[60:63],a[180:183]\n"
DT "a[228:229],v23 offset:0\n"
MF "a[148:151],a[232:235],v[44:47],a[148:151]\n"
DT "a[230:231],v23 offset:4608\n"
MF "a[184:187],a[232:235],v[60:63],a[184:187]\n"
DT "a[232:233],v23 offset:32\n"
MF "a[152:155],a[236:239],v[44:47],a[152:155]\n"
DT "a[234:235],v23 offset:4640\n"
BL "v[72:75],v246,s[28:31],s44 offen offset:2048 sc1\n"
MF "a[188:191],a[236:239],v[60:63],a[188:191]\n"
DT "a[236:237],v24 offset:0\n"
MF "a[156:159],a[240:243],v[44:47],a[156:159]\n"
DT "a[238:239],v24 offset:4608\n"
MF "a[192:195],a[240:243],v[60:63],a[192:195]\n"
DT "a[240:241],v24 offset:32\n"
MF "a[160:163],a[244:247],v[44:47],a[160:163]\n"
DT "a[242:243],v24 offset:4640\n"
MF "a[196:199],a[244:247],v[60:63],a[196:199]\n"
DT "a[244:245],v25 offset:0\n"
DT "a[246:247],v25 offset:4608\n"
MF "v[80:83],a[64:67],a[0:3],v[104:107]\n"
"s_add_u32 m0,s50,s48\n"
DT "a[112:113],v7 offset:128\n"
CV "v120,v88,v89\n"
MF "v[84:87],a[68:71],a[0:3],v[104:107]\n"
DT "a[114:115],v7 offset:640\n"
BL "v8,s[12:15],s78 offen nt lds\n"
MF "v[80:83],a[72:75],a[4:7],v[80:83]\n"
DT "a[116:117],v7 offset:160\n"
CV "v121,v90,v91\n"
MF "v[84:87],a[76:79],a[4:7],v[84:87]\n"
DT "a[118:119],v7 offset:672\n"
CV "v122,v92,v93\n"
MF "v[80:83],a[80:83],a[8:11],v[80:83]\n"
DT "a[120:121],v7 offset:192\n"
CV "v123,v94,v95\n"
MF "v[84:87],a[84:87],a[8:11],v[84:87]\n"
DT "a[122:123],v7 offset:704\n"
BL "v9,s[12:15],s78 offen offset:1056 nt lds\n"
MF "v[80:83],a[88:91],a[12:15],v[80:83]\n"
"s_add_u32 m0,m0,8448\n"
DT "a[124:125],v7 offset:224\n"
MF "v[84:87],a[92:95],a[12:15],v[84:87]\n"
DT "a[126:127],v7 offset:736\n"
MF "v[88:91],a[64:67],a[16:19],v[108:111]\n"
"v_add_u32 v6,s49,v4\n"
"v_add_u32 v7,s49,v5\n"
MF "v[92:95],a[68:71],a[16:19],v[108:111]\n"
"s_mov_b32 s50,s42\n"
BL "v10,s[16:19],s79 offen nt lds\n"
EX "v80,v80\n"
MF "v[88:91],a[72:75],a[20:23],v[88:91]\n"
"s_mov_b32 s42,s49\n"
EX "v81,v81\n"
MF "v[92:95],a[76:79],a[20:23],v[92:95]\n"
EX "v82,v82\n"
MF "v[88:91],a[80:83],a[24:27],v[88:91]\n"
EX "v83,v83\n"
MF "v[92:95],a[84:87],a[24:27],v[92:95]\n"
BL "v11,s[16:19],s79 offen offset:1056 nt lds\n"
EX "v84,v84\n"
MF "v[88:91],a[88:91],a[28:31],v[88:91]\n"
"s_add_u32 s51,s51,8192\n"
EX "v85,v85\n"
MF "v[92:95],a[92:95],a[28:31],v[92:95]\n"
"s_mov_b32 s79,s78\n"
WT "vmcnt(24)\n"
WT "lgkmcnt(0)\n"
"s_barrier\n"
MF "v[208:211],a[112:115],v[124:127],v[208:211]\n"
"s_xor_b32 s78,s51,s41\n"
DR "a[64:67],v6 offset:0\n"
EX "v86,v86\n"
MF "v[212:215],a[116:119],v[124:127],v[212:215]\n"
DR "a[68:71],v6 offset:512\n"
EX "v87,v87\n"
MF "v[216:219],a[120:123],v[124:127],v[216:219]\n"
DR "a[72:75],v6 offset:64\n"
MF "v[220:223],a[124:127],v[124:127],v[220:223]\n"
DR "a[76:79],v6 offset:576\n"
EX "v88,v88\n"
MF "v[240:243],a[48:51],v[124:127],v[240:243]\n"
DR "a[80:83],v6 offset:128\n"
EX "v89,v89\n"
MF "v[136:139],a[96:99],v[116:119],v[136:139]\n"
DR "a[84:87],v6 offset:640\n"
MF "v[140:143],a[100:103],v[116:119],v[140:143]\n"
DR "a[88:91],v6 offset:192\n"
MF "v[144:147],a[104:107],v[116:119],v[144:147]\n"
DR "a[92:95],v6 offset:704\n"
MF "v[148:151],a[108:111],v[116:119],v[148:151]\n"
DR "v[124:127],v254\n"
"v_xor_b32 v254,0x400,v254\n"
MF "v[152:155],a[112:115],v[116:119],v[152:155]\n"
EX "v90,v90\n"
MF "v[156:159],a[116:119],v[116:119],v[156:159]\n"
EX "v91,v91\n"
MF "v[160:163],a[120:123],v[116:119],v[160:163]\n"
"s_add_u32 s49,s49,16896\n"
EX "v92,v92\n"
MF "v[164:167],a[124:127],v[116:119],v[164:167]\n"
"s_cmp_eq_u32 s49,84480\n"
EX "v93,v93\n"
MF "v[168:171],a[48:51],v[116:119],v[168:171]\n"
"s_cselect_b32 s49,0,s49\n"
EX "v94,v94\n"
MF "v[172:175],a[96:99],v[120:123],v[172:175]\n"
EX "v95,v95\n"
MF "v[176:179],a[100:103],v[120:123],v[176:179]\n"
DT "a[96:97],v7 offset:0\n"
CV "v116,v80,v81\n"
MF "v[180:183],a[104:107],v[120:123],v[180:183]\n"
DT "a[98:99],v7 offset:512\n"
CV "v117,v82,v83\n"
MF "v[184:187],a[108:111],v[120:123],v[184:187]\n"
DT "a[100:101],v7 offset:32\n"
CV "v118,v84,v85\n"
MF "v[188:191],a[112:115],v[120:123],v[188:191]\n"
DT "a[102:103],v7 offset:544\n"
CV "v119,v86,v87\n"
MF "v[192:195],a[116:119],v[120:123],v[192:195]\n"
DT "a[104:105],v7 offset:64\n"
MF "v[196:199],a[120:123],v[120:123],v[196:199]\n"
DT "a[106:107],v7 offset:576\n"
MF "v[200:203],a[124:127],v[120:123],v[200:203]\n"
WT "lgkmcnt(6)\n"
DT "a[108:109],v7 offset:96\n"
MF "v[204:207],a[48:51],v[120:123],v[204:207]\n"
DT "a[110:111],v7 offset:608\n"
WT "vmcnt(21)\n"
MF "a[128:131],a[212:215],v[32:35],a[128:131]\n"
MF "a[164:167],a[212:215],v[48:51],a[164:167]\n"
"s_add_u32 s44,s57,0\n"
MF "a[200:203],a[212:215],v[64:67],a[200:203]\n"
DT "a[212:213],v21 offset:9216\n"
BL "v[44:47],v244,s[28:31],s44 offen offset:3072 sc1\n"
MF "a[132:135],a[216:219],v[32:35],a[132:135]\n"
DT "a[214:215],v21 offset:13824\n"
MF "a[168:171],a[216:219],v[48:51],a[168:171]\n"
BL "v[60:63],v245,s[28:31],s44 offen offset:3072 sc1\n"
MF "a[204:207],a[216:219],v[64:67],a[204:207]\n"
DT "a[216:217],v21 offset:9248\n"
MF "a[136:139],a[220:223],v[32:35],a[136:139]\n"
DT "a[218:219],v21 offset:13856\n"
BL "v[76:79],v246,s[28:31],s44 offen offset:3072 sc1\n"
MF "a[172:175],a[220:223],v[48:51],a[172:175]\n"
"s_add_u32 m0,s76,84480\n"
"s_add_u32 s67,s70,0\n"
DT "a[220:221],v22 offset:9216\n"
MF "a[140:143],a[224:227],v[32:35],a[140:143]\n"
DT "a[222:223],v22 offset:13824\n"
BL "v12,s[24:27],s67 offen offset:0 lds\n"
MF "a[176:179],a[224:227],v[48:51],a[176:179]\n"
DT "a[224:225],v22 offset:9248\n"
MF "a[144:147],a[228:231],v[32:35],a[144:147]\n"
DT "a[226:227],v22 offset:13856\n"
BL "v13,s[24:27],s67 offen offset:1024 lds\n"
MF "a[180:183],a[228:231],v[48:51],a[180:183]\n"
DT "a[228:229],v23 offset:9216\n"
MF "a[148:151],a[232:235],v[32:35],a[148:151]\n"
DT "a[230:231],v23 offset:13824\n"
BL "v14,s[24:27],s67 offen offset:2048 lds\n"
MF "a[184:187],a[232:235],v[48:51],a[184:187]\n"
"s_add_u32 m0,s76,87552\n"
"s_add_u32 s67,s70,0\n"
DT "a[232:233],v23 offset:9248\n"
MF "a[152:155],a[236:239],v[32:35],a[152:155]\n"
DT "a[234:235],v23 offset:13856\n"
BL "v15,s[24:27],s67 offen offset:0 lds\n"
MF "a[188:191],a[236:239],v[48:51],a[188:191]\n"
DT "a[236:237],v24 offset:9216\n"
MF "a[156:159],a[240:243],v[32:35],a[156:159]\n"
DT "a[238:239],v24 offset:13824\n"
BL "v16,s[24:27],s67 offen offset:1024 lds\n"
MF "a[192:195],a[240:243],v[48:51],a[192:195]\n"
DT "a[240:241],v24 offset:9248\n"
MF "a[160:163],a[244:247],v[32:35],a[160:163]\n"
DT "a[242:243],v24 offset:13856\n"
BL "v17,s[24:27],s67 offen offset:2048 lds\n"
MF "a[196:199],a[244:247],v[48:51],a[196:199]\n"
DT "a[244:245],v25 offset:9216\n"
DT "a[246:247],v25 offset:13824\n"
MF "v[80:83],a[64:67],a[0:3],v[104:107]\n"
"s_add_u32 m0,s50,s48\n"
"s_add_u32 m0,m0,8448\n"
DT "a[112:113],v7 offset:128\n"
CV "v120,v88,v89\n"
MF "v[84:87],a[68:71],a[0:3],v[104:107]\n"
DT "a[114:115],v7 offset:640\n"
BL "v10,s[16:19],s79 offen nt lds\n"
MF "v[80:83],a[72:75],a[4:7],v[80:83]\n"
DT "a[116:117],v7 offset:160\n"
CV "v121,v90,v91\n"
MF "v[84:87],a[76:79],a[4:7],v[84:87]\n"
DT "a[118:119],v7 offset:672\n"
CV "v122,v92,v93\n"
MF "v[80:83],a[80:83],a[8:11],v[80:83]\n"
DT "a[120:121],v7 offset:192\n"
CV "v123,v94,v95\n"
MF "v[84:87],a[84:87],a[8:11],v[84:87]\n"
DT "a[122:123],v7 offset:704\n"
BL "v11,s[16:19],s79 offen offset:1056 nt lds\n"
MF "v[80:83],a[88:91],a[12:15],v[80:83]\n"
"s_add_u32 s51,s51,8192\n"
"s_mov_b32 s79,s78\n"
"s_xor_b32 s78,s51,s41\n"
DT "a[124:125],v7 offset:224\n"
MF "v[84:87],a[92:95],a[12:15],v[84:87]\n"
DT "a[126:127],v7 offset:736\n"
MF "v[88:91],a[64:67],a[16:19],v[108:111]\n"
"v_add_u32 v6,s49,v4\n"
"v_add_u32 v7,s49,v5\n"
MF "v[92:95],a[68:71],a[16:19],v[108:111]\n"
"s_mov_b32 s50,s42\n"
EX "v80,v80\n"
MF "v[88:91],a[72:75],a[20:23],v[88:91]\n"
"s_mov_b32 s42,s49\n"
EX "v81,v81\n"
MF "v[92:95],a[76:79],a[20:23],v[92:95]\n"
EX "v82,v82\n"
MF "v[88:91],a[80:83],a[24:27],v[88:91]\n"
EX "v83,v83\n"
MF "v[92:95],a[84:87],a[24:27],v[92:95]\n"
EX "v84,v84\n"
MF "v[88:91],a[88:91],a[28:31],v[88:91]\n"
EX "v85,v85\n"
MF "v[92:95],a[92:95],a[28:31],v[92:95]\n"
WT "vmcnt(25)\n"
WT "lgkmcnt(0)\n"
"s_barrier\n"
MF "v[208:211],a[112:115],v[124:127],v[208:211]\n"
DR "a[64:67],v6 offset:0\n"
EX "v86,v86\n"
MF "v[212:215],a[116:119],v[124:127],v[212:215]\n"
DR "a[68:71],v6 offset:512\n"
EX "v87,v87\n"
MF "v[216:219],a[120:123],v[124:127],v[216:219]\n"
DR "a[72:75],v6 offset:64\n"
MF "v[220:223],a[124:127],v[124:127],v[220:223]\n"
DR "a[76:79],v6 offset:576\n"
EX "v88,v88\n"
MF "v[240:243],a[48:51],v[124:127],v[240:243]\n"
DR "a[80:83],v6 offset:128\n"
EX "v89,v89\n"
MF "v[136:139],a[96:99],v[116:119],v[136:139]\n"
DR "a[84:87],v6 offset:640\n"
MF "v[140:143],a[100:103],v[116:119],v[140:143]\n"
DR "a[88:91],v6 offset:192\n"
MF "v[144:147],a[104:107],v[116:119],v[144:147]\n"
DR "a[92:95],v6 offset:704\n"
MF "v[148:151],a[108:111],v[116:119],v[148:151]\n"
DR "v[124:127],v254\n"
"v_xor_b32 v254,0x400,v254\n"
MF "v[152:155],a[112:115],v[116:119],v[152:155]\n"
EX "v90,v90\n"
MF "v[156:159],a[116:119],v[116:119],v[156:159]\n"
EX "v91,v91\n"
MF "v[160:163],a[120:123],v[116:119],v[160:163]\n"
"s_add_u32 s49,s49,16896\n"
EX "v92,v92\n"
MF "v[164:167],a[124:127],v[116:119],v[164:167]\n"
"s_cmp_eq_u32 s49,84480\n"
EX "v93,v93\n"
MF "v[168:171],a[48:51],v[116:119],v[168:171]\n"
"s_cselect_b32 s49,0,s49\n"
EX "v94,v94\n"
MF "v[172:175],a[96:99],v[120:123],v[172:175]\n"
EX "v95,v95\n"
MF "v[176:179],a[100:103],v[120:123],v[176:179]\n"
DT "a[96:97],v7 offset:0\n"
CV "v116,v80,v81\n"
MF "v[180:183],a[104:107],v[120:123],v[180:183]\n"
DT "a[98:99],v7 offset:512\n"
CV "v117,v82,v83\n"
MF "v[184:187],a[108:111],v[120:123],v[184:187]\n"
DT "a[100:101],v7 offset:32\n"
CV "v118,v84,v85\n"
MF "v[188:191],a[112:115],v[120:123],v[188:191]\n"
DT "a[102:103],v7 offset:544\n"
CV "v119,v86,v87\n"
MF "v[192:195],a[116:119],v[120:123],v[192:195]\n"
DT "a[104:105],v7 offset:64\n"
MF "v[196:199],a[120:123],v[120:123],v[196:199]\n"
DT "a[106:107],v7 offset:576\n"
MF "v[200:203],a[124:127],v[120:123],v[200:203]\n"
WT "lgkmcnt(6)\n"
DT "a[108:109],v7 offset:96\n"
MF "v[204:207],a[48:51],v[120:123],v[204:207]\n"
DT "a[110:111],v7 offset:608\n"
WT "vmcnt(22)\n"
MF "a[128:131],a[212:215],v[36:39],a[128:131]\n"
MF "a[164:167],a[212:215],v[52:55],a[164:167]\n"
"s_add_u32 s44,s58,0\n"
MF "a[200:203],a[212:215],v[68:71],a[200:203]\n"
DT "a[212:213],v21 offset:18432\n"
BL "v[32:35],v244,s[28:31],s44 offen offset:0 sc1\n"
MF "a[132:135],a[216:219],v[36:39],a[132:135]\n"
DT "a[214:215],v21 offset:23040\n"
MF "a[168:171],a[216:219],v[52:55],a[168:171]\n"
MF "a[204:207],a[216:219],v[68:71],a[204:207]\n"
DT "a[216:217],v21 offset:18464\n"
BL "v[48:51],v245,s[28:31],s44 offen offset:0 sc1\n"
MF "a[136:139],a[220:223],v[36:39],a[136:139]\n"
DT "a[218:219],v21 offset:23072\n"
MF "a[172:175],a[220:223],v[52:55],a[172:175]\n"
DT "a[220:221],v22 offset:18432\n"
MF "a[140:143],a[224:227],v[36:39],a[140:143]\n"
DT "a[222:223],v22 offset:23040\n"
BL "v[64:67],v246,s[28:31],s44 offen offset:0 sc1\n"
MF "a[176:179],a[224:227],v[52:55],a[176:179]\n"
"s_add_u32 m0,s76,90624\n"
DT "a[224:225],v22 offset:18464\n"
MF "a[144:147],a[228:231],v[36:39],a[144:147]\n"
"s_add_u32 s67,s70,0\n"
DT "a[226:227],v22 offset:23072\n"
MF "a[180:183],a[228:231],v[52:55],a[180:183]\n"
DT "a[228:229],v23 offset:18432\n"
BL "v18,s[24:27],s67 offen offset:0 lds\n"
MF "a[148:151],a[232:235],v[36:39],a[148:151]\n"
DT "a[230:231],v23 offset:23040\n"
MF "a[184:187],a[232:235],v[52:55],a[184:187]\n"
DT "a[232:233],v23 offset:18464\n"
MF "a[152:155],a[236:239],v[36:39],a[152:155]\n"
DT "a[234:235],v23 offset:23072\n"
BL "v19,s[24:27],s67 offen offset:1024 lds\n"
MF "a[188:191],a[236:239],v[52:55],a[188:191]\n"
DT "a[236:237],v24 offset:18432\n"
MF "a[156:159],a[240:243],v[36:39],a[156:159]\n"
DT "a[238:239],v24 offset:23040\n"
MF "a[192:195],a[240:243],v[52:55],a[192:195]\n"
DT "a[240:241],v24 offset:18464\n"
BL "v20,s[24:27],s67 offen offset:2048 lds\n"
MF "a[160:163],a[244:247],v[36:39],a[160:163]\n"
DT "a[242:243],v24 offset:23072\n"
MF "a[196:199],a[244:247],v[52:55],a[196:199]\n"
DT "a[244:245],v25 offset:18432\n"
DT "a[246:247],v25 offset:23040\n"
MF "v[80:83],a[64:67],a[0:3],v[104:107]\n"
DT "a[112:113],v7 offset:128\n"
CV "v120,v88,v89\n"
MF "v[84:87],a[68:71],a[0:3],v[104:107]\n"
DT "a[114:115],v7 offset:640\n"
MF "v[80:83],a[72:75],a[4:7],v[80:83]\n"
DT "a[116:117],v7 offset:160\n"
CV "v121,v90,v91\n"
MF "v[84:87],a[76:79],a[4:7],v[84:87]\n"
DT "a[118:119],v7 offset:672\n"
CV "v122,v92,v93\n"
MF "v[80:83],a[80:83],a[8:11],v[80:83]\n"
DT "a[120:121],v7 offset:192\n"
CV "v123,v94,v95\n"
MF "v[84:87],a[84:87],a[8:11],v[84:87]\n"
DT "a[122:123],v7 offset:704\n"
MF "v[80:83],a[88:91],a[12:15],v[80:83]\n"
DT "a[124:125],v7 offset:224\n"
MF "v[84:87],a[92:95],a[12:15],v[84:87]\n"
DT "a[126:127],v7 offset:736\n"
MF "v[88:91],a[64:67],a[16:19],v[108:111]\n"
"v_add_u32 v6,s49,v4\n"
"v_add_u32 v7,s49,v5\n"
MF "v[92:95],a[68:71],a[16:19],v[108:111]\n"
"s_mov_b32 s50,s42\n"
EX "v80,v80\n"
MF "v[88:91],a[72:75],a[20:23],v[88:91]\n"
"s_mov_b32 s42,s49\n"
EX "v81,v81\n"
MF "v[92:95],a[76:79],a[20:23],v[92:95]\n"
EX "v82,v82\n"
MF "v[88:91],a[80:83],a[24:27],v[88:91]\n"
EX "v83,v83\n"
MF "v[92:95],a[84:87],a[24:27],v[92:95]\n"
EX "v84,v84\n"
MF "v[88:91],a[88:91],a[28:31],v[88:91]\n"
EX "v85,v85\n"
MF "v[92:95],a[92:95],a[28:31],v[92:95]\n"
WT "vmcnt(24)\n"
WT "lgkmcnt(0)\n"
"s_barrier\n"
MF "v[208:211],a[112:115],v[124:127],v[208:211]\n"
DR "a[64:67],v6 offset:0\n"
EX "v86,v86\n"
MF "v[212:215],a[116:119],v[124:127],v[212:215]\n"
DR "a[68:71],v6 offset:512\n"
EX "v87,v87\n"
MF "v[216:219],a[120:123],v[124:127],v[216:219]\n"
DR "a[72:75],v6 offset:64\n"
MF "v[220:223],a[124:127],v[124:127],v[220:223]\n"
DR "a[76:79],v6 offset:576\n"
EX "v88,v88\n"
MF "v[240:243],a[48:51],v[124:127],v[240:243]\n"
DR "a[80:83],v6 offset:128\n"
EX "v89,v89\n"
MF "v[136:139],a[96:99],v[116:119],v[136:139]\n"
DR "a[84:87],v6 offset:640\n"
MF "v[140:143],a[100:103],v[116:119],v[140:143]\n"
DR "a[88:91],v6 offset:192\n"
MF "v[144:147],a[104:107],v[116:119],v[144:147]\n"
DR "a[92:95],v6 offset:704\n"
MF "v[148:151],a[108:111],v[116:119],v[148:151]\n"
DR "v[124:127],v254\n"
"v_xor_b32 v254,0x400,v254\n"
MF "v[152:155],a[112:115],v[116:119],v[152:155]\n"
EX "v90,v90\n"
MF "v[156:159],a[116:119],v[116:119],v[156:159]\n"
EX "v91,v91\n"
MF "v[160:163],a[120:123],v[116:119],v[160:163]\n"
"s_add_u32 s49,s49,16896\n"
EX "v92,v92\n"
MF "v[164:167],a[124:127],v[116:119],v[164:167]\n"
"s_cmp_eq_u32 s49,84480\n"
EX "v93,v93\n"
MF "v[168:171],a[48:51],v[116:119],v[168:171]\n"
"s_cselect_b32 s49,0,s49\n"
EX "v94,v94\n"
MF "v[172:175],a[96:99],v[120:123],v[172:175]\n"
EX "v95,v95\n"
MF "v[176:179],a[100:103],v[120:123],v[176:179]\n"
DT "a[96:97],v7 offset:0\n"
CV "v116,v80,v81\n"
MF "v[180:183],a[104:107],v[120:123],v[180:183]\n"
DT "a[98:99],v7 offset:512\n"
CV "v117,v82,v83\n"
MF "v[184:187],a[108:111],v[120:123],v[184:187]\n"
DT "a[100:101],v7 offset:32\n"
CV "v118,v84,v85\n"
MF "v[188:191],a[112:115],v[120:123],v[188:191]\n"
DT "a[102:103],v7 offset:544\n"
CV "v119,v86,v87\n"
MF "v[192:195],a[116:119],v[120:123],v[192:195]\n"
DT "a[104:105],v7 offset:64\n"
MF "v[196:199],a[120:123],v[120:123],v[196:199]\n"
DT "a[106:107],v7 offset:576\n"
MF "v[200:203],a[124:127],v[120:123],v[200:203]\n"
WT "lgkmcnt(6)\n"
DT "a[108:109],v7 offset:96\n"
MF "v[204:207],a[48:51],v[120:123],v[204:207]\n"
DT "a[110:111],v7 offset:608\n"
WT "vmcnt(21)\n"
MF "a[128:131],a[212:215],v[40:43],a[128:131]\n"
MF "a[164:167],a[212:215],v[56:59],a[164:167]\n"
"s_add_u32 s44,s58,0\n"
MF "a[200:203],a[212:215],v[72:75],a[200:203]\n"
DT "a[212:213],v21 offset:27648\n"
BL "v[36:39],v244,s[28:31],s44 offen offset:1024 sc1\n"
MF "a[132:135],a[216:219],v[40:43],a[132:135]\n"
DT "a[214:215],v21 offset:32256\n"
MF "a[168:171],a[216:219],v[56:59],a[168:171]\n"
MF "a[204:207],a[216:219],v[72:75],a[204:207]\n"
DT "a[216:217],v21 offset:27680\n"
MF "a[136:139],a[220:223],v[40:43],a[136:139]\n"
DT "a[218:219],v21 offset:32288\n"
"v_subrev_u32 v21,36864,v21\n"
MF "a[172:175],a[220:223],v[56:59],a[172:175]\n"
DT "a[220:221],v22 offset:27648\n"
MF "a[140:143],a[224:227],v[40:43],a[140:143]\n"
DT "a[222:223],v22 offset:32256\n"
BL "v[52:55],v245,s[28:31],s44 offen offset:1024 sc1\n"
MF "a[176:179],a[224:227],v[56:59],a[176:179]\n"
DT "a[224:225],v22 offset:27680\n"
MF "a[144:147],a[228:231],v[40:43],a[144:147]\n"
DT "a[226:227],v22 offset:32288\n"
"v_subrev_u32 v22,36864,v22\n"
MF "a[180:183],a[228:231],v[56:59],a[180:183]\n"
DT "a[228:229],v23 offset:27648\n"
MF "a[148:151],a[232:235],v[40:43],a[148:151]\n"
DT "a[230:231],v23 offset:32256\n"
MF "a[184:187],a[232:235],v[56:59],a[184:187]\n"
DT "a[232:233],v23 offset:27680\n"
MF "a[152:155],a[236:239],v[40:43],a[152:155]\n"
DT "a[234:235],v23 offset:32288\n"
BL "v[68:71],v246,s[28:31],s44 offen offset:1024 sc1\n"
"v_subrev_u32 v23,36864,v23\n"
MF "a[188:191],a[236:239],v[56:59],a[188:191]\n"
DT "a[236:237],v24 offset:27648\n"
MF "a[156:159],a[240:243],v[40:43],a[156:159]\n"
DT "a[238:239],v24 offset:32256\n"
MF "a[192:195],a[240:243],v[56:59],a[192:195]\n"
DT "a[240:241],v24 offset:27680\n"
MF "a[160:163],a[244:247],v[40:43],a[160:163]\n"
DT "a[242:243],v24 offset:32288\n"
"v_subrev_u32 v24,36864,v24\n"
MF "a[196:199],a[244:247],v[56:59],a[196:199]\n"
DT "a[244:245],v25 offset:27648\n"
DT "a[246:247],v25 offset:32256\n"
"v_subrev_u32 v25,36864,v25\n"
MF "v[80:83],a[64:67],a[0:3],v[104:107]\n"
DT "a[112:113],v7 offset:128\n"
CV "v120,v88,v89\n"
MF "v[84:87],a[68:71],a[0:3],v[104:107]\n"
DT "a[114:115],v7 offset:640\n"
MF "v[80:83],a[72:75],a[4:7],v[80:83]\n"
DT "a[116:117],v7 offset:160\n"
CV "v121,v90,v91\n"
MF "v[84:87],a[76:79],a[4:7],v[84:87]\n"
DT "a[118:119],v7 offset:672\n"
CV "v122,v92,v93\n"
MF "v[80:83],a[80:83],a[8:11],v[80:83]\n"
DT "a[120:121],v7 offset:192\n"
CV "v123,v94,v95\n"
MF "v[84:87],a[84:87],a[8:11],v[84:87]\n"
DT "a[122:123],v7 offset:704\n"
MF "v[80:83],a[88:91],a[12:15],v[80:83]\n"
DT "a[124:125],v7 offset:224\n"
MF "v[84:87],a[92:95],a[12:15],v[84:87]\n"
DT "a[126:127],v7 offset:736\n"
MF "v[88:91],a[64:67],a[16:19],v[108:111]\n"
"v_add_u32 v6,s49,v4\n"
"v_add_u32 v7,s49,v5\n"
MF "v[92:95],a[68:71],a[16:19],v[108:111]\n"
"s_mov_b32 s50,s42\n"
EX "v80,v80\n"
MF "v[88:91],a[72:75],a[20:23],v[88:91]\n"
"s_mov_b32 s42,s49\n"
EX "v81,v81\n"
MF "v[92:95],a[76:79],a[20:23],v[92:95]\n"
EX "v82,v82\n"
MF "v[88:91],a[80:83],a[24:27],v[88:91]\n"
EX "v83,v83\n"
MF "v[92:95],a[84:87],a[24:27],v[92:95]\n"
EX "v84,v84\n"
MF "v[88:91],a[88:91],a[28:31],v[88:91]\n"
EX "v85,v85\n"
MF "v[92:95],a[92:95],a[28:31],v[92:95]\n"
WT "vmcnt(20)\n"
WT "vmcnt(3)\n"
WT "lgkmcnt(0)\n"
"s_barrier\n"
MF "v[208:211],a[112:115],v[124:127],v[208:211]\n"
DR "a[64:67],v6 offset:0\n"
EX "v86,v86\n"
MF "v[212:215],a[116:119],v[124:127],v[212:215]\n"
DR "a[68:71],v6 offset:512\n"
EX "v87,v87\n"
MF "v[216:219],a[120:123],v[124:127],v[216:219]\n"
DR "a[72:75],v6 offset:64\n"
MF "v[220:223],a[124:127],v[124:127],v[220:223]\n"
DR "a[76:79],v6 offset:576\n"
EX "v88,v88\n"
MF "v[240:243],a[48:51],v[124:127],v[240:243]\n"
DR "a[80:83],v6 offset:128\n"
EX "v89,v89\n"
MF "v[136:139],a[96:99],v[116:119],v[136:139]\n"
DR "a[84:87],v6 offset:640\n"
MF "v[140:143],a[100:103],v[116:119],v[140:143]\n"
DR "a[88:91],v6 offset:192\n"
MF "v[144:147],a[104:107],v[116:119],v[144:147]\n"
DR "a[92:95],v6 offset:704\n"
MF "v[148:151],a[108:111],v[116:119],v[148:151]\n"
DR "v[124:127],v254\n"
"v_xor_b32 v254,0x400,v254\n"
MF "v[152:155],a[112:115],v[116:119],v[152:155]\n"
EX "v90,v90\n"
MF "v[156:159],a[116:119],v[116:119],v[156:159]\n"
EX "v91,v91\n"
MF "v[160:163],a[120:123],v[116:119],v[160:163]\n"
"s_add_u32 s49,s49,16896\n"
EX "v92,v92\n"
MF "v[164:167],a[124:127],v[116:119],v[164:167]\n"
"s_cmp_eq_u32 s49,84480\n"
EX "v93,v93\n"
MF "v[168:171],a[48:51],v[116:119],v[168:171]\n"
"s_cselect_b32 s49,0,s49\n"
EX "v94,v94\n"
MF "v[172:175],a[96:99],v[120:123],v[172:175]\n"
EX "v95,v95\n"
MF "v[176:179],a[100:103],v[120:123],v[176:179]\n"
DT "a[96:97],v7 offset:0\n"
CV "v116,v80,v81\n"
MF "v[180:183],a[104:107],v[120:123],v[180:183]\n"
DT "a[98:99],v7 offset:512\n"
CV "v117,v82,v83\n"
MF "v[184:187],a[108:111],v[120:123],v[184:187]\n"
DT "a[100:101],v7 offset:32\n"
CV "v118,v84,v85\n"
MF "v[188:191],a[112:115],v[120:123],v[188:191]\n"
DT "a[102:103],v7 offset:544\n"
CV "v119,v86,v87\n"
MF "v[192:195],a[116:119],v[120:123],v[192:195]\n"
DT "a[104:105],v7 offset:64\n"
MF "v[196:199],a[120:123],v[120:123],v[196:199]\n"
DT "a[106:107],v7 offset:576\n"
MF "v[200:203],a[124:127],v[120:123],v[200:203]\n"
WT "lgkmcnt(6)\n"
DT "a[108:109],v7 offset:96\n"
MF "v[204:207],a[48:51],v[120:123],v[204:207]\n"
DT "a[110:111],v7 offset:608\n"
WT "vmcnt(17)\n"
MF "a[128:131],a[212:215],v[44:47],a[128:131]\n"
MF "a[164:167],a[212:215],v[60:63],a[164:167]\n"
"s_add_u32 s44,s58,0\n"
MF "a[200:203],a[212:215],v[76:79],a[200:203]\n"
DT "a[212:213],v21 offset:0\n"
BL "v[40:43],v244,s[28:31],s44 offen offset:2048 sc1\n"
MF "a[132:135],a[216:219],v[44:47],a[132:135]\n"
DT "a[214:215],v21 offset:4608\n"
MF "a[168:171],a[216:219],v[60:63],a[168:171]\n"
MF "a[204:207],a[216:219],v[76:79],a[204:207]\n"
DT "a[216:217],v21 offset:32\n"
MF "a[136:139],a[220:223],v[44:47],a[136:139]\n"
DT "a[218:219],v21 offset:4640\n"
MF "a[172:175],a[220:223],v[60:63],a[172:175]\n"
DT "a[220:221],v22 offset:0\n"
MF "a[140:143],a[224:227],v[44:47],a[140:143]\n"
DT "a[222:223],v22 offset:4608\n"
BL "v[56:59],v245,s[28:31],s44 offen offset:2048 sc1\n"
MF "a[176:179],a[224:227],v[60:63],a[176:179]\n"
DT "a[224:225],v22 offset:32\n"
MF "a[144:147],a[228:231],v[44:47],a[144:147]\n"
DT "a[226:227],v22 offset:4640\n"
MF "a[180:183],a[228:231],v[60:63],a[180:183]\n"
DT "a[228:229],v23 offset:0\n"
MF "a[148:151],a[232:235],v[44:47],a[148:151]\n"
DT "a[230:231],v23 offset:4608\n"
MF "a[184:187],a[232:235],v[60:63],a[184:187]\n"
DT "a[232:233],v23 offset:32\n"
MF "a[152:155],a[236:239],v[44:47],a[152:155]\n"
DT "a[234:235],v23 offset:4640\n"
BL "v[72:75],v246,s[28:31],s44 offen offset:2048 sc1\n"
MF "a[188:191],a[236:239],v[60:63],a[188:191]\n"
DT "a[236:237],v24 offset:0\n"
MF "a[156:159],a[240:243],v[44:47],a[156:159]\n"
DT "a[238:239],v24 offset:4608\n"
MF "a[192:195],a[240:243],v[60:63],a[192:195]\n"
DT "a[240:241],v24 offset:32\n"
MF "a[160:163],a[244:247],v[44:47],a[160:163]\n"
DT "a[242:243],v24 offset:4640\n"
MF "a[196:199],a[244:247],v[60:63],a[196:199]\n"
DT "a[244:245],v25 offset:0\n"
DT "a[246:247],v25 offset:4608\n"
MF "v[80:83],a[64:67],a[0:3],v[104:107]\n"
"s_add_u32 s44,s58,0\n"
DT "a[112:113],v7 offset:128\n"
CV "v120,v88,v89\n"
MF "v[84:87],a[68:71],a[0:3],v[104:107]\n"
DT "a[114:115],v7 offset:640\n"
BL "v[44:47],v244,s[28:31],s44 offen offset:3072 sc1\n"
MF "v[80:83],a[72:75],a[4:7],v[80:83]\n"
DT "a[116:117],v7 offset:160\n"
CV "v121,v90,v91\n"
MF "v[84:87],a[76:79],a[4:7],v[84:87]\n"
DT "a[118:119],v7 offset:672\n"
CV "v122,v92,v93\n"
MF "v[80:83],a[80:83],a[8:11],v[80:83]\n"
DT "a[120:121],v7 offset:192\n"
BL "v[60:63],v245,s[28:31],s44 offen offset:3072 sc1\n"
MF "v[84:87],a[84:87],a[8:11],v[84:87]\n"
DT "a[122:123],v7 offset:704\n"
CV "v123,v94,v95\n"
MF "v[80:83],a[88:91],a[12:15],v[80:83]\n"
DT "a[124:125],v7 offset:224\n"
MF "v[84:87],a[92:95],a[12:15],v[84:87]\n"
DT "a[126:127],v7 offset:736\n"
BL "v[76:79],v246,s[28:31],s44 offen offset:3072 sc1\n"
MF "v[88:91],a[64:67],a[16:19],v[108:111]\n"
"s_add_u32 m0,s76,121344\n"
"s_add_u32 s67,s68,147456\n"
"v_add_u32 v6,s49,v4\n"
"v_add_u32 v7,s49,v5\n"
MF "v[92:95],a[68:71],a[16:19],v[108:111]\n"
"s_mov_b32 s50,s42\n"
EX "v80,v80\n"
MF "v[88:91],a[72:75],a[20:23],v[88:91]\n"
"s_mov_b32 s42,s49\n"
BL "v12,s[24:27],s67 offen offset:0 lds\n"
EX "v81,v81\n"
MF "v[92:95],a[76:79],a[20:23],v[92:95]\n"
EX "v82,v82\n"
MF "v[88:91],a[80:83],a[24:27],v[88:91]\n"
EX "v83,v83\n"
MF "v[92:95],a[84:87],a[24:27],v[92:95]\n"
BL "v13,s[24:27],s67 offen offset:1024 lds\n"
EX "v84,v84\n"
MF "v[88:91],a[88:91],a[28:31],v[88:91]\n"
EX "v85,v85\n"
MF "v[92:95],a[92:95],a[28:31],v[92:95]\n"
WT "vmcnt(17)\n"
WT "lgkmcnt(0)\n"
"s_barrier\n"
MF "v[208:211],a[112:115],v[124:127],v[208:211]\n"
BL "v14,s[24:27],s67 offen offset:2048 lds\n"
EX "v86,v86\n"
MF "v[212:215],a[116:119],v[124:127],v[212:215]\n"
"s_add_u32 m0,s76,124416\n"
"s_add_u32 s67,s68,147456\n"
EX "v87,v87\n"
MF "v[216:219],a[120:123],v[124:127],v[216:219]\n"
MF "v[220:223],a[124:127],v[124:127],v[220:223]\n"
EX "v88,v88\n"
MF "v[240:243],a[48:51],v[124:127],v[240:243]\n"
BL "v15,s[24:27],s67 offen offset:0 lds\n"
EX "v89,v89\n"
MF "v[136:139],a[96:99],v[116:119],v[136:139]\n"
MF "v[140:143],a[100:103],v[116:119],v[140:143]\n"
MF "v[144:147],a[104:107],v[116:119],v[144:147]\n"
BL "v16,s[24:27],s67 offen offset:1024 lds\n"
MF "v[148:151],a[108:111],v[116:119],v[148:151]\n"
DR "v[124:127],v254\n"
"v_xor_b32 v254,0x400,v254\n"
MF "v[152:155],a[112:115],v[116:119],v[152:155]\n"
EX "v90,v90\n"
MF "v[156:159],a[116:119],v[116:119],v[156:159]\n"
BL "v17,s[24:27],s67 offen offset:2048 lds\n"
EX "v91,v91\n"
MF "v[160:163],a[120:123],v[116:119],v[160:163]\n"
"s_add_u32 m0,s76,127488\n"
"s_add_u32 s67,s68,147456\n"
"s_add_u32 s49,s49,16896\n"
EX "v92,v92\n"
MF "v[164:167],a[124:127],v[116:119],v[164:167]\n"
"s_cmp_eq_u32 s49,84480\n"
EX "v93,v93\n"
MF "v[168:171],a[48:51],v[116:119],v[168:171]\n"
"s_cselect_b32 s49,0,s49\n"
BL "v18,s[24:27],s67 offen offset:0 lds\n"
EX "v94,v94\n"
MF "v[172:175],a[96:99],v[120:123],v[172:175]\n"
DT "a[96:97],v7 offset:0\n"
DT "a[98:99],v7 offset:512\n"
EX "v95,v95\n"
MF "v[176:179],a[100:103],v[120:123],v[176:179]\n"
DT "a[100:101],v7 offset:32\n"
DT "a[102:103],v7 offset:544\n"
CV "v116,v80,v81\n"
MF "v[180:183],a[104:107],v[120:123],v[180:183]\n"
DT "a[104:105],v7 offset:64\n"
DT "a[106:107],v7 offset:576\n"
BL "v19,s[24:27],s67 offen offset:1024 lds\n"
CV "v117,v82,v83\n"
MF "v[184:187],a[108:111],v[120:123],v[184:187]\n"
DT "a[108:109],v7 offset:96\n"
DT "a[110:111],v7 offset:608\n"
CV "v118,v84,v85\n"
MF "v[188:191],a[112:115],v[120:123],v[188:191]\n"
DT "a[112:113],v7 offset:128\n"
DT "a[114:115],v7 offset:640\n"
CV "v119,v86,v87\n"
MF "v[192:195],a[116:119],v[120:123],v[192:195]\n"
DT "a[116:117],v7 offset:160\n"
DT "a[118:119],v7 offset:672\n"
MF "v[196:199],a[120:123],v[120:123],v[196:199]\n"
DT "a[120:121],v7 offset:192\n"
DT "a[122:123],v7 offset:704\n"
BL "v20,s[24:27],s67 offen offset:2048 lds\n"
MF "v[200:203],a[124:127],v[120:123],v[200:203]\n"
DT "a[124:125],v7 offset:224\n"
DT "a[126:127],v7 offset:736\n"
MF "v[204:207],a[48:51],v[120:123],v[204:207]\n"
CV "v120,v88,v89\n"
CV "v121,v90,v91\n"
CV "v122,v92,v93\n"
CV "v123,v94,v95\n"
WT "vmcnt(24)\n"
WT "lgkmcnt(0)\n"
"s_barrier\n"
MF "v[208:211],a[112:115],v[124:127],v[208:211]\n"
MF "v[212:215],a[116:119],v[124:127],v[212:215]\n"
MF "v[216:219],a[120:123],v[124:127],v[216:219]\n"
MF "v[220:223],a[124:127],v[124:127],v[220:223]\n"
MF "v[240:243],a[48:51],v[124:127],v[240:243]\n"
MF "v[136:139],a[96:99],v[116:119],v[136:139]\n"
MF "v[140:143],a[100:103],v[116:119],v[140:143]\n"
MF "v[144:147],a[104:107],v[116:119],v[144:147]\n"
MF "v[148:151],a[108:111],v[116:119],v[148:151]\n"
MF "v[152:155],a[112:115],v[116:119],v[152:155]\n"
MF "v[156:159],a[116:119],v[116:119],v[156:159]\n"
MF "v[160:163],a[120:123],v[116:119],v[160:163]\n"
MF "v[164:167],a[124:127],v[116:119],v[164:167]\n"
MF "v[168:171],a[48:51],v[116:119],v[168:171]\n"
MF "v[172:175],a[96:99],v[120:123],v[172:175]\n"
MF "v[176:179],a[100:103],v[120:123],v[176:179]\n"
MF "v[180:183],a[104:107],v[120:123],v[180:183]\n"
MF "v[184:187],a[108:111],v[120:123],v[184:187]\n"
MF "v[188:191],a[112:115],v[120:123],v[188:191]\n"
MF "v[192:195],a[116:119],v[120:123],v[192:195]\n"
MF "v[196:199],a[120:123],v[120:123],v[196:199]\n"
MF "v[200:203],a[124:127],v[120:123],v[200:203]\n"
MF "v[204:207],a[48:51],v[120:123],v[204:207]\n"
"s_nop 1\n"
"v_rcp_f32 v130,v240\n"
"s_add_u32 s66,s56,36864\n"
PM "v[208:209],v[208:209],v[130:131] op_sel_hi:[1,0]\n"
PM "v[210:211],v[210:211],v[130:131] op_sel_hi:[1,0]\n"
PM "v[212:213],v[212:213],v[130:131] op_sel_hi:[1,0]\n"
PM "v[214:215],v[214:215],v[130:131] op_sel_hi:[1,0]\n"
CV "v224,v208,v209\n"
CV "v225,v210,v211\n"
CV "v226,v212,v213\n"
CV "v227,v214,v215\n"
"buffer_store_dwordx4 v[224:227],v246,s[28:31],s66 offen offset:2048 sc1\n"
"ds_write_b128 v247,v[224:227] offset:2048\n"
PM "v[216:217],v[216:217],v[130:131] op_sel_hi:[1,0]\n"
PM "v[218:219],v[218:219],v[130:131] op_sel_hi:[1,0]\n"
PM "v[220:221],v[220:221],v[130:131] op_sel_hi:[1,0]\n"
PM "v[222:223],v[222:223],v[130:131] op_sel_hi:[1,0]\n"
CV "v228,v216,v217\n"
CV "v229,v218,v219\n"
CV "v230,v220,v221\n"
CV "v231,v222,v223\n"
"buffer_store_dwordx4 v[228:231],v246,s[28:31],s66 offen offset:3072 sc1\n"
"ds_write_b128 v247,v[228:231] offset:3072\n"
"v_rcp_f32 v130,v168\n"
"s_add_u32 s66,s56,36864\n"
PM "v[136:137],v[136:137],v[130:131] op_sel_hi:[1,0]\n"
PM "v[138:139],v[138:139],v[130:131] op_sel_hi:[1,0]\n"
PM "v[140:141],v[140:141],v[130:131] op_sel_hi:[1,0]\n"
PM "v[142:143],v[142:143],v[130:131] op_sel_hi:[1,0]\n"
CV "v80,v136,v137\n"
CV "v81,v138,v139\n"
CV "v82,v140,v141\n"
CV "v83,v142,v143\n"
"buffer_store_dwordx4 v[80:83],v244,s[28:31],s66 offen offset:0 sc1\n"
PM "v[144:145],v[144:145],v[130:131] op_sel_hi:[1,0]\n"
PM "v[146:147],v[146:147],v[130:131] op_sel_hi:[1,0]\n"
PM "v[148:149],v[148:149],v[130:131] op_sel_hi:[1,0]\n"
PM "v[150:151],v[150:151],v[130:131] op_sel_hi:[1,0]\n"
CV "v84,v144,v145\n"
CV "v85,v146,v147\n"
CV "v86,v148,v149\n"
CV "v87,v150,v151\n"
"buffer_store_dwordx4 v[84:87],v244,s[28:31],s66 offen offset:1024 sc1\n"
PM "v[152:153],v[152:153],v[130:131] op_sel_hi:[1,0]\n"
PM "v[154:155],v[154:155],v[130:131] op_sel_hi:[1,0]\n"
PM "v[156:157],v[156:157],v[130:131] op_sel_hi:[1,0]\n"
PM "v[158:159],v[158:159],v[130:131] op_sel_hi:[1,0]\n"
CV "v88,v152,v153\n"
CV "v89,v154,v155\n"
CV "v90,v156,v157\n"
CV "v91,v158,v159\n"
"buffer_store_dwordx4 v[88:91],v244,s[28:31],s66 offen offset:2048 sc1\n"
PM "v[160:161],v[160:161],v[130:131] op_sel_hi:[1,0]\n"
PM "v[162:163],v[162:163],v[130:131] op_sel_hi:[1,0]\n"
PM "v[164:165],v[164:165],v[130:131] op_sel_hi:[1,0]\n"
PM "v[166:167],v[166:167],v[130:131] op_sel_hi:[1,0]\n"
CV "v92,v160,v161\n"
CV "v93,v162,v163\n"
CV "v94,v164,v165\n"
CV "v95,v166,v167\n"
"buffer_store_dwordx4 v[92:95],v244,s[28:31],s66 offen offset:3072 sc1\n"
"v_rcp_f32 v130,v204\n"
"s_add_u32 s66,s56,36864\n"
PM "v[172:173],v[172:173],v[130:131] op_sel_hi:[1,0]\n"
PM "v[174:175],v[174:175],v[130:131] op_sel_hi:[1,0]\n"
PM "v[176:177],v[176:177],v[130:131] op_sel_hi:[1,0]\n"
PM "v[178:179],v[178:179],v[130:131] op_sel_hi:[1,0]\n"
CV "v96,v172,v173\n"
CV "v97,v174,v175\n"
CV "v98,v176,v177\n"
CV "v99,v178,v179\n"
"buffer_store_dwordx4 v[96:99],v245,s[28:31],s66 offen offset:0 sc1\n"
PM "v[180:181],v[180:181],v[130:131] op_sel_hi:[1,0]\n"
PM "v[182:183],v[182:183],v[130:131] op_sel_hi:[1,0]\n"
PM "v[184:185],v[184:185],v[130:131] op_sel_hi:[1,0]\n"
PM "v[186:187],v[186:187],v[130:131] op_sel_hi:[1,0]\n"
CV "v100,v180,v181\n"
CV "v101,v182,v183\n"
CV "v102,v184,v185\n"
CV "v103,v186,v187\n"
"buffer_store_dwordx4 v[100:103],v245,s[28:31],s66 offen offset:1024 sc1\n"
PM "v[188:189],v[188:189],v[130:131] op_sel_hi:[1,0]\n"
PM "v[190:191],v[190:191],v[130:131] op_sel_hi:[1,0]\n"
PM "v[192:193],v[192:193],v[130:131] op_sel_hi:[1,0]\n"
PM "v[194:195],v[194:195],v[130:131] op_sel_hi:[1,0]\n"
CV "v104,v188,v189\n"
CV "v105,v190,v191\n"
CV "v106,v192,v193\n"
CV "v107,v194,v195\n"
"buffer_store_dwordx4 v[104:107],v245,s[28:31],s66 offen offset:2048 sc1\n"
PM "v[196:197],v[196:197],v[130:131] op_sel_hi:[1,0]\n"
PM "v[198:199],v[198:199],v[130:131] op_sel_hi:[1,0]\n"
PM "v[200:201],v[200:201],v[130:131] op_sel_hi:[1,0]\n"
PM "v[202:203],v[202:203],v[130:131] op_sel_hi:[1,0]\n"
CV "v108,v196,v197\n"
CV "v109,v198,v199\n"
CV "v110,v200,v201\n"
CV "v111,v202,v203\n"
"buffer_store_dwordx4 v[108:111],v245,s[28:31],s66 offen offset:3072 sc1\n"
"v_add_f32 v128,v168,v204\n"
"v_add_f32 v128,v128,v240\n"
"v_cmp_ngt_f32 vcc,s43,v128\n"
"s_cmp_lg_u64 vcc,0\n"
"s_cselect_b32 s66,1,0\n"
"v_mov_b32 v128,s66\n"
"ds_write_b32 v251,v128 offset:16\n"
"s_branch LO15_%=\n"
"Lslow9_%=:\n"
"s_nop 7\n"
"v_mov_b32 v129,v128\n"
"s_nop 1\n"
"v_permlane32_swap_b32 v129,v128\n"
"v_max_f32 v128,v129,v128\n"
"v_mov_b32 v129,v128\n"
"s_nop 1\n"
"v_permlane16_swap_b32 v129,v128\n"
"v_max_f32 v128,v129,v128\n"
"v_max_f32 v128,0,v128\n"
EX "v130,-v128\n"
"v_sub_f32 v80,v80,v128\n"
"v_sub_f32 v81,v81,v128\n"
"v_sub_f32 v82,v82,v128\n"
"v_sub_f32 v83,v83,v128\n"
"v_sub_f32 v84,v84,v128\n"
"v_sub_f32 v85,v85,v128\n"
"v_sub_f32 v86,v86,v128\n"
"v_sub_f32 v87,v87,v128\n"
"v_sub_f32 v104,v104,v128\n"
"v_sub_f32 v105,v105,v128\n"
"v_sub_f32 v106,v106,v128\n"
"v_sub_f32 v107,v107,v128\n"
"v_mul_f32 v136,v130,v136\n"
"v_mul_f32 v137,v130,v137\n"
"v_mul_f32 v138,v130,v138\n"
"v_mul_f32 v139,v130,v139\n"
"v_mul_f32 v140,v130,v140\n"
"v_mul_f32 v141,v130,v141\n"
"v_mul_f32 v142,v130,v142\n"
"v_mul_f32 v143,v130,v143\n"
"v_mul_f32 v144,v130,v144\n"
"v_mul_f32 v145,v130,v145\n"
"v_mul_f32 v146,v130,v146\n"
"v_mul_f32 v147,v130,v147\n"
"v_mul_f32 v148,v130,v148\n"
"v_mul_f32 v149,v130,v149\n"
"v_mul_f32 v150,v130,v150\n"
"v_mul_f32 v151,v130,v151\n"
"v_mul_f32 v152,v130,v152\n"
"v_mul_f32 v153,v130,v153\n"
"v_mul_f32 v154,v130,v154\n"
"v_mul_f32 v155,v130,v155\n"
"v_mul_f32 v156,v130,v156\n"
"v_mul_f32 v157,v130,v157\n"
"v_mul_f32 v158,v130,v158\n"
"v_mul_f32 v159,v130,v159\n"
"v_mul_f32 v160,v130,v160\n"
"v_mul_f32 v161,v130,v161\n"
"v_mul_f32 v162,v130,v162\n"
"v_mul_f32 v163,v130,v163\n"
"v_mul_f32 v164,v130,v164\n"
"v_mul_f32 v165,v130,v165\n"
"v_mul_f32 v166,v130,v166\n"
"v_mul_f32 v167,v130,v167\n"
"v_mul_f32 v168,v130,v168\n"
"v_mul_f32 v169,v130,v169\n"
"v_mul_f32 v170,v130,v170\n"
"v_mul_f32 v171,v130,v171\n"
"s_nop 1\n"
"s_setpc_b64 s[60:61]\n"
"Lslow10_%=:\n"
"s_nop 7\n"
"v_mov_b32 v129,v128\n"
"s_nop 1\n"
"v_permlane32_swap_b32 v129,v128\n"
"v_max_f32 v128,v129,v128\n"
"v_mov_b32 v129,v128\n"
"s_nop 1\n"
"v_permlane16_swap_b32 v129,v128\n"
"v_max_f32 v128,v129,v128\n"
"v_max_f32 v128,0,v128\n"
EX "v130,-v128\n"
"v_sub_f32 v88,v88,v128\n"
"v_sub_f32 v89,v89,v128\n"
"v_sub_f32 v90,v90,v128\n"
"v_sub_f32 v91,v91,v128\n"
"v_sub_f32 v92,v92,v128\n"
"v_sub_f32 v93,v93,v128\n"
"v_sub_f32 v94,v94,v128\n"
"v_sub_f32 v95,v95,v128\n"
"v_sub_f32 v108,v108,v128\n"
"v_sub_f32 v109,v109,v128\n"
"v_sub_f32 v110,v110,v128\n"
"v_sub_f32 v111,v111,v128\n"
"v_mul_f32 v172,v130,v172\n"
"v_mul_f32 v173,v130,v173\n"
"v_mul_f32 v174,v130,v174\n"
"v_mul_f32 v175,v130,v175\n"
"v_mul_f32 v176,v130,v176\n"
"v_mul_f32 v177,v130,v177\n"
"v_mul_f32 v178,v130,v178\n"
"v_mul_f32 v179,v130,v179\n"
"v_mul_f32 v180,v130,v180\n"
"v_mul_f32 v181,v130,v181\n"
"v_mul_f32 v182,v130,v182\n"
"v_mul_f32 v183,v130,v183\n"
"v_mul_f32 v184,v130,v184\n"
"v_mul_f32 v185,v130,v185\n"
"v_mul_f32 v186,v130,v186\n"
"v_mul_f32 v187,v130,v187\n"
"v_mul_f32 v188,v130,v188\n"
"v_mul_f32 v189,v130,v189\n"
"v_mul_f32 v190,v130,v190\n"
"v_mul_f32 v191,v130,v191\n"
"v_mul_f32 v192,v130,v192\n"
"v_mul_f32 v193,v130,v193\n"
"v_mul_f32 v194,v130,v194\n"
"v_mul_f32 v195,v130,v195\n"
"v_mul_f32 v196,v130,v196\n"
"v_mul_f32 v197,v130,v197\n"
"v_mul_f32 v198,v130,v198\n"
"v_mul_f32 v199,v130,v199\n"
"v_mul_f32 v200,v130,v200\n"
"v_mul_f32 v201,v130,v201\n"
"v_mul_f32 v202,v130,v202\n"
"v_mul_f32 v203,v130,v203\n"
"v_mul_f32 v204,v130,v204\n"
"v_mul_f32 v205,v130,v205\n"
"v_mul_f32 v206,v130,v206\n"
"v_mul_f32 v207,v130,v207\n"
"s_nop 1\n"
"s_setpc_b64 s[60:61]\n"
"Lslow11_%=:\n"
"s_nop 7\n"
"v_mov_b32 v129,v128\n"
"s_nop 1\n"
"v_permlane32_swap_b32 v129,v128\n"
"v_max_f32 v128,v129,v128\n"
"v_mov_b32 v129,v128\n"
"s_nop 1\n"
"v_permlane16_swap_b32 v129,v128\n"
"v_max_f32 v128,v129,v128\n"
"v_max_f32 v128,0,v128\n"
EX "v130,-v128\n"
"v_sub_f32 v96,v96,v128\n"
"v_sub_f32 v97,v97,v128\n"
"v_sub_f32 v98,v98,v128\n"
"v_sub_f32 v99,v99,v128\n"
"v_sub_f32 v100,v100,v128\n"
"v_sub_f32 v101,v101,v128\n"
"v_sub_f32 v102,v102,v128\n"
"v_sub_f32 v103,v103,v128\n"
"v_sub_f32 v112,v112,v128\n"
"v_sub_f32 v113,v113,v128\n"
"v_sub_f32 v114,v114,v128\n"
"v_sub_f32 v115,v115,v128\n"
"v_mul_f32 v208,v130,v208\n"
"v_mul_f32 v209,v130,v209\n"
"v_mul_f32 v210,v130,v210\n"
"v_mul_f32 v211,v130,v211\n"
"v_mul_f32 v212,v130,v212\n"
"v_mul_f32 v213,v130,v213\n"
"v_mul_f32 v214,v130,v214\n"
"v_mul_f32 v215,v130,v215\n"
"v_mul_f32 v216,v130,v216\n"
"v_mul_f32 v217,v130,v217\n"
"v_mul_f32 v218,v130,v218\n"
"v_mul_f32 v219,v130,v219\n"
"v_mul_f32 v220,v130,v220\n"
"v_mul_f32 v221,v130,v221\n"
"v_mul_f32 v222,v130,v222\n"
"v_mul_f32 v223,v130,v223\n"
"v_mul_f32 v224,v130,v224\n"
"v_mul_f32 v225,v130,v225\n"
"v_mul_f32 v226,v130,v226\n"
"v_mul_f32 v227,v130,v227\n"
"v_mul_f32 v228,v130,v228\n"
"v_mul_f32 v229,v130,v229\n"
"v_mul_f32 v230,v130,v230\n"
"v_mul_f32 v231,v130,v231\n"
"v_mul_f32 v232,v130,v232\n"
"v_mul_f32 v233,v130,v233\n"
"v_mul_f32 v234,v130,v234\n"
"v_mul_f32 v235,v130,v235\n"
"v_mul_f32 v236,v130,v236\n"
"v_mul_f32 v237,v130,v237\n"
"v_mul_f32 v238,v130,v238\n"
"v_mul_f32 v239,v130,v239\n"
"v_mul_f32 v240,v130,v240\n"
"v_mul_f32 v241,v130,v241\n"
"v_mul_f32 v242,v130,v242\n"
"v_mul_f32 v243,v130,v243\n"
"s_nop 1\n"
"s_setpc_b64 s[60:61]\n"
"Lr18_%=:\n"
WT "vmcnt(0) lgkmcnt(0)\n"
"s_barrier\n"
"s_mov_b32 s98,2\n"
"s_mov_b32 s99,0\n"
"s_branch LR1_%=\n"
"Lp19_%=:\n"
"s_mov_b32 s77,4000000\n"
"Lp21_%=:\n"
"buffer_load_dword v31,v26,s[28:31],0 offen offset:0 sc1\n"
WT "vmcnt(0)\n"
"v_cmp_eq_u32 vcc,v31,v28\n"
"s_andn2_b64 vcc,vcc,s[62:63]\n"
"s_cbranch_scc0 Lp20_%=\n"
"s_sub_u32 s77,s77,1\n"
"s_cmp_lg_u32 s77,0\n"
"s_cbranch_scc1 Lp21_%=\n"
"s_branch Lp20_%=\n"
"Lr22_%=:\n"
WT "vmcnt(0) lgkmcnt(0)\n"
"s_barrier\n"
"s_mov_b32 s98,2\n"
"s_mov_b32 s99,1073741824\n"
"s_branch LR1_%=\n"
"Lp23_%=:\n"
"s_mov_b32 s77,4000000\n"
"Lp25_%=:\n"
"buffer_load_dword v31,v26,s[28:31],0 offen offset:128 sc1\n"
WT "vmcnt(0)\n"
"v_cmp_eq_u32 vcc,v31,v29\n"
"s_andn2_b64 vcc,vcc,s[62:63]\n"
"s_cbranch_scc0 Lp24_%=\n"
"s_sub_u32 s77,s77,1\n"
"s_cmp_lg_u32 s77,0\n"
"s_cbranch_scc1 Lp25_%=\n"
"s_branch Lp24_%=\n"
"Lp30_%=:\n"
"s_mov_b32 s77,4000000\n"
"Lp32_%=:\n"
"buffer_load_dword v31,v27,s[28:31],0 offen offset:256 sc1\n"
WT "vmcnt(0)\n"
"v_cmp_eq_u32 vcc,v31,v30\n"
"s_andn2_b64 vcc,vcc,s[64:65]\n"
"s_cbranch_scc0 Lp31_%=\n"
"s_sub_u32 s77,s77,1\n"
"s_cmp_lg_u32 s77,0\n"
"s_cbranch_scc1 Lp32_%=\n"
"s_branch Lp31_%=\n"
"Ls34_%=:\n"
"s_call_b64 s[60:61],Lslow9_%=\n"
"s_branch Ls35_%=\n"
"Ls36_%=:\n"
"s_call_b64 s[60:61],Lslow10_%=\n"
"s_branch Ls37_%=\n"
"Ls38_%=:\n"
"s_call_b64 s[60:61],Lslow11_%=\n"
"s_branch Ls39_%=\n"
"Ls125_%=:\n"
"s_call_b64 s[60:61],Lslow9_%=\n"
"s_branch Ls126_%=\n"
"Ls127_%=:\n"
"s_call_b64 s[60:61],Lslow10_%=\n"
"s_branch Ls128_%=\n"
"Ls129_%=:\n"
"s_call_b64 s[60:61],Lslow11_%=\n"
"s_branch Ls130_%=\n"
"Ls149_%=:\n"
"s_call_b64 s[60:61],Lslow9_%=\n"
"s_branch Ls150_%=\n"
"Ls151_%=:\n"
"s_call_b64 s[60:61],Lslow10_%=\n"
"s_branch Ls152_%=\n"
"Ls153_%=:\n"
"s_call_b64 s[60:61],Lslow11_%=\n"
"s_branch Ls154_%=\n"
"Ls155_%=:\n"
"s_call_b64 s[60:61],Lslow9_%=\n"
"s_branch Ls156_%=\n"
"Ls157_%=:\n"
"s_call_b64 s[60:61],Lslow10_%=\n"
"s_branch Ls158_%=\n"
"Ls159_%=:\n"
"s_call_b64 s[60:61],Lslow11_%=\n"
"s_branch Ls160_%=\n"
"Lp161_%=:\n"
"s_mov_b32 s77,4000000\n"
"Lp163_%=:\n"
"buffer_load_dword v31,v26,s[28:31],0 offen offset:0 sc1\n"
WT "vmcnt(0)\n"
"v_cmp_eq_u32 vcc,v31,v28\n"
"s_andn2_b64 vcc,vcc,s[62:63]\n"
"s_cbranch_scc0 Lp162_%=\n"
"s_sub_u32 s77,s77,1\n"
"s_cmp_lg_u32 s77,0\n"
"s_cbranch_scc1 Lp163_%=\n"
"s_branch Lp162_%=\n"
"Ls164_%=:\n"
"s_call_b64 s[60:61],Lslow9_%=\n"
"s_branch Ls165_%=\n"
"Ls166_%=:\n"
"s_call_b64 s[60:61],Lslow10_%=\n"
"s_branch Ls167_%=\n"
"Ls168_%=:\n"
"s_call_b64 s[60:61],Lslow11_%=\n"
"s_branch Ls169_%=\n"
"Ls170_%=:\n"
"s_call_b64 s[60:61],Lslow9_%=\n"
"s_branch Ls171_%=\n"
"Ls172_%=:\n"
"s_call_b64 s[60:61],Lslow10_%=\n"
"s_branch Ls173_%=\n"
"Ls174_%=:\n"
"s_call_b64 s[60:61],Lslow11_%=\n"
"s_branch Ls175_%=\n"
"Ls176_%=:\n"
"s_call_b64 s[60:61],Lslow9_%=\n"
"s_branch Ls177_%=\n"
"Ls178_%=:\n"
"s_call_b64 s[60:61],Lslow10_%=\n"
"s_branch Ls179_%=\n"
"Ls180_%=:\n"
"s_call_b64 s[60:61],Lslow11_%=\n"
"s_branch Ls181_%=\n"
"Ls182_%=:\n"
"s_call_b64 s[60:61],Lslow9_%=\n"
"s_branch Ls183_%=\n"
"Ls184_%=:\n"
"s_call_b64 s[60:61],Lslow10_%=\n"
"s_branch Ls185_%=\n"
"Ls186_%=:\n"
"s_call_b64 s[60:61],Lslow11_%=\n"
"s_branch Ls187_%=\n"
"Ls188_%=:\n"
"s_call_b64 s[60:61],Lslow9_%=\n"
"s_branch Ls189_%=\n"
"Ls190_%=:\n"
"s_call_b64 s[60:61],Lslow10_%=\n"
"s_branch Ls191_%=\n"
"Ls192_%=:\n"
"s_call_b64 s[60:61],Lslow11_%=\n"
"s_branch Ls193_%=\n"
"Ls194_%=:\n"
"s_call_b64 s[60:61],Lslow9_%=\n"
"s_branch Ls195_%=\n"
"Ls196_%=:\n"
"s_call_b64 s[60:61],Lslow10_%=\n"
"s_branch Ls197_%=\n"
"Ls198_%=:\n"
"s_call_b64 s[60:61],Lslow11_%=\n"
"s_branch Ls199_%=\n"
"Ls200_%=:\n"
"s_call_b64 s[60:61],Lslow9_%=\n"
"s_branch Ls201_%=\n"
"Ls202_%=:\n"
"s_call_b64 s[60:61],Lslow10_%=\n"
"s_branch Ls203_%=\n"
"Ls204_%=:\n"
"s_call_b64 s[60:61],Lslow11_%=\n"
"s_branch Ls205_%=\n"
"Ls206_%=:\n"
"s_call_b64 s[60:61],Lslow9_%=\n"
"s_branch Ls207_%=\n"
"Ls208_%=:\n"
"s_call_b64 s[60:61],Lslow10_%=\n"
"s_branch Ls209_%=\n"
"Ls210_%=:\n"
"s_call_b64 s[60:61],Lslow11_%=\n"
"s_branch Ls211_%=\n"
"Ls212_%=:\n"
"s_call_b64 s[60:61],Lslow9_%=\n"
"s_branch Ls213_%=\n"
"Ls214_%=:\n"
"s_call_b64 s[60:61],Lslow10_%=\n"
"s_branch Ls215_%=\n"
"Ls216_%=:\n"
"s_call_b64 s[60:61],Lslow11_%=\n"
"s_branch Ls217_%=\n"
"Lr220_%=:\n"
WT "vmcnt(0) lgkmcnt(0)\n"
"s_barrier\n"
"s_mov_b32 s98,2\n"
"s_mov_b32 s99,0\n"
"s_branch LR1_%=\n"
"Lp221_%=:\n"
"s_mov_b32 s77,4000000\n"
"Lp223_%=:\n"
"buffer_load_dword v31,v26,s[28:31],0 offen offset:0 sc1\n"
WT "vmcnt(0)\n"
"v_cmp_eq_u32 vcc,v31,v28\n"
"s_andn2_b64 vcc,vcc,s[62:63]\n"
"s_cbranch_scc0 Lp222_%=\n"
"s_sub_u32 s77,s77,1\n"
"s_cmp_lg_u32 s77,0\n"
"s_cbranch_scc1 Lp223_%=\n"
"s_branch Lp222_%=\n"
"Lr224_%=:\n"
WT "vmcnt(0) lgkmcnt(0)\n"
"s_barrier\n"
"s_mov_b32 s98,2\n"
"s_mov_b32 s99,1073741824\n"
"s_branch LR1_%=\n"
"Lp225_%=:\n"
"s_mov_b32 s77,4000000\n"
"Lp227_%=:\n"
"buffer_load_dword v31,v26,s[28:31],0 offen offset:128 sc1\n"
WT "vmcnt(0)\n"
"v_cmp_eq_u32 vcc,v31,v29\n"
"s_andn2_b64 vcc,vcc,s[62:63]\n"
"s_cbranch_scc0 Lp226_%=\n"
"s_sub_u32 s77,s77,1\n"
"s_cmp_lg_u32 s77,0\n"
"s_cbranch_scc1 Lp227_%=\n"
"s_branch Lp226_%=\n"
"Lp232_%=:\n"
"s_mov_b32 s77,4000000\n"
"Lp234_%=:\n"
"buffer_load_dword v31,v27,s[28:31],0 offen offset:256 sc1\n"
WT "vmcnt(0)\n"
"v_cmp_eq_u32 vcc,v31,v30\n"
"s_andn2_b64 vcc,vcc,s[64:65]\n"
"s_cbranch_scc0 Lp233_%=\n"
"s_sub_u32 s77,s77,1\n"
"s_cmp_lg_u32 s77,0\n"
"s_cbranch_scc1 Lp234_%=\n"
"s_branch Lp233_%=\n"
"Ls236_%=:\n"
"s_call_b64 s[60:61],Lslow9_%=\n"
"s_branch Ls237_%=\n"
"Ls238_%=:\n"
"s_call_b64 s[60:61],Lslow10_%=\n"
"s_branch Ls239_%=\n"
"Ls297_%=:\n"
"s_call_b64 s[60:61],Lslow9_%=\n"
"s_branch Ls298_%=\n"
"Ls299_%=:\n"
"s_call_b64 s[60:61],Lslow10_%=\n"
"s_branch Ls300_%=\n"
"Ls313_%=:\n"
"s_call_b64 s[60:61],Lslow9_%=\n"
"s_branch Ls314_%=\n"
"Ls315_%=:\n"
"s_call_b64 s[60:61],Lslow10_%=\n"
"s_branch Ls316_%=\n"
"Ls317_%=:\n"
"s_call_b64 s[60:61],Lslow9_%=\n"
"s_branch Ls318_%=\n"
"Ls319_%=:\n"
"s_call_b64 s[60:61],Lslow10_%=\n"
"s_branch Ls320_%=\n"
"Lp321_%=:\n"
"s_mov_b32 s77,4000000\n"
"Lp323_%=:\n"
"buffer_load_dword v31,v26,s[28:31],0 offen offset:0 sc1\n"
WT "vmcnt(0)\n"
"v_cmp_eq_u32 vcc,v31,v28\n"
"s_andn2_b64 vcc,vcc,s[62:63]\n"
"s_cbranch_scc0 Lp322_%=\n"
"s_sub_u32 s77,s77,1\n"
"s_cmp_lg_u32 s77,0\n"
"s_cbranch_scc1 Lp323_%=\n"
"s_branch Lp322_%=\n"
"Ls324_%=:\n"
"s_call_b64 s[60:61],Lslow9_%=\n"
"s_branch Ls325_%=\n"
"Ls326_%=:\n"
"s_call_b64 s[60:61],Lslow10_%=\n"
"s_branch Ls327_%=\n"
"Ls328_%=:\n"
"s_call_b64 s[60:61],Lslow9_%=\n"
"s_branch Ls329_%=\n"
"Ls330_%=:\n"
"s_call_b64 s[60:61],Lslow10_%=\n"
"s_branch Ls331_%=\n"
"Ls332_%=:\n"
"s_call_b64 s[60:61],Lslow9_%=\n"
"s_branch Ls333_%=\n"
"Ls334_%=:\n"
"s_call_b64 s[60:61],Lslow10_%=\n"
"s_branch Ls335_%=\n"
"Ls336_%=:\n"
"s_call_b64 s[60:61],Lslow9_%=\n"
"s_branch Ls337_%=\n"
"Ls338_%=:\n"
"s_call_b64 s[60:61],Lslow10_%=\n"
"s_branch Ls339_%=\n"
"Ls340_%=:\n"
"s_call_b64 s[60:61],Lslow9_%=\n"
"s_branch Ls341_%=\n"
"Ls342_%=:\n"
"s_call_b64 s[60:61],Lslow10_%=\n"
"s_branch Ls343_%=\n"
"Ls344_%=:\n"
"s_call_b64 s[60:61],Lslow9_%=\n"
"s_branch Ls345_%=\n"
"Ls346_%=:\n"
"s_call_b64 s[60:61],Lslow10_%=\n"
"s_branch Ls347_%=\n"
"Ls348_%=:\n"
"s_call_b64 s[60:61],Lslow9_%=\n"
"s_branch Ls349_%=\n"
"Ls350_%=:\n"
"s_call_b64 s[60:61],Lslow10_%=\n"
"s_branch Ls351_%=\n"
"Ls352_%=:\n"
"s_call_b64 s[60:61],Lslow9_%=\n"
"s_branch Ls353_%=\n"
"Ls354_%=:\n"
"s_call_b64 s[60:61],Lslow10_%=\n"
"s_branch Ls355_%=\n"
"Ls356_%=:\n"
"s_call_b64 s[60:61],Lslow9_%=\n"
"s_branch Ls357_%=\n"
"Ls358_%=:\n"
"s_call_b64 s[60:61],Lslow10_%=\n"
"s_branch Ls359_%=\n"
"Lr362_%=:\n"
WT "vmcnt(0) lgkmcnt(0)\n"
"s_barrier\n"
"s_mov_b32 s98,2\n"
"s_mov_b32 s99,0\n"
"s_branch LR1_%=\n"
"Lp363_%=:\n"
"s_mov_b32 s77,4000000\n"
"Lp365_%=:\n"
"buffer_load_dword v31,v26,s[28:31],0 offen offset:0 sc1\n"
WT "vmcnt(0)\n"
"v_cmp_eq_u32 vcc,v31,v28\n"
"s_andn2_b64 vcc,vcc,s[62:63]\n"
"s_cbranch_scc0 Lp364_%=\n"
"s_sub_u32 s77,s77,1\n"
"s_cmp_lg_u32 s77,0\n"
"s_cbranch_scc1 Lp365_%=\n"
"s_branch Lp364_%=\n"
"Lr368_%=:\n"
WT "vmcnt(0) lgkmcnt(0)\n"
"s_barrier\n"
"s_mov_b32 s98,2\n"
"s_mov_b32 s99,0\n"
"s_branch LR1_%=\n"
"Lp369_%=:\n"
"s_mov_b32 s77,4000000\n"
"Lp371_%=:\n"
"buffer_load_dword v31,v26,s[28:31],0 offen offset:0 sc1\n"
WT "vmcnt(0)\n"
"v_cmp_eq_u32 vcc,v31,v28\n"
"s_andn2_b64 vcc,vcc,s[62:63]\n"
"s_cbranch_scc0 Lp370_%=\n"
"s_sub_u32 s77,s77,1\n"
"s_cmp_lg_u32 s77,0\n"
"s_cbranch_scc1 Lp371_%=\n"
"s_branch Lp370_%=\n"
"LS12_%=:\n"
DR "v[64:67],v253 offset:0\n"
DR "v[68:71],v253 offset:1024\n"
DR "v[72:75],v253 offset:2048\n"
DR "v[76:79],v253 offset:3072\n"
DR "a[32:35],v253 offset:4096\n"
DR "a[36:39],v253 offset:5120\n"
DR "a[40:43],v253 offset:6144\n"
DR "a[44:47],v253 offset:7168\n"
WT "lgkmcnt(0)\n"
MF "v[80:83],a[64:67],v[32:35],0\n"
"v_mov_b64 v[136:137],0\n"
"v_mov_b64 v[138:139],0\n"
"v_mov_b64 v[140:141],0\n"
MF "v[84:87],a[68:71],v[32:35],0\n"
"v_mov_b64 v[142:143],0\n"
"v_mov_b64 v[144:145],0\n"
"v_mov_b64 v[146:147],0\n"
MF "v[80:83],a[72:75],v[36:39],v[80:83]\n"
"v_mov_b64 v[148:149],0\n"
"v_mov_b64 v[150:151],0\n"
MF "v[84:87],a[76:79],v[36:39],v[84:87]\n"
"v_mov_b64 v[152:153],0\n"
"v_mov_b64 v[154:155],0\n"
"v_mov_b64 v[156:157],0\n"
MF "v[80:83],a[80:83],v[40:43],v[80:83]\n"
"v_add_u32 v6,s49,v4\n"
"s_mov_b32 s50,s42\n"
"v_mov_b64 v[158:159],0\n"
"v_mov_b64 v[160:161],0\n"
MF "v[84:87],a[84:87],v[40:43],v[84:87]\n"
"v_add_u32 v7,s49,v5\n"
"s_mov_b32 s42,s49\n"
"v_mov_b64 v[162:163],0\n"
"v_mov_b64 v[164:165],0\n"
"v_mov_b64 v[166:167],0\n"
MF "v[80:83],a[88:91],v[44:47],v[80:83]\n"
"s_add_u32 s49,s49,16896\n"
"s_cmp_eq_u32 s49,84480\n"
"s_cselect_b32 s49,0,s49\n"
"v_mov_b64 v[168:169],0\n"
"v_mov_b64 v[170:171],0\n"
MF "v[84:87],a[92:95],v[44:47],v[84:87]\n"
"s_nop 7\n"
"v_max3_f32 v128,v80,v81,v82\n"
"v_max3_f32 v128,v128,v83,v84\n"
"v_max3_f32 v128,v128,v85,v86\n"
"v_max_f32 v128,v128,v87\n"
"v_mov_b32 v129,v128\n"
"s_nop 1\n"
"v_permlane32_swap_b32 v129,v128\n"
"v_max_f32 v128,v129,v128\n"
"v_mov_b32 v129,v128\n"
"s_nop 1\n"
"v_permlane16_swap_b32 v129,v128\n"
"v_max_f32 v128,v129,v128\n"
"v_sub_f32 v80,v80,v128\n"
"v_sub_f32 v81,v81,v128\n"
"v_sub_f32 v82,v82,v128\n"
"v_sub_f32 v83,v83,v128\n"
"v_sub_f32 v84,v84,v128\n"
"v_sub_f32 v85,v85,v128\n"
"v_sub_f32 v86,v86,v128\n"
"v_sub_f32 v87,v87,v128\n"
"v_sub_f32 v104,0,v128\n"
"v_sub_f32 v105,0,v128\n"
"v_sub_f32 v106,0,v128\n"
"v_sub_f32 v107,0,v128\n"
MF "v[88:91],a[64:67],v[48:51],0\n"
"v_mov_b64 v[172:173],0\n"
"v_mov_b64 v[174:175],0\n"
"v_mov_b64 v[176:177],0\n"
MF "v[92:95],a[68:71],v[48:51],0\n"
"v_mov_b64 v[178:179],0\n"
"v_mov_b64 v[180:181],0\n"
"v_mov_b64 v[182:183],0\n"
MF "v[88:91],a[72:75],v[52:55],v[88:91]\n"
"v_mov_b64 v[184:185],0\n"
"v_mov_b64 v[186:187],0\n"
MF "v[92:95],a[76:79],v[52:55],v[92:95]\n"
"v_mov_b64 v[188:189],0\n"
"v_mov_b64 v[190:191],0\n"
"v_mov_b64 v[192:193],0\n"
MF "v[88:91],a[80:83],v[56:59],v[88:91]\n"
"v_mov_b64 v[194:195],0\n"
"v_mov_b64 v[196:197],0\n"
MF "v[92:95],a[84:87],v[56:59],v[92:95]\n"
"v_mov_b64 v[198:199],0\n"
"v_mov_b64 v[200:201],0\n"
"v_mov_b64 v[202:203],0\n"
MF "v[88:91],a[88:91],v[60:63],v[88:91]\n"
"v_mov_b64 v[204:205],0\n"
"v_mov_b64 v[206:207],0\n"
MF "v[92:95],a[92:95],v[60:63],v[92:95]\n"
"s_nop 7\n"
"v_max3_f32 v128,v88,v89,v90\n"
"v_max3_f32 v128,v128,v91,v92\n"
"v_max3_f32 v128,v128,v93,v94\n"
"v_max_f32 v128,v128,v95\n"
"v_mov_b32 v129,v128\n"
"s_nop 1\n"
"v_permlane32_swap_b32 v129,v128\n"
"v_max_f32 v128,v129,v128\n"
"v_mov_b32 v129,v128\n"
"s_nop 1\n"
"v_permlane16_swap_b32 v129,v128\n"
"v_max_f32 v128,v129,v128\n"
"v_sub_f32 v88,v88,v128\n"
"v_sub_f32 v89,v89,v128\n"
"v_sub_f32 v90,v90,v128\n"
"v_sub_f32 v91,v91,v128\n"
"v_sub_f32 v92,v92,v128\n"
"v_sub_f32 v93,v93,v128\n"
"v_sub_f32 v94,v94,v128\n"
"v_sub_f32 v95,v95,v128\n"
"v_sub_f32 v108,0,v128\n"
"v_sub_f32 v109,0,v128\n"
"v_sub_f32 v110,0,v128\n"
"v_sub_f32 v111,0,v128\n"
WT "vmcnt(12)\n"
"s_barrier\n"
MF "v[96:99],a[64:67],v[64:67],0\n"
EX "v80,v80\n"
DR "a[64:67],v6 offset:0\n"
"v_mov_b64 v[208:209],0\n"
"v_mov_b64 v[210:211],0\n"
"v_mov_b64 v[212:213],0\n"
MF "v[100:103],a[68:71],v[64:67],0\n"
EX "v81,v81\n"
DR "a[68:71],v6 offset:512\n"
"v_mov_b64 v[214:215],0\n"
"v_mov_b64 v[216:217],0\n"
"v_mov_b64 v[218:219],0\n"
MF "v[96:99],a[72:75],v[68:71],v[96:99]\n"
EX "v82,v82\n"
DR "a[72:75],v6 offset:64\n"
"v_mov_b64 v[220:221],0\n"
"v_mov_b64 v[222:223],0\n"
MF "v[100:103],a[76:79],v[68:71],v[100:103]\n"
EX "v83,v83\n"
DR "a[76:79],v6 offset:576\n"
"v_mov_b64 v[224:225],0\n"
"v_mov_b64 v[226:227],0\n"
"v_mov_b64 v[228:229],0\n"
MF "v[96:99],a[80:83],v[72:75],v[96:99]\n"
CV "v116,v80,v81\n"
CV "v117,v82,v83\n"
DR "a[80:83],v6 offset:128\n"
"v_mov_b64 v[230:231],0\n"
"v_mov_b64 v[232:233],0\n"
MF "v[100:103],a[84:87],v[72:75],v[100:103]\n"
EX "v84,v84\n"
DR "a[84:87],v6 offset:640\n"
"v_mov_b64 v[234:235],0\n"
"v_mov_b64 v[236:237],0\n"
"v_mov_b64 v[238:239],0\n"
MF "v[96:99],a[88:91],v[76:79],v[96:99]\n"
EX "v85,v85\n"
DR "a[88:91],v6 offset:192\n"
"v_mov_b64 v[240:241],0\n"
"v_mov_b64 v[242:243],0\n"
MF "v[100:103],a[92:95],v[76:79],v[100:103]\n"
EX "v86,v86\n"
DR "a[92:95],v6 offset:704\n"
EX "v87,v87\n"
DT "a[96:97],v7 offset:0\n"
DT "a[98:99],v7 offset:512\n"
CV "v118,v84,v85\n"
CV "v119,v86,v87\n"
DT "a[100:101],v7 offset:32\n"
DT "a[102:103],v7 offset:544\n"
DT "a[104:105],v7 offset:64\n"
DT "a[106:107],v7 offset:576\n"
DT "a[108:109],v7 offset:96\n"
DT "a[110:111],v7 offset:608\n"
DT "a[112:113],v7 offset:128\n"
DT "a[114:115],v7 offset:640\n"
DT "a[116:117],v7 offset:160\n"
DT "a[118:119],v7 offset:672\n"
DT "a[120:121],v7 offset:192\n"
DT "a[122:123],v7 offset:704\n"
DT "a[124:125],v7 offset:224\n"
DT "a[126:127],v7 offset:736\n"
"s_nop 7\n"
"v_max3_f32 v128,v96,v97,v98\n"
"v_max3_f32 v128,v128,v99,v100\n"
"v_max3_f32 v128,v128,v101,v102\n"
"v_max_f32 v128,v128,v103\n"
"v_mov_b32 v129,v128\n"
"s_nop 1\n"
"v_permlane32_swap_b32 v129,v128\n"
"v_max_f32 v128,v129,v128\n"
"v_mov_b32 v129,v128\n"
"s_nop 1\n"
"v_permlane16_swap_b32 v129,v128\n"
"v_max_f32 v128,v129,v128\n"
"v_sub_f32 v96,v96,v128\n"
"v_sub_f32 v97,v97,v128\n"
"v_sub_f32 v98,v98,v128\n"
"v_sub_f32 v99,v99,v128\n"
"v_sub_f32 v100,v100,v128\n"
"v_sub_f32 v101,v101,v128\n"
"v_sub_f32 v102,v102,v128\n"
"v_sub_f32 v103,v103,v128\n"
"v_sub_f32 v112,0,v128\n"
"v_sub_f32 v113,0,v128\n"
"v_sub_f32 v114,0,v128\n"
"v_sub_f32 v115,0,v128\n"
"s_mov_b32 s52,163840\n"
"Ll33_%=:\n"
WT "lgkmcnt(15)\n"
MF "v[80:83],a[64:67],v[32:35],v[104:107]\n"
EX "v88,v88\n"
MF "v[84:87],a[68:71],v[32:35],v[104:107]\n"
EX "v89,v89\n"
MF "v[80:83],a[72:75],v[36:39],v[80:83]\n"
EX "v90,v90\n"
MF "v[84:87],a[76:79],v[36:39],v[84:87]\n"
EX "v91,v91\n"
MF "v[80:83],a[80:83],v[40:43],v[80:83]\n"
CV "v120,v88,v89\n"
CV "v121,v90,v91\n"
MF "v[84:87],a[84:87],v[40:43],v[84:87]\n"
EX "v92,v92\n"
MF "v[80:83],a[88:91],v[44:47],v[80:83]\n"
EX "v93,v93\n"
MF "v[84:87],a[92:95],v[44:47],v[84:87]\n"
EX "v94,v94\n"
WT "lgkmcnt(0)\n"
MF "v[136:139],a[96:99],v[116:119],v[136:139]\n"
EX "v95,v95\n"
"s_add_u32 m0,s50,s48\n"
MF "v[140:143],a[100:103],v[116:119],v[140:143]\n"
CV "v122,v92,v93\n"
CV "v123,v94,v95\n"
BL "v8,s[12:15],s78 offen nt lds\n"
MF "v[144:147],a[104:107],v[116:119],v[144:147]\n"
BL "v9,s[12:15],s78 offen offset:1056 nt lds\n"
"s_add_u32 m0,m0,8448\n"
MF "v[148:151],a[108:111],v[116:119],v[148:151]\n"
BL "v10,s[16:19],s79 offen nt lds\n"
MF "v[152:155],a[112:115],v[116:119],v[152:155]\n"
BL "v11,s[16:19],s79 offen offset:1056 nt lds\n"
"s_add_u32 s51,s51,8192\n"
"s_mov_b32 s79,s78\n"
"s_xor_b32 s78,s51,s41\n"
MF "v[156:159],a[116:119],v[116:119],v[156:159]\n"
"v_add_u32 v6,s49,v4\n"
"s_mov_b32 s50,s42\n"
"v_max3_f32 v128,v80,v81,v82\n"
"v_max3_f32 v128,v128,v83,v84\n"
MF "v[160:163],a[120:123],v[116:119],v[160:163]\n"
"v_add_u32 v7,s49,v5\n"
"s_mov_b32 s42,s49\n"
"v_max3_f32 v128,v128,v85,v86\n"
"v_max_f32 v128,v128,v87\n"
MF "v[164:167],a[124:127],v[116:119],v[164:167]\n"
"s_add_u32 s49,s49,16896\n"
"s_cmp_eq_u32 s49,84480\n"
"s_cselect_b32 s49,0,s49\n"
"v_cmp_lt_f32 vcc,s53,v128\n"
MF "v[168:171],a[48:51],v[116:119],v[168:171]\n"
"s_cbranch_vccnz Ls34_%=\n"
"Ls35_%=:\n"
MF "v[88:91],a[64:67],v[48:51],v[108:111]\n"
EX "v96,v96\n"
MF "v[92:95],a[68:71],v[48:51],v[108:111]\n"
EX "v97,v97\n"
MF "v[88:91],a[72:75],v[52:55],v[88:91]\n"
EX "v98,v98\n"
MF "v[92:95],a[76:79],v[52:55],v[92:95]\n"
EX "v99,v99\n"
MF "v[88:91],a[80:83],v[56:59],v[88:91]\n"
CV "v124,v96,v97\n"
CV "v125,v98,v99\n"
MF "v[92:95],a[84:87],v[56:59],v[92:95]\n"
EX "v100,v100\n"
MF "v[88:91],a[88:91],v[60:63],v[88:91]\n"
EX "v101,v101\n"
MF "v[92:95],a[92:95],v[60:63],v[92:95]\n"
EX "v102,v102\n"
MF "v[172:175],a[96:99],v[120:123],v[172:175]\n"
EX "v103,v103\n"
MF "v[176:179],a[100:103],v[120:123],v[176:179]\n"
CV "v126,v100,v101\n"
CV "v127,v102,v103\n"
MF "v[180:183],a[104:107],v[120:123],v[180:183]\n"
MF "v[184:187],a[108:111],v[120:123],v[184:187]\n"
MF "v[188:191],a[112:115],v[120:123],v[188:191]\n"
MF "v[192:195],a[116:119],v[120:123],v[192:195]\n"
"v_max3_f32 v128,v88,v89,v90\n"
"v_max3_f32 v128,v128,v91,v92\n"
MF "v[196:199],a[120:123],v[120:123],v[196:199]\n"
"v_max3_f32 v128,v128,v93,v94\n"
"v_max_f32 v128,v128,v95\n"
MF "v[200:203],a[124:127],v[120:123],v[200:203]\n"
"v_cmp_lt_f32 vcc,s53,v128\n"
MF "v[204:207],a[48:51],v[120:123],v[204:207]\n"
"s_cbranch_vccnz Ls36_%=\n"
"Ls37_%=:\n"
WT "vmcnt(12)\n"
"s_barrier\n"
MF "v[96:99],a[64:67],v[64:67],v[112:115]\n"
EX "v80,v80\n"
DR "a[64:67],v6 offset:0\n"
MF "v[100:103],a[68:71],v[64:67],v[112:115]\n"
EX "v81,v81\n"
DR "a[68:71],v6 offset:512\n"
MF "v[96:99],a[72:75],v[68:71],v[96:99]\n"
EX "v82,v82\n"
DR "a[72:75],v6 offset:64\n"
MF "v[100:103],a[76:79],v[68:71],v[100:103]\n"
EX "v83,v83\n"
DR "a[76:79],v6 offset:576\n"
MF "v[96:99],a[80:83],v[72:75],v[96:99]\n"
CV "v116,v80,v81\n"
CV "v117,v82,v83\n"
DR "a[80:83],v6 offset:128\n"
MF "v[100:103],a[84:87],v[72:75],v[100:103]\n"
EX "v84,v84\n"
DR "a[84:87],v6 offset:640\n"
MF "v[96:99],a[88:91],v[76:79],v[96:99]\n"
EX "v85,v85\n"
DR "a[88:91],v6 offset:192\n"
MF "v[100:103],a[92:95],v[76:79],v[100:103]\n"
EX "v86,v86\n"
DR "a[92:95],v6 offset:704\n"
MF "v[208:211],a[96:99],v[124:127],v[208:211]\n"
EX "v87,v87\n"
DT "a[96:97],v7 offset:0\n"
DT "a[98:99],v7 offset:512\n"
MF "v[212:215],a[100:103],v[124:127],v[212:215]\n"
CV "v118,v84,v85\n"
CV "v119,v86,v87\n"
DT "a[100:101],v7 offset:32\n"
DT "a[102:103],v7 offset:544\n"
MF "v[216:219],a[104:107],v[124:127],v[216:219]\n"
DT "a[104:105],v7 offset:64\n"
DT "a[106:107],v7 offset:576\n"
MF "v[220:223],a[108:111],v[124:127],v[220:223]\n"
DT "a[108:109],v7 offset:96\n"
DT "a[110:111],v7 offset:608\n"
MF "v[224:227],a[112:115],v[124:127],v[224:227]\n"
DT "a[112:113],v7 offset:128\n"
DT "a[114:115],v7 offset:640\n"
MF "v[228:231],a[116:119],v[124:127],v[228:231]\n"
DT "a[116:117],v7 offset:160\n"
DT "a[118:119],v7 offset:672\n"
"v_max3_f32 v128,v96,v97,v98\n"
"v_max3_f32 v128,v128,v99,v100\n"
MF "v[232:235],a[120:123],v[124:127],v[232:235]\n"
DT "a[120:121],v7 offset:192\n"
DT "a[122:123],v7 offset:704\n"
"v_max3_f32 v128,v128,v101,v102\n"
"v_max_f32 v128,v128,v103\n"
MF "v[236:239],a[124:127],v[124:127],v[236:239]\n"
DT "a[124:125],v7 offset:224\n"
DT "a[126:127],v7 offset:736\n"
"v_cmp_lt_f32 vcc,s53,v128\n"
MF "v[240:243],a[48:51],v[124:127],v[240:243]\n"
"s_cbranch_vccnz Ls38_%=\n"
"Ls39_%=:\n"
"s_cmp_lg_u32 s51,s52\n"
"s_cbranch_scc1 Ll33_%=\n"
WT "lgkmcnt(15)\n"
MF "v[80:83],a[64:67],a[0:3],0\n"
EX "v88,v88\n"
MF "v[84:87],a[68:71],a[0:3],0\n"
EX "v89,v89\n"
MF "v[80:83],a[72:75],a[4:7],v[80:83]\n"
EX "v90,v90\n"
MF "v[84:87],a[76:79],a[4:7],v[84:87]\n"
EX "v91,v91\n"
MF "v[80:83],a[80:83],a[8:11],v[80:83]\n"
CV "v120,v88,v89\n"
CV "v121,v90,v91\n"
MF "v[84:87],a[84:87],a[8:11],v[84:87]\n"
EX "v92,v92\n"
MF "v[80:83],a[88:91],a[12:15],v[80:83]\n"
EX "v93,v93\n"
MF "v[84:87],a[92:95],a[12:15],v[84:87]\n"
EX "v94,v94\n"
WT "lgkmcnt(0)\n"
MF "v[136:139],a[96:99],v[116:119],v[136:139]\n"
EX "v95,v95\n"
"s_add_u32 m0,s50,s48\n"
MF "v[140:143],a[100:103],v[116:119],v[140:143]\n"
CV "v122,v92,v93\n"
CV "v123,v94,v95\n"
BL "v8,s[12:15],s78 offen nt lds\n"
MF "v[144:147],a[104:107],v[116:119],v[144:147]\n"
BL "v9,s[12:15],s78 offen offset:1056 nt lds\n"
"s_add_u32 m0,m0,8448\n"
MF "v[148:151],a[108:111],v[116:119],v[148:151]\n"
BL "v10,s[16:19],s79 offen nt lds\n"
MF "v[152:155],a[112:115],v[116:119],v[152:155]\n"
BL "v11,s[16:19],s79 offen offset:1056 nt lds\n"
"s_add_u32 s51,s51,8192\n"
"s_mov_b32 s79,s78\n"
"s_xor_b32 s78,s51,s41\n"
MF "v[156:159],a[116:119],v[116:119],v[156:159]\n"
"v_add_u32 v6,s49,v4\n"
"s_mov_b32 s50,s42\n"
MF "v[160:163],a[120:123],v[116:119],v[160:163]\n"
"v_add_u32 v7,s49,v5\n"
"s_mov_b32 s42,s49\n"
MF "v[164:167],a[124:127],v[116:119],v[164:167]\n"
"s_add_u32 s49,s49,16896\n"
"s_cmp_eq_u32 s49,84480\n"
"s_cselect_b32 s49,0,s49\n"
MF "v[168:171],a[48:51],v[116:119],v[168:171]\n"
"v_max3_f32 v128,v80,v81,v82\n"
"v_max3_f32 v128,v128,v83,v84\n"
"v_max3_f32 v128,v128,v85,v86\n"
"v_max_f32 v128,v128,v87\n"
"v_mov_b32 v129,v128\n"
"s_nop 1\n"
"v_permlane32_swap_b32 v129,v128\n"
"v_max_f32 v128,v129,v128\n"
"v_mov_b32 v129,v128\n"
"s_nop 1\n"
"v_permlane16_swap_b32 v129,v128\n"
"v_max_f32 v128,v129,v128\n"
"v_sub_f32 v80,v80,v128\n"
"v_sub_f32 v81,v81,v128\n"
"v_sub_f32 v82,v82,v128\n"
"v_sub_f32 v83,v83,v128\n"
"v_sub_f32 v84,v84,v128\n"
"v_sub_f32 v85,v85,v128\n"
"v_sub_f32 v86,v86,v128\n"
"v_sub_f32 v87,v87,v128\n"
"v_sub_f32 v104,0,v128\n"
"v_sub_f32 v105,0,v128\n"
"v_sub_f32 v106,0,v128\n"
"v_sub_f32 v107,0,v128\n"
"v_rcp_f32 v130,v168\n"
"s_add_u32 s66,s56,s99\n"
PM "v[136:137],v[136:137],v[130:131] op_sel_hi:[1,0]\n"
PM "v[138:139],v[138:139],v[130:131] op_sel_hi:[1,0]\n"
PM "v[140:141],v[140:141],v[130:131] op_sel_hi:[1,0]\n"
PM "v[142:143],v[142:143],v[130:131] op_sel_hi:[1,0]\n"
CV "v32,v136,v137\n"
CV "v33,v138,v139\n"
CV "v34,v140,v141\n"
CV "v35,v142,v143\n"
"buffer_store_dwordx4 v[32:35],v244,s[28:31],s66 offen offset:0 sc1\n"
PM "v[144:145],v[144:145],v[130:131] op_sel_hi:[1,0]\n"
PM "v[146:147],v[146:147],v[130:131] op_sel_hi:[1,0]\n"
PM "v[148:149],v[148:149],v[130:131] op_sel_hi:[1,0]\n"
PM "v[150:151],v[150:151],v[130:131] op_sel_hi:[1,0]\n"
CV "v36,v144,v145\n"
CV "v37,v146,v147\n"
CV "v38,v148,v149\n"
CV "v39,v150,v151\n"
"buffer_store_dwordx4 v[36:39],v244,s[28:31],s66 offen offset:1024 sc1\n"
PM "v[152:153],v[152:153],v[130:131] op_sel_hi:[1,0]\n"
PM "v[154:155],v[154:155],v[130:131] op_sel_hi:[1,0]\n"
PM "v[156:157],v[156:157],v[130:131] op_sel_hi:[1,0]\n"
PM "v[158:159],v[158:159],v[130:131] op_sel_hi:[1,0]\n"
CV "v40,v152,v153\n"
CV "v41,v154,v155\n"
CV "v42,v156,v157\n"
CV "v43,v158,v159\n"
"buffer_store_dwordx4 v[40:43],v244,s[28:31],s66 offen offset:2048 sc1\n"
PM "v[160:161],v[160:161],v[130:131] op_sel_hi:[1,0]\n"
PM "v[162:163],v[162:163],v[130:131] op_sel_hi:[1,0]\n"
PM "v[164:165],v[164:165],v[130:131] op_sel_hi:[1,0]\n"
PM "v[166:167],v[166:167],v[130:131] op_sel_hi:[1,0]\n"
CV "v44,v160,v161\n"
CV "v45,v162,v163\n"
CV "v46,v164,v165\n"
CV "v47,v166,v167\n"
"buffer_store_dwordx4 v[44:47],v244,s[28:31],s66 offen offset:3072 sc1\n"
"v_mov_b64 v[136:137],0\n"
"v_mov_b64 v[138:139],0\n"
"v_mov_b64 v[140:141],0\n"
"v_mov_b64 v[142:143],0\n"
"v_mov_b64 v[144:145],0\n"
"v_mov_b64 v[146:147],0\n"
"v_mov_b64 v[148:149],0\n"
"v_mov_b64 v[150:151],0\n"
"v_mov_b64 v[152:153],0\n"
"v_mov_b64 v[154:155],0\n"
"v_mov_b64 v[156:157],0\n"
"v_mov_b64 v[158:159],0\n"
"v_mov_b64 v[160:161],0\n"
"v_mov_b64 v[162:163],0\n"
"v_mov_b64 v[164:165],0\n"
"v_mov_b64 v[166:167],0\n"
"v_mov_b64 v[168:169],0\n"
"v_mov_b64 v[170:171],0\n"
MF "v[88:91],a[64:67],a[16:19],0\n"
EX "v96,v96\n"
MF "v[92:95],a[68:71],a[16:19],0\n"
EX "v97,v97\n"
MF "v[88:91],a[72:75],a[20:23],v[88:91]\n"
EX "v98,v98\n"
MF "v[92:95],a[76:79],a[20:23],v[92:95]\n"
EX "v99,v99\n"
MF "v[88:91],a[80:83],a[24:27],v[88:91]\n"
CV "v124,v96,v97\n"
CV "v125,v98,v99\n"
MF "v[92:95],a[84:87],a[24:27],v[92:95]\n"
EX "v100,v100\n"
MF "v[88:91],a[88:91],a[28:31],v[88:91]\n"
EX "v101,v101\n"
MF "v[92:95],a[92:95],a[28:31],v[92:95]\n"
EX "v102,v102\n"
MF "v[172:175],a[96:99],v[120:123],v[172:175]\n"
EX "v103,v103\n"
MF "v[176:179],a[100:103],v[120:123],v[176:179]\n"
CV "v126,v100,v101\n"
CV "v127,v102,v103\n"
MF "v[180:183],a[104:107],v[120:123],v[180:183]\n"
MF "v[184:187],a[108:111],v[120:123],v[184:187]\n"
MF "v[188:191],a[112:115],v[120:123],v[188:191]\n"
MF "v[192:195],a[116:119],v[120:123],v[192:195]\n"
MF "v[196:199],a[120:123],v[120:123],v[196:199]\n"
MF "v[200:203],a[124:127],v[120:123],v[200:203]\n"
MF "v[204:207],a[48:51],v[120:123],v[204:207]\n"
"v_max3_f32 v128,v88,v89,v90\n"
"v_max3_f32 v128,v128,v91,v92\n"
"v_max3_f32 v128,v128,v93,v94\n"
"v_max_f32 v128,v128,v95\n"
"v_mov_b32 v129,v128\n"
"s_nop 1\n"
"v_permlane32_swap_b32 v129,v128\n"
"v_max_f32 v128,v129,v128\n"
"v_mov_b32 v129,v128\n"
"s_nop 1\n"
"v_permlane16_swap_b32 v129,v128\n"
"v_max_f32 v128,v129,v128\n"
"v_sub_f32 v88,v88,v128\n"
"v_sub_f32 v89,v89,v128\n"
"v_sub_f32 v90,v90,v128\n"
"v_sub_f32 v91,v91,v128\n"
"v_sub_f32 v92,v92,v128\n"
"v_sub_f32 v93,v93,v128\n"
"v_sub_f32 v94,v94,v128\n"
"v_sub_f32 v95,v95,v128\n"
"v_sub_f32 v108,0,v128\n"
"v_sub_f32 v109,0,v128\n"
"v_sub_f32 v110,0,v128\n"
"v_sub_f32 v111,0,v128\n"
"v_rcp_f32 v130,v204\n"
"s_add_u32 s66,s56,s99\n"
PM "v[172:173],v[172:173],v[130:131] op_sel_hi:[1,0]\n"
PM "v[174:175],v[174:175],v[130:131] op_sel_hi:[1,0]\n"
PM "v[176:177],v[176:177],v[130:131] op_sel_hi:[1,0]\n"
PM "v[178:179],v[178:179],v[130:131] op_sel_hi:[1,0]\n"
CV "v48,v172,v173\n"
CV "v49,v174,v175\n"
CV "v50,v176,v177\n"
CV "v51,v178,v179\n"
"buffer_store_dwordx4 v[48:51],v245,s[28:31],s66 offen offset:0 sc1\n"
PM "v[180:181],v[180:181],v[130:131] op_sel_hi:[1,0]\n"
PM "v[182:183],v[182:183],v[130:131] op_sel_hi:[1,0]\n"
PM "v[184:185],v[184:185],v[130:131] op_sel_hi:[1,0]\n"
PM "v[186:187],v[186:187],v[130:131] op_sel_hi:[1,0]\n"
CV "v52,v180,v181\n"
CV "v53,v182,v183\n"
CV "v54,v184,v185\n"
CV "v55,v186,v187\n"
"buffer_store_dwordx4 v[52:55],v245,s[28:31],s66 offen offset:1024 sc1\n"
PM "v[188:189],v[188:189],v[130:131] op_sel_hi:[1,0]\n"
PM "v[190:191],v[190:191],v[130:131] op_sel_hi:[1,0]\n"
PM "v[192:193],v[192:193],v[130:131] op_sel_hi:[1,0]\n"
PM "v[194:195],v[194:195],v[130:131] op_sel_hi:[1,0]\n"
CV "v56,v188,v189\n"
CV "v57,v190,v191\n"
CV "v58,v192,v193\n"
CV "v59,v194,v195\n"
"buffer_store_dwordx4 v[56:59],v245,s[28:31],s66 offen offset:2048 sc1\n"
PM "v[196:197],v[196:197],v[130:131] op_sel_hi:[1,0]\n"
PM "v[198:199],v[198:199],v[130:131] op_sel_hi:[1,0]\n"
PM "v[200:201],v[200:201],v[130:131] op_sel_hi:[1,0]\n"
PM "v[202:203],v[202:203],v[130:131] op_sel_hi:[1,0]\n"
CV "v60,v196,v197\n"
CV "v61,v198,v199\n"
CV "v62,v200,v201\n"
CV "v63,v202,v203\n"
"buffer_store_dwordx4 v[60:63],v245,s[28:31],s66 offen offset:3072 sc1\n"
"v_mov_b64 v[172:173],0\n"
"v_mov_b64 v[174:175],0\n"
"v_mov_b64 v[176:177],0\n"
"v_mov_b64 v[178:179],0\n"
"v_mov_b64 v[180:181],0\n"
"v_mov_b64 v[182:183],0\n"
"v_mov_b64 v[184:185],0\n"
"v_mov_b64 v[186:187],0\n"
"v_mov_b64 v[188:189],0\n"
"v_mov_b64 v[190:191],0\n"
"v_mov_b64 v[192:193],0\n"
"v_mov_b64 v[194:195],0\n"
"v_mov_b64 v[196:197],0\n"
"v_mov_b64 v[198:199],0\n"
"v_mov_b64 v[200:201],0\n"
"v_mov_b64 v[202:203],0\n"
"v_mov_b64 v[204:205],0\n"
"v_mov_b64 v[206:207],0\n"
WT "vmcnt(20)\n"
"s_barrier\n"
MF "v[96:99],a[64:67],a[32:35],0\n"
EX "v80,v80\n"
DR "a[64:67],v6 offset:0\n"
MF "v[100:103],a[68:71],a[32:35],0\n"
EX "v81,v81\n"
DR "a[68:71],v6 offset:512\n"
MF "v[96:99],a[72:75],a[36:39],v[96:99]\n"
EX "v82,v82\n"
DR "a[72:75],v6 offset:64\n"
MF "v[100:103],a[76:79],a[36:39],v[100:103]\n"
EX "v83,v83\n"
DR "a[76:79],v6 offset:576\n"
MF "v[96:99],a[80:83],a[40:43],v[96:99]\n"
CV "v116,v80,v81\n"
CV "v117,v82,v83\n"
DR "a[80:83],v6 offset:128\n"
MF "v[100:103],a[84:87],a[40:43],v[100:103]\n"
EX "v84,v84\n"
DR "a[84:87],v6 offset:640\n"
MF "v[96:99],a[88:91],a[44:47],v[96:99]\n"
EX "v85,v85\n"
DR "a[88:91],v6 offset:192\n"
MF "v[100:103],a[92:95],a[44:47],v[100:103]\n"
EX "v86,v86\n"
DR "a[92:95],v6 offset:704\n"
MF "v[208:211],a[96:99],v[124:127],v[208:211]\n"
EX "v87,v87\n"
DT "a[96:97],v7 offset:0\n"
DT "a[98:99],v7 offset:512\n"
MF "v[212:215],a[100:103],v[124:127],v[212:215]\n"
CV "v118,v84,v85\n"
CV "v119,v86,v87\n"
DT "a[100:101],v7 offset:32\n"
DT "a[102:103],v7 offset:544\n"
MF "v[216:219],a[104:107],v[124:127],v[216:219]\n"
DT "a[104:105],v7 offset:64\n"
DT "a[106:107],v7 offset:576\n"
MF "v[220:223],a[108:111],v[124:127],v[220:223]\n"
DT "a[108:109],v7 offset:96\n"
DT "a[110:111],v7 offset:608\n"
MF "v[224:227],a[112:115],v[124:127],v[224:227]\n"
DT "a[112:113],v7 offset:128\n"
DT "a[114:115],v7 offset:640\n"
MF "v[228:231],a[116:119],v[124:127],v[228:231]\n"
DT "a[116:117],v7 offset:160\n"
DT "a[118:119],v7 offset:672\n"
MF "v[232:235],a[120:123],v[124:127],v[232:235]\n"
DT "a[120:121],v7 offset:192\n"
DT "a[122:123],v7 offset:704\n"
MF "v[236:239],a[124:127],v[124:127],v[236:239]\n"
DT "a[124:125],v7 offset:224\n"
DT "a[126:127],v7 offset:736\n"
MF "v[240:243],a[48:51],v[124:127],v[240:243]\n"
"v_max3_f32 v128,v96,v97,v98\n"
"v_max3_f32 v128,v128,v99,v100\n"
"v_max3_f32 v128,v128,v101,v102\n"
"v_max_f32 v128,v128,v103\n"
"v_mov_b32 v129,v128\n"
"s_nop 1\n"
"v_permlane32_swap_b32 v129,v128\n"
"v_max_f32 v128,v129,v128\n"
"v_mov_b32 v129,v128\n"
"s_nop 1\n"
"v_permlane16_swap_b32 v129,v128\n"
"v_max_f32 v128,v129,v128\n"
"v_sub_f32 v96,v96,v128\n"
"v_sub_f32 v97,v97,v128\n"
"v_sub_f32 v98,v98,v128\n"
"v_sub_f32 v99,v99,v128\n"
"v_sub_f32 v100,v100,v128\n"
"v_sub_f32 v101,v101,v128\n"
"v_sub_f32 v102,v102,v128\n"
"v_sub_f32 v103,v103,v128\n"
"v_sub_f32 v112,0,v128\n"
"v_sub_f32 v113,0,v128\n"
"v_sub_f32 v114,0,v128\n"
"v_sub_f32 v115,0,v128\n"
"v_rcp_f32 v130,v240\n"
"s_add_u32 s66,s56,s99\n"
PM "v[208:209],v[208:209],v[130:131] op_sel_hi:[1,0]\n"
PM "v[210:211],v[210:211],v[130:131] op_sel_hi:[1,0]\n"
PM "v[212:213],v[212:213],v[130:131] op_sel_hi:[1,0]\n"
PM "v[214:215],v[214:215],v[130:131] op_sel_hi:[1,0]\n"
CV "v64,v208,v209\n"
CV "v65,v210,v211\n"
CV "v66,v212,v213\n"
CV "v67,v214,v215\n"
"buffer_store_dwordx4 v[64:67],v246,s[28:31],s66 offen offset:0 sc1\n"
"ds_write_b128 v253,v[64:67] offset:36864\n"
PM "v[216:217],v[216:217],v[130:131] op_sel_hi:[1,0]\n"
PM "v[218:219],v[218:219],v[130:131] op_sel_hi:[1,0]\n"
PM "v[220:221],v[220:221],v[130:131] op_sel_hi:[1,0]\n"
PM "v[222:223],v[222:223],v[130:131] op_sel_hi:[1,0]\n"
CV "v68,v216,v217\n"
CV "v69,v218,v219\n"
CV "v70,v220,v221\n"
CV "v71,v222,v223\n"
"buffer_store_dwordx4 v[68:71],v246,s[28:31],s66 offen offset:1024 sc1\n"
"ds_write_b128 v253,v[68:71] offset:37888\n"
PM "v[224:225],v[224:225],v[130:131] op_sel_hi:[1,0]\n"
PM "v[226:227],v[226:227],v[130:131] op_sel_hi:[1,0]\n"
PM "v[228:229],v[228:229],v[130:131] op_sel_hi:[1,0]\n"
PM "v[230:231],v[230:231],v[130:131] op_sel_hi:[1,0]\n"
CV "v72,v224,v225\n"
CV "v73,v226,v227\n"
CV "v74,v228,v229\n"
CV "v75,v230,v231\n"
"buffer_store_dwordx4 v[72:75],v246,s[28:31],s66 offen offset:2048 sc1\n"
"ds_write_b128 v253,v[72:75] offset:38912\n"
PM "v[232:233],v[232:233],v[130:131] op_sel_hi:[1,0]\n"
PM "v[234:235],v[234:235],v[130:131] op_sel_hi:[1,0]\n"
PM "v[236:237],v[236:237],v[130:131] op_sel_hi:[1,0]\n"
PM "v[238:239],v[238:239],v[130:131] op_sel_hi:[1,0]\n"
CV "v76,v232,v233\n"
CV "v77,v234,v235\n"
CV "v78,v236,v237\n"
CV "v79,v238,v239\n"
"buffer_store_dwordx4 v[76:79],v246,s[28:31],s66 offen offset:3072 sc1\n"
"ds_write_b128 v253,v[76:79] offset:39936\n"
"v_mov_b64 v[208:209],0\n"
"v_mov_b64 v[210:211],0\n"
"v_mov_b64 v[212:213],0\n"
"v_mov_b64 v[214:215],0\n"
"v_mov_b64 v[216:217],0\n"
"v_mov_b64 v[218:219],0\n"
"v_mov_b64 v[220:221],0\n"
"v_mov_b64 v[222:223],0\n"
"v_mov_b64 v[224:225],0\n"
"v_mov_b64 v[226:227],0\n"
"v_mov_b64 v[228:229],0\n"
"v_mov_b64 v[230:231],0\n"
"v_mov_b64 v[232:233],0\n"
"v_mov_b64 v[234:235],0\n"
"v_mov_b64 v[236:237],0\n"
"v_mov_b64 v[238:239],0\n"
"v_mov_b64 v[240:241],0\n"
"v_mov_b64 v[242:243],0\n"
WT "lgkmcnt(0)\n"
"s_mov_b32 s52,204800\n"
"Ll124_%=:\n"
WT "lgkmcnt(15)\n"
MF "v[80:83],a[64:67],a[0:3],v[104:107]\n"
EX "v88,v88\n"
MF "v[84:87],a[68:71],a[0:3],v[104:107]\n"
EX "v89,v89\n"
MF "v[80:83],a[72:75],a[4:7],v[80:83]\n"
EX "v90,v90\n"
MF "v[84:87],a[76:79],a[4:7],v[84:87]\n"
EX "v91,v91\n"
MF "v[80:83],a[80:83],a[8:11],v[80:83]\n"
CV "v120,v88,v89\n"
CV "v121,v90,v91\n"
MF "v[84:87],a[84:87],a[8:11],v[84:87]\n"
EX "v92,v92\n"
MF "v[80:83],a[88:91],a[12:15],v[80:83]\n"
EX "v93,v93\n"
MF "v[84:87],a[92:95],a[12:15],v[84:87]\n"
EX "v94,v94\n"
WT "lgkmcnt(0)\n"
MF "v[136:139],a[96:99],v[116:119],v[136:139]\n"
EX "v95,v95\n"
"s_add_u32 m0,s50,s48\n"
MF "v[140:143],a[100:103],v[116:119],v[140:143]\n"
CV "v122,v92,v93\n"
CV "v123,v94,v95\n"
BL "v8,s[12:15],s78 offen nt lds\n"
MF "v[144:147],a[104:107],v[116:119],v[144:147]\n"
BL "v9,s[12:15],s78 offen offset:1056 nt lds\n"
"s_add_u32 m0,m0,8448\n"
MF "v[148:151],a[108:111],v[116:119],v[148:151]\n"
BL "v10,s[16:19],s79 offen nt lds\n"
MF "v[152:155],a[112:115],v[116:119],v[152:155]\n"
BL "v11,s[16:19],s79 offen offset:1056 nt lds\n"
"s_add_u32 s51,s51,8192\n"
"s_mov_b32 s79,s78\n"
"s_xor_b32 s78,s51,s41\n"
MF "v[156:159],a[116:119],v[116:119],v[156:159]\n"
"v_add_u32 v6,s49,v4\n"
"s_mov_b32 s50,s42\n"
"v_max3_f32 v128,v80,v81,v82\n"
"v_max3_f32 v128,v128,v83,v84\n"
MF "v[160:163],a[120:123],v[116:119],v[160:163]\n"
"v_add_u32 v7,s49,v5\n"
"s_mov_b32 s42,s49\n"
"v_max3_f32 v128,v128,v85,v86\n"
"v_max_f32 v128,v128,v87\n"
MF "v[164:167],a[124:127],v[116:119],v[164:167]\n"
"s_add_u32 s49,s49,16896\n"
"s_cmp_eq_u32 s49,84480\n"
"s_cselect_b32 s49,0,s49\n"
"v_cmp_lt_f32 vcc,s53,v128\n"
MF "v[168:171],a[48:51],v[116:119],v[168:171]\n"
"s_cbranch_vccnz Ls125_%=\n"
"Ls126_%=:\n"
MF "v[88:91],a[64:67],a[16:19],v[108:111]\n"
EX "v96,v96\n"
MF "v[92:95],a[68:71],a[16:19],v[108:111]\n"
EX "v97,v97\n"
MF "v[88:91],a[72:75],a[20:23],v[88:91]\n"
EX "v98,v98\n"
MF "v[92:95],a[76:79],a[20:23],v[92:95]\n"
EX "v99,v99\n"
MF "v[88:91],a[80:83],a[24:27],v[88:91]\n"
CV "v124,v96,v97\n"
CV "v125,v98,v99\n"
MF "v[92:95],a[84:87],a[24:27],v[92:95]\n"
EX "v100,v100\n"
MF "v[88:91],a[88:91],a[28:31],v[88:91]\n"
EX "v101,v101\n"
MF "v[92:95],a[92:95],a[28:31],v[92:95]\n"
EX "v102,v102\n"
MF "v[172:175],a[96:99],v[120:123],v[172:175]\n"
EX "v103,v103\n"
MF "v[176:179],a[100:103],v[120:123],v[176:179]\n"
CV "v126,v100,v101\n"
CV "v127,v102,v103\n"
MF "v[180:183],a[104:107],v[120:123],v[180:183]\n"
MF "v[184:187],a[108:111],v[120:123],v[184:187]\n"
MF "v[188:191],a[112:115],v[120:123],v[188:191]\n"
MF "v[192:195],a[116:119],v[120:123],v[192:195]\n"
"v_max3_f32 v128,v88,v89,v90\n"
"v_max3_f32 v128,v128,v91,v92\n"
MF "v[196:199],a[120:123],v[120:123],v[196:199]\n"
"v_max3_f32 v128,v128,v93,v94\n"
"v_max_f32 v128,v128,v95\n"
MF "v[200:203],a[124:127],v[120:123],v[200:203]\n"
"v_cmp_lt_f32 vcc,s53,v128\n"
MF "v[204:207],a[48:51],v[120:123],v[204:207]\n"
"s_cbranch_vccnz Ls127_%=\n"
"Ls128_%=:\n"
WT "vmcnt(12)\n"
"s_barrier\n"
MF "v[96:99],a[64:67],a[32:35],v[112:115]\n"
EX "v80,v80\n"
DR "a[64:67],v6 offset:0\n"
MF "v[100:103],a[68:71],a[32:35],v[112:115]\n"
EX "v81,v81\n"
DR "a[68:71],v6 offset:512\n"
MF "v[96:99],a[72:75],a[36:39],v[96:99]\n"
EX "v82,v82\n"
DR "a[72:75],v6 offset:64\n"
MF "v[100:103],a[76:79],a[36:39],v[100:103]\n"
EX "v83,v83\n"
DR "a[76:79],v6 offset:576\n"
MF "v[96:99],a[80:83],a[40:43],v[96:99]\n"
CV "v116,v80,v81\n"
CV "v117,v82,v83\n"
DR "a[80:83],v6 offset:128\n"
MF "v[100:103],a[84:87],a[40:43],v[100:103]\n"
EX "v84,v84\n"
DR "a[84:87],v6 offset:640\n"
MF "v[96:99],a[88:91],a[44:47],v[96:99]\n"
EX "v85,v85\n"
DR "a[88:91],v6 offset:192\n"
MF "v[100:103],a[92:95],a[44:47],v[100:103]\n"
EX "v86,v86\n"
DR "a[92:95],v6 offset:704\n"
MF "v[208:211],a[96:99],v[124:127],v[208:211]\n"
EX "v87,v87\n"
DT "a[96:97],v7 offset:0\n"
DT "a[98:99],v7 offset:512\n"
MF "v[212:215],a[100:103],v[124:127],v[212:215]\n"
CV "v118,v84,v85\n"
CV "v119,v86,v87\n"
DT "a[100:101],v7 offset:32\n"
DT "a[102:103],v7 offset:544\n"
MF "v[216:219],a[104:107],v[124:127],v[216:219]\n"
DT "a[104:105],v7 offset:64\n"
DT "a[106:107],v7 offset:576\n"
MF "v[220:223],a[108:111],v[124:127],v[220:223]\n"
DT "a[108:109],v7 offset:96\n"
DT "a[110:111],v7 offset:608\n"
MF "v[224:227],a[112:115],v[124:127],v[224:227]\n"
DT "a[112:113],v7 offset:128\n"
DT "a[114:115],v7 offset:640\n"
MF "v[228:231],a[116:119],v[124:127],v[228:231]\n"
DT "a[116:117],v7 offset:160\n"
DT "a[118:119],v7 offset:672\n"
"v_max3_f32 v128,v96,v97,v98\n"
"v_max3_f32 v128,v128,v99,v100\n"
MF "v[232:235],a[120:123],v[124:127],v[232:235]\n"
DT "a[120:121],v7 offset:192\n"
DT "a[122:123],v7 offset:704\n"
"v_max3_f32 v128,v128,v101,v102\n"
"v_max_f32 v128,v128,v103\n"
MF "v[236:239],a[124:127],v[124:127],v[236:239]\n"
DT "a[124:125],v7 offset:224\n"
DT "a[126:127],v7 offset:736\n"
"v_cmp_lt_f32 vcc,s53,v128\n"
MF "v[240:243],a[48:51],v[124:127],v[240:243]\n"
"s_cbranch_vccnz Ls129_%=\n"
"Ls130_%=:\n"
"s_cmp_lg_u32 s51,s52\n"
"s_cbranch_scc1 Ll124_%=\n"
DR "v[128:131],v252 offset:0\n"
DR "v[64:67],v253 offset:36864\n"
DR "v[68:71],v253 offset:37888\n"
DR "v[72:75],v253 offset:38912\n"
DR "v[76:79],v253 offset:39936\n"
WT "lgkmcnt(0)\n"
"v_add_u32 v3,1,v28\n"
"s_lshl_b64 exec,1,s40\n"
"buffer_store_dword v3,v26,s[28:31],0 offen offset:0 sc1\n"
"s_mov_b64 exec,-1\n"
"s_add_u32 m0,s76,84480\n"
"s_add_u32 s67,s68,0\n"
BL "v12,s[24:27],s67 offen offset:0 lds\n"
BL "v13,s[24:27],s67 offen offset:1024 lds\n"
BL "v14,s[24:27],s67 offen offset:2048 lds\n"
"s_add_u32 m0,s76,87552\n"
"s_add_u32 s67,s68,0\n"
BL "v15,s[24:27],s67 offen offset:0 lds\n"
BL "v16,s[24:27],s67 offen offset:1024 lds\n"
BL "v17,s[24:27],s67 offen offset:2048 lds\n"
"s_add_u32 m0,s76,90624\n"
"s_add_u32 s67,s68,0\n"
BL "v18,s[24:27],s67 offen offset:0 lds\n"
BL "v19,s[24:27],s67 offen offset:1024 lds\n"
BL "v20,s[24:27],s67 offen offset:2048 lds\n"
WT "lgkmcnt(15)\n"
MF "v[80:83],a[64:67],a[0:3],v[104:107]\n"
EX "v88,v88\n"
MF "v[84:87],a[68:71],a[0:3],v[104:107]\n"
EX "v89,v89\n"
MF "v[80:83],a[72:75],a[4:7],v[80:83]\n"
EX "v90,v90\n"
MF "v[84:87],a[76:79],a[4:7],v[84:87]\n"
EX "v91,v91\n"
MF "v[80:83],a[80:83],a[8:11],v[80:83]\n"
CV "v120,v88,v89\n"
CV "v121,v90,v91\n"
MF "v[84:87],a[84:87],a[8:11],v[84:87]\n"
EX "v92,v92\n"
MF "v[80:83],a[88:91],a[12:15],v[80:83]\n"
EX "v93,v93\n"
MF "v[84:87],a[92:95],a[12:15],v[84:87]\n"
EX "v94,v94\n"
WT "lgkmcnt(0)\n"
"s_add_u32 m0,s50,s48\n"
MF "v[136:139],a[96:99],v[116:119],v[136:139]\n"
EX "v95,v95\n"
BL "v8,s[12:15],s78 offen nt lds\n"
MF "v[140:143],a[100:103],v[116:119],v[140:143]\n"
CV "v122,v92,v93\n"
CV "v123,v94,v95\n"
BL "v9,s[12:15],s78 offen offset:1056 nt lds\n"
"s_add_u32 m0,m0,8448\n"
MF "v[144:147],a[104:107],v[116:119],v[144:147]\n"
BL "v10,s[16:19],s79 offen nt lds\n"
MF "v[148:151],a[108:111],v[116:119],v[148:151]\n"
BL "v11,s[16:19],s79 offen offset:1056 nt lds\n"
"s_add_u32 s51,s51,8192\n"
"s_mov_b32 s79,s78\n"
"s_xor_b32 s78,s51,s41\n"
MF "v[152:155],a[112:115],v[116:119],v[152:155]\n"
"buffer_load_dword v31,v26,s[28:31],0 offen offset:0 sc1\n"
MF "v[156:159],a[116:119],v[116:119],v[156:159]\n"
"v_add_u32 v6,s49,v4\n"
"s_mov_b32 s50,s42\n"
"v_max3_f32 v128,v80,v81,v82\n"
"v_max3_f32 v128,v128,v83,v84\n"
MF "v[160:163],a[120:123],v[116:119],v[160:163]\n"
"v_add_u32 v7,s49,v5\n"
"s_mov_b32 s42,s49\n"
"v_max3_f32 v128,v128,v85,v86\n"
"v_max_f32 v128,v128,v87\n"
MF "v[164:167],a[124:127],v[116:119],v[164:167]\n"
"s_add_u32 s49,s49,16896\n"
"s_cmp_eq_u32 s49,84480\n"
"s_cselect_b32 s49,0,s49\n"
"v_cmp_lt_f32 vcc,s53,v128\n"
MF "v[168:171],a[48:51],v[116:119],v[168:171]\n"
"s_cbranch_vccnz Ls149_%=\n"
"Ls150_%=:\n"
MF "v[88:91],a[64:67],a[16:19],v[108:111]\n"
EX "v96,v96\n"
MF "v[92:95],a[68:71],a[16:19],v[108:111]\n"
EX "v97,v97\n"
MF "v[88:91],a[72:75],a[20:23],v[88:91]\n"
EX "v98,v98\n"
MF "v[92:95],a[76:79],a[20:23],v[92:95]\n"
EX "v99,v99\n"
MF "v[88:91],a[80:83],a[24:27],v[88:91]\n"
CV "v124,v96,v97\n"
CV "v125,v98,v99\n"
MF "v[92:95],a[84:87],a[24:27],v[92:95]\n"
EX "v100,v100\n"
MF "v[88:91],a[88:91],a[28:31],v[88:91]\n"
EX "v101,v101\n"
MF "v[92:95],a[92:95],a[28:31],v[92:95]\n"
EX "v102,v102\n"
MF "v[172:175],a[96:99],v[120:123],v[172:175]\n"
EX "v103,v103\n"
MF "v[176:179],a[100:103],v[120:123],v[176:179]\n"
CV "v126,v100,v101\n"
CV "v127,v102,v103\n"
MF "v[180:183],a[104:107],v[120:123],v[180:183]\n"
MF "v[184:187],a[108:111],v[120:123],v[184:187]\n"
MF "v[188:191],a[112:115],v[120:123],v[188:191]\n"
MF "v[192:195],a[116:119],v[120:123],v[192:195]\n"
"v_max3_f32 v128,v88,v89,v90\n"
"v_max3_f32 v128,v128,v91,v92\n"
MF "v[196:199],a[120:123],v[120:123],v[196:199]\n"
"v_max3_f32 v128,v128,v93,v94\n"
"v_max_f32 v128,v128,v95\n"
MF "v[200:203],a[124:127],v[120:123],v[200:203]\n"
"v_cmp_lt_f32 vcc,s53,v128\n"
MF "v[204:207],a[48:51],v[120:123],v[204:207]\n"
"s_cbranch_vccnz Ls151_%=\n"
"Ls152_%=:\n"
WT "vmcnt(23)\n"
"s_barrier\n"
MF "v[96:99],a[64:67],a[32:35],v[112:115]\n"
EX "v80,v80\n"
DR "a[64:67],v6 offset:0\n"
MF "v[100:103],a[68:71],a[32:35],v[112:115]\n"
EX "v81,v81\n"
DR "a[68:71],v6 offset:512\n"
MF "v[96:99],a[72:75],a[36:39],v[96:99]\n"
EX "v82,v82\n"
DR "a[72:75],v6 offset:64\n"
MF "v[100:103],a[76:79],a[36:39],v[100:103]\n"
EX "v83,v83\n"
DR "a[76:79],v6 offset:576\n"
MF "v[96:99],a[80:83],a[40:43],v[96:99]\n"
CV "v116,v80,v81\n"
CV "v117,v82,v83\n"
DR "a[80:83],v6 offset:128\n"
MF "v[100:103],a[84:87],a[40:43],v[100:103]\n"
EX "v84,v84\n"
DR "a[84:87],v6 offset:640\n"
MF "v[96:99],a[88:91],a[44:47],v[96:99]\n"
EX "v85,v85\n"
DR "a[88:91],v6 offset:192\n"
MF "v[100:103],a[92:95],a[44:47],v[100:103]\n"
EX "v86,v86\n"
DR "a[92:95],v6 offset:704\n"
MF "v[208:211],a[96:99],v[124:127],v[208:211]\n"
EX "v87,v87\n"
DT "a[96:97],v7 offset:0\n"
DT "a[98:99],v7 offset:512\n"
MF "v[212:215],a[100:103],v[124:127],v[212:215]\n"
CV "v118,v84,v85\n"
CV "v119,v86,v87\n"
DT "a[100:101],v7 offset:32\n"
DT "a[102:103],v7 offset:544\n"
MF "v[216:219],a[104:107],v[124:127],v[216:219]\n"
DT "a[104:105],v7 offset:64\n"
DT "a[106:107],v7 offset:576\n"
MF "v[220:223],a[108:111],v[124:127],v[220:223]\n"
DT "a[108:109],v7 offset:96\n"
DT "a[110:111],v7 offset:608\n"
MF "v[224:227],a[112:115],v[124:127],v[224:227]\n"
DT "a[112:113],v7 offset:128\n"
DT "a[114:115],v7 offset:640\n"
MF "v[228:231],a[116:119],v[124:127],v[228:231]\n"
DT "a[116:117],v7 offset:160\n"
DT "a[118:119],v7 offset:672\n"
"v_max3_f32 v128,v96,v97,v98\n"
"v_max3_f32 v128,v128,v99,v100\n"
MF "v[232:235],a[120:123],v[124:127],v[232:235]\n"
DT "a[120:121],v7 offset:192\n"
DT "a[122:123],v7 offset:704\n"
"v_max3_f32 v128,v128,v101,v102\n"
"v_max_f32 v128,v128,v103\n"
MF "v[236:239],a[124:127],v[124:127],v[236:239]\n"
DT "a[124:125],v7 offset:224\n"
DT "a[126:127],v7 offset:736\n"
"v_cmp_lt_f32 vcc,s53,v128\n"
MF "v[240:243],a[48:51],v[124:127],v[240:243]\n"
"s_cbranch_vccnz Ls153_%=\n"
"Ls154_%=:\n"
WT "lgkmcnt(15)\n"
MF "v[80:83],a[64:67],a[0:3],v[104:107]\n"
EX "v88,v88\n"
MF "v[84:87],a[68:71],a[0:3],v[104:107]\n"
EX "v89,v89\n"
MF "v[80:83],a[72:75],a[4:7],v[80:83]\n"
EX "v90,v90\n"
MF "v[84:87],a[76:79],a[4:7],v[84:87]\n"
EX "v91,v91\n"
MF "v[80:83],a[80:83],a[8:11],v[80:83]\n"
CV "v120,v88,v89\n"
CV "v121,v90,v91\n"
MF "v[84:87],a[84:87],a[8:11],v[84:87]\n"
EX "v92,v92\n"
MF "v[80:83],a[88:91],a[12:15],v[80:83]\n"
EX "v93,v93\n"
MF "v[84:87],a[92:95],a[12:15],v[84:87]\n"
EX "v94,v94\n"
WT "lgkmcnt(0)\n"
MF "v[136:139],a[96:99],v[116:119],v[136:139]\n"
EX "v95,v95\n"
"s_add_u32 m0,s50,s48\n"
MF "v[140:143],a[100:103],v[116:119],v[140:143]\n"
CV "v122,v92,v93\n"
CV "v123,v94,v95\n"
BL "v8,s[12:15],s78 offen nt lds\n"
MF "v[144:147],a[104:107],v[116:119],v[144:147]\n"
BL "v9,s[12:15],s78 offen offset:1056 nt lds\n"
"s_add_u32 m0,m0,8448\n"
MF "v[148:151],a[108:111],v[116:119],v[148:151]\n"
BL "v10,s[16:19],s79 offen nt lds\n"
MF "v[152:155],a[112:115],v[116:119],v[152:155]\n"
BL "v11,s[16:19],s79 offen offset:1056 nt lds\n"
"s_add_u32 s51,s51,8192\n"
"s_mov_b32 s79,s78\n"
"s_xor_b32 s78,s51,s41\n"
MF "v[156:159],a[116:119],v[116:119],v[156:159]\n"
"v_add_u32 v6,s49,v4\n"
"s_mov_b32 s50,s42\n"
"v_max3_f32 v128,v80,v81,v82\n"
"v_max3_f32 v128,v128,v83,v84\n"
MF "v[160:163],a[120:123],v[116:119],v[160:163]\n"
"v_add_u32 v7,s49,v5\n"
"s_mov_b32 s42,s49\n"
"v_max3_f32 v128,v128,v85,v86\n"
"v_max_f32 v128,v128,v87\n"
MF "v[164:167],a[124:127],v[116:119],v[164:167]\n"
"s_add_u32 s49,s49,16896\n"
"s_cmp_eq_u32 s49,84480\n"
"s_cselect_b32 s49,0,s49\n"
"v_cmp_lt_f32 vcc,s53,v128\n"
MF "v[168:171],a[48:51],v[116:119],v[168:171]\n"
"s_cbranch_vccnz Ls155_%=\n"
"Ls156_%=:\n"
MF "v[88:91],a[64:67],a[16:19],v[108:111]\n"
EX "v96,v96\n"
MF "v[92:95],a[68:71],a[16:19],v[108:111]\n"
EX "v97,v97\n"
MF "v[88:91],a[72:75],a[20:23],v[88:91]\n"
EX "v98,v98\n"
MF "v[92:95],a[76:79],a[20:23],v[92:95]\n"
EX "v99,v99\n"
MF "v[88:91],a[80:83],a[24:27],v[88:91]\n"
CV "v124,v96,v97\n"
CV "v125,v98,v99\n"
MF "v[92:95],a[84:87],a[24:27],v[92:95]\n"
EX "v100,v100\n"
MF "v[88:91],a[88:91],a[28:31],v[88:91]\n"
EX "v101,v101\n"
MF "v[92:95],a[92:95],a[28:31],v[92:95]\n"
EX "v102,v102\n"
MF "v[172:175],a[96:99],v[120:123],v[172:175]\n"
EX "v103,v103\n"
MF "v[176:179],a[100:103],v[120:123],v[176:179]\n"
CV "v126,v100,v101\n"
CV "v127,v102,v103\n"
MF "v[180:183],a[104:107],v[120:123],v[180:183]\n"
MF "v[184:187],a[108:111],v[120:123],v[184:187]\n"
MF "v[188:191],a[112:115],v[120:123],v[188:191]\n"
MF "v[192:195],a[116:119],v[120:123],v[192:195]\n"
"v_max3_f32 v128,v88,v89,v90\n"
"v_max3_f32 v128,v128,v91,v92\n"
MF "v[196:199],a[120:123],v[120:123],v[196:199]\n"
"v_max3_f32 v128,v128,v93,v94\n"
"v_max_f32 v128,v128,v95\n"
MF "v[200:203],a[124:127],v[120:123],v[200:203]\n"
"v_cmp_lt_f32 vcc,s53,v128\n"
MF "v[204:207],a[48:51],v[120:123],v[204:207]\n"
"s_cbranch_vccnz Ls157_%=\n"
"Ls158_%=:\n"
WT "vmcnt(23)\n"
WT "vmcnt(9)\n"
"s_barrier\n"
MF "v[96:99],a[64:67],a[32:35],v[112:115]\n"
EX "v80,v80\n"
DR "a[64:67],v6 offset:0\n"
MF "v[100:103],a[68:71],a[32:35],v[112:115]\n"
EX "v81,v81\n"
DR "a[68:71],v6 offset:512\n"
MF "v[96:99],a[72:75],a[36:39],v[96:99]\n"
EX "v82,v82\n"
DR "a[72:75],v6 offset:64\n"
MF "v[100:103],a[76:79],a[36:39],v[100:103]\n"
EX "v83,v83\n"
DR "a[76:79],v6 offset:576\n"
MF "v[96:99],a[80:83],a[40:43],v[96:99]\n"
CV "v116,v80,v81\n"
CV "v117,v82,v83\n"
DR "a[80:83],v6 offset:128\n"
MF "v[100:103],a[84:87],a[40:43],v[100:103]\n"
EX "v84,v84\n"
DR "a[84:87],v6 offset:640\n"
MF "v[96:99],a[88:91],a[44:47],v[96:99]\n"
EX "v85,v85\n"
DR "a[88:91],v6 offset:192\n"
MF "v[100:103],a[92:95],a[44:47],v[100:103]\n"
EX "v86,v86\n"
DR "a[92:95],v6 offset:704\n"
MF "v[208:211],a[96:99],v[124:127],v[208:211]\n"
EX "v87,v87\n"
DT "a[96:97],v7 offset:0\n"
DT "a[98:99],v7 offset:512\n"
MF "v[212:215],a[100:103],v[124:127],v[212:215]\n"
CV "v118,v84,v85\n"
CV "v119,v86,v87\n"
DT "a[100:101],v7 offset:32\n"
DT "a[102:103],v7 offset:544\n"
MF "v[216:219],a[104:107],v[124:127],v[216:219]\n"
DT "a[104:105],v7 offset:64\n"
DT "a[106:107],v7 offset:576\n"
MF "v[220:223],a[108:111],v[124:127],v[220:223]\n"
DT "a[108:109],v7 offset:96\n"
DT "a[110:111],v7 offset:608\n"
MF "v[224:227],a[112:115],v[124:127],v[224:227]\n"
DT "a[112:113],v7 offset:128\n"
DT "a[114:115],v7 offset:640\n"
MF "v[228:231],a[116:119],v[124:127],v[228:231]\n"
DT "a[116:117],v7 offset:160\n"
DT "a[118:119],v7 offset:672\n"
"v_max3_f32 v128,v96,v97,v98\n"
"v_max3_f32 v128,v128,v99,v100\n"
MF "v[232:235],a[120:123],v[124:127],v[232:235]\n"
DT "a[120:121],v7 offset:192\n"
DT "a[122:123],v7 offset:704\n"
"v_max3_f32 v128,v128,v101,v102\n"
"v_max_f32 v128,v128,v103\n"
MF "v[236:239],a[124:127],v[124:127],v[236:239]\n"
DT "a[124:125],v7 offset:224\n"
DT "a[126:127],v7 offset:736\n"
"v_cmp_lt_f32 vcc,s53,v128\n"
MF "v[240:243],a[48:51],v[124:127],v[240:243]\n"
"s_cbranch_vccnz Ls159_%=\n"
"Ls160_%=:\n"
DT "a[212:213],v21 offset:0\n"
DT "a[214:215],v21 offset:4608\n"
DT "a[216:217],v21 offset:32\n"
DT "a[218:219],v21 offset:4640\n"
DT "a[220:221],v22 offset:0\n"
DT "a[222:223],v22 offset:4608\n"
DT "a[224:225],v22 offset:32\n"
DT "a[226:227],v22 offset:4640\n"
DT "a[228:229],v23 offset:0\n"
DT "a[230:231],v23 offset:4608\n"
DT "a[232:233],v23 offset:32\n"
DT "a[234:235],v23 offset:4640\n"
DT "a[236:237],v24 offset:0\n"
DT "a[238:239],v24 offset:4608\n"
DT "a[240:241],v24 offset:32\n"
DT "a[242:243],v24 offset:4640\n"
DT "a[244:245],v25 offset:0\n"
DT "a[246:247],v25 offset:4608\n"
WT "vmcnt(4)\n"
"v_cmp_eq_u32 vcc,v31,v28\n"
"s_andn2_b64 vcc,vcc,s[62:63]\n"
"s_cbranch_scc1 Lp161_%=\n"
"Lp162_%=:\n"
"v_add_u32 v3,2,v28\n"
"s_lshl_b64 exec,1,s40\n"
"buffer_store_dword v3,v26,s[28:31],0 offen offset:0 sc1\n"
"s_mov_b64 exec,-1\n"
WT "lgkmcnt(15)\n"
MF "v[80:83],a[64:67],a[0:3],v[104:107]\n"
EX "v88,v88\n"
MF "v[84:87],a[68:71],a[0:3],v[104:107]\n"
EX "v89,v89\n"
MF "v[80:83],a[72:75],a[4:7],v[80:83]\n"
EX "v90,v90\n"
MF "v[84:87],a[76:79],a[4:7],v[84:87]\n"
EX "v91,v91\n"
MF "v[80:83],a[80:83],a[8:11],v[80:83]\n"
CV "v120,v88,v89\n"
CV "v121,v90,v91\n"
MF "v[84:87],a[84:87],a[8:11],v[84:87]\n"
EX "v92,v92\n"
MF "v[80:83],a[88:91],a[12:15],v[80:83]\n"
EX "v93,v93\n"
MF "v[84:87],a[92:95],a[12:15],v[84:87]\n"
EX "v94,v94\n"
WT "lgkmcnt(0)\n"
MF "v[136:139],a[96:99],v[116:119],v[136:139]\n"
EX "v95,v95\n"
"s_add_u32 m0,s50,s48\n"
MF "v[140:143],a[100:103],v[116:119],v[140:143]\n"
CV "v122,v92,v93\n"
CV "v123,v94,v95\n"
BL "v8,s[12:15],s78 offen nt lds\n"
MF "v[144:147],a[104:107],v[116:119],v[144:147]\n"
BL "v9,s[12:15],s78 offen offset:1056 nt lds\n"
"s_add_u32 m0,m0,8448\n"
MF "v[148:151],a[108:111],v[116:119],v[148:151]\n"
BL "v10,s[16:19],s79 offen nt lds\n"
MF "v[152:155],a[112:115],v[116:119],v[152:155]\n"
BL "v11,s[16:19],s79 offen offset:1056 nt lds\n"
"s_add_u32 s51,s51,8192\n"
"s_mov_b32 s79,s78\n"
"s_xor_b32 s78,s51,s41\n"
MF "v[156:159],a[116:119],v[116:119],v[156:159]\n"
"v_add_u32 v6,s49,v4\n"
"s_mov_b32 s50,s42\n"
"v_max3_f32 v128,v80,v81,v82\n"
"v_max3_f32 v128,v128,v83,v84\n"
MF "v[160:163],a[120:123],v[116:119],v[160:163]\n"
"v_add_u32 v7,s49,v5\n"
"s_mov_b32 s42,s49\n"
"v_max3_f32 v128,v128,v85,v86\n"
"v_max_f32 v128,v128,v87\n"
MF "v[164:167],a[124:127],v[116:119],v[164:167]\n"
"s_add_u32 s49,s49,16896\n"
"s_cmp_eq_u32 s49,84480\n"
"s_cselect_b32 s49,0,s49\n"
"v_cmp_lt_f32 vcc,s53,v128\n"
MF "v[168:171],a[48:51],v[116:119],v[168:171]\n"
"s_cbranch_vccnz Ls164_%=\n"
"Ls165_%=:\n"
MF "v[88:91],a[64:67],a[16:19],v[108:111]\n"
EX "v96,v96\n"
MF "v[92:95],a[68:71],a[16:19],v[108:111]\n"
EX "v97,v97\n"
MF "v[88:91],a[72:75],a[20:23],v[88:91]\n"
EX "v98,v98\n"
MF "v[92:95],a[76:79],a[20:23],v[92:95]\n"
EX "v99,v99\n"
MF "v[88:91],a[80:83],a[24:27],v[88:91]\n"
CV "v124,v96,v97\n"
CV "v125,v98,v99\n"
MF "v[92:95],a[84:87],a[24:27],v[92:95]\n"
EX "v100,v100\n"
MF "v[88:91],a[88:91],a[28:31],v[88:91]\n"
EX "v101,v101\n"
MF "v[92:95],a[92:95],a[28:31],v[92:95]\n"
EX "v102,v102\n"
MF "v[172:175],a[96:99],v[120:123],v[172:175]\n"
EX "v103,v103\n"
MF "v[176:179],a[100:103],v[120:123],v[176:179]\n"
CV "v126,v100,v101\n"
CV "v127,v102,v103\n"
MF "v[180:183],a[104:107],v[120:123],v[180:183]\n"
MF "v[184:187],a[108:111],v[120:123],v[184:187]\n"
MF "v[188:191],a[112:115],v[120:123],v[188:191]\n"
MF "v[192:195],a[116:119],v[120:123],v[192:195]\n"
"v_max3_f32 v128,v88,v89,v90\n"
"v_max3_f32 v128,v128,v91,v92\n"
MF "v[196:199],a[120:123],v[120:123],v[196:199]\n"
"v_max3_f32 v128,v128,v93,v94\n"
"v_max_f32 v128,v128,v95\n"
MF "v[200:203],a[124:127],v[120:123],v[200:203]\n"
"v_cmp_lt_f32 vcc,s53,v128\n"
MF "v[204:207],a[48:51],v[120:123],v[204:207]\n"
"s_cbranch_vccnz Ls166_%=\n"
"Ls167_%=:\n"
WT "vmcnt(24)\n"
"s_barrier\n"
MF "v[96:99],a[64:67],a[32:35],v[112:115]\n"
EX "v80,v80\n"
DR "a[64:67],v6 offset:0\n"
MF "v[100:103],a[68:71],a[32:35],v[112:115]\n"
EX "v81,v81\n"
DR "a[68:71],v6 offset:512\n"
MF "v[96:99],a[72:75],a[36:39],v[96:99]\n"
EX "v82,v82\n"
DR "a[72:75],v6 offset:64\n"
MF "v[100:103],a[76:79],a[36:39],v[100:103]\n"
EX "v83,v83\n"
DR "a[76:79],v6 offset:576\n"
MF "v[96:99],a[80:83],a[40:43],v[96:99]\n"
CV "v116,v80,v81\n"
CV "v117,v82,v83\n"
DR "a[80:83],v6 offset:128\n"
MF "v[100:103],a[84:87],a[40:43],v[100:103]\n"
EX "v84,v84\n"
DR "a[84:87],v6 offset:640\n"
MF "v[96:99],a[88:91],a[44:47],v[96:99]\n"
EX "v85,v85\n"
DR "a[88:91],v6 offset:192\n"
MF "v[100:103],a[92:95],a[44:47],v[100:103]\n"
EX "v86,v86\n"
DR "a[92:95],v6 offset:704\n"
MF "v[208:211],a[96:99],v[124:127],v[208:211]\n"
EX "v87,v87\n"
DT "a[96:97],v7 offset:0\n"
DT "a[98:99],v7 offset:512\n"
MF "v[212:215],a[100:103],v[124:127],v[212:215]\n"
CV "v118,v84,v85\n"
CV "v119,v86,v87\n"
DT "a[100:101],v7 offset:32\n"
DT "a[102:103],v7 offset:544\n"
MF "v[216:219],a[104:107],v[124:127],v[216:219]\n"
DT "a[104:105],v7 offset:64\n"
DT "a[106:107],v7 offset:576\n"
MF "v[220:223],a[108:111],v[124:127],v[220:223]\n"
DT "a[108:109],v7 offset:96\n"
DT "a[110:111],v7 offset:608\n"
MF "v[224:227],a[112:115],v[124:127],v[224:227]\n"
DT "a[112:113],v7 offset:128\n"
DT "a[114:115],v7 offset:640\n"
MF "v[228:231],a[116:119],v[124:127],v[228:231]\n"
DT "a[116:117],v7 offset:160\n"
DT "a[118:119],v7 offset:672\n"
"v_max3_f32 v128,v96,v97,v98\n"
"v_max3_f32 v128,v128,v99,v100\n"
MF "v[232:235],a[120:123],v[124:127],v[232:235]\n"
DT "a[120:121],v7 offset:192\n"
DT "a[122:123],v7 offset:704\n"
"v_max3_f32 v128,v128,v101,v102\n"
"v_max_f32 v128,v128,v103\n"
MF "v[236:239],a[124:127],v[124:127],v[236:239]\n"
DT "a[124:125],v7 offset:224\n"
DT "a[126:127],v7 offset:736\n"
"v_cmp_lt_f32 vcc,s53,v128\n"
MF "v[240:243],a[48:51],v[124:127],v[240:243]\n"
"s_cbranch_vccnz Ls168_%=\n"
"Ls169_%=:\n"
MF "a[128:131],a[212:215],v[32:35],0\n"
"s_add_u32 m0,s76,121344\n"
MF "a[164:167],a[212:215],v[48:51],0\n"
"s_add_u32 s67,s69,0\n"
MF "a[200:203],a[212:215],v[64:67],0\n"
DT "a[212:213],v21 offset:9216\n"
BL "v12,s[24:27],s67 offen offset:0 lds\n"
MF "a[132:135],a[216:219],v[32:35],0\n"
DT "a[214:215],v21 offset:13824\n"
MF "a[168:171],a[216:219],v[48:51],0\n"
MF "a[204:207],a[216:219],v[64:67],0\n"
DT "a[216:217],v21 offset:9248\n"
BL "v13,s[24:27],s67 offen offset:1024 lds\n"
MF "a[136:139],a[220:223],v[32:35],0\n"
DT "a[218:219],v21 offset:13856\n"
MF "a[172:175],a[220:223],v[48:51],0\n"
DT "a[220:221],v22 offset:9216\n"
MF "a[140:143],a[224:227],v[32:35],0\n"
DT "a[222:223],v22 offset:13824\n"
BL "v14,s[24:27],s67 offen offset:2048 lds\n"
MF "a[176:179],a[224:227],v[48:51],0\n"
"s_add_u32 m0,s76,124416\n"
DT "a[224:225],v22 offset:9248\n"
MF "a[144:147],a[228:231],v[32:35],0\n"
"s_add_u32 s67,s69,0\n"
DT "a[226:227],v22 offset:13856\n"
MF "a[180:183],a[228:231],v[48:51],0\n"
DT "a[228:229],v23 offset:9216\n"
BL "v15,s[24:27],s67 offen offset:0 lds\n"
MF "a[148:151],a[232:235],v[32:35],0\n"
DT "a[230:231],v23 offset:13824\n"
MF "a[184:187],a[232:235],v[48:51],0\n"
DT "a[232:233],v23 offset:9248\n"
MF "a[152:155],a[236:239],v[32:35],0\n"
DT "a[234:235],v23 offset:13856\n"
BL "v16,s[24:27],s67 offen offset:1024 lds\n"
MF "a[188:191],a[236:239],v[48:51],0\n"
DT "a[236:237],v24 offset:9216\n"
MF "a[156:159],a[240:243],v[32:35],0\n"
DT "a[238:239],v24 offset:13824\n"
MF "a[192:195],a[240:243],v[48:51],0\n"
DT "a[240:241],v24 offset:9248\n"
BL "v17,s[24:27],s67 offen offset:2048 lds\n"
MF "a[160:163],a[244:247],v[32:35],0\n"
DT "a[242:243],v24 offset:13856\n"
MF "a[196:199],a[244:247],v[48:51],0\n"
MF "a[208:211],a[244:247],v[64:67],0\n"
DT "a[244:245],v25 offset:9216\n"
DT "a[246:247],v25 offset:13824\n"
WT "lgkmcnt(15)\n"
MF "v[80:83],a[64:67],a[0:3],v[104:107]\n"
EX "v88,v88\n"
MF "v[84:87],a[68:71],a[0:3],v[104:107]\n"
EX "v89,v89\n"
MF "v[80:83],a[72:75],a[4:7],v[80:83]\n"
EX "v90,v90\n"
MF "v[84:87],a[76:79],a[4:7],v[84:87]\n"
EX "v91,v91\n"
MF "v[80:83],a[80:83],a[8:11],v[80:83]\n"
CV "v120,v88,v89\n"
CV "v121,v90,v91\n"
MF "v[84:87],a[84:87],a[8:11],v[84:87]\n"
EX "v92,v92\n"
MF "v[80:83],a[88:91],a[12:15],v[80:83]\n"
EX "v93,v93\n"
MF "v[84:87],a[92:95],a[12:15],v[84:87]\n"
EX "v94,v94\n"
WT "lgkmcnt(0)\n"
MF "v[136:139],a[96:99],v[116:119],v[136:139]\n"
EX "v95,v95\n"
"s_add_u32 m0,s50,s48\n"
MF "v[140:143],a[100:103],v[116:119],v[140:143]\n"
CV "v122,v92,v93\n"
CV "v123,v94,v95\n"
BL "v8,s[12:15],s78 offen nt lds\n"
MF "v[144:147],a[104:107],v[116:119],v[144:147]\n"
BL "v9,s[12:15],s78 offen offset:1056 nt lds\n"
"s_add_u32 m0,m0,8448\n"
MF "v[148:151],a[108:111],v[116:119],v[148:151]\n"
BL "v10,s[16:19],s79 offen nt lds\n"
MF "v[152:155],a[112:115],v[116:119],v[152:155]\n"
BL "v11,s[16:19],s79 offen offset:1056 nt lds\n"
"s_add_u32 s51,s51,8192\n"
"s_mov_b32 s79,s78\n"
"s_xor_b32 s78,s51,s41\n"
MF "v[156:159],a[116:119],v[116:119],v[156:159]\n"
"v_add_u32 v6,s49,v4\n"
"s_mov_b32 s50,s42\n"
"v_max3_f32 v128,v80,v81,v82\n"
"v_max3_f32 v128,v128,v83,v84\n"
MF "v[160:163],a[120:123],v[116:119],v[160:163]\n"
"v_add_u32 v7,s49,v5\n"
"s_mov_b32 s42,s49\n"
"v_max3_f32 v128,v128,v85,v86\n"
"v_max_f32 v128,v128,v87\n"
MF "v[164:167],a[124:127],v[116:119],v[164:167]\n"
"s_add_u32 s49,s49,16896\n"
"s_cmp_eq_u32 s49,84480\n"
"s_cselect_b32 s49,0,s49\n"
"v_cmp_lt_f32 vcc,s53,v128\n"
MF "v[168:171],a[48:51],v[116:119],v[168:171]\n"
"s_cbranch_vccnz Ls170_%=\n"
"Ls171_%=:\n"
MF "v[88:91],a[64:67],a[16:19],v[108:111]\n"
EX "v96,v96\n"
MF "v[92:95],a[68:71],a[16:19],v[108:111]\n"
EX "v97,v97\n"
MF "v[88:91],a[72:75],a[20:23],v[88:91]\n"
EX "v98,v98\n"
MF "v[92:95],a[76:79],a[20:23],v[92:95]\n"
EX "v99,v99\n"
MF "v[88:91],a[80:83],a[24:27],v[88:91]\n"
CV "v124,v96,v97\n"
CV "v125,v98,v99\n"
MF "v[92:95],a[84:87],a[24:27],v[92:95]\n"
EX "v100,v100\n"
MF "v[88:91],a[88:91],a[28:31],v[88:91]\n"
EX "v101,v101\n"
MF "v[92:95],a[92:95],a[28:31],v[92:95]\n"
EX "v102,v102\n"
MF "v[172:175],a[96:99],v[120:123],v[172:175]\n"
EX "v103,v103\n"
MF "v[176:179],a[100:103],v[120:123],v[176:179]\n"
CV "v126,v100,v101\n"
CV "v127,v102,v103\n"
MF "v[180:183],a[104:107],v[120:123],v[180:183]\n"
MF "v[184:187],a[108:111],v[120:123],v[184:187]\n"
MF "v[188:191],a[112:115],v[120:123],v[188:191]\n"
MF "v[192:195],a[116:119],v[120:123],v[192:195]\n"
"v_max3_f32 v128,v88,v89,v90\n"
"v_max3_f32 v128,v128,v91,v92\n"
MF "v[196:199],a[120:123],v[120:123],v[196:199]\n"
"v_max3_f32 v128,v128,v93,v94\n"
"v_max_f32 v128,v128,v95\n"
MF "v[200:203],a[124:127],v[120:123],v[200:203]\n"
"v_cmp_lt_f32 vcc,s53,v128\n"
MF "v[204:207],a[48:51],v[120:123],v[204:207]\n"
"s_cbranch_vccnz Ls172_%=\n"
"Ls173_%=:\n"
WT "vmcnt(20)\n"
"s_barrier\n"
MF "v[96:99],a[64:67],a[32:35],v[112:115]\n"
EX "v80,v80\n"
DR "a[64:67],v6 offset:0\n"
MF "v[100:103],a[68:71],a[32:35],v[112:115]\n"
EX "v81,v81\n"
DR "a[68:71],v6 offset:512\n"
MF "v[96:99],a[72:75],a[36:39],v[96:99]\n"
EX "v82,v82\n"
DR "a[72:75],v6 offset:64\n"
MF "v[100:103],a[76:79],a[36:39],v[100:103]\n"
EX "v83,v83\n"
DR "a[76:79],v6 offset:576\n"
MF "v[96:99],a[80:83],a[40:43],v[96:99]\n"
CV "v116,v80,v81\n"
CV "v117,v82,v83\n"
DR "a[80:83],v6 offset:128\n"
MF "v[100:103],a[84:87],a[40:43],v[100:103]\n"
EX "v84,v84\n"
DR "a[84:87],v6 offset:640\n"
MF "v[96:99],a[88:91],a[44:47],v[96:99]\n"
EX "v85,v85\n"
DR "a[88:91],v6 offset:192\n"
MF "v[100:103],a[92:95],a[44:47],v[100:103]\n"
EX "v86,v86\n"
DR "a[92:95],v6 offset:704\n"
MF "v[208:211],a[96:99],v[124:127],v[208:211]\n"
EX "v87,v87\n"
DT "a[96:97],v7 offset:0\n"
DT "a[98:99],v7 offset:512\n"
MF "v[212:215],a[100:103],v[124:127],v[212:215]\n"
CV "v118,v84,v85\n"
CV "v119,v86,v87\n"
DT "a[100:101],v7 offset:32\n"
DT "a[102:103],v7 offset:544\n"
MF "v[216:219],a[104:107],v[124:127],v[216:219]\n"
DT "a[104:105],v7 offset:64\n"
DT "a[106:107],v7 offset:576\n"
MF "v[220:223],a[108:111],v[124:127],v[220:223]\n"
DT "a[108:109],v7 offset:96\n"
DT "a[110:111],v7 offset:608\n"
MF "v[224:227],a[112:115],v[124:127],v[224:227]\n"
DT "a[112:113],v7 offset:128\n"
DT "a[114:115],v7 offset:640\n"
MF "v[228:231],a[116:119],v[124:127],v[228:231]\n"
DT "a[116:117],v7 offset:160\n"
DT "a[118:119],v7 offset:672\n"
"v_max3_f32 v128,v96,v97,v98\n"
"v_max3_f32 v128,v128,v99,v100\n"
MF "v[232:235],a[120:123],v[124:127],v[232:235]\n"
DT "a[120:121],v7 offset:192\n"
DT "a[122:123],v7 offset:704\n"
"v_max3_f32 v128,v128,v101,v102\n"
"v_max_f32 v128,v128,v103\n"
MF "v[236:239],a[124:127],v[124:127],v[236:239]\n"
DT "a[124:125],v7 offset:224\n"
DT "a[126:127],v7 offset:736\n"
"v_cmp_lt_f32 vcc,s53,v128\n"
MF "v[240:243],a[48:51],v[124:127],v[240:243]\n"
"s_cbranch_vccnz Ls174_%=\n"
"Ls175_%=:\n"
MF "a[128:131],a[212:215],v[36:39],a[128:131]\n"
MF "a[164:167],a[212:215],v[52:55],a[164:167]\n"
"s_add_u32 s44,s57,0\n"
MF "a[200:203],a[212:215],v[68:71],a[200:203]\n"
DT "a[212:213],v21 offset:18432\n"
BL "v[32:35],v244,s[28:31],s44 offen offset:0 sc1\n"
MF "a[132:135],a[216:219],v[36:39],a[132:135]\n"
DT "a[214:215],v21 offset:23040\n"
MF "a[168:171],a[216:219],v[52:55],a[168:171]\n"
MF "a[204:207],a[216:219],v[68:71],a[204:207]\n"
DT "a[216:217],v21 offset:18464\n"
BL "v[48:51],v245,s[28:31],s44 offen offset:0 sc1\n"
MF "a[136:139],a[220:223],v[36:39],a[136:139]\n"
DT "a[218:219],v21 offset:23072\n"
MF "a[172:175],a[220:223],v[52:55],a[172:175]\n"
DT "a[220:221],v22 offset:18432\n"
MF "a[140:143],a[224:227],v[36:39],a[140:143]\n"
DT "a[222:223],v22 offset:23040\n"
BL "v[64:67],v246,s[28:31],s44 offen offset:0 sc1\n"
MF "a[176:179],a[224:227],v[52:55],a[176:179]\n"
"s_add_u32 m0,s76,127488\n"
DT "a[224:225],v22 offset:18464\n"
MF "a[144:147],a[228:231],v[36:39],a[144:147]\n"
"s_add_u32 s67,s69,0\n"
DT "a[226:227],v22 offset:23072\n"
MF "a[180:183],a[228:231],v[52:55],a[180:183]\n"
DT "a[228:229],v23 offset:18432\n"
BL "v18,s[24:27],s67 offen offset:0 lds\n"
MF "a[148:151],a[232:235],v[36:39],a[148:151]\n"
DT "a[230:231],v23 offset:23040\n"
MF "a[184:187],a[232:235],v[52:55],a[184:187]\n"
DT "a[232:233],v23 offset:18464\n"
MF "a[152:155],a[236:239],v[36:39],a[152:155]\n"
DT "a[234:235],v23 offset:23072\n"
BL "v19,s[24:27],s67 offen offset:1024 lds\n"
MF "a[188:191],a[236:239],v[52:55],a[188:191]\n"
DT "a[236:237],v24 offset:18432\n"
MF "a[156:159],a[240:243],v[36:39],a[156:159]\n"
DT "a[238:239],v24 offset:23040\n"
MF "a[192:195],a[240:243],v[52:55],a[192:195]\n"
DT "a[240:241],v24 offset:18464\n"
BL "v20,s[24:27],s67 offen offset:2048 lds\n"
MF "a[160:163],a[244:247],v[36:39],a[160:163]\n"
DT "a[242:243],v24 offset:23072\n"
MF "a[196:199],a[244:247],v[52:55],a[196:199]\n"
MF "a[208:211],a[244:247],v[68:71],a[208:211]\n"
DT "a[244:245],v25 offset:18432\n"
DT "a[246:247],v25 offset:23040\n"
WT "lgkmcnt(15)\n"
MF "v[80:83],a[64:67],a[0:3],v[104:107]\n"
EX "v88,v88\n"
MF "v[84:87],a[68:71],a[0:3],v[104:107]\n"
EX "v89,v89\n"
MF "v[80:83],a[72:75],a[4:7],v[80:83]\n"
EX "v90,v90\n"
MF "v[84:87],a[76:79],a[4:7],v[84:87]\n"
EX "v91,v91\n"
MF "v[80:83],a[80:83],a[8:11],v[80:83]\n"
CV "v120,v88,v89\n"
CV "v121,v90,v91\n"
MF "v[84:87],a[84:87],a[8:11],v[84:87]\n"
EX "v92,v92\n"
MF "v[80:83],a[88:91],a[12:15],v[80:83]\n"
EX "v93,v93\n"
MF "v[84:87],a[92:95],a[12:15],v[84:87]\n"
EX "v94,v94\n"
WT "lgkmcnt(0)\n"
MF "v[136:139],a[96:99],v[116:119],v[136:139]\n"
EX "v95,v95\n"
"s_add_u32 m0,s50,s48\n"
MF "v[140:143],a[100:103],v[116:119],v[140:143]\n"
CV "v122,v92,v93\n"
CV "v123,v94,v95\n"
BL "v8,s[12:15],s78 offen nt lds\n"
MF "v[144:147],a[104:107],v[116:119],v[144:147]\n"
BL "v9,s[12:15],s78 offen offset:1056 nt lds\n"
"s_add_u32 m0,m0,8448\n"
MF "v[148:151],a[108:111],v[116:119],v[148:151]\n"
BL "v10,s[16:19],s79 offen nt lds\n"
MF "v[152:155],a[112:115],v[116:119],v[152:155]\n"
BL "v11,s[16:19],s79 offen offset:1056 nt lds\n"
"s_add_u32 s51,s51,8192\n"
"s_mov_b32 s79,s78\n"
"s_xor_b32 s78,s51,s41\n"
MF "v[156:159],a[116:119],v[116:119],v[156:159]\n"
"v_add_u32 v6,s49,v4\n"
"s_mov_b32 s50,s42\n"
"v_max3_f32 v128,v80,v81,v82\n"
"v_max3_f32 v128,v128,v83,v84\n"
MF "v[160:163],a[120:123],v[116:119],v[160:163]\n"
"v_add_u32 v7,s49,v5\n"
"s_mov_b32 s42,s49\n"
"v_max3_f32 v128,v128,v85,v86\n"
"v_max_f32 v128,v128,v87\n"
MF "v[164:167],a[124:127],v[116:119],v[164:167]\n"
"s_add_u32 s49,s49,16896\n"
"s_cmp_eq_u32 s49,84480\n"
"s_cselect_b32 s49,0,s49\n"
"v_cmp_lt_f32 vcc,s53,v128\n"
MF "v[168:171],a[48:51],v[116:119],v[168:171]\n"
"s_cbranch_vccnz Ls176_%=\n"
"Ls177_%=:\n"
MF "v[88:91],a[64:67],a[16:19],v[108:111]\n"
EX "v96,v96\n"
MF "v[92:95],a[68:71],a[16:19],v[108:111]\n"
EX "v97,v97\n"
MF "v[88:91],a[72:75],a[20:23],v[88:91]\n"
EX "v98,v98\n"
MF "v[92:95],a[76:79],a[20:23],v[92:95]\n"
EX "v99,v99\n"
MF "v[88:91],a[80:83],a[24:27],v[88:91]\n"
CV "v124,v96,v97\n"
CV "v125,v98,v99\n"
MF "v[92:95],a[84:87],a[24:27],v[92:95]\n"
EX "v100,v100\n"
MF "v[88:91],a[88:91],a[28:31],v[88:91]\n"
EX "v101,v101\n"
MF "v[92:95],a[92:95],a[28:31],v[92:95]\n"
EX "v102,v102\n"
MF "v[172:175],a[96:99],v[120:123],v[172:175]\n"
EX "v103,v103\n"
MF "v[176:179],a[100:103],v[120:123],v[176:179]\n"
CV "v126,v100,v101\n"
CV "v127,v102,v103\n"
MF "v[180:183],a[104:107],v[120:123],v[180:183]\n"
MF "v[184:187],a[108:111],v[120:123],v[184:187]\n"
MF "v[188:191],a[112:115],v[120:123],v[188:191]\n"
MF "v[192:195],a[116:119],v[120:123],v[192:195]\n"
"v_max3_f32 v128,v88,v89,v90\n"
"v_max3_f32 v128,v128,v91,v92\n"
MF "v[196:199],a[120:123],v[120:123],v[196:199]\n"
"v_max3_f32 v128,v128,v93,v94\n"
"v_max_f32 v128,v128,v95\n"
MF "v[200:203],a[124:127],v[120:123],v[200:203]\n"
"v_cmp_lt_f32 vcc,s53,v128\n"
MF "v[204:207],a[48:51],v[120:123],v[204:207]\n"
"s_cbranch_vccnz Ls178_%=\n"
"Ls179_%=:\n"
WT "vmcnt(25)\n"
"s_barrier\n"
MF "v[96:99],a[64:67],a[32:35],v[112:115]\n"
EX "v80,v80\n"
DR "a[64:67],v6 offset:0\n"
MF "v[100:103],a[68:71],a[32:35],v[112:115]\n"
EX "v81,v81\n"
DR "a[68:71],v6 offset:512\n"
MF "v[96:99],a[72:75],a[36:39],v[96:99]\n"
EX "v82,v82\n"
DR "a[72:75],v6 offset:64\n"
MF "v[100:103],a[76:79],a[36:39],v[100:103]\n"
EX "v83,v83\n"
DR "a[76:79],v6 offset:576\n"
MF "v[96:99],a[80:83],a[40:43],v[96:99]\n"
CV "v116,v80,v81\n"
CV "v117,v82,v83\n"
DR "a[80:83],v6 offset:128\n"
MF "v[100:103],a[84:87],a[40:43],v[100:103]\n"
EX "v84,v84\n"
DR "a[84:87],v6 offset:640\n"
MF "v[96:99],a[88:91],a[44:47],v[96:99]\n"
EX "v85,v85\n"
DR "a[88:91],v6 offset:192\n"
MF "v[100:103],a[92:95],a[44:47],v[100:103]\n"
EX "v86,v86\n"
DR "a[92:95],v6 offset:704\n"
MF "v[208:211],a[96:99],v[124:127],v[208:211]\n"
EX "v87,v87\n"
DT "a[96:97],v7 offset:0\n"
DT "a[98:99],v7 offset:512\n"
MF "v[212:215],a[100:103],v[124:127],v[212:215]\n"
CV "v118,v84,v85\n"
CV "v119,v86,v87\n"
DT "a[100:101],v7 offset:32\n"
DT "a[102:103],v7 offset:544\n"
MF "v[216:219],a[104:107],v[124:127],v[216:219]\n"
DT "a[104:105],v7 offset:64\n"
DT "a[106:107],v7 offset:576\n"
MF "v[220:223],a[108:111],v[124:127],v[220:223]\n"
DT "a[108:109],v7 offset:96\n"
DT "a[110:111],v7 offset:608\n"
MF "v[224:227],a[112:115],v[124:127],v[224:227]\n"
DT "a[112:113],v7 offset:128\n"
DT "a[114:115],v7 offset:640\n"
MF "v[228:231],a[116:119],v[124:127],v[228:231]\n"
DT "a[116:117],v7 offset:160\n"
DT "a[118:119],v7 offset:672\n"
"v_max3_f32 v128,v96,v97,v98\n"
"v_max3_f32 v128,v128,v99,v100\n"
MF "v[232:235],a[120:123],v[124:127],v[232:235]\n"
DT "a[120:121],v7 offset:192\n"
DT "a[122:123],v7 offset:704\n"
"v_max3_f32 v128,v128,v101,v102\n"
"v_max_f32 v128,v128,v103\n"
MF "v[236:239],a[124:127],v[124:127],v[236:239]\n"
DT "a[124:125],v7 offset:224\n"
DT "a[126:127],v7 offset:736\n"
"v_cmp_lt_f32 vcc,s53,v128\n"
MF "v[240:243],a[48:51],v[124:127],v[240:243]\n"
"s_cbranch_vccnz Ls180_%=\n"
"Ls181_%=:\n"
MF "a[128:131],a[212:215],v[40:43],a[128:131]\n"
MF "a[164:167],a[212:215],v[56:59],a[164:167]\n"
"s_add_u32 s44,s57,0\n"
MF "a[200:203],a[212:215],v[72:75],a[200:203]\n"
DT "a[212:213],v21 offset:27648\n"
BL "v[36:39],v244,s[28:31],s44 offen offset:1024 sc1\n"
MF "a[132:135],a[216:219],v[40:43],a[132:135]\n"
DT "a[214:215],v21 offset:32256\n"
MF "a[168:171],a[216:219],v[56:59],a[168:171]\n"
MF "a[204:207],a[216:219],v[72:75],a[204:207]\n"
DT "a[216:217],v21 offset:27680\n"
MF "a[136:139],a[220:223],v[40:43],a[136:139]\n"
DT "a[218:219],v21 offset:32288\n"
"v_add_u32 v21,36864,v21\n"
MF "a[172:175],a[220:223],v[56:59],a[172:175]\n"
DT "a[220:221],v22 offset:27648\n"
MF "a[140:143],a[224:227],v[40:43],a[140:143]\n"
DT "a[222:223],v22 offset:32256\n"
BL "v[52:55],v245,s[28:31],s44 offen offset:1024 sc1\n"
MF "a[176:179],a[224:227],v[56:59],a[176:179]\n"
DT "a[224:225],v22 offset:27680\n"
MF "a[144:147],a[228:231],v[40:43],a[144:147]\n"
DT "a[226:227],v22 offset:32288\n"
"v_add_u32 v22,36864,v22\n"
MF "a[180:183],a[228:231],v[56:59],a[180:183]\n"
DT "a[228:229],v23 offset:27648\n"
MF "a[148:151],a[232:235],v[40:43],a[148:151]\n"
DT "a[230:231],v23 offset:32256\n"
MF "a[184:187],a[232:235],v[56:59],a[184:187]\n"
DT "a[232:233],v23 offset:27680\n"
MF "a[152:155],a[236:239],v[40:43],a[152:155]\n"
DT "a[234:235],v23 offset:32288\n"
BL "v[68:71],v246,s[28:31],s44 offen offset:1024 sc1\n"
"v_add_u32 v23,36864,v23\n"
MF "a[188:191],a[236:239],v[56:59],a[188:191]\n"
DT "a[236:237],v24 offset:27648\n"
MF "a[156:159],a[240:243],v[40:43],a[156:159]\n"
DT "a[238:239],v24 offset:32256\n"
MF "a[192:195],a[240:243],v[56:59],a[192:195]\n"
DT "a[240:241],v24 offset:27680\n"
MF "a[160:163],a[244:247],v[40:43],a[160:163]\n"
DT "a[242:243],v24 offset:32288\n"
"v_add_u32 v24,36864,v24\n"
MF "a[196:199],a[244:247],v[56:59],a[196:199]\n"
MF "a[208:211],a[244:247],v[72:75],a[208:211]\n"
DT "a[244:245],v25 offset:27648\n"
DT "a[246:247],v25 offset:32256\n"
"v_add_u32 v25,36864,v25\n"
WT "lgkmcnt(15)\n"
MF "v[80:83],a[64:67],a[0:3],v[104:107]\n"
EX "v88,v88\n"
MF "v[84:87],a[68:71],a[0:3],v[104:107]\n"
EX "v89,v89\n"
MF "v[80:83],a[72:75],a[4:7],v[80:83]\n"
EX "v90,v90\n"
MF "v[84:87],a[76:79],a[4:7],v[84:87]\n"
EX "v91,v91\n"
MF "v[80:83],a[80:83],a[8:11],v[80:83]\n"
CV "v120,v88,v89\n"
CV "v121,v90,v91\n"
MF "v[84:87],a[84:87],a[8:11],v[84:87]\n"
EX "v92,v92\n"
MF "v[80:83],a[88:91],a[12:15],v[80:83]\n"
EX "v93,v93\n"
MF "v[84:87],a[92:95],a[12:15],v[84:87]\n"
EX "v94,v94\n"
WT "lgkmcnt(0)\n"
MF "v[136:139],a[96:99],v[116:119],v[136:139]\n"
EX "v95,v95\n"
"s_add_u32 m0,s50,s48\n"
MF "v[140:143],a[100:103],v[116:119],v[140:143]\n"
CV "v122,v92,v93\n"
CV "v123,v94,v95\n"
BL "v8,s[12:15],s78 offen nt lds\n"
MF "v[144:147],a[104:107],v[116:119],v[144:147]\n"
BL "v9,s[12:15],s78 offen offset:1056 nt lds\n"
"s_add_u32 m0,m0,8448\n"
MF "v[148:151],a[108:111],v[116:119],v[148:151]\n"
BL "v10,s[16:19],s79 offen nt lds\n"
MF "v[152:155],a[112:115],v[116:119],v[152:155]\n"
BL "v11,s[16:19],s79 offen offset:1056 nt lds\n"
"s_add_u32 s51,s51,8192\n"
"s_mov_b32 s79,s78\n"
"s_xor_b32 s78,s51,s41\n"
MF "v[156:159],a[116:119],v[116:119],v[156:159]\n"
"v_add_u32 v6,s49,v4\n"
"s_mov_b32 s50,s42\n"
"v_max3_f32 v128,v80,v81,v82\n"
"v_max3_f32 v128,v128,v83,v84\n"
MF "v[160:163],a[120:123],v[116:119],v[160:163]\n"
"v_add_u32 v7,s49,v5\n"
"s_mov_b32 s42,s49\n"
"v_max3_f32 v128,v128,v85,v86\n"
"v_max_f32 v128,v128,v87\n"
MF "v[164:167],a[124:127],v[116:119],v[164:167]\n"
"s_add_u32 s49,s49,16896\n"
"s_cmp_eq_u32 s49,84480\n"
"s_cselect_b32 s49,0,s49\n"
"v_cmp_lt_f32 vcc,s53,v128\n"
MF "v[168:171],a[48:51],v[116:119],v[168:171]\n"
"s_cbranch_vccnz Ls182_%=\n"
"Ls183_%=:\n"
MF "v[88:91],a[64:67],a[16:19],v[108:111]\n"
EX "v96,v96\n"
MF "v[92:95],a[68:71],a[16:19],v[108:111]\n"
EX "v97,v97\n"
MF "v[88:91],a[72:75],a[20:23],v[88:91]\n"
EX "v98,v98\n"
MF "v[92:95],a[76:79],a[20:23],v[92:95]\n"
EX "v99,v99\n"
MF "v[88:91],a[80:83],a[24:27],v[88:91]\n"
CV "v124,v96,v97\n"
CV "v125,v98,v99\n"
MF "v[92:95],a[84:87],a[24:27],v[92:95]\n"
EX "v100,v100\n"
MF "v[88:91],a[88:91],a[28:31],v[88:91]\n"
EX "v101,v101\n"
MF "v[92:95],a[92:95],a[28:31],v[92:95]\n"
EX "v102,v102\n"
MF "v[172:175],a[96:99],v[120:123],v[172:175]\n"
EX "v103,v103\n"
MF "v[176:179],a[100:103],v[120:123],v[176:179]\n"
CV "v126,v100,v101\n"
CV "v127,v102,v103\n"
MF "v[180:183],a[104:107],v[120:123],v[180:183]\n"
MF "v[184:187],a[108:111],v[120:123],v[184:187]\n"
MF "v[188:191],a[112:115],v[120:123],v[188:191]\n"
MF "v[192:195],a[116:119],v[120:123],v[192:195]\n"
"v_max3_f32 v128,v88,v89,v90\n"
"v_max3_f32 v128,v128,v91,v92\n"
MF "v[196:199],a[120:123],v[120:123],v[196:199]\n"
"v_max3_f32 v128,v128,v93,v94\n"
"v_max_f32 v128,v128,v95\n"
MF "v[200:203],a[124:127],v[120:123],v[200:203]\n"
"v_cmp_lt_f32 vcc,s53,v128\n"
MF "v[204:207],a[48:51],v[120:123],v[204:207]\n"
"s_cbranch_vccnz Ls184_%=\n"
"Ls185_%=:\n"
WT "vmcnt(27)\n"
WT "vmcnt(11)\n"
"s_barrier\n"
MF "v[96:99],a[64:67],a[32:35],v[112:115]\n"
EX "v80,v80\n"
DR "a[64:67],v6 offset:0\n"
MF "v[100:103],a[68:71],a[32:35],v[112:115]\n"
EX "v81,v81\n"
DR "a[68:71],v6 offset:512\n"
MF "v[96:99],a[72:75],a[36:39],v[96:99]\n"
EX "v82,v82\n"
DR "a[72:75],v6 offset:64\n"
MF "v[100:103],a[76:79],a[36:39],v[100:103]\n"
EX "v83,v83\n"
DR "a[76:79],v6 offset:576\n"
MF "v[96:99],a[80:83],a[40:43],v[96:99]\n"
CV "v116,v80,v81\n"
CV "v117,v82,v83\n"
DR "a[80:83],v6 offset:128\n"
MF "v[100:103],a[84:87],a[40:43],v[100:103]\n"
EX "v84,v84\n"
DR "a[84:87],v6 offset:640\n"
MF "v[96:99],a[88:91],a[44:47],v[96:99]\n"
EX "v85,v85\n"
DR "a[88:91],v6 offset:192\n"
MF "v[100:103],a[92:95],a[44:47],v[100:103]\n"
EX "v86,v86\n"
DR "a[92:95],v6 offset:704\n"
MF "v[208:211],a[96:99],v[124:127],v[208:211]\n"
EX "v87,v87\n"
DT "a[96:97],v7 offset:0\n"
DT "a[98:99],v7 offset:512\n"
MF "v[212:215],a[100:103],v[124:127],v[212:215]\n"
CV "v118,v84,v85\n"
CV "v119,v86,v87\n"
DT "a[100:101],v7 offset:32\n"
DT "a[102:103],v7 offset:544\n"
MF "v[216:219],a[104:107],v[124:127],v[216:219]\n"
DT "a[104:105],v7 offset:64\n"
DT "a[106:107],v7 offset:576\n"
MF "v[220:223],a[108:111],v[124:127],v[220:223]\n"
DT "a[108:109],v7 offset:96\n"
DT "a[110:111],v7 offset:608\n"
MF "v[224:227],a[112:115],v[124:127],v[224:227]\n"
DT "a[112:113],v7 offset:128\n"
DT "a[114:115],v7 offset:640\n"
MF "v[228:231],a[116:119],v[124:127],v[228:231]\n"
DT "a[116:117],v7 offset:160\n"
DT "a[118:119],v7 offset:672\n"
"v_max3_f32 v128,v96,v97,v98\n"
"v_max3_f32 v128,v128,v99,v100\n"
MF "v[232:235],a[120:123],v[124:127],v[232:235]\n"
DT "a[120:121],v7 offset:192\n"
DT "a[122:123],v7 offset:704\n"
"v_max3_f32 v128,v128,v101,v102\n"
"v_max_f32 v128,v128,v103\n"
MF "v[236:239],a[124:127],v[124:127],v[236:239]\n"
DT "a[124:125],v7 offset:224\n"
DT "a[126:127],v7 offset:736\n"
"v_cmp_lt_f32 vcc,s53,v128\n"
MF "v[240:243],a[48:51],v[124:127],v[240:243]\n"
"s_cbranch_vccnz Ls186_%=\n"
"Ls187_%=:\n"
MF "a[128:131],a[212:215],v[44:47],a[128:131]\n"
MF "a[164:167],a[212:215],v[60:63],a[164:167]\n"
"s_add_u32 s44,s57,0\n"
MF "a[200:203],a[212:215],v[76:79],a[200:203]\n"
DT "a[212:213],v21 offset:0\n"
BL "v[40:43],v244,s[28:31],s44 offen offset:2048 sc1\n"
MF "a[132:135],a[216:219],v[44:47],a[132:135]\n"
DT "a[214:215],v21 offset:4608\n"
MF "a[168:171],a[216:219],v[60:63],a[168:171]\n"
MF "a[204:207],a[216:219],v[76:79],a[204:207]\n"
DT "a[216:217],v21 offset:32\n"
MF "a[136:139],a[220:223],v[44:47],a[136:139]\n"
DT "a[218:219],v21 offset:4640\n"
MF "a[172:175],a[220:223],v[60:63],a[172:175]\n"
DT "a[220:221],v22 offset:0\n"
MF "a[140:143],a[224:227],v[44:47],a[140:143]\n"
DT "a[222:223],v22 offset:4608\n"
BL "v[56:59],v245,s[28:31],s44 offen offset:2048 sc1\n"
MF "a[176:179],a[224:227],v[60:63],a[176:179]\n"
DT "a[224:225],v22 offset:32\n"
MF "a[144:147],a[228:231],v[44:47],a[144:147]\n"
DT "a[226:227],v22 offset:4640\n"
MF "a[180:183],a[228:231],v[60:63],a[180:183]\n"
DT "a[228:229],v23 offset:0\n"
MF "a[148:151],a[232:235],v[44:47],a[148:151]\n"
DT "a[230:231],v23 offset:4608\n"
MF "a[184:187],a[232:235],v[60:63],a[184:187]\n"
DT "a[232:233],v23 offset:32\n"
MF "a[152:155],a[236:239],v[44:47],a[152:155]\n"
DT "a[234:235],v23 offset:4640\n"
BL "v[72:75],v246,s[28:31],s44 offen offset:2048 sc1\n"
MF "a[188:191],a[236:239],v[60:63],a[188:191]\n"
DT "a[236:237],v24 offset:0\n"
MF "a[156:159],a[240:243],v[44:47],a[156:159]\n"
DT "a[238:239],v24 offset:4608\n"
MF "a[192:195],a[240:243],v[60:63],a[192:195]\n"
DT "a[240:241],v24 offset:32\n"
MF "a[160:163],a[244:247],v[44:47],a[160:163]\n"
DT "a[242:243],v24 offset:4640\n"
MF "a[196:199],a[244:247],v[60:63],a[196:199]\n"
MF "a[208:211],a[244:247],v[76:79],a[208:211]\n"
DT "a[244:245],v25 offset:0\n"
DT "a[246:247],v25 offset:4608\n"
WT "lgkmcnt(15)\n"
MF "v[80:83],a[64:67],a[0:3],v[104:107]\n"
EX "v88,v88\n"
MF "v[84:87],a[68:71],a[0:3],v[104:107]\n"
EX "v89,v89\n"
MF "v[80:83],a[72:75],a[4:7],v[80:83]\n"
EX "v90,v90\n"
MF "v[84:87],a[76:79],a[4:7],v[84:87]\n"
EX "v91,v91\n"
MF "v[80:83],a[80:83],a[8:11],v[80:83]\n"
CV "v120,v88,v89\n"
CV "v121,v90,v91\n"
MF "v[84:87],a[84:87],a[8:11],v[84:87]\n"
EX "v92,v92\n"
MF "v[80:83],a[88:91],a[12:15],v[80:83]\n"
EX "v93,v93\n"
MF "v[84:87],a[92:95],a[12:15],v[84:87]\n"
EX "v94,v94\n"
WT "lgkmcnt(0)\n"
MF "v[136:139],a[96:99],v[116:119],v[136:139]\n"
EX "v95,v95\n"
"s_add_u32 m0,s50,s48\n"
MF "v[140:143],a[100:103],v[116:119],v[140:143]\n"
CV "v122,v92,v93\n"
CV "v123,v94,v95\n"
BL "v8,s[12:15],s78 offen nt lds\n"
MF "v[144:147],a[104:107],v[116:119],v[144:147]\n"
BL "v9,s[12:15],s78 offen offset:1056 nt lds\n"
"s_add_u32 m0,m0,8448\n"
MF "v[148:151],a[108:111],v[116:119],v[148:151]\n"
BL "v10,s[16:19],s79 offen nt lds\n"
MF "v[152:155],a[112:115],v[116:119],v[152:155]\n"
BL "v11,s[16:19],s79 offen offset:1056 nt lds\n"
"s_add_u32 s51,s51,8192\n"
"s_mov_b32 s79,s78\n"
"s_xor_b32 s78,s51,s41\n"
MF "v[156:159],a[116:119],v[116:119],v[156:159]\n"
"v_add_u32 v6,s49,v4\n"
"s_mov_b32 s50,s42\n"
"v_max3_f32 v128,v80,v81,v82\n"
"v_max3_f32 v128,v128,v83,v84\n"
MF "v[160:163],a[120:123],v[116:119],v[160:163]\n"
"v_add_u32 v7,s49,v5\n"
"s_mov_b32 s42,s49\n"
"v_max3_f32 v128,v128,v85,v86\n"
"v_max_f32 v128,v128,v87\n"
MF "v[164:167],a[124:127],v[116:119],v[164:167]\n"
"s_add_u32 s49,s49,16896\n"
"s_cmp_eq_u32 s49,84480\n"
"s_cselect_b32 s49,0,s49\n"
"v_cmp_lt_f32 vcc,s53,v128\n"
MF "v[168:171],a[48:51],v[116:119],v[168:171]\n"
"s_cbranch_vccnz Ls188_%=\n"
"Ls189_%=:\n"
MF "v[88:91],a[64:67],a[16:19],v[108:111]\n"
EX "v96,v96\n"
MF "v[92:95],a[68:71],a[16:19],v[108:111]\n"
EX "v97,v97\n"
MF "v[88:91],a[72:75],a[20:23],v[88:91]\n"
EX "v98,v98\n"
MF "v[92:95],a[76:79],a[20:23],v[92:95]\n"
EX "v99,v99\n"
MF "v[88:91],a[80:83],a[24:27],v[88:91]\n"
CV "v124,v96,v97\n"
CV "v125,v98,v99\n"
MF "v[92:95],a[84:87],a[24:27],v[92:95]\n"
EX "v100,v100\n"
MF "v[88:91],a[88:91],a[28:31],v[88:91]\n"
EX "v101,v101\n"
MF "v[92:95],a[92:95],a[28:31],v[92:95]\n"
EX "v102,v102\n"
MF "v[172:175],a[96:99],v[120:123],v[172:175]\n"
EX "v103,v103\n"
MF "v[176:179],a[100:103],v[120:123],v[176:179]\n"
CV "v126,v100,v101\n"
CV "v127,v102,v103\n"
MF "v[180:183],a[104:107],v[120:123],v[180:183]\n"
MF "v[184:187],a[108:111],v[120:123],v[184:187]\n"
MF "v[188:191],a[112:115],v[120:123],v[188:191]\n"
MF "v[192:195],a[116:119],v[120:123],v[192:195]\n"
"v_max3_f32 v128,v88,v89,v90\n"
"v_max3_f32 v128,v128,v91,v92\n"
MF "v[196:199],a[120:123],v[120:123],v[196:199]\n"
"v_max3_f32 v128,v128,v93,v94\n"
"v_max_f32 v128,v128,v95\n"
MF "v[200:203],a[124:127],v[120:123],v[200:203]\n"
"v_cmp_lt_f32 vcc,s53,v128\n"
MF "v[204:207],a[48:51],v[120:123],v[204:207]\n"
"s_cbranch_vccnz Ls190_%=\n"
"Ls191_%=:\n"
WT "vmcnt(24)\n"
"s_barrier\n"
MF "v[96:99],a[64:67],a[32:35],v[112:115]\n"
EX "v80,v80\n"
DR "a[64:67],v6 offset:0\n"
MF "v[100:103],a[68:71],a[32:35],v[112:115]\n"
EX "v81,v81\n"
DR "a[68:71],v6 offset:512\n"
MF "v[96:99],a[72:75],a[36:39],v[96:99]\n"
EX "v82,v82\n"
DR "a[72:75],v6 offset:64\n"
MF "v[100:103],a[76:79],a[36:39],v[100:103]\n"
EX "v83,v83\n"
DR "a[76:79],v6 offset:576\n"
MF "v[96:99],a[80:83],a[40:43],v[96:99]\n"
CV "v116,v80,v81\n"
CV "v117,v82,v83\n"
DR "a[80:83],v6 offset:128\n"
MF "v[100:103],a[84:87],a[40:43],v[100:103]\n"
EX "v84,v84\n"
DR "a[84:87],v6 offset:640\n"
MF "v[96:99],a[88:91],a[44:47],v[96:99]\n"
EX "v85,v85\n"
DR "a[88:91],v6 offset:192\n"
MF "v[100:103],a[92:95],a[44:47],v[100:103]\n"
EX "v86,v86\n"
DR "a[92:95],v6 offset:704\n"
MF "v[208:211],a[96:99],v[124:127],v[208:211]\n"
EX "v87,v87\n"
DT "a[96:97],v7 offset:0\n"
DT "a[98:99],v7 offset:512\n"
MF "v[212:215],a[100:103],v[124:127],v[212:215]\n"
CV "v118,v84,v85\n"
CV "v119,v86,v87\n"
DT "a[100:101],v7 offset:32\n"
DT "a[102:103],v7 offset:544\n"
MF "v[216:219],a[104:107],v[124:127],v[216:219]\n"
DT "a[104:105],v7 offset:64\n"
DT "a[106:107],v7 offset:576\n"
MF "v[220:223],a[108:111],v[124:127],v[220:223]\n"
DT "a[108:109],v7 offset:96\n"
DT "a[110:111],v7 offset:608\n"
MF "v[224:227],a[112:115],v[124:127],v[224:227]\n"
DT "a[112:113],v7 offset:128\n"
DT "a[114:115],v7 offset:640\n"
MF "v[228:231],a[116:119],v[124:127],v[228:231]\n"
DT "a[116:117],v7 offset:160\n"
DT "a[118:119],v7 offset:672\n"
"v_max3_f32 v128,v96,v97,v98\n"
"v_max3_f32 v128,v128,v99,v100\n"
MF "v[232:235],a[120:123],v[124:127],v[232:235]\n"
DT "a[120:121],v7 offset:192\n"
DT "a[122:123],v7 offset:704\n"
"v_max3_f32 v128,v128,v101,v102\n"
"v_max_f32 v128,v128,v103\n"
MF "v[236:239],a[124:127],v[124:127],v[236:239]\n"
DT "a[124:125],v7 offset:224\n"
DT "a[126:127],v7 offset:736\n"
"v_cmp_lt_f32 vcc,s53,v128\n"
MF "v[240:243],a[48:51],v[124:127],v[240:243]\n"
"s_cbranch_vccnz Ls192_%=\n"
"Ls193_%=:\n"
WT "vmcnt(21)\n"
MF "a[128:131],a[212:215],v[32:35],a[128:131]\n"
MF "a[164:167],a[212:215],v[48:51],a[164:167]\n"
"s_add_u32 s44,s57,0\n"
MF "a[200:203],a[212:215],v[64:67],a[200:203]\n"
DT "a[212:213],v21 offset:9216\n"
BL "v[44:47],v244,s[28:31],s44 offen offset:3072 sc1\n"
MF "a[132:135],a[216:219],v[32:35],a[132:135]\n"
DT "a[214:215],v21 offset:13824\n"
MF "a[168:171],a[216:219],v[48:51],a[168:171]\n"
BL "v[60:63],v245,s[28:31],s44 offen offset:3072 sc1\n"
MF "a[204:207],a[216:219],v[64:67],a[204:207]\n"
DT "a[216:217],v21 offset:9248\n"
MF "a[136:139],a[220:223],v[32:35],a[136:139]\n"
DT "a[218:219],v21 offset:13856\n"
BL "v[76:79],v246,s[28:31],s44 offen offset:3072 sc1\n"
MF "a[172:175],a[220:223],v[48:51],a[172:175]\n"
"s_add_u32 m0,s76,84480\n"
"s_add_u32 s67,s70,0\n"
DT "a[220:221],v22 offset:9216\n"
MF "a[140:143],a[224:227],v[32:35],a[140:143]\n"
DT "a[222:223],v22 offset:13824\n"
BL "v12,s[24:27],s67 offen offset:0 lds\n"
MF "a[176:179],a[224:227],v[48:51],a[176:179]\n"
DT "a[224:225],v22 offset:9248\n"
MF "a[144:147],a[228:231],v[32:35],a[144:147]\n"
DT "a[226:227],v22 offset:13856\n"
BL "v13,s[24:27],s67 offen offset:1024 lds\n"
MF "a[180:183],a[228:231],v[48:51],a[180:183]\n"
DT "a[228:229],v23 offset:9216\n"
MF "a[148:151],a[232:235],v[32:35],a[148:151]\n"
DT "a[230:231],v23 offset:13824\n"
BL "v14,s[24:27],s67 offen offset:2048 lds\n"
MF "a[184:187],a[232:235],v[48:51],a[184:187]\n"
"s_add_u32 m0,s76,87552\n"
"s_add_u32 s67,s70,0\n"
DT "a[232:233],v23 offset:9248\n"
MF "a[152:155],a[236:239],v[32:35],a[152:155]\n"
DT "a[234:235],v23 offset:13856\n"
BL "v15,s[24:27],s67 offen offset:0 lds\n"
MF "a[188:191],a[236:239],v[48:51],a[188:191]\n"
DT "a[236:237],v24 offset:9216\n"
MF "a[156:159],a[240:243],v[32:35],a[156:159]\n"
DT "a[238:239],v24 offset:13824\n"
BL "v16,s[24:27],s67 offen offset:1024 lds\n"
MF "a[192:195],a[240:243],v[48:51],a[192:195]\n"
DT "a[240:241],v24 offset:9248\n"
MF "a[160:163],a[244:247],v[32:35],a[160:163]\n"
DT "a[242:243],v24 offset:13856\n"
BL "v17,s[24:27],s67 offen offset:2048 lds\n"
MF "a[196:199],a[244:247],v[48:51],a[196:199]\n"
MF "a[208:211],a[244:247],v[64:67],a[208:211]\n"
DT "a[244:245],v25 offset:9216\n"
DT "a[246:247],v25 offset:13824\n"
WT "lgkmcnt(15)\n"
MF "v[80:83],a[64:67],a[0:3],v[104:107]\n"
EX "v88,v88\n"
MF "v[84:87],a[68:71],a[0:3],v[104:107]\n"
EX "v89,v89\n"
MF "v[80:83],a[72:75],a[4:7],v[80:83]\n"
EX "v90,v90\n"
MF "v[84:87],a[76:79],a[4:7],v[84:87]\n"
EX "v91,v91\n"
MF "v[80:83],a[80:83],a[8:11],v[80:83]\n"
CV "v120,v88,v89\n"
CV "v121,v90,v91\n"
MF "v[84:87],a[84:87],a[8:11],v[84:87]\n"
EX "v92,v92\n"
MF "v[80:83],a[88:91],a[12:15],v[80:83]\n"
EX "v93,v93\n"
MF "v[84:87],a[92:95],a[12:15],v[84:87]\n"
EX "v94,v94\n"
WT "lgkmcnt(0)\n"
MF "v[136:139],a[96:99],v[116:119],v[136:139]\n"
EX "v95,v95\n"
MF "v[140:143],a[100:103],v[116:119],v[140:143]\n"
CV "v122,v92,v93\n"
CV "v123,v94,v95\n"
MF "v[144:147],a[104:107],v[116:119],v[144:147]\n"
"s_add_u32 m0,s50,s48\n"
"s_add_u32 m0,m0,8448\n"
MF "v[148:151],a[108:111],v[116:119],v[148:151]\n"
BL "v10,s[16:19],s79 offen nt lds\n"
MF "v[152:155],a[112:115],v[116:119],v[152:155]\n"
BL "v11,s[16:19],s79 offen offset:1056 nt lds\n"
"s_add_u32 s51,s51,8192\n"
"s_mov_b32 s79,s78\n"
"s_xor_b32 s78,s51,s41\n"
MF "v[156:159],a[116:119],v[116:119],v[156:159]\n"
"v_add_u32 v6,s49,v4\n"
"s_mov_b32 s50,s42\n"
"v_max3_f32 v128,v80,v81,v82\n"
"v_max3_f32 v128,v128,v83,v84\n"
MF "v[160:163],a[120:123],v[116:119],v[160:163]\n"
"v_add_u32 v7,s49,v5\n"
"s_mov_b32 s42,s49\n"
"v_max3_f32 v128,v128,v85,v86\n"
"v_max_f32 v128,v128,v87\n"
MF "v[164:167],a[124:127],v[116:119],v[164:167]\n"
"s_add_u32 s49,s49,16896\n"
"s_cmp_eq_u32 s49,84480\n"
"s_cselect_b32 s49,0,s49\n"
"v_cmp_lt_f32 vcc,s53,v128\n"
MF "v[168:171],a[48:51],v[116:119],v[168:171]\n"
"s_cbranch_vccnz Ls194_%=\n"
"Ls195_%=:\n"
MF "v[88:91],a[64:67],a[16:19],v[108:111]\n"
EX "v96,v96\n"
MF "v[92:95],a[68:71],a[16:19],v[108:111]\n"
EX "v97,v97\n"
MF "v[88:91],a[72:75],a[20:23],v[88:91]\n"
EX "v98,v98\n"
MF "v[92:95],a[76:79],a[20:23],v[92:95]\n"
EX "v99,v99\n"
MF "v[88:91],a[80:83],a[24:27],v[88:91]\n"
CV "v124,v96,v97\n"
CV "v125,v98,v99\n"
MF "v[92:95],a[84:87],a[24:27],v[92:95]\n"
EX "v100,v100\n"
MF "v[88:91],a[88:91],a[28:31],v[88:91]\n"
EX "v101,v101\n"
MF "v[92:95],a[92:95],a[28:31],v[92:95]\n"
EX "v102,v102\n"
MF "v[172:175],a[96:99],v[120:123],v[172:175]\n"
EX "v103,v103\n"
MF "v[176:179],a[100:103],v[120:123],v[176:179]\n"
CV "v126,v100,v101\n"
CV "v127,v102,v103\n"
MF "v[180:183],a[104:107],v[120:123],v[180:183]\n"
MF "v[184:187],a[108:111],v[120:123],v[184:187]\n"
MF "v[188:191],a[112:115],v[120:123],v[188:191]\n"
MF "v[192:195],a[116:119],v[120:123],v[192:195]\n"
"v_max3_f32 v128,v88,v89,v90\n"
"v_max3_f32 v128,v128,v91,v92\n"
MF "v[196:199],a[120:123],v[120:123],v[196:199]\n"
"v_max3_f32 v128,v128,v93,v94\n"
"v_max_f32 v128,v128,v95\n"
MF "v[200:203],a[124:127],v[120:123],v[200:203]\n"
"v_cmp_lt_f32 vcc,s53,v128\n"
MF "v[204:207],a[48:51],v[120:123],v[204:207]\n"
"s_cbranch_vccnz Ls196_%=\n"
"Ls197_%=:\n"
WT "vmcnt(25)\n"
WT "lgkmcnt(0)\n"
"s_barrier\n"
MF "v[96:99],a[64:67],a[32:35],v[112:115]\n"
EX "v80,v80\n"
DR "a[64:67],v6 offset:0\n"
MF "v[100:103],a[68:71],a[32:35],v[112:115]\n"
EX "v81,v81\n"
DR "a[68:71],v6 offset:512\n"
MF "v[96:99],a[72:75],a[36:39],v[96:99]\n"
EX "v82,v82\n"
DR "a[72:75],v6 offset:64\n"
MF "v[100:103],a[76:79],a[36:39],v[100:103]\n"
EX "v83,v83\n"
DR "a[76:79],v6 offset:576\n"
MF "v[96:99],a[80:83],a[40:43],v[96:99]\n"
CV "v116,v80,v81\n"
CV "v117,v82,v83\n"
DR "a[80:83],v6 offset:128\n"
MF "v[100:103],a[84:87],a[40:43],v[100:103]\n"
EX "v84,v84\n"
DR "a[84:87],v6 offset:640\n"
MF "v[96:99],a[88:91],a[44:47],v[96:99]\n"
EX "v85,v85\n"
DR "a[88:91],v6 offset:192\n"
MF "v[100:103],a[92:95],a[44:47],v[100:103]\n"
EX "v86,v86\n"
DR "a[92:95],v6 offset:704\n"
MF "v[208:211],a[96:99],v[124:127],v[208:211]\n"
EX "v87,v87\n"
DT "a[96:97],v7 offset:0\n"
DT "a[98:99],v7 offset:512\n"
MF "v[212:215],a[100:103],v[124:127],v[212:215]\n"
CV "v118,v84,v85\n"
CV "v119,v86,v87\n"
DT "a[100:101],v7 offset:32\n"
DT "a[102:103],v7 offset:544\n"
MF "v[216:219],a[104:107],v[124:127],v[216:219]\n"
DT "a[104:105],v7 offset:64\n"
DT "a[106:107],v7 offset:576\n"
MF "v[220:223],a[108:111],v[124:127],v[220:223]\n"
DT "a[108:109],v7 offset:96\n"
DT "a[110:111],v7 offset:608\n"
MF "v[224:227],a[112:115],v[124:127],v[224:227]\n"
DT "a[112:113],v7 offset:128\n"
DT "a[114:115],v7 offset:640\n"
MF "v[228:231],a[116:119],v[124:127],v[228:231]\n"
DT "a[116:117],v7 offset:160\n"
DT "a[118:119],v7 offset:672\n"
"v_max3_f32 v128,v96,v97,v98\n"
"v_max3_f32 v128,v128,v99,v100\n"
MF "v[232:235],a[120:123],v[124:127],v[232:235]\n"
DT "a[120:121],v7 offset:192\n"
DT "a[122:123],v7 offset:704\n"
"v_max3_f32 v128,v128,v101,v102\n"
"v_max_f32 v128,v128,v103\n"
MF "v[236:239],a[124:127],v[124:127],v[236:239]\n"
DT "a[124:125],v7 offset:224\n"
DT "a[126:127],v7 offset:736\n"
"v_cmp_lt_f32 vcc,s53,v128\n"
MF "v[240:243],a[48:51],v[124:127],v[240:243]\n"
"s_cbranch_vccnz Ls198_%=\n"
"Ls199_%=:\n"
WT "vmcnt(22)\n"
MF "a[128:131],a[212:215],v[36:39],a[128:131]\n"
MF "a[164:167],a[212:215],v[52:55],a[164:167]\n"
"s_add_u32 s44,s58,0\n"
MF "a[200:203],a[212:215],v[68:71],a[200:203]\n"
DT "a[212:213],v21 offset:18432\n"
BL "v[32:35],v244,s[28:31],s44 offen offset:0 sc1\n"
MF "a[132:135],a[216:219],v[36:39],a[132:135]\n"
DT "a[214:215],v21 offset:23040\n"
MF "a[168:171],a[216:219],v[52:55],a[168:171]\n"
MF "a[204:207],a[216:219],v[68:71],a[204:207]\n"
DT "a[216:217],v21 offset:18464\n"
BL "v[48:51],v245,s[28:31],s44 offen offset:0 sc1\n"
MF "a[136:139],a[220:223],v[36:39],a[136:139]\n"
DT "a[218:219],v21 offset:23072\n"
MF "a[172:175],a[220:223],v[52:55],a[172:175]\n"
DT "a[220:221],v22 offset:18432\n"
MF "a[140:143],a[224:227],v[36:39],a[140:143]\n"
DT "a[222:223],v22 offset:23040\n"
BL "v[64:67],v246,s[28:31],s44 offen offset:0 sc1\n"
MF "a[176:179],a[224:227],v[52:55],a[176:179]\n"
"s_add_u32 m0,s76,90624\n"
DT "a[224:225],v22 offset:18464\n"
MF "a[144:147],a[228:231],v[36:39],a[144:147]\n"
"s_add_u32 s67,s70,0\n"
DT "a[226:227],v22 offset:23072\n"
MF "a[180:183],a[228:231],v[52:55],a[180:183]\n"
DT "a[228:229],v23 offset:18432\n"
BL "v18,s[24:27],s67 offen offset:0 lds\n"
MF "a[148:151],a[232:235],v[36:39],a[148:151]\n"
DT "a[230:231],v23 offset:23040\n"
MF "a[184:187],a[232:235],v[52:55],a[184:187]\n"
DT "a[232:233],v23 offset:18464\n"
MF "a[152:155],a[236:239],v[36:39],a[152:155]\n"
DT "a[234:235],v23 offset:23072\n"
BL "v19,s[24:27],s67 offen offset:1024 lds\n"
MF "a[188:191],a[236:239],v[52:55],a[188:191]\n"
DT "a[236:237],v24 offset:18432\n"
MF "a[156:159],a[240:243],v[36:39],a[156:159]\n"
DT "a[238:239],v24 offset:23040\n"
MF "a[192:195],a[240:243],v[52:55],a[192:195]\n"
DT "a[240:241],v24 offset:18464\n"
BL "v20,s[24:27],s67 offen offset:2048 lds\n"
MF "a[160:163],a[244:247],v[36:39],a[160:163]\n"
DT "a[242:243],v24 offset:23072\n"
MF "a[196:199],a[244:247],v[52:55],a[196:199]\n"
MF "a[208:211],a[244:247],v[68:71],a[208:211]\n"
DT "a[244:245],v25 offset:18432\n"
DT "a[246:247],v25 offset:23040\n"
WT "lgkmcnt(15)\n"
MF "v[80:83],a[64:67],a[0:3],v[104:107]\n"
EX "v88,v88\n"
MF "v[84:87],a[68:71],a[0:3],v[104:107]\n"
EX "v89,v89\n"
MF "v[80:83],a[72:75],a[4:7],v[80:83]\n"
EX "v90,v90\n"
MF "v[84:87],a[76:79],a[4:7],v[84:87]\n"
EX "v91,v91\n"
MF "v[80:83],a[80:83],a[8:11],v[80:83]\n"
CV "v120,v88,v89\n"
CV "v121,v90,v91\n"
MF "v[84:87],a[84:87],a[8:11],v[84:87]\n"
EX "v92,v92\n"
MF "v[80:83],a[88:91],a[12:15],v[80:83]\n"
EX "v93,v93\n"
MF "v[84:87],a[92:95],a[12:15],v[84:87]\n"
EX "v94,v94\n"
WT "lgkmcnt(0)\n"
MF "v[136:139],a[96:99],v[116:119],v[136:139]\n"
EX "v95,v95\n"
MF "v[140:143],a[100:103],v[116:119],v[140:143]\n"
CV "v122,v92,v93\n"
CV "v123,v94,v95\n"
MF "v[144:147],a[104:107],v[116:119],v[144:147]\n"
"v_add_u32 v6,s49,v4\n"
"s_mov_b32 s50,s42\n"
MF "v[148:151],a[108:111],v[116:119],v[148:151]\n"
MF "v[152:155],a[112:115],v[116:119],v[152:155]\n"
"v_add_u32 v7,s49,v5\n"
"s_mov_b32 s42,s49\n"
MF "v[156:159],a[116:119],v[116:119],v[156:159]\n"
"v_max3_f32 v128,v80,v81,v82\n"
"v_max3_f32 v128,v128,v83,v84\n"
MF "v[160:163],a[120:123],v[116:119],v[160:163]\n"
"s_add_u32 s49,s49,16896\n"
"s_cmp_eq_u32 s49,84480\n"
"s_cselect_b32 s49,0,s49\n"
"v_max3_f32 v128,v128,v85,v86\n"
"v_max_f32 v128,v128,v87\n"
MF "v[164:167],a[124:127],v[116:119],v[164:167]\n"
"v_cmp_lt_f32 vcc,s53,v128\n"
MF "v[168:171],a[48:51],v[116:119],v[168:171]\n"
"s_cbranch_vccnz Ls200_%=\n"
"Ls201_%=:\n"
MF "v[88:91],a[64:67],a[16:19],v[108:111]\n"
EX "v96,v96\n"
MF "v[92:95],a[68:71],a[16:19],v[108:111]\n"
EX "v97,v97\n"
MF "v[88:91],a[72:75],a[20:23],v[88:91]\n"
EX "v98,v98\n"
MF "v[92:95],a[76:79],a[20:23],v[92:95]\n"
EX "v99,v99\n"
MF "v[88:91],a[80:83],a[24:27],v[88:91]\n"
CV "v124,v96,v97\n"
CV "v125,v98,v99\n"
MF "v[92:95],a[84:87],a[24:27],v[92:95]\n"
EX "v100,v100\n"
MF "v[88:91],a[88:91],a[28:31],v[88:91]\n"
EX "v101,v101\n"
MF "v[92:95],a[92:95],a[28:31],v[92:95]\n"
EX "v102,v102\n"
MF "v[172:175],a[96:99],v[120:123],v[172:175]\n"
EX "v103,v103\n"
MF "v[176:179],a[100:103],v[120:123],v[176:179]\n"
CV "v126,v100,v101\n"
CV "v127,v102,v103\n"
MF "v[180:183],a[104:107],v[120:123],v[180:183]\n"
MF "v[184:187],a[108:111],v[120:123],v[184:187]\n"
MF "v[188:191],a[112:115],v[120:123],v[188:191]\n"
MF "v[192:195],a[116:119],v[120:123],v[192:195]\n"
"v_max3_f32 v128,v88,v89,v90\n"
"v_max3_f32 v128,v128,v91,v92\n"
MF "v[196:199],a[120:123],v[120:123],v[196:199]\n"
"v_max3_f32 v128,v128,v93,v94\n"
"v_max_f32 v128,v128,v95\n"
MF "v[200:203],a[124:127],v[120:123],v[200:203]\n"
"v_cmp_lt_f32 vcc,s53,v128\n"
MF "v[204:207],a[48:51],v[120:123],v[204:207]\n"
"s_cbranch_vccnz Ls202_%=\n"
"Ls203_%=:\n"
WT "vmcnt(24)\n"
"s_barrier\n"
MF "v[96:99],a[64:67],a[32:35],v[112:115]\n"
EX "v80,v80\n"
DR "a[64:67],v6 offset:0\n"
MF "v[100:103],a[68:71],a[32:35],v[112:115]\n"
EX "v81,v81\n"
DR "a[68:71],v6 offset:512\n"
MF "v[96:99],a[72:75],a[36:39],v[96:99]\n"
EX "v82,v82\n"
DR "a[72:75],v6 offset:64\n"
MF "v[100:103],a[76:79],a[36:39],v[100:103]\n"
EX "v83,v83\n"
DR "a[76:79],v6 offset:576\n"
MF "v[96:99],a[80:83],a[40:43],v[96:99]\n"
CV "v116,v80,v81\n"
CV "v117,v82,v83\n"
DR "a[80:83],v6 offset:128\n"
MF "v[100:103],a[84:87],a[40:43],v[100:103]\n"
EX "v84,v84\n"
DR "a[84:87],v6 offset:640\n"
MF "v[96:99],a[88:91],a[44:47],v[96:99]\n"
EX "v85,v85\n"
DR "a[88:91],v6 offset:192\n"
MF "v[100:103],a[92:95],a[44:47],v[100:103]\n"
EX "v86,v86\n"
DR "a[92:95],v6 offset:704\n"
MF "v[208:211],a[96:99],v[124:127],v[208:211]\n"
EX "v87,v87\n"
DT "a[96:97],v7 offset:0\n"
DT "a[98:99],v7 offset:512\n"
MF "v[212:215],a[100:103],v[124:127],v[212:215]\n"
CV "v118,v84,v85\n"
CV "v119,v86,v87\n"
DT "a[100:101],v7 offset:32\n"
DT "a[102:103],v7 offset:544\n"
MF "v[216:219],a[104:107],v[124:127],v[216:219]\n"
DT "a[104:105],v7 offset:64\n"
DT "a[106:107],v7 offset:576\n"
MF "v[220:223],a[108:111],v[124:127],v[220:223]\n"
DT "a[108:109],v7 offset:96\n"
DT "a[110:111],v7 offset:608\n"
MF "v[224:227],a[112:115],v[124:127],v[224:227]\n"
DT "a[112:113],v7 offset:128\n"
DT "a[114:115],v7 offset:640\n"
MF "v[228:231],a[116:119],v[124:127],v[228:231]\n"
DT "a[116:117],v7 offset:160\n"
DT "a[118:119],v7 offset:672\n"
"v_max3_f32 v128,v96,v97,v98\n"
"v_max3_f32 v128,v128,v99,v100\n"
MF "v[232:235],a[120:123],v[124:127],v[232:235]\n"
DT "a[120:121],v7 offset:192\n"
DT "a[122:123],v7 offset:704\n"
"v_max3_f32 v128,v128,v101,v102\n"
"v_max_f32 v128,v128,v103\n"
MF "v[236:239],a[124:127],v[124:127],v[236:239]\n"
DT "a[124:125],v7 offset:224\n"
DT "a[126:127],v7 offset:736\n"
"v_cmp_lt_f32 vcc,s53,v128\n"
MF "v[240:243],a[48:51],v[124:127],v[240:243]\n"
"s_cbranch_vccnz Ls204_%=\n"
"Ls205_%=:\n"
WT "vmcnt(21)\n"
MF "a[128:131],a[212:215],v[40:43],a[128:131]\n"
MF "a[164:167],a[212:215],v[56:59],a[164:167]\n"
"s_add_u32 s44,s58,0\n"
MF "a[200:203],a[212:215],v[72:75],a[200:203]\n"
DT "a[212:213],v21 offset:27648\n"
BL "v[36:39],v244,s[28:31],s44 offen offset:1024 sc1\n"
MF "a[132:135],a[216:219],v[40:43],a[132:135]\n"
DT "a[214:215],v21 offset:32256\n"
MF "a[168:171],a[216:219],v[56:59],a[168:171]\n"
MF "a[204:207],a[216:219],v[72:75],a[204:207]\n"
DT "a[216:217],v21 offset:27680\n"
MF "a[136:139],a[220:223],v[40:43],a[136:139]\n"
DT "a[218:219],v21 offset:32288\n"
"v_subrev_u32 v21,36864,v21\n"
MF "a[172:175],a[220:223],v[56:59],a[172:175]\n"
DT "a[220:221],v22 offset:27648\n"
MF "a[140:143],a[224:227],v[40:43],a[140:143]\n"
DT "a[222:223],v22 offset:32256\n"
BL "v[52:55],v245,s[28:31],s44 offen offset:1024 sc1\n"
MF "a[176:179],a[224:227],v[56:59],a[176:179]\n"
DT "a[224:225],v22 offset:27680\n"
MF "a[144:147],a[228:231],v[40:43],a[144:147]\n"
DT "a[226:227],v22 offset:32288\n"
"v_subrev_u32 v22,36864,v22\n"
MF "a[180:183],a[228:231],v[56:59],a[180:183]\n"
DT "a[228:229],v23 offset:27648\n"
MF "a[148:151],a[232:235],v[40:43],a[148:151]\n"
DT "a[230:231],v23 offset:32256\n"
MF "a[184:187],a[232:235],v[56:59],a[184:187]\n"
DT "a[232:233],v23 offset:27680\n"
MF "a[152:155],a[236:239],v[40:43],a[152:155]\n"
DT "a[234:235],v23 offset:32288\n"
BL "v[68:71],v246,s[28:31],s44 offen offset:1024 sc1\n"
"v_subrev_u32 v23,36864,v23\n"
MF "a[188:191],a[236:239],v[56:59],a[188:191]\n"
DT "a[236:237],v24 offset:27648\n"
MF "a[156:159],a[240:243],v[40:43],a[156:159]\n"
DT "a[238:239],v24 offset:32256\n"
MF "a[192:195],a[240:243],v[56:59],a[192:195]\n"
DT "a[240:241],v24 offset:27680\n"
MF "a[160:163],a[244:247],v[40:43],a[160:163]\n"
DT "a[242:243],v24 offset:32288\n"
"v_subrev_u32 v24,36864,v24\n"
MF "a[196:199],a[244:247],v[56:59],a[196:199]\n"
MF "a[208:211],a[244:247],v[72:75],a[208:211]\n"
DT "a[244:245],v25 offset:27648\n"
DT "a[246:247],v25 offset:32256\n"
"v_subrev_u32 v25,36864,v25\n"
WT "lgkmcnt(15)\n"
MF "v[80:83],a[64:67],a[0:3],v[104:107]\n"
EX "v88,v88\n"
MF "v[84:87],a[68:71],a[0:3],v[104:107]\n"
EX "v89,v89\n"
MF "v[80:83],a[72:75],a[4:7],v[80:83]\n"
EX "v90,v90\n"
MF "v[84:87],a[76:79],a[4:7],v[84:87]\n"
EX "v91,v91\n"
MF "v[80:83],a[80:83],a[8:11],v[80:83]\n"
CV "v120,v88,v89\n"
CV "v121,v90,v91\n"
MF "v[84:87],a[84:87],a[8:11],v[84:87]\n"
EX "v92,v92\n"
MF "v[80:83],a[88:91],a[12:15],v[80:83]\n"
EX "v93,v93\n"
MF "v[84:87],a[92:95],a[12:15],v[84:87]\n"
EX "v94,v94\n"
WT "lgkmcnt(0)\n"
MF "v[136:139],a[96:99],v[116:119],v[136:139]\n"
EX "v95,v95\n"
MF "v[140:143],a[100:103],v[116:119],v[140:143]\n"
CV "v122,v92,v93\n"
CV "v123,v94,v95\n"
MF "v[144:147],a[104:107],v[116:119],v[144:147]\n"
"v_add_u32 v6,s49,v4\n"
"s_mov_b32 s50,s42\n"
MF "v[148:151],a[108:111],v[116:119],v[148:151]\n"
MF "v[152:155],a[112:115],v[116:119],v[152:155]\n"
"v_add_u32 v7,s49,v5\n"
"s_mov_b32 s42,s49\n"
MF "v[156:159],a[116:119],v[116:119],v[156:159]\n"
"v_max3_f32 v128,v80,v81,v82\n"
"v_max3_f32 v128,v128,v83,v84\n"
MF "v[160:163],a[120:123],v[116:119],v[160:163]\n"
"s_add_u32 s49,s49,16896\n"
"s_cmp_eq_u32 s49,84480\n"
"s_cselect_b32 s49,0,s49\n"
"v_max3_f32 v128,v128,v85,v86\n"
"v_max_f32 v128,v128,v87\n"
MF "v[164:167],a[124:127],v[116:119],v[164:167]\n"
"v_cmp_lt_f32 vcc,s53,v128\n"
MF "v[168:171],a[48:51],v[116:119],v[168:171]\n"
"s_cbranch_vccnz Ls206_%=\n"
"Ls207_%=:\n"
MF "v[88:91],a[64:67],a[16:19],v[108:111]\n"
EX "v96,v96\n"
MF "v[92:95],a[68:71],a[16:19],v[108:111]\n"
EX "v97,v97\n"
MF "v[88:91],a[72:75],a[20:23],v[88:91]\n"
EX "v98,v98\n"
MF "v[92:95],a[76:79],a[20:23],v[92:95]\n"
EX "v99,v99\n"
MF "v[88:91],a[80:83],a[24:27],v[88:91]\n"
CV "v124,v96,v97\n"
CV "v125,v98,v99\n"
MF "v[92:95],a[84:87],a[24:27],v[92:95]\n"
EX "v100,v100\n"
MF "v[88:91],a[88:91],a[28:31],v[88:91]\n"
EX "v101,v101\n"
MF "v[92:95],a[92:95],a[28:31],v[92:95]\n"
EX "v102,v102\n"
MF "v[172:175],a[96:99],v[120:123],v[172:175]\n"
EX "v103,v103\n"
MF "v[176:179],a[100:103],v[120:123],v[176:179]\n"
CV "v126,v100,v101\n"
CV "v127,v102,v103\n"
MF "v[180:183],a[104:107],v[120:123],v[180:183]\n"
MF "v[184:187],a[108:111],v[120:123],v[184:187]\n"
MF "v[188:191],a[112:115],v[120:123],v[188:191]\n"
MF "v[192:195],a[116:119],v[120:123],v[192:195]\n"
"v_max3_f32 v128,v88,v89,v90\n"
"v_max3_f32 v128,v128,v91,v92\n"
MF "v[196:199],a[120:123],v[120:123],v[196:199]\n"
"v_max3_f32 v128,v128,v93,v94\n"
"v_max_f32 v128,v128,v95\n"
MF "v[200:203],a[124:127],v[120:123],v[200:203]\n"
"v_cmp_lt_f32 vcc,s53,v128\n"
MF "v[204:207],a[48:51],v[120:123],v[204:207]\n"
"s_cbranch_vccnz Ls208_%=\n"
"Ls209_%=:\n"
WT "vmcnt(20)\n"
WT "vmcnt(3)\n"
"s_barrier\n"
MF "v[96:99],a[64:67],a[32:35],v[112:115]\n"
EX "v80,v80\n"
DR "a[64:67],v6 offset:0\n"
MF "v[100:103],a[68:71],a[32:35],v[112:115]\n"
EX "v81,v81\n"
DR "a[68:71],v6 offset:512\n"
MF "v[96:99],a[72:75],a[36:39],v[96:99]\n"
EX "v82,v82\n"
DR "a[72:75],v6 offset:64\n"
MF "v[100:103],a[76:79],a[36:39],v[100:103]\n"
EX "v83,v83\n"
DR "a[76:79],v6 offset:576\n"
MF "v[96:99],a[80:83],a[40:43],v[96:99]\n"
CV "v116,v80,v81\n"
CV "v117,v82,v83\n"
DR "a[80:83],v6 offset:128\n"
MF "v[100:103],a[84:87],a[40:43],v[100:103]\n"
EX "v84,v84\n"
DR "a[84:87],v6 offset:640\n"
MF "v[96:99],a[88:91],a[44:47],v[96:99]\n"
EX "v85,v85\n"
DR "a[88:91],v6 offset:192\n"
MF "v[100:103],a[92:95],a[44:47],v[100:103]\n"
EX "v86,v86\n"
DR "a[92:95],v6 offset:704\n"
MF "v[208:211],a[96:99],v[124:127],v[208:211]\n"
EX "v87,v87\n"
DT "a[96:97],v7 offset:0\n"
DT "a[98:99],v7 offset:512\n"
MF "v[212:215],a[100:103],v[124:127],v[212:215]\n"
CV "v118,v84,v85\n"
CV "v119,v86,v87\n"
DT "a[100:101],v7 offset:32\n"
DT "a[102:103],v7 offset:544\n"
MF "v[216:219],a[104:107],v[124:127],v[216:219]\n"
DT "a[104:105],v7 offset:64\n"
DT "a[106:107],v7 offset:576\n"
MF "v[220:223],a[108:111],v[124:127],v[220:223]\n"
DT "a[108:109],v7 offset:96\n"
DT "a[110:111],v7 offset:608\n"
MF "v[224:227],a[112:115],v[124:127],v[224:227]\n"
DT "a[112:113],v7 offset:128\n"
DT "a[114:115],v7 offset:640\n"
MF "v[228:231],a[116:119],v[124:127],v[228:231]\n"
DT "a[116:117],v7 offset:160\n"
DT "a[118:119],v7 offset:672\n"
"v_max3_f32 v128,v96,v97,v98\n"
"v_max3_f32 v128,v128,v99,v100\n"
MF "v[232:235],a[120:123],v[124:127],v[232:235]\n"
DT "a[120:121],v7 offset:192\n"
DT "a[122:123],v7 offset:704\n"
"v_max3_f32 v128,v128,v101,v102\n"
"v_max_f32 v128,v128,v103\n"
MF "v[236:239],a[124:127],v[124:127],v[236:239]\n"
DT "a[124:125],v7 offset:224\n"
DT "a[126:127],v7 offset:736\n"
"v_cmp_lt_f32 vcc,s53,v128\n"
MF "v[240:243],a[48:51],v[124:127],v[240:243]\n"
"s_cbranch_vccnz Ls210_%=\n"
"Ls211_%=:\n"
WT "vmcnt(17)\n"
MF "a[128:131],a[212:215],v[44:47],a[128:131]\n"
MF "a[164:167],a[212:215],v[60:63],a[164:167]\n"
"s_add_u32 s44,s58,0\n"
MF "a[200:203],a[212:215],v[76:79],a[200:203]\n"
DT "a[212:213],v21 offset:0\n"
BL "v[40:43],v244,s[28:31],s44 offen offset:2048 sc1\n"
MF "a[132:135],a[216:219],v[44:47],a[132:135]\n"
DT "a[214:215],v21 offset:4608\n"
MF "a[168:171],a[216:219],v[60:63],a[168:171]\n"
MF "a[204:207],a[216:219],v[76:79],a[204:207]\n"
DT "a[216:217],v21 offset:32\n"
MF "a[136:139],a[220:223],v[44:47],a[136:139]\n"
DT "a[218:219],v21 offset:4640\n"
MF "a[172:175],a[220:223],v[60:63],a[172:175]\n"
DT "a[220:221],v22 offset:0\n"
MF "a[140:143],a[224:227],v[44:47],a[140:143]\n"
DT "a[222:223],v22 offset:4608\n"
BL "v[56:59],v245,s[28:31],s44 offen offset:2048 sc1\n"
MF "a[176:179],a[224:227],v[60:63],a[176:179]\n"
DT "a[224:225],v22 offset:32\n"
MF "a[144:147],a[228:231],v[44:47],a[144:147]\n"
DT "a[226:227],v22 offset:4640\n"
MF "a[180:183],a[228:231],v[60:63],a[180:183]\n"
DT "a[228:229],v23 offset:0\n"
MF "a[148:151],a[232:235],v[44:47],a[148:151]\n"
DT "a[230:231],v23 offset:4608\n"
MF "a[184:187],a[232:235],v[60:63],a[184:187]\n"
DT "a[232:233],v23 offset:32\n"
MF "a[152:155],a[236:239],v[44:47],a[152:155]\n"
DT "a[234:235],v23 offset:4640\n"
BL "v[72:75],v246,s[28:31],s44 offen offset:2048 sc1\n"
MF "a[188:191],a[236:239],v[60:63],a[188:191]\n"
DT "a[236:237],v24 offset:0\n"
MF "a[156:159],a[240:243],v[44:47],a[156:159]\n"
DT "a[238:239],v24 offset:4608\n"
MF "a[192:195],a[240:243],v[60:63],a[192:195]\n"
DT "a[240:241],v24 offset:32\n"
MF "a[160:163],a[244:247],v[44:47],a[160:163]\n"
DT "a[242:243],v24 offset:4640\n"
MF "a[196:199],a[244:247],v[60:63],a[196:199]\n"
MF "a[208:211],a[244:247],v[76:79],a[208:211]\n"
DT "a[244:245],v25 offset:0\n"
DT "a[246:247],v25 offset:4608\n"
WT "lgkmcnt(15)\n"
MF "v[80:83],a[64:67],a[0:3],v[104:107]\n"
EX "v88,v88\n"
MF "v[84:87],a[68:71],a[0:3],v[104:107]\n"
EX "v89,v89\n"
"s_add_u32 s44,s58,0\n"
BL "v[44:47],v244,s[28:31],s44 offen offset:3072 sc1\n"
MF "v[80:83],a[72:75],a[4:7],v[80:83]\n"
EX "v90,v90\n"
BL "v[60:63],v245,s[28:31],s44 offen offset:3072 sc1\n"
MF "v[84:87],a[76:79],a[4:7],v[84:87]\n"
EX "v91,v91\n"
BL "v[76:79],v246,s[28:31],s44 offen offset:3072 sc1\n"
MF "v[80:83],a[80:83],a[8:11],v[80:83]\n"
CV "v120,v88,v89\n"
CV "v121,v90,v91\n"
"s_add_u32 m0,s76,121344\n"
"s_add_u32 s67,s68,147456\n"
BL "v12,s[24:27],s67 offen offset:0 lds\n"
MF "v[84:87],a[84:87],a[8:11],v[84:87]\n"
EX "v92,v92\n"
BL "v13,s[24:27],s67 offen offset:1024 lds\n"
MF "v[80:83],a[88:91],a[12:15],v[80:83]\n"
EX "v93,v93\n"
BL "v14,s[24:27],s67 offen offset:2048 lds\n"
MF "v[84:87],a[92:95],a[12:15],v[84:87]\n"
EX "v94,v94\n"
WT "lgkmcnt(0)\n"
"s_add_u32 m0,s76,124416\n"
"s_add_u32 s67,s68,147456\n"
BL "v15,s[24:27],s67 offen offset:0 lds\n"
MF "v[136:139],a[96:99],v[116:119],v[136:139]\n"
EX "v95,v95\n"
BL "v16,s[24:27],s67 offen offset:1024 lds\n"
MF "v[140:143],a[100:103],v[116:119],v[140:143]\n"
CV "v122,v92,v93\n"
CV "v123,v94,v95\n"
BL "v17,s[24:27],s67 offen offset:2048 lds\n"
MF "v[144:147],a[104:107],v[116:119],v[144:147]\n"
"s_add_u32 m0,s76,127488\n"
"s_add_u32 s67,s68,147456\n"
BL "v18,s[24:27],s67 offen offset:0 lds\n"
MF "v[148:151],a[108:111],v[116:119],v[148:151]\n"
BL "v19,s[24:27],s67 offen offset:1024 lds\n"
MF "v[152:155],a[112:115],v[116:119],v[152:155]\n"
BL "v20,s[24:27],s67 offen offset:2048 lds\n"
MF "v[156:159],a[116:119],v[116:119],v[156:159]\n"
"v_add_u32 v6,s49,v4\n"
"s_mov_b32 s50,s42\n"
"v_max3_f32 v128,v80,v81,v82\n"
"v_max3_f32 v128,v128,v83,v84\n"
MF "v[160:163],a[120:123],v[116:119],v[160:163]\n"
"v_add_u32 v7,s49,v5\n"
"s_mov_b32 s42,s49\n"
"v_max3_f32 v128,v128,v85,v86\n"
"v_max_f32 v128,v128,v87\n"
MF "v[164:167],a[124:127],v[116:119],v[164:167]\n"
"s_add_u32 s49,s49,16896\n"
"s_cmp_eq_u32 s49,84480\n"
"s_cselect_b32 s49,0,s49\n"
"v_cmp_lt_f32 vcc,s53,v128\n"
MF "v[168:171],a[48:51],v[116:119],v[168:171]\n"
"s_cbranch_vccnz Ls212_%=\n"
"Ls213_%=:\n"
MF "v[88:91],a[64:67],a[16:19],v[108:111]\n"
EX "v96,v96\n"
MF "v[92:95],a[68:71],a[16:19],v[108:111]\n"
EX "v97,v97\n"
MF "v[88:91],a[72:75],a[20:23],v[88:91]\n"
EX "v98,v98\n"
MF "v[92:95],a[76:79],a[20:23],v[92:95]\n"
EX "v99,v99\n"
MF "v[88:91],a[80:83],a[24:27],v[88:91]\n"
CV "v124,v96,v97\n"
CV "v125,v98,v99\n"
MF "v[92:95],a[84:87],a[24:27],v[92:95]\n"
EX "v100,v100\n"
MF "v[88:91],a[88:91],a[28:31],v[88:91]\n"
EX "v101,v101\n"
MF "v[92:95],a[92:95],a[28:31],v[92:95]\n"
EX "v102,v102\n"
MF "v[172:175],a[96:99],v[120:123],v[172:175]\n"
EX "v103,v103\n"
MF "v[176:179],a[100:103],v[120:123],v[176:179]\n"
CV "v126,v100,v101\n"
CV "v127,v102,v103\n"
MF "v[180:183],a[104:107],v[120:123],v[180:183]\n"
MF "v[184:187],a[108:111],v[120:123],v[184:187]\n"
MF "v[188:191],a[112:115],v[120:123],v[188:191]\n"
MF "v[192:195],a[116:119],v[120:123],v[192:195]\n"
"v_max3_f32 v128,v88,v89,v90\n"
"v_max3_f32 v128,v128,v91,v92\n"
MF "v[196:199],a[120:123],v[120:123],v[196:199]\n"
"v_max3_f32 v128,v128,v93,v94\n"
"v_max_f32 v128,v128,v95\n"
MF "v[200:203],a[124:127],v[120:123],v[200:203]\n"
"v_cmp_lt_f32 vcc,s53,v128\n"
MF "v[204:207],a[48:51],v[120:123],v[204:207]\n"
"s_cbranch_vccnz Ls214_%=\n"
"Ls215_%=:\n"
WT "vmcnt(24)\n"
"s_barrier\n"
MF "v[96:99],a[64:67],a[32:35],v[112:115]\n"
EX "v80,v80\n"
MF "v[100:103],a[68:71],a[32:35],v[112:115]\n"
EX "v81,v81\n"
MF "v[96:99],a[72:75],a[36:39],v[96:99]\n"
EX "v82,v82\n"
MF "v[100:103],a[76:79],a[36:39],v[100:103]\n"
EX "v83,v83\n"
MF "v[96:99],a[80:83],a[40:43],v[96:99]\n"
CV "v116,v80,v81\n"
CV "v117,v82,v83\n"
MF "v[100:103],a[84:87],a[40:43],v[100:103]\n"
EX "v84,v84\n"
MF "v[96:99],a[88:91],a[44:47],v[96:99]\n"
EX "v85,v85\n"
MF "v[100:103],a[92:95],a[44:47],v[100:103]\n"
EX "v86,v86\n"
MF "v[208:211],a[96:99],v[124:127],v[208:211]\n"
EX "v87,v87\n"
DT "a[96:97],v7 offset:0\n"
DT "a[98:99],v7 offset:512\n"
MF "v[212:215],a[100:103],v[124:127],v[212:215]\n"
CV "v118,v84,v85\n"
CV "v119,v86,v87\n"
DT "a[100:101],v7 offset:32\n"
DT "a[102:103],v7 offset:544\n"
MF "v[216:219],a[104:107],v[124:127],v[216:219]\n"
DT "a[104:105],v7 offset:64\n"
DT "a[106:107],v7 offset:576\n"
MF "v[220:223],a[108:111],v[124:127],v[220:223]\n"
DT "a[108:109],v7 offset:96\n"
DT "a[110:111],v7 offset:608\n"
MF "v[224:227],a[112:115],v[124:127],v[224:227]\n"
DT "a[112:113],v7 offset:128\n"
DT "a[114:115],v7 offset:640\n"
MF "v[228:231],a[116:119],v[124:127],v[228:231]\n"
DT "a[116:117],v7 offset:160\n"
DT "a[118:119],v7 offset:672\n"
"v_max3_f32 v128,v96,v97,v98\n"
"v_max3_f32 v128,v128,v99,v100\n"
MF "v[232:235],a[120:123],v[124:127],v[232:235]\n"
DT "a[120:121],v7 offset:192\n"
DT "a[122:123],v7 offset:704\n"
"v_max3_f32 v128,v128,v101,v102\n"
"v_max_f32 v128,v128,v103\n"
MF "v[236:239],a[124:127],v[124:127],v[236:239]\n"
DT "a[124:125],v7 offset:224\n"
DT "a[126:127],v7 offset:736\n"
"v_cmp_lt_f32 vcc,s53,v128\n"
MF "v[240:243],a[48:51],v[124:127],v[240:243]\n"
"s_cbranch_vccnz Ls216_%=\n"
"Ls217_%=:\n"
WT "lgkmcnt(0)\n"
MF "v[136:139],a[96:99],v[116:119],v[136:139]\n"
EX "v88,v88\n"
MF "v[140:143],a[100:103],v[116:119],v[140:143]\n"
EX "v89,v89\n"
MF "v[144:147],a[104:107],v[116:119],v[144:147]\n"
EX "v90,v90\n"
MF "v[148:151],a[108:111],v[116:119],v[148:151]\n"
EX "v91,v91\n"
MF "v[152:155],a[112:115],v[116:119],v[152:155]\n"
CV "v120,v88,v89\n"
CV "v121,v90,v91\n"
MF "v[156:159],a[116:119],v[116:119],v[156:159]\n"
EX "v92,v92\n"
MF "v[160:163],a[120:123],v[116:119],v[160:163]\n"
EX "v93,v93\n"
MF "v[164:167],a[124:127],v[116:119],v[164:167]\n"
EX "v94,v94\n"
MF "v[168:171],a[48:51],v[116:119],v[168:171]\n"
EX "v95,v95\n"
CV "v122,v92,v93\n"
CV "v123,v94,v95\n"
"s_nop 1\n"
MF "v[172:175],a[96:99],v[120:123],v[172:175]\n"
EX "v96,v96\n"
MF "v[176:179],a[100:103],v[120:123],v[176:179]\n"
EX "v97,v97\n"
MF "v[180:183],a[104:107],v[120:123],v[180:183]\n"
EX "v98,v98\n"
MF "v[184:187],a[108:111],v[120:123],v[184:187]\n"
EX "v99,v99\n"
MF "v[188:191],a[112:115],v[120:123],v[188:191]\n"
CV "v124,v96,v97\n"
CV "v125,v98,v99\n"
MF "v[192:195],a[116:119],v[120:123],v[192:195]\n"
EX "v100,v100\n"
MF "v[196:199],a[120:123],v[120:123],v[196:199]\n"
EX "v101,v101\n"
MF "v[200:203],a[124:127],v[120:123],v[200:203]\n"
EX "v102,v102\n"
MF "v[204:207],a[48:51],v[120:123],v[204:207]\n"
EX "v103,v103\n"
CV "v126,v100,v101\n"
CV "v127,v102,v103\n"
"s_nop 1\n"
WT "vmcnt(24)\n"
"s_barrier\n"
MF "v[208:211],a[96:99],v[124:127],v[208:211]\n"
MF "v[212:215],a[100:103],v[124:127],v[212:215]\n"
MF "v[216:219],a[104:107],v[124:127],v[216:219]\n"
MF "v[220:223],a[108:111],v[124:127],v[220:223]\n"
MF "v[224:227],a[112:115],v[124:127],v[224:227]\n"
MF "v[228:231],a[116:119],v[124:127],v[228:231]\n"
MF "v[232:235],a[120:123],v[124:127],v[232:235]\n"
MF "v[236:239],a[124:127],v[124:127],v[236:239]\n"
MF "v[240:243],a[48:51],v[124:127],v[240:243]\n"
"s_nop 1\n"
"v_rcp_f32 v130,v168\n"
"s_add_u32 s66,s56,36864\n"
PM "v[136:137],v[136:137],v[130:131] op_sel_hi:[1,0]\n"
PM "v[138:139],v[138:139],v[130:131] op_sel_hi:[1,0]\n"
PM "v[140:141],v[140:141],v[130:131] op_sel_hi:[1,0]\n"
PM "v[142:143],v[142:143],v[130:131] op_sel_hi:[1,0]\n"
CV "v80,v136,v137\n"
CV "v81,v138,v139\n"
CV "v82,v140,v141\n"
CV "v83,v142,v143\n"
"buffer_store_dwordx4 v[80:83],v244,s[28:31],s66 offen offset:0 sc1\n"
PM "v[144:145],v[144:145],v[130:131] op_sel_hi:[1,0]\n"
PM "v[146:147],v[146:147],v[130:131] op_sel_hi:[1,0]\n"
PM "v[148:149],v[148:149],v[130:131] op_sel_hi:[1,0]\n"
PM "v[150:151],v[150:151],v[130:131] op_sel_hi:[1,0]\n"
CV "v84,v144,v145\n"
CV "v85,v146,v147\n"
CV "v86,v148,v149\n"
CV "v87,v150,v151\n"
"buffer_store_dwordx4 v[84:87],v244,s[28:31],s66 offen offset:1024 sc1\n"
PM "v[152:153],v[152:153],v[130:131] op_sel_hi:[1,0]\n"
PM "v[154:155],v[154:155],v[130:131] op_sel_hi:[1,0]\n"
PM "v[156:157],v[156:157],v[130:131] op_sel_hi:[1,0]\n"
PM "v[158:159],v[158:159],v[130:131] op_sel_hi:[1,0]\n"
CV "v88,v152,v153\n"
CV "v89,v154,v155\n"
CV "v90,v156,v157\n"
CV "v91,v158,v159\n"
"buffer_store_dwordx4 v[88:91],v244,s[28:31],s66 offen offset:2048 sc1\n"
PM "v[160:161],v[160:161],v[130:131] op_sel_hi:[1,0]\n"
PM "v[162:163],v[162:163],v[130:131] op_sel_hi:[1,0]\n"
PM "v[164:165],v[164:165],v[130:131] op_sel_hi:[1,0]\n"
PM "v[166:167],v[166:167],v[130:131] op_sel_hi:[1,0]\n"
CV "v92,v160,v161\n"
CV "v93,v162,v163\n"
CV "v94,v164,v165\n"
CV "v95,v166,v167\n"
"buffer_store_dwordx4 v[92:95],v244,s[28:31],s66 offen offset:3072 sc1\n"
"v_rcp_f32 v130,v204\n"
"s_add_u32 s66,s56,36864\n"
PM "v[172:173],v[172:173],v[130:131] op_sel_hi:[1,0]\n"
PM "v[174:175],v[174:175],v[130:131] op_sel_hi:[1,0]\n"
PM "v[176:177],v[176:177],v[130:131] op_sel_hi:[1,0]\n"
PM "v[178:179],v[178:179],v[130:131] op_sel_hi:[1,0]\n"
CV "v96,v172,v173\n"
CV "v97,v174,v175\n"
CV "v98,v176,v177\n"
CV "v99,v178,v179\n"
"buffer_store_dwordx4 v[96:99],v245,s[28:31],s66 offen offset:0 sc1\n"
PM "v[180:181],v[180:181],v[130:131] op_sel_hi:[1,0]\n"
PM "v[182:183],v[182:183],v[130:131] op_sel_hi:[1,0]\n"
PM "v[184:185],v[184:185],v[130:131] op_sel_hi:[1,0]\n"
PM "v[186:187],v[186:187],v[130:131] op_sel_hi:[1,0]\n"
CV "v100,v180,v181\n"
CV "v101,v182,v183\n"
CV "v102,v184,v185\n"
CV "v103,v186,v187\n"
"buffer_store_dwordx4 v[100:103],v245,s[28:31],s66 offen offset:1024 sc1\n"
PM "v[188:189],v[188:189],v[130:131] op_sel_hi:[1,0]\n"
PM "v[190:191],v[190:191],v[130:131] op_sel_hi:[1,0]\n"
PM "v[192:193],v[192:193],v[130:131] op_sel_hi:[1,0]\n"
PM "v[194:195],v[194:195],v[130:131] op_sel_hi:[1,0]\n"
CV "v104,v188,v189\n"
CV "v105,v190,v191\n"
CV "v106,v192,v193\n"
CV "v107,v194,v195\n"
"buffer_store_dwordx4 v[104:107],v245,s[28:31],s66 offen offset:2048 sc1\n"
PM "v[196:197],v[196:197],v[130:131] op_sel_hi:[1,0]\n"
PM "v[198:199],v[198:199],v[130:131] op_sel_hi:[1,0]\n"
PM "v[200:201],v[200:201],v[130:131] op_sel_hi:[1,0]\n"
PM "v[202:203],v[202:203],v[130:131] op_sel_hi:[1,0]\n"
CV "v108,v196,v197\n"
CV "v109,v198,v199\n"
CV "v110,v200,v201\n"
CV "v111,v202,v203\n"
"buffer_store_dwordx4 v[108:111],v245,s[28:31],s66 offen offset:3072 sc1\n"
"v_rcp_f32 v130,v240\n"
"s_add_u32 s66,s56,36864\n"
PM "v[208:209],v[208:209],v[130:131] op_sel_hi:[1,0]\n"
PM "v[210:211],v[210:211],v[130:131] op_sel_hi:[1,0]\n"
PM "v[212:213],v[212:213],v[130:131] op_sel_hi:[1,0]\n"
PM "v[214:215],v[214:215],v[130:131] op_sel_hi:[1,0]\n"
CV "v112,v208,v209\n"
CV "v113,v210,v211\n"
CV "v114,v212,v213\n"
CV "v115,v214,v215\n"
"buffer_store_dwordx4 v[112:115],v246,s[28:31],s66 offen offset:0 sc1\n"
"ds_write_b128 v247,v[112:115] offset:0\n"
PM "v[216:217],v[216:217],v[130:131] op_sel_hi:[1,0]\n"
PM "v[218:219],v[218:219],v[130:131] op_sel_hi:[1,0]\n"
PM "v[220:221],v[220:221],v[130:131] op_sel_hi:[1,0]\n"
PM "v[222:223],v[222:223],v[130:131] op_sel_hi:[1,0]\n"
CV "v116,v216,v217\n"
CV "v117,v218,v219\n"
CV "v118,v220,v221\n"
CV "v119,v222,v223\n"
"buffer_store_dwordx4 v[116:119],v246,s[28:31],s66 offen offset:1024 sc1\n"
"ds_write_b128 v247,v[116:119] offset:1024\n"
PM "v[224:225],v[224:225],v[130:131] op_sel_hi:[1,0]\n"
PM "v[226:227],v[226:227],v[130:131] op_sel_hi:[1,0]\n"
PM "v[228:229],v[228:229],v[130:131] op_sel_hi:[1,0]\n"
PM "v[230:231],v[230:231],v[130:131] op_sel_hi:[1,0]\n"
CV "v120,v224,v225\n"
CV "v121,v226,v227\n"
CV "v122,v228,v229\n"
CV "v123,v230,v231\n"
"buffer_store_dwordx4 v[120:123],v246,s[28:31],s66 offen offset:2048 sc1\n"
"ds_write_b128 v247,v[120:123] offset:2048\n"
PM "v[232:233],v[232:233],v[130:131] op_sel_hi:[1,0]\n"
PM "v[234:235],v[234:235],v[130:131] op_sel_hi:[1,0]\n"
PM "v[236:237],v[236:237],v[130:131] op_sel_hi:[1,0]\n"
PM "v[238:239],v[238:239],v[130:131] op_sel_hi:[1,0]\n"
CV "v124,v232,v233\n"
CV "v125,v234,v235\n"
CV "v126,v236,v237\n"
CV "v127,v238,v239\n"
"buffer_store_dwordx4 v[124:127],v246,s[28:31],s66 offen offset:3072 sc1\n"
"ds_write_b128 v247,v[124:127] offset:3072\n"
"v_mov_b32 v128,0\n"
"ds_write_b32 v251,v128 offset:16\n"
"s_branch LO14_%=\n"
"LS13_%=:\n"
WT "lgkmcnt(0)\n"
MF "v[80:83],a[64:67],v[32:35],0\n"
"v_mov_b64 v[136:137],0\n"
"v_mov_b64 v[138:139],0\n"
"v_mov_b64 v[140:141],0\n"
MF "v[84:87],a[68:71],v[32:35],0\n"
"v_mov_b64 v[142:143],0\n"
"v_mov_b64 v[144:145],0\n"
"v_mov_b64 v[146:147],0\n"
MF "v[80:83],a[72:75],v[36:39],v[80:83]\n"
"v_mov_b64 v[148:149],0\n"
"v_mov_b64 v[150:151],0\n"
MF "v[84:87],a[76:79],v[36:39],v[84:87]\n"
"v_mov_b64 v[152:153],0\n"
"v_mov_b64 v[154:155],0\n"
"v_mov_b64 v[156:157],0\n"
MF "v[80:83],a[80:83],v[40:43],v[80:83]\n"
"v_add_u32 v6,s49,v4\n"
"s_mov_b32 s50,s42\n"
"v_mov_b64 v[158:159],0\n"
"v_mov_b64 v[160:161],0\n"
MF "v[84:87],a[84:87],v[40:43],v[84:87]\n"
"v_add_u32 v7,s49,v5\n"
"s_mov_b32 s42,s49\n"
"v_mov_b64 v[162:163],0\n"
"v_mov_b64 v[164:165],0\n"
"v_mov_b64 v[166:167],0\n"
MF "v[80:83],a[88:91],v[44:47],v[80:83]\n"
"s_add_u32 s49,s49,16896\n"
"s_cmp_eq_u32 s49,84480\n"
"s_cselect_b32 s49,0,s49\n"
"v_mov_b64 v[168:169],0\n"
"v_mov_b64 v[170:171],0\n"
MF "v[84:87],a[92:95],v[44:47],v[84:87]\n"
"s_nop 7\n"
"v_max3_f32 v128,v80,v81,v82\n"
"v_max3_f32 v128,v128,v83,v84\n"
"v_max3_f32 v128,v128,v85,v86\n"
"v_max_f32 v128,v128,v87\n"
"v_mov_b32 v129,v128\n"
"s_nop 1\n"
"v_permlane32_swap_b32 v129,v128\n"
"v_max_f32 v128,v129,v128\n"
"v_mov_b32 v129,v128\n"
"s_nop 1\n"
"v_permlane16_swap_b32 v129,v128\n"
"v_max_f32 v128,v129,v128\n"
"v_sub_f32 v80,v80,v128\n"
"v_sub_f32 v81,v81,v128\n"
"v_sub_f32 v82,v82,v128\n"
"v_sub_f32 v83,v83,v128\n"
"v_sub_f32 v84,v84,v128\n"
"v_sub_f32 v85,v85,v128\n"
"v_sub_f32 v86,v86,v128\n"
"v_sub_f32 v87,v87,v128\n"
"v_sub_f32 v104,0,v128\n"
"v_sub_f32 v105,0,v128\n"
"v_sub_f32 v106,0,v128\n"
"v_sub_f32 v107,0,v128\n"
WT "vmcnt(12)\n"
"s_barrier\n"
MF "v[88:91],a[64:67],v[48:51],0\n"
EX "v80,v80\n"
DR "a[64:67],v6 offset:0\n"
"v_mov_b64 v[172:173],0\n"
"v_mov_b64 v[174:175],0\n"
"v_mov_b64 v[176:177],0\n"
MF "v[92:95],a[68:71],v[48:51],0\n"
EX "v81,v81\n"
DR "a[68:71],v6 offset:512\n"
"v_mov_b64 v[178:179],0\n"
"v_mov_b64 v[180:181],0\n"
"v_mov_b64 v[182:183],0\n"
MF "v[88:91],a[72:75],v[52:55],v[88:91]\n"
EX "v82,v82\n"
DR "a[72:75],v6 offset:64\n"
"v_mov_b64 v[184:185],0\n"
"v_mov_b64 v[186:187],0\n"
MF "v[92:95],a[76:79],v[52:55],v[92:95]\n"
EX "v83,v83\n"
DR "a[76:79],v6 offset:576\n"
"v_mov_b64 v[188:189],0\n"
"v_mov_b64 v[190:191],0\n"
"v_mov_b64 v[192:193],0\n"
MF "v[88:91],a[80:83],v[56:59],v[88:91]\n"
CV "v116,v80,v81\n"
CV "v117,v82,v83\n"
DR "a[80:83],v6 offset:128\n"
"v_mov_b64 v[194:195],0\n"
"v_mov_b64 v[196:197],0\n"
MF "v[92:95],a[84:87],v[56:59],v[92:95]\n"
EX "v84,v84\n"
DR "a[84:87],v6 offset:640\n"
"v_mov_b64 v[198:199],0\n"
"v_mov_b64 v[200:201],0\n"
"v_mov_b64 v[202:203],0\n"
MF "v[88:91],a[88:91],v[60:63],v[88:91]\n"
EX "v85,v85\n"
DR "a[88:91],v6 offset:192\n"
"v_mov_b64 v[204:205],0\n"
"v_mov_b64 v[206:207],0\n"
MF "v[92:95],a[92:95],v[60:63],v[92:95]\n"
EX "v86,v86\n"
DR "a[92:95],v6 offset:704\n"
EX "v87,v87\n"
DT "a[96:97],v7 offset:0\n"
DT "a[98:99],v7 offset:512\n"
CV "v118,v84,v85\n"
CV "v119,v86,v87\n"
DT "a[100:101],v7 offset:32\n"
DT "a[102:103],v7 offset:544\n"
DT "a[104:105],v7 offset:64\n"
DT "a[106:107],v7 offset:576\n"
DT "a[108:109],v7 offset:96\n"
DT "a[110:111],v7 offset:608\n"
DT "a[112:113],v7 offset:128\n"
DT "a[114:115],v7 offset:640\n"
DT "a[116:117],v7 offset:160\n"
DT "a[118:119],v7 offset:672\n"
DT "a[120:121],v7 offset:192\n"
DT "a[122:123],v7 offset:704\n"
DT "a[124:125],v7 offset:224\n"
DT "a[126:127],v7 offset:736\n"
"s_nop 7\n"
"v_max3_f32 v128,v88,v89,v90\n"
"v_max3_f32 v128,v128,v91,v92\n"
"v_max3_f32 v128,v128,v93,v94\n"
"v_max_f32 v128,v128,v95\n"
"v_mov_b32 v129,v128\n"
"s_nop 1\n"
"v_permlane32_swap_b32 v129,v128\n"
"v_max_f32 v128,v129,v128\n"
"v_mov_b32 v129,v128\n"
"s_nop 1\n"
"v_permlane16_swap_b32 v129,v128\n"
"v_max_f32 v128,v129,v128\n"
"v_sub_f32 v88,v88,v128\n"
"v_sub_f32 v89,v89,v128\n"
"v_sub_f32 v90,v90,v128\n"
"v_sub_f32 v91,v91,v128\n"
"v_sub_f32 v92,v92,v128\n"
"v_sub_f32 v93,v93,v128\n"
"v_sub_f32 v94,v94,v128\n"
"v_sub_f32 v95,v95,v128\n"
"v_sub_f32 v108,0,v128\n"
"v_sub_f32 v109,0,v128\n"
"v_sub_f32 v110,0,v128\n"
"v_sub_f32 v111,0,v128\n"
"s_mov_b32 s52,163840\n"
"Ll235_%=:\n"
WT "lgkmcnt(15)\n"
MF "v[80:83],a[64:67],v[32:35],v[104:107]\n"
EX "v88,v88\n"
MF "v[84:87],a[68:71],v[32:35],v[104:107]\n"
EX "v89,v89\n"
MF "v[80:83],a[72:75],v[36:39],v[80:83]\n"
EX "v90,v90\n"
MF "v[84:87],a[76:79],v[36:39],v[84:87]\n"
EX "v91,v91\n"
MF "v[80:83],a[80:83],v[40:43],v[80:83]\n"
CV "v120,v88,v89\n"
CV "v121,v90,v91\n"
MF "v[84:87],a[84:87],v[40:43],v[84:87]\n"
EX "v92,v92\n"
MF "v[80:83],a[88:91],v[44:47],v[80:83]\n"
EX "v93,v93\n"
MF "v[84:87],a[92:95],v[44:47],v[84:87]\n"
EX "v94,v94\n"
WT "lgkmcnt(0)\n"
MF "v[136:139],a[96:99],v[116:119],v[136:139]\n"
EX "v95,v95\n"
"s_add_u32 m0,s50,s48\n"
MF "v[140:143],a[100:103],v[116:119],v[140:143]\n"
CV "v122,v92,v93\n"
CV "v123,v94,v95\n"
BL "v8,s[12:15],s78 offen nt lds\n"
MF "v[144:147],a[104:107],v[116:119],v[144:147]\n"
BL "v9,s[12:15],s78 offen offset:1056 nt lds\n"
"s_add_u32 m0,m0,8448\n"
MF "v[148:151],a[108:111],v[116:119],v[148:151]\n"
BL "v10,s[16:19],s79 offen nt lds\n"
MF "v[152:155],a[112:115],v[116:119],v[152:155]\n"
BL "v11,s[16:19],s79 offen offset:1056 nt lds\n"
"s_add_u32 s51,s51,8192\n"
"s_mov_b32 s79,s78\n"
"s_xor_b32 s78,s51,s41\n"
MF "v[156:159],a[116:119],v[116:119],v[156:159]\n"
"v_add_u32 v6,s49,v4\n"
"s_mov_b32 s50,s42\n"
"v_max3_f32 v128,v80,v81,v82\n"
"v_max3_f32 v128,v128,v83,v84\n"
MF "v[160:163],a[120:123],v[116:119],v[160:163]\n"
"v_add_u32 v7,s49,v5\n"
"s_mov_b32 s42,s49\n"
"v_max3_f32 v128,v128,v85,v86\n"
"v_max_f32 v128,v128,v87\n"
MF "v[164:167],a[124:127],v[116:119],v[164:167]\n"
"s_add_u32 s49,s49,16896\n"
"s_cmp_eq_u32 s49,84480\n"
"s_cselect_b32 s49,0,s49\n"
"v_cmp_lt_f32 vcc,s53,v128\n"
MF "v[168:171],a[48:51],v[116:119],v[168:171]\n"
"s_cbranch_vccnz Ls236_%=\n"
"Ls237_%=:\n"
WT "vmcnt(12)\n"
"s_barrier\n"
MF "v[88:91],a[64:67],v[48:51],v[108:111]\n"
EX "v80,v80\n"
DR "a[64:67],v6 offset:0\n"
MF "v[92:95],a[68:71],v[48:51],v[108:111]\n"
EX "v81,v81\n"
DR "a[68:71],v6 offset:512\n"
MF "v[88:91],a[72:75],v[52:55],v[88:91]\n"
EX "v82,v82\n"
DR "a[72:75],v6 offset:64\n"
MF "v[92:95],a[76:79],v[52:55],v[92:95]\n"
EX "v83,v83\n"
DR "a[76:79],v6 offset:576\n"
MF "v[88:91],a[80:83],v[56:59],v[88:91]\n"
CV "v116,v80,v81\n"
CV "v117,v82,v83\n"
DR "a[80:83],v6 offset:128\n"
MF "v[92:95],a[84:87],v[56:59],v[92:95]\n"
EX "v84,v84\n"
DR "a[84:87],v6 offset:640\n"
MF "v[88:91],a[88:91],v[60:63],v[88:91]\n"
EX "v85,v85\n"
DR "a[88:91],v6 offset:192\n"
MF "v[92:95],a[92:95],v[60:63],v[92:95]\n"
EX "v86,v86\n"
DR "a[92:95],v6 offset:704\n"
MF "v[172:175],a[96:99],v[120:123],v[172:175]\n"
EX "v87,v87\n"
DT "a[96:97],v7 offset:0\n"
DT "a[98:99],v7 offset:512\n"
MF "v[176:179],a[100:103],v[120:123],v[176:179]\n"
CV "v118,v84,v85\n"
CV "v119,v86,v87\n"
DT "a[100:101],v7 offset:32\n"
DT "a[102:103],v7 offset:544\n"
MF "v[180:183],a[104:107],v[120:123],v[180:183]\n"
DT "a[104:105],v7 offset:64\n"
DT "a[106:107],v7 offset:576\n"
MF "v[184:187],a[108:111],v[120:123],v[184:187]\n"
DT "a[108:109],v7 offset:96\n"
DT "a[110:111],v7 offset:608\n"
MF "v[188:191],a[112:115],v[120:123],v[188:191]\n"
DT "a[112:113],v7 offset:128\n"
DT "a[114:115],v7 offset:640\n"
MF "v[192:195],a[116:119],v[120:123],v[192:195]\n"
DT "a[116:117],v7 offset:160\n"
DT "a[118:119],v7 offset:672\n"
"v_max3_f32 v128,v88,v89,v90\n"
"v_max3_f32 v128,v128,v91,v92\n"
MF "v[196:199],a[120:123],v[120:123],v[196:199]\n"
DT "a[120:121],v7 offset:192\n"
DT "a[122:123],v7 offset:704\n"
"v_max3_f32 v128,v128,v93,v94\n"
"v_max_f32 v128,v128,v95\n"
MF "v[200:203],a[124:127],v[120:123],v[200:203]\n"
DT "a[124:125],v7 offset:224\n"
DT "a[126:127],v7 offset:736\n"
"v_cmp_lt_f32 vcc,s53,v128\n"
MF "v[204:207],a[48:51],v[120:123],v[204:207]\n"
"s_cbranch_vccnz Ls238_%=\n"
"Ls239_%=:\n"
"s_cmp_lg_u32 s51,s52\n"
"s_cbranch_scc1 Ll235_%=\n"
WT "lgkmcnt(15)\n"
MF "v[80:83],a[64:67],a[0:3],0\n"
EX "v88,v88\n"
MF "v[84:87],a[68:71],a[0:3],0\n"
EX "v89,v89\n"
MF "v[80:83],a[72:75],a[4:7],v[80:83]\n"
EX "v90,v90\n"
MF "v[84:87],a[76:79],a[4:7],v[84:87]\n"
EX "v91,v91\n"
MF "v[80:83],a[80:83],a[8:11],v[80:83]\n"
CV "v120,v88,v89\n"
CV "v121,v90,v91\n"
MF "v[84:87],a[84:87],a[8:11],v[84:87]\n"
EX "v92,v92\n"
MF "v[80:83],a[88:91],a[12:15],v[80:83]\n"
EX "v93,v93\n"
MF "v[84:87],a[92:95],a[12:15],v[84:87]\n"
EX "v94,v94\n"
WT "lgkmcnt(0)\n"
MF "v[136:139],a[96:99],v[116:119],v[136:139]\n"
EX "v95,v95\n"
"s_add_u32 m0,s50,s48\n"
MF "v[140:143],a[100:103],v[116:119],v[140:143]\n"
CV "v122,v92,v93\n"
CV "v123,v94,v95\n"
BL "v8,s[12:15],s78 offen nt lds\n"
MF "v[144:147],a[104:107],v[116:119],v[144:147]\n"
BL "v9,s[12:15],s78 offen offset:1056 nt lds\n"
"s_add_u32 m0,m0,8448\n"
MF "v[148:151],a[108:111],v[116:119],v[148:151]\n"
BL "v10,s[16:19],s79 offen nt lds\n"
MF "v[152:155],a[112:115],v[116:119],v[152:155]\n"
BL "v11,s[16:19],s79 offen offset:1056 nt lds\n"
"s_add_u32 s51,s51,8192\n"
"s_mov_b32 s79,s78\n"
"s_xor_b32 s78,s51,s41\n"
MF "v[156:159],a[116:119],v[116:119],v[156:159]\n"
"v_add_u32 v6,s49,v4\n"
"s_mov_b32 s50,s42\n"
MF "v[160:163],a[120:123],v[116:119],v[160:163]\n"
"v_add_u32 v7,s49,v5\n"
"s_mov_b32 s42,s49\n"
MF "v[164:167],a[124:127],v[116:119],v[164:167]\n"
"s_add_u32 s49,s49,16896\n"
"s_cmp_eq_u32 s49,84480\n"
"s_cselect_b32 s49,0,s49\n"
MF "v[168:171],a[48:51],v[116:119],v[168:171]\n"
"v_max3_f32 v128,v80,v81,v82\n"
"v_max3_f32 v128,v128,v83,v84\n"
"v_max3_f32 v128,v128,v85,v86\n"
"v_max_f32 v128,v128,v87\n"
"v_mov_b32 v129,v128\n"
"s_nop 1\n"
"v_permlane32_swap_b32 v129,v128\n"
"v_max_f32 v128,v129,v128\n"
"v_mov_b32 v129,v128\n"
"s_nop 1\n"
"v_permlane16_swap_b32 v129,v128\n"
"v_max_f32 v128,v129,v128\n"
"v_sub_f32 v80,v80,v128\n"
"v_sub_f32 v81,v81,v128\n"
"v_sub_f32 v82,v82,v128\n"
"v_sub_f32 v83,v83,v128\n"
"v_sub_f32 v84,v84,v128\n"
"v_sub_f32 v85,v85,v128\n"
"v_sub_f32 v86,v86,v128\n"
"v_sub_f32 v87,v87,v128\n"
"v_sub_f32 v104,0,v128\n"
"v_sub_f32 v105,0,v128\n"
"v_sub_f32 v106,0,v128\n"
"v_sub_f32 v107,0,v128\n"
"v_rcp_f32 v130,v168\n"
"s_add_u32 s66,s56,s99\n"
PM "v[136:137],v[136:137],v[130:131] op_sel_hi:[1,0]\n"
PM "v[138:139],v[138:139],v[130:131] op_sel_hi:[1,0]\n"
PM "v[140:141],v[140:141],v[130:131] op_sel_hi:[1,0]\n"
PM "v[142:143],v[142:143],v[130:131] op_sel_hi:[1,0]\n"
CV "v32,v136,v137\n"
CV "v33,v138,v139\n"
CV "v34,v140,v141\n"
CV "v35,v142,v143\n"
"buffer_store_dwordx4 v[32:35],v244,s[28:31],s66 offen offset:0 sc1\n"
PM "v[144:145],v[144:145],v[130:131] op_sel_hi:[1,0]\n"
PM "v[146:147],v[146:147],v[130:131] op_sel_hi:[1,0]\n"
PM "v[148:149],v[148:149],v[130:131] op_sel_hi:[1,0]\n"
PM "v[150:151],v[150:151],v[130:131] op_sel_hi:[1,0]\n"
CV "v36,v144,v145\n"
CV "v37,v146,v147\n"
CV "v38,v148,v149\n"
CV "v39,v150,v151\n"
"buffer_store_dwordx4 v[36:39],v244,s[28:31],s66 offen offset:1024 sc1\n"
PM "v[152:153],v[152:153],v[130:131] op_sel_hi:[1,0]\n"
PM "v[154:155],v[154:155],v[130:131] op_sel_hi:[1,0]\n"
PM "v[156:157],v[156:157],v[130:131] op_sel_hi:[1,0]\n"
PM "v[158:159],v[158:159],v[130:131] op_sel_hi:[1,0]\n"
CV "v40,v152,v153\n"
CV "v41,v154,v155\n"
CV "v42,v156,v157\n"
CV "v43,v158,v159\n"
"buffer_store_dwordx4 v[40:43],v244,s[28:31],s66 offen offset:2048 sc1\n"
PM "v[160:161],v[160:161],v[130:131] op_sel_hi:[1,0]\n"
PM "v[162:163],v[162:163],v[130:131] op_sel_hi:[1,0]\n"
PM "v[164:165],v[164:165],v[130:131] op_sel_hi:[1,0]\n"
PM "v[166:167],v[166:167],v[130:131] op_sel_hi:[1,0]\n"
CV "v44,v160,v161\n"
CV "v45,v162,v163\n"
CV "v46,v164,v165\n"
CV "v47,v166,v167\n"
"buffer_store_dwordx4 v[44:47],v244,s[28:31],s66 offen offset:3072 sc1\n"
"v_mov_b64 v[136:137],0\n"
"v_mov_b64 v[138:139],0\n"
"v_mov_b64 v[140:141],0\n"
"v_mov_b64 v[142:143],0\n"
"v_mov_b64 v[144:145],0\n"
"v_mov_b64 v[146:147],0\n"
"v_mov_b64 v[148:149],0\n"
"v_mov_b64 v[150:151],0\n"
"v_mov_b64 v[152:153],0\n"
"v_mov_b64 v[154:155],0\n"
"v_mov_b64 v[156:157],0\n"
"v_mov_b64 v[158:159],0\n"
"v_mov_b64 v[160:161],0\n"
"v_mov_b64 v[162:163],0\n"
"v_mov_b64 v[164:165],0\n"
"v_mov_b64 v[166:167],0\n"
"v_mov_b64 v[168:169],0\n"
"v_mov_b64 v[170:171],0\n"
WT "vmcnt(16)\n"
"s_barrier\n"
MF "v[88:91],a[64:67],a[16:19],0\n"
EX "v80,v80\n"
DR "a[64:67],v6 offset:0\n"
MF "v[92:95],a[68:71],a[16:19],0\n"
EX "v81,v81\n"
DR "a[68:71],v6 offset:512\n"
MF "v[88:91],a[72:75],a[20:23],v[88:91]\n"
EX "v82,v82\n"
DR "a[72:75],v6 offset:64\n"
MF "v[92:95],a[76:79],a[20:23],v[92:95]\n"
EX "v83,v83\n"
DR "a[76:79],v6 offset:576\n"
MF "v[88:91],a[80:83],a[24:27],v[88:91]\n"
CV "v116,v80,v81\n"
CV "v117,v82,v83\n"
DR "a[80:83],v6 offset:128\n"
MF "v[92:95],a[84:87],a[24:27],v[92:95]\n"
EX "v84,v84\n"
DR "a[84:87],v6 offset:640\n"
MF "v[88:91],a[88:91],a[28:31],v[88:91]\n"
EX "v85,v85\n"
DR "a[88:91],v6 offset:192\n"
MF "v[92:95],a[92:95],a[28:31],v[92:95]\n"
EX "v86,v86\n"
DR "a[92:95],v6 offset:704\n"
MF "v[172:175],a[96:99],v[120:123],v[172:175]\n"
EX "v87,v87\n"
DT "a[96:97],v7 offset:0\n"
DT "a[98:99],v7 offset:512\n"
MF "v[176:179],a[100:103],v[120:123],v[176:179]\n"
CV "v118,v84,v85\n"
CV "v119,v86,v87\n"
DT "a[100:101],v7 offset:32\n"
DT "a[102:103],v7 offset:544\n"
MF "v[180:183],a[104:107],v[120:123],v[180:183]\n"
DT "a[104:105],v7 offset:64\n"
DT "a[106:107],v7 offset:576\n"
MF "v[184:187],a[108:111],v[120:123],v[184:187]\n"
DT "a[108:109],v7 offset:96\n"
DT "a[110:111],v7 offset:608\n"
MF "v[188:191],a[112:115],v[120:123],v[188:191]\n"
DT "a[112:113],v7 offset:128\n"
DT "a[114:115],v7 offset:640\n"
MF "v[192:195],a[116:119],v[120:123],v[192:195]\n"
DT "a[116:117],v7 offset:160\n"
DT "a[118:119],v7 offset:672\n"
MF "v[196:199],a[120:123],v[120:123],v[196:199]\n"
DT "a[120:121],v7 offset:192\n"
DT "a[122:123],v7 offset:704\n"
MF "v[200:203],a[124:127],v[120:123],v[200:203]\n"
DT "a[124:125],v7 offset:224\n"
DT "a[126:127],v7 offset:736\n"
MF "v[204:207],a[48:51],v[120:123],v[204:207]\n"
"v_max3_f32 v128,v88,v89,v90\n"
"v_max3_f32 v128,v128,v91,v92\n"
"v_max3_f32 v128,v128,v93,v94\n"
"v_max_f32 v128,v128,v95\n"
"v_mov_b32 v129,v128\n"
"s_nop 1\n"
"v_permlane32_swap_b32 v129,v128\n"
"v_max_f32 v128,v129,v128\n"
"v_mov_b32 v129,v128\n"
"s_nop 1\n"
"v_permlane16_swap_b32 v129,v128\n"
"v_max_f32 v128,v129,v128\n"
"v_sub_f32 v88,v88,v128\n"
"v_sub_f32 v89,v89,v128\n"
"v_sub_f32 v90,v90,v128\n"
"v_sub_f32 v91,v91,v128\n"
"v_sub_f32 v92,v92,v128\n"
"v_sub_f32 v93,v93,v128\n"
"v_sub_f32 v94,v94,v128\n"
"v_sub_f32 v95,v95,v128\n"
"v_sub_f32 v108,0,v128\n"
"v_sub_f32 v109,0,v128\n"
"v_sub_f32 v110,0,v128\n"
"v_sub_f32 v111,0,v128\n"
"v_rcp_f32 v130,v204\n"
"s_add_u32 s66,s56,s99\n"
PM "v[172:173],v[172:173],v[130:131] op_sel_hi:[1,0]\n"
PM "v[174:175],v[174:175],v[130:131] op_sel_hi:[1,0]\n"
PM "v[176:177],v[176:177],v[130:131] op_sel_hi:[1,0]\n"
PM "v[178:179],v[178:179],v[130:131] op_sel_hi:[1,0]\n"
CV "v48,v172,v173\n"
CV "v49,v174,v175\n"
CV "v50,v176,v177\n"
CV "v51,v178,v179\n"
"buffer_store_dwordx4 v[48:51],v245,s[28:31],s66 offen offset:0 sc1\n"
PM "v[180:181],v[180:181],v[130:131] op_sel_hi:[1,0]\n"
PM "v[182:183],v[182:183],v[130:131] op_sel_hi:[1,0]\n"
PM "v[184:185],v[184:185],v[130:131] op_sel_hi:[1,0]\n"
PM "v[186:187],v[186:187],v[130:131] op_sel_hi:[1,0]\n"
CV "v52,v180,v181\n"
CV "v53,v182,v183\n"
CV "v54,v184,v185\n"
CV "v55,v186,v187\n"
"buffer_store_dwordx4 v[52:55],v245,s[28:31],s66 offen offset:1024 sc1\n"
PM "v[188:189],v[188:189],v[130:131] op_sel_hi:[1,0]\n"
PM "v[190:191],v[190:191],v[130:131] op_sel_hi:[1,0]\n"
PM "v[192:193],v[192:193],v[130:131] op_sel_hi:[1,0]\n"
PM "v[194:195],v[194:195],v[130:131] op_sel_hi:[1,0]\n"
CV "v56,v188,v189\n"
CV "v57,v190,v191\n"
CV "v58,v192,v193\n"
CV "v59,v194,v195\n"
"buffer_store_dwordx4 v[56:59],v245,s[28:31],s66 offen offset:2048 sc1\n"
PM "v[196:197],v[196:197],v[130:131] op_sel_hi:[1,0]\n"
PM "v[198:199],v[198:199],v[130:131] op_sel_hi:[1,0]\n"
PM "v[200:201],v[200:201],v[130:131] op_sel_hi:[1,0]\n"
PM "v[202:203],v[202:203],v[130:131] op_sel_hi:[1,0]\n"
CV "v60,v196,v197\n"
CV "v61,v198,v199\n"
CV "v62,v200,v201\n"
CV "v63,v202,v203\n"
"buffer_store_dwordx4 v[60:63],v245,s[28:31],s66 offen offset:3072 sc1\n"
"v_mov_b64 v[172:173],0\n"
"v_mov_b64 v[174:175],0\n"
"v_mov_b64 v[176:177],0\n"
"v_mov_b64 v[178:179],0\n"
"v_mov_b64 v[180:181],0\n"
"v_mov_b64 v[182:183],0\n"
"v_mov_b64 v[184:185],0\n"
"v_mov_b64 v[186:187],0\n"
"v_mov_b64 v[188:189],0\n"
"v_mov_b64 v[190:191],0\n"
"v_mov_b64 v[192:193],0\n"
"v_mov_b64 v[194:195],0\n"
"v_mov_b64 v[196:197],0\n"
"v_mov_b64 v[198:199],0\n"
"v_mov_b64 v[200:201],0\n"
"v_mov_b64 v[202:203],0\n"
"v_mov_b64 v[204:205],0\n"
"v_mov_b64 v[206:207],0\n"
WT "lgkmcnt(0)\n"
"s_mov_b32 s52,204800\n"
"Ll296_%=:\n"
WT "lgkmcnt(15)\n"
MF "v[80:83],a[64:67],a[0:3],v[104:107]\n"
EX "v88,v88\n"
MF "v[84:87],a[68:71],a[0:3],v[104:107]\n"
EX "v89,v89\n"
MF "v[80:83],a[72:75],a[4:7],v[80:83]\n"
EX "v90,v90\n"
MF "v[84:87],a[76:79],a[4:7],v[84:87]\n"
EX "v91,v91\n"
MF "v[80:83],a[80:83],a[8:11],v[80:83]\n"
CV "v120,v88,v89\n"
CV "v121,v90,v91\n"
MF "v[84:87],a[84:87],a[8:11],v[84:87]\n"
EX "v92,v92\n"
MF "v[80:83],a[88:91],a[12:15],v[80:83]\n"
EX "v93,v93\n"
MF "v[84:87],a[92:95],a[12:15],v[84:87]\n"
EX "v94,v94\n"
WT "lgkmcnt(0)\n"
MF "v[136:139],a[96:99],v[116:119],v[136:139]\n"
EX "v95,v95\n"
"s_add_u32 m0,s50,s48\n"
MF "v[140:143],a[100:103],v[116:119],v[140:143]\n"
CV "v122,v92,v93\n"
CV "v123,v94,v95\n"
BL "v8,s[12:15],s78 offen nt lds\n"
MF "v[144:147],a[104:107],v[116:119],v[144:147]\n"
BL "v9,s[12:15],s78 offen offset:1056 nt lds\n"
"s_add_u32 m0,m0,8448\n"
MF "v[148:151],a[108:111],v[116:119],v[148:151]\n"
BL "v10,s[16:19],s79 offen nt lds\n"
MF "v[152:155],a[112:115],v[116:119],v[152:155]\n"
BL "v11,s[16:19],s79 offen offset:1056 nt lds\n"
"s_add_u32 s51,s51,8192\n"
"s_mov_b32 s79,s78\n"
"s_xor_b32 s78,s51,s41\n"
MF "v[156:159],a[116:119],v[116:119],v[156:159]\n"
"v_add_u32 v6,s49,v4\n"
"s_mov_b32 s50,s42\n"
"v_max3_f32 v128,v80,v81,v82\n"
"v_max3_f32 v128,v128,v83,v84\n"
MF "v[160:163],a[120:123],v[116:119],v[160:163]\n"
"v_add_u32 v7,s49,v5\n"
"s_mov_b32 s42,s49\n"
"v_max3_f32 v128,v128,v85,v86\n"
"v_max_f32 v128,v128,v87\n"
MF "v[164:167],a[124:127],v[116:119],v[164:167]\n"
"s_add_u32 s49,s49,16896\n"
"s_cmp_eq_u32 s49,84480\n"
"s_cselect_b32 s49,0,s49\n"
"v_cmp_lt_f32 vcc,s53,v128\n"
MF "v[168:171],a[48:51],v[116:119],v[168:171]\n"
"s_cbranch_vccnz Ls297_%=\n"
"Ls298_%=:\n"
WT "vmcnt(12)\n"
"s_barrier\n"
MF "v[88:91],a[64:67],a[16:19],v[108:111]\n"
EX "v80,v80\n"
DR "a[64:67],v6 offset:0\n"
MF "v[92:95],a[68:71],a[16:19],v[108:111]\n"
EX "v81,v81\n"
DR "a[68:71],v6 offset:512\n"
MF "v[88:91],a[72:75],a[20:23],v[88:91]\n"
EX "v82,v82\n"
DR "a[72:75],v6 offset:64\n"
MF "v[92:95],a[76:79],a[20:23],v[92:95]\n"
EX "v83,v83\n"
DR "a[76:79],v6 offset:576\n"
MF "v[88:91],a[80:83],a[24:27],v[88:91]\n"
CV "v116,v80,v81\n"
CV "v117,v82,v83\n"
DR "a[80:83],v6 offset:128\n"
MF "v[92:95],a[84:87],a[24:27],v[92:95]\n"
EX "v84,v84\n"
DR "a[84:87],v6 offset:640\n"
MF "v[88:91],a[88:91],a[28:31],v[88:91]\n"
EX "v85,v85\n"
DR "a[88:91],v6 offset:192\n"
MF "v[92:95],a[92:95],a[28:31],v[92:95]\n"
EX "v86,v86\n"
DR "a[92:95],v6 offset:704\n"
MF "v[172:175],a[96:99],v[120:123],v[172:175]\n"
EX "v87,v87\n"
DT "a[96:97],v7 offset:0\n"
DT "a[98:99],v7 offset:512\n"
MF "v[176:179],a[100:103],v[120:123],v[176:179]\n"
CV "v118,v84,v85\n"
CV "v119,v86,v87\n"
DT "a[100:101],v7 offset:32\n"
DT "a[102:103],v7 offset:544\n"
MF "v[180:183],a[104:107],v[120:123],v[180:183]\n"
DT "a[104:105],v7 offset:64\n"
DT "a[106:107],v7 offset:576\n"
MF "v[184:187],a[108:111],v[120:123],v[184:187]\n"
DT "a[108:109],v7 offset:96\n"
DT "a[110:111],v7 offset:608\n"
MF "v[188:191],a[112:115],v[120:123],v[188:191]\n"
DT "a[112:113],v7 offset:128\n"
DT "a[114:115],v7 offset:640\n"
MF "v[192:195],a[116:119],v[120:123],v[192:195]\n"
DT "a[116:117],v7 offset:160\n"
DT "a[118:119],v7 offset:672\n"
"v_max3_f32 v128,v88,v89,v90\n"
"v_max3_f32 v128,v128,v91,v92\n"
MF "v[196:199],a[120:123],v[120:123],v[196:199]\n"
DT "a[120:121],v7 offset:192\n"
DT "a[122:123],v7 offset:704\n"
"v_max3_f32 v128,v128,v93,v94\n"
"v_max_f32 v128,v128,v95\n"
MF "v[200:203],a[124:127],v[120:123],v[200:203]\n"
DT "a[124:125],v7 offset:224\n"
DT "a[126:127],v7 offset:736\n"
"v_cmp_lt_f32 vcc,s53,v128\n"
MF "v[204:207],a[48:51],v[120:123],v[204:207]\n"
"s_cbranch_vccnz Ls299_%=\n"
"Ls300_%=:\n"
"s_cmp_lg_u32 s51,s52\n"
"s_cbranch_scc1 Ll296_%=\n"
DR "v[128:131],v252 offset:0\n"
DR "v[64:67],v253 offset:36864\n"
DR "v[68:71],v253 offset:37888\n"
DR "v[72:75],v253 offset:38912\n"
DR "v[76:79],v253 offset:39936\n"
WT "lgkmcnt(0)\n"
"s_add_u32 m0,s76,84480\n"
"s_add_u32 s67,s68,0\n"
BL "v12,s[24:27],s67 offen offset:0 lds\n"
BL "v13,s[24:27],s67 offen offset:1024 lds\n"
BL "v14,s[24:27],s67 offen offset:2048 lds\n"
"s_add_u32 m0,s76,87552\n"
"s_add_u32 s67,s68,0\n"
BL "v15,s[24:27],s67 offen offset:0 lds\n"
BL "v16,s[24:27],s67 offen offset:1024 lds\n"
BL "v17,s[24:27],s67 offen offset:2048 lds\n"
"s_add_u32 m0,s76,90624\n"
"s_add_u32 s67,s68,0\n"
BL "v18,s[24:27],s67 offen offset:0 lds\n"
BL "v19,s[24:27],s67 offen offset:1024 lds\n"
BL "v20,s[24:27],s67 offen offset:2048 lds\n"
WT "lgkmcnt(15)\n"
MF "v[80:83],a[64:67],a[0:3],v[104:107]\n"
EX "v88,v88\n"
MF "v[84:87],a[68:71],a[0:3],v[104:107]\n"
EX "v89,v89\n"
MF "v[80:83],a[72:75],a[4:7],v[80:83]\n"
EX "v90,v90\n"
MF "v[84:87],a[76:79],a[4:7],v[84:87]\n"
EX "v91,v91\n"
MF "v[80:83],a[80:83],a[8:11],v[80:83]\n"
CV "v120,v88,v89\n"
CV "v121,v90,v91\n"
MF "v[84:87],a[84:87],a[8:11],v[84:87]\n"
EX "v92,v92\n"
MF "v[80:83],a[88:91],a[12:15],v[80:83]\n"
EX "v93,v93\n"
MF "v[84:87],a[92:95],a[12:15],v[84:87]\n"
EX "v94,v94\n"
WT "lgkmcnt(0)\n"
"s_add_u32 m0,s50,s48\n"
MF "v[136:139],a[96:99],v[116:119],v[136:139]\n"
EX "v95,v95\n"
BL "v8,s[12:15],s78 offen nt lds\n"
MF "v[140:143],a[100:103],v[116:119],v[140:143]\n"
CV "v122,v92,v93\n"
CV "v123,v94,v95\n"
BL "v9,s[12:15],s78 offen offset:1056 nt lds\n"
"s_add_u32 m0,m0,8448\n"
MF "v[144:147],a[104:107],v[116:119],v[144:147]\n"
BL "v10,s[16:19],s79 offen nt lds\n"
MF "v[148:151],a[108:111],v[116:119],v[148:151]\n"
BL "v11,s[16:19],s79 offen offset:1056 nt lds\n"
"s_add_u32 s51,s51,8192\n"
"s_mov_b32 s79,s78\n"
"s_xor_b32 s78,s51,s41\n"
MF "v[152:155],a[112:115],v[116:119],v[152:155]\n"
"buffer_load_dword v31,v26,s[28:31],0 offen offset:0 sc1\n"
MF "v[156:159],a[116:119],v[116:119],v[156:159]\n"
"v_add_u32 v6,s49,v4\n"
"s_mov_b32 s50,s42\n"
"v_max3_f32 v128,v80,v81,v82\n"
"v_max3_f32 v128,v128,v83,v84\n"
MF "v[160:163],a[120:123],v[116:119],v[160:163]\n"
"v_add_u32 v7,s49,v5\n"
"s_mov_b32 s42,s49\n"
"v_max3_f32 v128,v128,v85,v86\n"
"v_max_f32 v128,v128,v87\n"
MF "v[164:167],a[124:127],v[116:119],v[164:167]\n"
"s_add_u32 s49,s49,16896\n"
"s_cmp_eq_u32 s49,84480\n"
"s_cselect_b32 s49,0,s49\n"
"v_cmp_lt_f32 vcc,s53,v128\n"
MF "v[168:171],a[48:51],v[116:119],v[168:171]\n"
"s_cbranch_vccnz Ls313_%=\n"
"Ls314_%=:\n"
WT "vmcnt(22)\n"
"s_barrier\n"
MF "v[88:91],a[64:67],a[16:19],v[108:111]\n"
EX "v80,v80\n"
DR "a[64:67],v6 offset:0\n"
MF "v[92:95],a[68:71],a[16:19],v[108:111]\n"
EX "v81,v81\n"
DR "a[68:71],v6 offset:512\n"
MF "v[88:91],a[72:75],a[20:23],v[88:91]\n"
EX "v82,v82\n"
DR "a[72:75],v6 offset:64\n"
MF "v[92:95],a[76:79],a[20:23],v[92:95]\n"
EX "v83,v83\n"
DR "a[76:79],v6 offset:576\n"
MF "v[88:91],a[80:83],a[24:27],v[88:91]\n"
CV "v116,v80,v81\n"
CV "v117,v82,v83\n"
DR "a[80:83],v6 offset:128\n"
MF "v[92:95],a[84:87],a[24:27],v[92:95]\n"
EX "v84,v84\n"
DR "a[84:87],v6 offset:640\n"
MF "v[88:91],a[88:91],a[28:31],v[88:91]\n"
EX "v85,v85\n"
DR "a[88:91],v6 offset:192\n"
MF "v[92:95],a[92:95],a[28:31],v[92:95]\n"
EX "v86,v86\n"
DR "a[92:95],v6 offset:704\n"
MF "v[172:175],a[96:99],v[120:123],v[172:175]\n"
EX "v87,v87\n"
DT "a[96:97],v7 offset:0\n"
DT "a[98:99],v7 offset:512\n"
MF "v[176:179],a[100:103],v[120:123],v[176:179]\n"
CV "v118,v84,v85\n"
CV "v119,v86,v87\n"
DT "a[100:101],v7 offset:32\n"
DT "a[102:103],v7 offset:544\n"
MF "v[180:183],a[104:107],v[120:123],v[180:183]\n"
DT "a[104:105],v7 offset:64\n"
DT "a[106:107],v7 offset:576\n"
MF "v[184:187],a[108:111],v[120:123],v[184:187]\n"
DT "a[108:109],v7 offset:96\n"
DT "a[110:111],v7 offset:608\n"
MF "v[188:191],a[112:115],v[120:123],v[188:191]\n"
DT "a[112:113],v7 offset:128\n"
DT "a[114:115],v7 offset:640\n"
MF "v[192:195],a[116:119],v[120:123],v[192:195]\n"
DT "a[116:117],v7 offset:160\n"
DT "a[118:119],v7 offset:672\n"
"v_max3_f32 v128,v88,v89,v90\n"
"v_max3_f32 v128,v128,v91,v92\n"
MF "v[196:199],a[120:123],v[120:123],v[196:199]\n"
DT "a[120:121],v7 offset:192\n"
DT "a[122:123],v7 offset:704\n"
"v_max3_f32 v128,v128,v93,v94\n"
"v_max_f32 v128,v128,v95\n"
MF "v[200:203],a[124:127],v[120:123],v[200:203]\n"
DT "a[124:125],v7 offset:224\n"
DT "a[126:127],v7 offset:736\n"
"v_cmp_lt_f32 vcc,s53,v128\n"
MF "v[204:207],a[48:51],v[120:123],v[204:207]\n"
"s_cbranch_vccnz Ls315_%=\n"
"Ls316_%=:\n"
WT "lgkmcnt(15)\n"
MF "v[80:83],a[64:67],a[0:3],v[104:107]\n"
EX "v88,v88\n"
MF "v[84:87],a[68:71],a[0:3],v[104:107]\n"
EX "v89,v89\n"
MF "v[80:83],a[72:75],a[4:7],v[80:83]\n"
EX "v90,v90\n"
MF "v[84:87],a[76:79],a[4:7],v[84:87]\n"
EX "v91,v91\n"
MF "v[80:83],a[80:83],a[8:11],v[80:83]\n"
CV "v120,v88,v89\n"
CV "v121,v90,v91\n"
MF "v[84:87],a[84:87],a[8:11],v[84:87]\n"
EX "v92,v92\n"
MF "v[80:83],a[88:91],a[12:15],v[80:83]\n"
EX "v93,v93\n"
MF "v[84:87],a[92:95],a[12:15],v[84:87]\n"
EX "v94,v94\n"
WT "lgkmcnt(0)\n"
MF "v[136:139],a[96:99],v[116:119],v[136:139]\n"
EX "v95,v95\n"
"s_add_u32 m0,s50,s48\n"
MF "v[140:143],a[100:103],v[116:119],v[140:143]\n"
CV "v122,v92,v93\n"
CV "v123,v94,v95\n"
BL "v8,s[12:15],s78 offen nt lds\n"
MF "v[144:147],a[104:107],v[116:119],v[144:147]\n"
BL "v9,s[12:15],s78 offen offset:1056 nt lds\n"
"s_add_u32 m0,m0,8448\n"
MF "v[148:151],a[108:111],v[116:119],v[148:151]\n"
BL "v10,s[16:19],s79 offen nt lds\n"
MF "v[152:155],a[112:115],v[116:119],v[152:155]\n"
BL "v11,s[16:19],s79 offen offset:1056 nt lds\n"
"s_add_u32 s51,s51,8192\n"
"s_mov_b32 s79,s78\n"
"s_xor_b32 s78,s51,s41\n"
MF "v[156:159],a[116:119],v[116:119],v[156:159]\n"
"v_add_u32 v6,s49,v4\n"
"s_mov_b32 s50,s42\n"
"v_max3_f32 v128,v80,v81,v82\n"
"v_max3_f32 v128,v128,v83,v84\n"
MF "v[160:163],a[120:123],v[116:119],v[160:163]\n"
"v_add_u32 v7,s49,v5\n"
"s_mov_b32 s42,s49\n"
"v_max3_f32 v128,v128,v85,v86\n"
"v_max_f32 v128,v128,v87\n"
MF "v[164:167],a[124:127],v[116:119],v[164:167]\n"
"s_add_u32 s49,s49,16896\n"
"s_cmp_eq_u32 s49,84480\n"
"s_cselect_b32 s49,0,s49\n"
"v_cmp_lt_f32 vcc,s53,v128\n"
MF "v[168:171],a[48:51],v[116:119],v[168:171]\n"
"s_cbranch_vccnz Ls317_%=\n"
"Ls318_%=:\n"
WT "vmcnt(22)\n"
WT "vmcnt(9)\n"
"s_barrier\n"
MF "v[88:91],a[64:67],a[16:19],v[108:111]\n"
EX "v80,v80\n"
DR "a[64:67],v6 offset:0\n"
MF "v[92:95],a[68:71],a[16:19],v[108:111]\n"
EX "v81,v81\n"
DR "a[68:71],v6 offset:512\n"
MF "v[88:91],a[72:75],a[20:23],v[88:91]\n"
EX "v82,v82\n"
DR "a[72:75],v6 offset:64\n"
MF "v[92:95],a[76:79],a[20:23],v[92:95]\n"
EX "v83,v83\n"
DR "a[76:79],v6 offset:576\n"
MF "v[88:91],a[80:83],a[24:27],v[88:91]\n"
CV "v116,v80,v81\n"
CV "v117,v82,v83\n"
DR "a[80:83],v6 offset:128\n"
MF "v[92:95],a[84:87],a[24:27],v[92:95]\n"
EX "v84,v84\n"
DR "a[84:87],v6 offset:640\n"
MF "v[88:91],a[88:91],a[28:31],v[88:91]\n"
EX "v85,v85\n"
DR "a[88:91],v6 offset:192\n"
MF "v[92:95],a[92:95],a[28:31],v[92:95]\n"
EX "v86,v86\n"
DR "a[92:95],v6 offset:704\n"
MF "v[172:175],a[96:99],v[120:123],v[172:175]\n"
EX "v87,v87\n"
DT "a[96:97],v7 offset:0\n"
DT "a[98:99],v7 offset:512\n"
MF "v[176:179],a[100:103],v[120:123],v[176:179]\n"
CV "v118,v84,v85\n"
CV "v119,v86,v87\n"
DT "a[100:101],v7 offset:32\n"
DT "a[102:103],v7 offset:544\n"
MF "v[180:183],a[104:107],v[120:123],v[180:183]\n"
DT "a[104:105],v7 offset:64\n"
DT "a[106:107],v7 offset:576\n"
MF "v[184:187],a[108:111],v[120:123],v[184:187]\n"
DT "a[108:109],v7 offset:96\n"
DT "a[110:111],v7 offset:608\n"
MF "v[188:191],a[112:115],v[120:123],v[188:191]\n"
DT "a[112:113],v7 offset:128\n"
DT "a[114:115],v7 offset:640\n"
MF "v[192:195],a[116:119],v[120:123],v[192:195]\n"
DT "a[116:117],v7 offset:160\n"
DT "a[118:119],v7 offset:672\n"
"v_max3_f32 v128,v88,v89,v90\n"
"v_max3_f32 v128,v128,v91,v92\n"
MF "v[196:199],a[120:123],v[120:123],v[196:199]\n"
DT "a[120:121],v7 offset:192\n"
DT "a[122:123],v7 offset:704\n"
"v_max3_f32 v128,v128,v93,v94\n"
"v_max_f32 v128,v128,v95\n"
MF "v[200:203],a[124:127],v[120:123],v[200:203]\n"
DT "a[124:125],v7 offset:224\n"
DT "a[126:127],v7 offset:736\n"
"v_cmp_lt_f32 vcc,s53,v128\n"
MF "v[204:207],a[48:51],v[120:123],v[204:207]\n"
"s_cbranch_vccnz Ls319_%=\n"
"Ls320_%=:\n"
DT "a[212:213],v21 offset:0\n"
DT "a[214:215],v21 offset:4608\n"
DT "a[216:217],v21 offset:32\n"
DT "a[218:219],v21 offset:4640\n"
DT "a[220:221],v22 offset:0\n"
DT "a[222:223],v22 offset:4608\n"
DT "a[224:225],v22 offset:32\n"
DT "a[226:227],v22 offset:4640\n"
DT "a[228:229],v23 offset:0\n"
DT "a[230:231],v23 offset:4608\n"
DT "a[232:233],v23 offset:32\n"
DT "a[234:235],v23 offset:4640\n"
DT "a[236:237],v24 offset:0\n"
DT "a[238:239],v24 offset:4608\n"
DT "a[240:241],v24 offset:32\n"
DT "a[242:243],v24 offset:4640\n"
DT "a[244:245],v25 offset:0\n"
DT "a[246:247],v25 offset:4608\n"
WT "vmcnt(4)\n"
"v_cmp_eq_u32 vcc,v31,v28\n"
"s_andn2_b64 vcc,vcc,s[62:63]\n"
"s_cbranch_scc1 Lp321_%=\n"
"Lp322_%=:\n"
WT "lgkmcnt(15)\n"
MF "v[80:83],a[64:67],a[0:3],v[104:107]\n"
EX "v88,v88\n"
MF "v[84:87],a[68:71],a[0:3],v[104:107]\n"
EX "v89,v89\n"
MF "v[80:83],a[72:75],a[4:7],v[80:83]\n"
EX "v90,v90\n"
MF "v[84:87],a[76:79],a[4:7],v[84:87]\n"
EX "v91,v91\n"
MF "v[80:83],a[80:83],a[8:11],v[80:83]\n"
CV "v120,v88,v89\n"
CV "v121,v90,v91\n"
MF "v[84:87],a[84:87],a[8:11],v[84:87]\n"
EX "v92,v92\n"
MF "v[80:83],a[88:91],a[12:15],v[80:83]\n"
EX "v93,v93\n"
MF "v[84:87],a[92:95],a[12:15],v[84:87]\n"
EX "v94,v94\n"
WT "lgkmcnt(0)\n"
MF "v[136:139],a[96:99],v[116:119],v[136:139]\n"
EX "v95,v95\n"
"s_add_u32 m0,s50,s48\n"
MF "v[140:143],a[100:103],v[116:119],v[140:143]\n"
CV "v122,v92,v93\n"
CV "v123,v94,v95\n"
BL "v8,s[12:15],s78 offen nt lds\n"
MF "v[144:147],a[104:107],v[116:119],v[144:147]\n"
BL "v9,s[12:15],s78 offen offset:1056 nt lds\n"
"s_add_u32 m0,m0,8448\n"
MF "v[148:151],a[108:111],v[116:119],v[148:151]\n"
BL "v10,s[16:19],s79 offen nt lds\n"
MF "v[152:155],a[112:115],v[116:119],v[152:155]\n"
BL "v11,s[16:19],s79 offen offset:1056 nt lds\n"
"s_add_u32 s51,s51,8192\n"
"s_mov_b32 s79,s78\n"
"s_xor_b32 s78,s51,s41\n"
MF "v[156:159],a[116:119],v[116:119],v[156:159]\n"
"v_add_u32 v6,s49,v4\n"
"s_mov_b32 s50,s42\n"
"v_max3_f32 v128,v80,v81,v82\n"
"v_max3_f32 v128,v128,v83,v84\n"
MF "v[160:163],a[120:123],v[116:119],v[160:163]\n"
"v_add_u32 v7,s49,v5\n"
"s_mov_b32 s42,s49\n"
"v_max3_f32 v128,v128,v85,v86\n"
"v_max_f32 v128,v128,v87\n"
MF "v[164:167],a[124:127],v[116:119],v[164:167]\n"
"s_add_u32 s49,s49,16896\n"
"s_cmp_eq_u32 s49,84480\n"
"s_cselect_b32 s49,0,s49\n"
"v_cmp_lt_f32 vcc,s53,v128\n"
MF "v[168:171],a[48:51],v[116:119],v[168:171]\n"
"s_cbranch_vccnz Ls324_%=\n"
"Ls325_%=:\n"
WT "vmcnt(22)\n"
"s_barrier\n"
MF "v[88:91],a[64:67],a[16:19],v[108:111]\n"
EX "v80,v80\n"
DR "a[64:67],v6 offset:0\n"
MF "v[92:95],a[68:71],a[16:19],v[108:111]\n"
EX "v81,v81\n"
DR "a[68:71],v6 offset:512\n"
MF "v[88:91],a[72:75],a[20:23],v[88:91]\n"
EX "v82,v82\n"
DR "a[72:75],v6 offset:64\n"
MF "v[92:95],a[76:79],a[20:23],v[92:95]\n"
EX "v83,v83\n"
DR "a[76:79],v6 offset:576\n"
MF "v[88:91],a[80:83],a[24:27],v[88:91]\n"
CV "v116,v80,v81\n"
CV "v117,v82,v83\n"
DR "a[80:83],v6 offset:128\n"
MF "v[92:95],a[84:87],a[24:27],v[92:95]\n"
EX "v84,v84\n"
DR "a[84:87],v6 offset:640\n"
MF "v[88:91],a[88:91],a[28:31],v[88:91]\n"
EX "v85,v85\n"
DR "a[88:91],v6 offset:192\n"
MF "v[92:95],a[92:95],a[28:31],v[92:95]\n"
EX "v86,v86\n"
DR "a[92:95],v6 offset:704\n"
MF "v[172:175],a[96:99],v[120:123],v[172:175]\n"
EX "v87,v87\n"
DT "a[96:97],v7 offset:0\n"
DT "a[98:99],v7 offset:512\n"
MF "v[176:179],a[100:103],v[120:123],v[176:179]\n"
CV "v118,v84,v85\n"
CV "v119,v86,v87\n"
DT "a[100:101],v7 offset:32\n"
DT "a[102:103],v7 offset:544\n"
MF "v[180:183],a[104:107],v[120:123],v[180:183]\n"
DT "a[104:105],v7 offset:64\n"
DT "a[106:107],v7 offset:576\n"
MF "v[184:187],a[108:111],v[120:123],v[184:187]\n"
DT "a[108:109],v7 offset:96\n"
DT "a[110:111],v7 offset:608\n"
MF "v[188:191],a[112:115],v[120:123],v[188:191]\n"
DT "a[112:113],v7 offset:128\n"
DT "a[114:115],v7 offset:640\n"
MF "v[192:195],a[116:119],v[120:123],v[192:195]\n"
DT "a[116:117],v7 offset:160\n"
DT "a[118:119],v7 offset:672\n"
"v_max3_f32 v128,v88,v89,v90\n"
"v_max3_f32 v128,v128,v91,v92\n"
MF "v[196:199],a[120:123],v[120:123],v[196:199]\n"
DT "a[120:121],v7 offset:192\n"
DT "a[122:123],v7 offset:704\n"
"v_max3_f32 v128,v128,v93,v94\n"
"v_max_f32 v128,v128,v95\n"
MF "v[200:203],a[124:127],v[120:123],v[200:203]\n"
DT "a[124:125],v7 offset:224\n"
DT "a[126:127],v7 offset:736\n"
"v_cmp_lt_f32 vcc,s53,v128\n"
MF "v[204:207],a[48:51],v[120:123],v[204:207]\n"
"s_cbranch_vccnz Ls326_%=\n"
"Ls327_%=:\n"
MF "a[128:131],a[212:215],v[32:35],0\n"
"s_add_u32 m0,s76,121344\n"
MF "a[164:167],a[212:215],v[48:51],0\n"
"s_add_u32 s67,s69,0\n"
MF "a[200:203],a[212:215],v[64:67],0\n"
DT "a[212:213],v21 offset:9216\n"
BL "v12,s[24:27],s67 offen offset:0 lds\n"
MF "a[132:135],a[216:219],v[32:35],0\n"
DT "a[214:215],v21 offset:13824\n"
MF "a[168:171],a[216:219],v[48:51],0\n"
MF "a[204:207],a[216:219],v[64:67],0\n"
DT "a[216:217],v21 offset:9248\n"
BL "v13,s[24:27],s67 offen offset:1024 lds\n"
MF "a[136:139],a[220:223],v[32:35],0\n"
DT "a[218:219],v21 offset:13856\n"
MF "a[172:175],a[220:223],v[48:51],0\n"
DT "a[220:221],v22 offset:9216\n"
MF "a[140:143],a[224:227],v[32:35],0\n"
DT "a[222:223],v22 offset:13824\n"
BL "v14,s[24:27],s67 offen offset:2048 lds\n"
MF "a[176:179],a[224:227],v[48:51],0\n"
"s_add_u32 m0,s76,124416\n"
DT "a[224:225],v22 offset:9248\n"
MF "a[144:147],a[228:231],v[32:35],0\n"
"s_add_u32 s67,s69,0\n"
DT "a[226:227],v22 offset:13856\n"
MF "a[180:183],a[228:231],v[48:51],0\n"
DT "a[228:229],v23 offset:9216\n"
BL "v15,s[24:27],s67 offen offset:0 lds\n"
MF "a[148:151],a[232:235],v[32:35],0\n"
DT "a[230:231],v23 offset:13824\n"
MF "a[184:187],a[232:235],v[48:51],0\n"
DT "a[232:233],v23 offset:9248\n"
MF "a[152:155],a[236:239],v[32:35],0\n"
DT "a[234:235],v23 offset:13856\n"
BL "v16,s[24:27],s67 offen offset:1024 lds\n"
MF "a[188:191],a[236:239],v[48:51],0\n"
DT "a[236:237],v24 offset:9216\n"
MF "a[156:159],a[240:243],v[32:35],0\n"
DT "a[238:239],v24 offset:13824\n"
MF "a[192:195],a[240:243],v[48:51],0\n"
DT "a[240:241],v24 offset:9248\n"
BL "v17,s[24:27],s67 offen offset:2048 lds\n"
MF "a[160:163],a[244:247],v[32:35],0\n"
DT "a[242:243],v24 offset:13856\n"
MF "a[196:199],a[244:247],v[48:51],0\n"
DT "a[244:245],v25 offset:9216\n"
DT "a[246:247],v25 offset:13824\n"
WT "lgkmcnt(15)\n"
MF "v[80:83],a[64:67],a[0:3],v[104:107]\n"
EX "v88,v88\n"
MF "v[84:87],a[68:71],a[0:3],v[104:107]\n"
EX "v89,v89\n"
MF "v[80:83],a[72:75],a[4:7],v[80:83]\n"
EX "v90,v90\n"
MF "v[84:87],a[76:79],a[4:7],v[84:87]\n"
EX "v91,v91\n"
MF "v[80:83],a[80:83],a[8:11],v[80:83]\n"
CV "v120,v88,v89\n"
CV "v121,v90,v91\n"
MF "v[84:87],a[84:87],a[8:11],v[84:87]\n"
EX "v92,v92\n"
MF "v[80:83],a[88:91],a[12:15],v[80:83]\n"
EX "v93,v93\n"
MF "v[84:87],a[92:95],a[12:15],v[84:87]\n"
EX "v94,v94\n"
WT "lgkmcnt(0)\n"
MF "v[136:139],a[96:99],v[116:119],v[136:139]\n"
EX "v95,v95\n"
"s_add_u32 m0,s50,s48\n"
MF "v[140:143],a[100:103],v[116:119],v[140:143]\n"
CV "v122,v92,v93\n"
CV "v123,v94,v95\n"
BL "v8,s[12:15],s78 offen nt lds\n"
MF "v[144:147],a[104:107],v[116:119],v[144:147]\n"
BL "v9,s[12:15],s78 offen offset:1056 nt lds\n"
"s_add_u32 m0,m0,8448\n"
MF "v[148:151],a[108:111],v[116:119],v[148:151]\n"
BL "v10,s[16:19],s79 offen nt lds\n"
MF "v[152:155],a[112:115],v[116:119],v[152:155]\n"
BL "v11,s[16:19],s79 offen offset:1056 nt lds\n"
"s_add_u32 s51,s51,8192\n"
"s_mov_b32 s79,s78\n"
"s_xor_b32 s78,s51,s41\n"
MF "v[156:159],a[116:119],v[116:119],v[156:159]\n"
"v_add_u32 v6,s49,v4\n"
"s_mov_b32 s50,s42\n"
"v_max3_f32 v128,v80,v81,v82\n"
"v_max3_f32 v128,v128,v83,v84\n"
MF "v[160:163],a[120:123],v[116:119],v[160:163]\n"
"v_add_u32 v7,s49,v5\n"
"s_mov_b32 s42,s49\n"
"v_max3_f32 v128,v128,v85,v86\n"
"v_max_f32 v128,v128,v87\n"
MF "v[164:167],a[124:127],v[116:119],v[164:167]\n"
"s_add_u32 s49,s49,16896\n"
"s_cmp_eq_u32 s49,84480\n"
"s_cselect_b32 s49,0,s49\n"
"v_cmp_lt_f32 vcc,s53,v128\n"
MF "v[168:171],a[48:51],v[116:119],v[168:171]\n"
"s_cbranch_vccnz Ls328_%=\n"
"Ls329_%=:\n"
WT "vmcnt(19)\n"
"s_barrier\n"
MF "v[88:91],a[64:67],a[16:19],v[108:111]\n"
EX "v80,v80\n"
DR "a[64:67],v6 offset:0\n"
MF "v[92:95],a[68:71],a[16:19],v[108:111]\n"
EX "v81,v81\n"
DR "a[68:71],v6 offset:512\n"
MF "v[88:91],a[72:75],a[20:23],v[88:91]\n"
EX "v82,v82\n"
DR "a[72:75],v6 offset:64\n"
MF "v[92:95],a[76:79],a[20:23],v[92:95]\n"
EX "v83,v83\n"
DR "a[76:79],v6 offset:576\n"
MF "v[88:91],a[80:83],a[24:27],v[88:91]\n"
CV "v116,v80,v81\n"
CV "v117,v82,v83\n"
DR "a[80:83],v6 offset:128\n"
MF "v[92:95],a[84:87],a[24:27],v[92:95]\n"
EX "v84,v84\n"
DR "a[84:87],v6 offset:640\n"
MF "v[88:91],a[88:91],a[28:31],v[88:91]\n"
EX "v85,v85\n"
DR "a[88:91],v6 offset:192\n"
MF "v[92:95],a[92:95],a[28:31],v[92:95]\n"
EX "v86,v86\n"
DR "a[92:95],v6 offset:704\n"
MF "v[172:175],a[96:99],v[120:123],v[172:175]\n"
EX "v87,v87\n"
DT "a[96:97],v7 offset:0\n"
DT "a[98:99],v7 offset:512\n"
MF "v[176:179],a[100:103],v[120:123],v[176:179]\n"
CV "v118,v84,v85\n"
CV "v119,v86,v87\n"
DT "a[100:101],v7 offset:32\n"
DT "a[102:103],v7 offset:544\n"
MF "v[180:183],a[104:107],v[120:123],v[180:183]\n"
DT "a[104:105],v7 offset:64\n"
DT "a[106:107],v7 offset:576\n"
MF "v[184:187],a[108:111],v[120:123],v[184:187]\n"
DT "a[108:109],v7 offset:96\n"
DT "a[110:111],v7 offset:608\n"
MF "v[188:191],a[112:115],v[120:123],v[188:191]\n"
DT "a[112:113],v7 offset:128\n"
DT "a[114:115],v7 offset:640\n"
MF "v[192:195],a[116:119],v[120:123],v[192:195]\n"
DT "a[116:117],v7 offset:160\n"
DT "a[118:119],v7 offset:672\n"
"v_max3_f32 v128,v88,v89,v90\n"
"v_max3_f32 v128,v128,v91,v92\n"
MF "v[196:199],a[120:123],v[120:123],v[196:199]\n"
DT "a[120:121],v7 offset:192\n"
DT "a[122:123],v7 offset:704\n"
"v_max3_f32 v128,v128,v93,v94\n"
"v_max_f32 v128,v128,v95\n"
MF "v[200:203],a[124:127],v[120:123],v[200:203]\n"
DT "a[124:125],v7 offset:224\n"
DT "a[126:127],v7 offset:736\n"
"v_cmp_lt_f32 vcc,s53,v128\n"
MF "v[204:207],a[48:51],v[120:123],v[204:207]\n"
"s_cbranch_vccnz Ls330_%=\n"
"Ls331_%=:\n"
MF "a[128:131],a[212:215],v[36:39],a[128:131]\n"
MF "a[164:167],a[212:215],v[52:55],a[164:167]\n"
"s_add_u32 s44,s57,0\n"
MF "a[200:203],a[212:215],v[68:71],a[200:203]\n"
DT "a[212:213],v21 offset:18432\n"
BL "v[32:35],v244,s[28:31],s44 offen offset:0 sc1\n"
MF "a[132:135],a[216:219],v[36:39],a[132:135]\n"
DT "a[214:215],v21 offset:23040\n"
MF "a[168:171],a[216:219],v[52:55],a[168:171]\n"
MF "a[204:207],a[216:219],v[68:71],a[204:207]\n"
DT "a[216:217],v21 offset:18464\n"
BL "v[48:51],v245,s[28:31],s44 offen offset:0 sc1\n"
MF "a[136:139],a[220:223],v[36:39],a[136:139]\n"
DT "a[218:219],v21 offset:23072\n"
MF "a[172:175],a[220:223],v[52:55],a[172:175]\n"
DT "a[220:221],v22 offset:18432\n"
MF "a[140:143],a[224:227],v[36:39],a[140:143]\n"
DT "a[222:223],v22 offset:23040\n"
BL "v[64:67],v246,s[28:31],s44 offen offset:0 sc1\n"
MF "a[176:179],a[224:227],v[52:55],a[176:179]\n"
"s_add_u32 m0,s76,127488\n"
DT "a[224:225],v22 offset:18464\n"
MF "a[144:147],a[228:231],v[36:39],a[144:147]\n"
"s_add_u32 s67,s69,0\n"
DT "a[226:227],v22 offset:23072\n"
MF "a[180:183],a[228:231],v[52:55],a[180:183]\n"
DT "a[228:229],v23 offset:18432\n"
BL "v18,s[24:27],s67 offen offset:0 lds\n"
MF "a[148:151],a[232:235],v[36:39],a[148:151]\n"
DT "a[230:231],v23 offset:23040\n"
MF "a[184:187],a[232:235],v[52:55],a[184:187]\n"
DT "a[232:233],v23 offset:18464\n"
MF "a[152:155],a[236:239],v[36:39],a[152:155]\n"
DT "a[234:235],v23 offset:23072\n"
BL "v19,s[24:27],s67 offen offset:1024 lds\n"
MF "a[188:191],a[236:239],v[52:55],a[188:191]\n"
DT "a[236:237],v24 offset:18432\n"
MF "a[156:159],a[240:243],v[36:39],a[156:159]\n"
DT "a[238:239],v24 offset:23040\n"
MF "a[192:195],a[240:243],v[52:55],a[192:195]\n"
DT "a[240:241],v24 offset:18464\n"
BL "v20,s[24:27],s67 offen offset:2048 lds\n"
MF "a[160:163],a[244:247],v[36:39],a[160:163]\n"
DT "a[242:243],v24 offset:23072\n"
MF "a[196:199],a[244:247],v[52:55],a[196:199]\n"
DT "a[244:245],v25 offset:18432\n"
DT "a[246:247],v25 offset:23040\n"
WT "lgkmcnt(15)\n"
MF "v[80:83],a[64:67],a[0:3],v[104:107]\n"
EX "v88,v88\n"
MF "v[84:87],a[68:71],a[0:3],v[104:107]\n"
EX "v89,v89\n"
MF "v[80:83],a[72:75],a[4:7],v[80:83]\n"
EX "v90,v90\n"
MF "v[84:87],a[76:79],a[4:7],v[84:87]\n"
EX "v91,v91\n"
MF "v[80:83],a[80:83],a[8:11],v[80:83]\n"
CV "v120,v88,v89\n"
CV "v121,v90,v91\n"
MF "v[84:87],a[84:87],a[8:11],v[84:87]\n"
EX "v92,v92\n"
MF "v[80:83],a[88:91],a[12:15],v[80:83]\n"
EX "v93,v93\n"
MF "v[84:87],a[92:95],a[12:15],v[84:87]\n"
EX "v94,v94\n"
WT "lgkmcnt(0)\n"
MF "v[136:139],a[96:99],v[116:119],v[136:139]\n"
EX "v95,v95\n"
"s_add_u32 m0,s50,s48\n"
MF "v[140:143],a[100:103],v[116:119],v[140:143]\n"
CV "v122,v92,v93\n"
CV "v123,v94,v95\n"
BL "v8,s[12:15],s78 offen nt lds\n"
MF "v[144:147],a[104:107],v[116:119],v[144:147]\n"
BL "v9,s[12:15],s78 offen offset:1056 nt lds\n"
"s_add_u32 m0,m0,8448\n"
MF "v[148:151],a[108:111],v[116:119],v[148:151]\n"
BL "v10,s[16:19],s79 offen nt lds\n"
MF "v[152:155],a[112:115],v[116:119],v[152:155]\n"
BL "v11,s[16:19],s79 offen offset:1056 nt lds\n"
"s_add_u32 s51,s51,8192\n"
"s_mov_b32 s79,s78\n"
"s_xor_b32 s78,s51,s41\n"
MF "v[156:159],a[116:119],v[116:119],v[156:159]\n"
"v_add_u32 v6,s49,v4\n"
"s_mov_b32 s50,s42\n"
"v_max3_f32 v128,v80,v81,v82\n"
"v_max3_f32 v128,v128,v83,v84\n"
MF "v[160:163],a[120:123],v[116:119],v[160:163]\n"
"v_add_u32 v7,s49,v5\n"
"s_mov_b32 s42,s49\n"
"v_max3_f32 v128,v128,v85,v86\n"
"v_max_f32 v128,v128,v87\n"
MF "v[164:167],a[124:127],v[116:119],v[164:167]\n"
"s_add_u32 s49,s49,16896\n"
"s_cmp_eq_u32 s49,84480\n"
"s_cselect_b32 s49,0,s49\n"
"v_cmp_lt_f32 vcc,s53,v128\n"
MF "v[168:171],a[48:51],v[116:119],v[168:171]\n"
"s_cbranch_vccnz Ls332_%=\n"
"Ls333_%=:\n"
WT "vmcnt(24)\n"
"s_barrier\n"
MF "v[88:91],a[64:67],a[16:19],v[108:111]\n"
EX "v80,v80\n"
DR "a[64:67],v6 offset:0\n"
MF "v[92:95],a[68:71],a[16:19],v[108:111]\n"
EX "v81,v81\n"
DR "a[68:71],v6 offset:512\n"
MF "v[88:91],a[72:75],a[20:23],v[88:91]\n"
EX "v82,v82\n"
DR "a[72:75],v6 offset:64\n"
MF "v[92:95],a[76:79],a[20:23],v[92:95]\n"
EX "v83,v83\n"
DR "a[76:79],v6 offset:576\n"
MF "v[88:91],a[80:83],a[24:27],v[88:91]\n"
CV "v116,v80,v81\n"
CV "v117,v82,v83\n"
DR "a[80:83],v6 offset:128\n"
MF "v[92:95],a[84:87],a[24:27],v[92:95]\n"
EX "v84,v84\n"
DR "a[84:87],v6 offset:640\n"
MF "v[88:91],a[88:91],a[28:31],v[88:91]\n"
EX "v85,v85\n"
DR "a[88:91],v6 offset:192\n"
MF "v[92:95],a[92:95],a[28:31],v[92:95]\n"
EX "v86,v86\n"
DR "a[92:95],v6 offset:704\n"
MF "v[172:175],a[96:99],v[120:123],v[172:175]\n"
EX "v87,v87\n"
DT "a[96:97],v7 offset:0\n"
DT "a[98:99],v7 offset:512\n"
MF "v[176:179],a[100:103],v[120:123],v[176:179]\n"
CV "v118,v84,v85\n"
CV "v119,v86,v87\n"
DT "a[100:101],v7 offset:32\n"
DT "a[102:103],v7 offset:544\n"
MF "v[180:183],a[104:107],v[120:123],v[180:183]\n"
DT "a[104:105],v7 offset:64\n"
DT "a[106:107],v7 offset:576\n"
MF "v[184:187],a[108:111],v[120:123],v[184:187]\n"
DT "a[108:109],v7 offset:96\n"
DT "a[110:111],v7 offset:608\n"
MF "v[188:191],a[112:115],v[120:123],v[188:191]\n"
DT "a[112:113],v7 offset:128\n"
DT "a[114:115],v7 offset:640\n"
MF "v[192:195],a[116:119],v[120:123],v[192:195]\n"
DT "a[116:117],v7 offset:160\n"
DT "a[118:119],v7 offset:672\n"
"v_max3_f32 v128,v88,v89,v90\n"
"v_max3_f32 v128,v128,v91,v92\n"
MF "v[196:199],a[120:123],v[120:123],v[196:199]\n"
DT "a[120:121],v7 offset:192\n"
DT "a[122:123],v7 offset:704\n"
"v_max3_f32 v128,v128,v93,v94\n"
"v_max_f32 v128,v128,v95\n"
MF "v[200:203],a[124:127],v[120:123],v[200:203]\n"
DT "a[124:125],v7 offset:224\n"
DT "a[126:127],v7 offset:736\n"
"v_cmp_lt_f32 vcc,s53,v128\n"
MF "v[204:207],a[48:51],v[120:123],v[204:207]\n"
"s_cbranch_vccnz Ls334_%=\n"
"Ls335_%=:\n"
MF "a[128:131],a[212:215],v[40:43],a[128:131]\n"
MF "a[164:167],a[212:215],v[56:59],a[164:167]\n"
"s_add_u32 s44,s57,0\n"
MF "a[200:203],a[212:215],v[72:75],a[200:203]\n"
DT "a[212:213],v21 offset:27648\n"
BL "v[36:39],v244,s[28:31],s44 offen offset:1024 sc1\n"
MF "a[132:135],a[216:219],v[40:43],a[132:135]\n"
DT "a[214:215],v21 offset:32256\n"
MF "a[168:171],a[216:219],v[56:59],a[168:171]\n"
MF "a[204:207],a[216:219],v[72:75],a[204:207]\n"
DT "a[216:217],v21 offset:27680\n"
MF "a[136:139],a[220:223],v[40:43],a[136:139]\n"
DT "a[218:219],v21 offset:32288\n"
"v_add_u32 v21,36864,v21\n"
MF "a[172:175],a[220:223],v[56:59],a[172:175]\n"
DT "a[220:221],v22 offset:27648\n"
MF "a[140:143],a[224:227],v[40:43],a[140:143]\n"
DT "a[222:223],v22 offset:32256\n"
BL "v[52:55],v245,s[28:31],s44 offen offset:1024 sc1\n"
MF "a[176:179],a[224:227],v[56:59],a[176:179]\n"
DT "a[224:225],v22 offset:27680\n"
MF "a[144:147],a[228:231],v[40:43],a[144:147]\n"
DT "a[226:227],v22 offset:32288\n"
"v_add_u32 v22,36864,v22\n"
MF "a[180:183],a[228:231],v[56:59],a[180:183]\n"
DT "a[228:229],v23 offset:27648\n"
MF "a[148:151],a[232:235],v[40:43],a[148:151]\n"
DT "a[230:231],v23 offset:32256\n"
MF "a[184:187],a[232:235],v[56:59],a[184:187]\n"
DT "a[232:233],v23 offset:27680\n"
MF "a[152:155],a[236:239],v[40:43],a[152:155]\n"
DT "a[234:235],v23 offset:32288\n"
BL "v[68:71],v246,s[28:31],s44 offen offset:1024 sc1\n"
"v_add_u32 v23,36864,v23\n"
MF "a[188:191],a[236:239],v[56:59],a[188:191]\n"
DT "a[236:237],v24 offset:27648\n"
MF "a[156:159],a[240:243],v[40:43],a[156:159]\n"
DT "a[238:239],v24 offset:32256\n"
MF "a[192:195],a[240:243],v[56:59],a[192:195]\n"
DT "a[240:241],v24 offset:27680\n"
MF "a[160:163],a[244:247],v[40:43],a[160:163]\n"
DT "a[242:243],v24 offset:32288\n"
"v_add_u32 v24,36864,v24\n"
MF "a[196:199],a[244:247],v[56:59],a[196:199]\n"
DT "a[244:245],v25 offset:27648\n"
DT "a[246:247],v25 offset:32256\n"
"v_add_u32 v25,36864,v25\n"
WT "lgkmcnt(15)\n"
MF "v[80:83],a[64:67],a[0:3],v[104:107]\n"
EX "v88,v88\n"
MF "v[84:87],a[68:71],a[0:3],v[104:107]\n"
EX "v89,v89\n"
MF "v[80:83],a[72:75],a[4:7],v[80:83]\n"
EX "v90,v90\n"
MF "v[84:87],a[76:79],a[4:7],v[84:87]\n"
EX "v91,v91\n"
MF "v[80:83],a[80:83],a[8:11],v[80:83]\n"
CV "v120,v88,v89\n"
CV "v121,v90,v91\n"
MF "v[84:87],a[84:87],a[8:11],v[84:87]\n"
EX "v92,v92\n"
MF "v[80:83],a[88:91],a[12:15],v[80:83]\n"
EX "v93,v93\n"
MF "v[84:87],a[92:95],a[12:15],v[84:87]\n"
EX "v94,v94\n"
WT "lgkmcnt(0)\n"
MF "v[136:139],a[96:99],v[116:119],v[136:139]\n"
EX "v95,v95\n"
"s_add_u32 m0,s50,s48\n"
MF "v[140:143],a[100:103],v[116:119],v[140:143]\n"
CV "v122,v92,v93\n"
CV "v123,v94,v95\n"
BL "v8,s[12:15],s78 offen nt lds\n"
MF "v[144:147],a[104:107],v[116:119],v[144:147]\n"
BL "v9,s[12:15],s78 offen offset:1056 nt lds\n"
"s_add_u32 m0,m0,8448\n"
MF "v[148:151],a[108:111],v[116:119],v[148:151]\n"
BL "v10,s[16:19],s79 offen nt lds\n"
MF "v[152:155],a[112:115],v[116:119],v[152:155]\n"
BL "v11,s[16:19],s79 offen offset:1056 nt lds\n"
"s_add_u32 s51,s51,8192\n"
"s_mov_b32 s79,s78\n"
"s_xor_b32 s78,s51,s41\n"
MF "v[156:159],a[116:119],v[116:119],v[156:159]\n"
"v_add_u32 v6,s49,v4\n"
"s_mov_b32 s50,s42\n"
"v_max3_f32 v128,v80,v81,v82\n"
"v_max3_f32 v128,v128,v83,v84\n"
MF "v[160:163],a[120:123],v[116:119],v[160:163]\n"
"v_add_u32 v7,s49,v5\n"
"s_mov_b32 s42,s49\n"
"v_max3_f32 v128,v128,v85,v86\n"
"v_max_f32 v128,v128,v87\n"
MF "v[164:167],a[124:127],v[116:119],v[164:167]\n"
"s_add_u32 s49,s49,16896\n"
"s_cmp_eq_u32 s49,84480\n"
"s_cselect_b32 s49,0,s49\n"
"v_cmp_lt_f32 vcc,s53,v128\n"
MF "v[168:171],a[48:51],v[116:119],v[168:171]\n"
"s_cbranch_vccnz Ls336_%=\n"
"Ls337_%=:\n"
WT "vmcnt(27)\n"
WT "vmcnt(11)\n"
"s_barrier\n"
MF "v[88:91],a[64:67],a[16:19],v[108:111]\n"
EX "v80,v80\n"
DR "a[64:67],v6 offset:0\n"
MF "v[92:95],a[68:71],a[16:19],v[108:111]\n"
EX "v81,v81\n"
DR "a[68:71],v6 offset:512\n"
MF "v[88:91],a[72:75],a[20:23],v[88:91]\n"
EX "v82,v82\n"
DR "a[72:75],v6 offset:64\n"
MF "v[92:95],a[76:79],a[20:23],v[92:95]\n"
EX "v83,v83\n"
DR "a[76:79],v6 offset:576\n"
MF "v[88:91],a[80:83],a[24:27],v[88:91]\n"
CV "v116,v80,v81\n"
CV "v117,v82,v83\n"
DR "a[80:83],v6 offset:128\n"
MF "v[92:95],a[84:87],a[24:27],v[92:95]\n"
EX "v84,v84\n"
DR "a[84:87],v6 offset:640\n"
MF "v[88:91],a[88:91],a[28:31],v[88:91]\n"
EX "v85,v85\n"
DR "a[88:91],v6 offset:192\n"
MF "v[92:95],a[92:95],a[28:31],v[92:95]\n"
EX "v86,v86\n"
DR "a[92:95],v6 offset:704\n"
MF "v[172:175],a[96:99],v[120:123],v[172:175]\n"
EX "v87,v87\n"
DT "a[96:97],v7 offset:0\n"
DT "a[98:99],v7 offset:512\n"
MF "v[176:179],a[100:103],v[120:123],v[176:179]\n"
CV "v118,v84,v85\n"
CV "v119,v86,v87\n"
DT "a[100:101],v7 offset:32\n"
DT "a[102:103],v7 offset:544\n"
MF "v[180:183],a[104:107],v[120:123],v[180:183]\n"
DT "a[104:105],v7 offset:64\n"
DT "a[106:107],v7 offset:576\n"
MF "v[184:187],a[108:111],v[120:123],v[184:187]\n"
DT "a[108:109],v7 offset:96\n"
DT "a[110:111],v7 offset:608\n"
MF "v[188:191],a[112:115],v[120:123],v[188:191]\n"
DT "a[112:113],v7 offset:128\n"
DT "a[114:115],v7 offset:640\n"
MF "v[192:195],a[116:119],v[120:123],v[192:195]\n"
DT "a[116:117],v7 offset:160\n"
DT "a[118:119],v7 offset:672\n"
"v_max3_f32 v128,v88,v89,v90\n"
"v_max3_f32 v128,v128,v91,v92\n"
MF "v[196:199],a[120:123],v[120:123],v[196:199]\n"
DT "a[120:121],v7 offset:192\n"
DT "a[122:123],v7 offset:704\n"
"v_max3_f32 v128,v128,v93,v94\n"
"v_max_f32 v128,v128,v95\n"
MF "v[200:203],a[124:127],v[120:123],v[200:203]\n"
DT "a[124:125],v7 offset:224\n"
DT "a[126:127],v7 offset:736\n"
"v_cmp_lt_f32 vcc,s53,v128\n"
MF "v[204:207],a[48:51],v[120:123],v[204:207]\n"
"s_cbranch_vccnz Ls338_%=\n"
"Ls339_%=:\n"
MF "a[128:131],a[212:215],v[44:47],a[128:131]\n"
MF "a[164:167],a[212:215],v[60:63],a[164:167]\n"
"s_add_u32 s44,s57,0\n"
MF "a[200:203],a[212:215],v[76:79],a[200:203]\n"
DT "a[212:213],v21 offset:0\n"
BL "v[40:43],v244,s[28:31],s44 offen offset:2048 sc1\n"
MF "a[132:135],a[216:219],v[44:47],a[132:135]\n"
DT "a[214:215],v21 offset:4608\n"
MF "a[168:171],a[216:219],v[60:63],a[168:171]\n"
MF "a[204:207],a[216:219],v[76:79],a[204:207]\n"
DT "a[216:217],v21 offset:32\n"
MF "a[136:139],a[220:223],v[44:47],a[136:139]\n"
DT "a[218:219],v21 offset:4640\n"
MF "a[172:175],a[220:223],v[60:63],a[172:175]\n"
DT "a[220:221],v22 offset:0\n"
MF "a[140:143],a[224:227],v[44:47],a[140:143]\n"
DT "a[222:223],v22 offset:4608\n"
BL "v[56:59],v245,s[28:31],s44 offen offset:2048 sc1\n"
MF "a[176:179],a[224:227],v[60:63],a[176:179]\n"
DT "a[224:225],v22 offset:32\n"
MF "a[144:147],a[228:231],v[44:47],a[144:147]\n"
DT "a[226:227],v22 offset:4640\n"
MF "a[180:183],a[228:231],v[60:63],a[180:183]\n"
DT "a[228:229],v23 offset:0\n"
MF "a[148:151],a[232:235],v[44:47],a[148:151]\n"
DT "a[230:231],v23 offset:4608\n"
MF "a[184:187],a[232:235],v[60:63],a[184:187]\n"
DT "a[232:233],v23 offset:32\n"
MF "a[152:155],a[236:239],v[44:47],a[152:155]\n"
DT "a[234:235],v23 offset:4640\n"
BL "v[72:75],v246,s[28:31],s44 offen offset:2048 sc1\n"
MF "a[188:191],a[236:239],v[60:63],a[188:191]\n"
DT "a[236:237],v24 offset:0\n"
MF "a[156:159],a[240:243],v[44:47],a[156:159]\n"
DT "a[238:239],v24 offset:4608\n"
MF "a[192:195],a[240:243],v[60:63],a[192:195]\n"
DT "a[240:241],v24 offset:32\n"
MF "a[160:163],a[244:247],v[44:47],a[160:163]\n"
DT "a[242:243],v24 offset:4640\n"
MF "a[196:199],a[244:247],v[60:63],a[196:199]\n"
DT "a[244:245],v25 offset:0\n"
DT "a[246:247],v25 offset:4608\n"
WT "lgkmcnt(15)\n"
MF "v[80:83],a[64:67],a[0:3],v[104:107]\n"
EX "v88,v88\n"
MF "v[84:87],a[68:71],a[0:3],v[104:107]\n"
EX "v89,v89\n"
MF "v[80:83],a[72:75],a[4:7],v[80:83]\n"
EX "v90,v90\n"
MF "v[84:87],a[76:79],a[4:7],v[84:87]\n"
EX "v91,v91\n"
MF "v[80:83],a[80:83],a[8:11],v[80:83]\n"
CV "v120,v88,v89\n"
CV "v121,v90,v91\n"
MF "v[84:87],a[84:87],a[8:11],v[84:87]\n"
EX "v92,v92\n"
MF "v[80:83],a[88:91],a[12:15],v[80:83]\n"
EX "v93,v93\n"
MF "v[84:87],a[92:95],a[12:15],v[84:87]\n"
EX "v94,v94\n"
WT "lgkmcnt(0)\n"
MF "v[136:139],a[96:99],v[116:119],v[136:139]\n"
EX "v95,v95\n"
"s_add_u32 m0,s50,s48\n"
MF "v[140:143],a[100:103],v[116:119],v[140:143]\n"
CV "v122,v92,v93\n"
CV "v123,v94,v95\n"
BL "v8,s[12:15],s78 offen nt lds\n"
MF "v[144:147],a[104:107],v[116:119],v[144:147]\n"
BL "v9,s[12:15],s78 offen offset:1056 nt lds\n"
"s_add_u32 m0,m0,8448\n"
MF "v[148:151],a[108:111],v[116:119],v[148:151]\n"
BL "v10,s[16:19],s79 offen nt lds\n"
MF "v[152:155],a[112:115],v[116:119],v[152:155]\n"
BL "v11,s[16:19],s79 offen offset:1056 nt lds\n"
"s_add_u32 s51,s51,8192\n"
"s_mov_b32 s79,s78\n"
"s_xor_b32 s78,s51,s41\n"
MF "v[156:159],a[116:119],v[116:119],v[156:159]\n"
"v_add_u32 v6,s49,v4\n"
"s_mov_b32 s50,s42\n"
"v_max3_f32 v128,v80,v81,v82\n"
"v_max3_f32 v128,v128,v83,v84\n"
MF "v[160:163],a[120:123],v[116:119],v[160:163]\n"
"v_add_u32 v7,s49,v5\n"
"s_mov_b32 s42,s49\n"
"v_max3_f32 v128,v128,v85,v86\n"
"v_max_f32 v128,v128,v87\n"
MF "v[164:167],a[124:127],v[116:119],v[164:167]\n"
"s_add_u32 s49,s49,16896\n"
"s_cmp_eq_u32 s49,84480\n"
"s_cselect_b32 s49,0,s49\n"
"v_cmp_lt_f32 vcc,s53,v128\n"
MF "v[168:171],a[48:51],v[116:119],v[168:171]\n"
"s_cbranch_vccnz Ls340_%=\n"
"Ls341_%=:\n"
WT "vmcnt(24)\n"
"s_barrier\n"
MF "v[88:91],a[64:67],a[16:19],v[108:111]\n"
EX "v80,v80\n"
DR "a[64:67],v6 offset:0\n"
MF "v[92:95],a[68:71],a[16:19],v[108:111]\n"
EX "v81,v81\n"
DR "a[68:71],v6 offset:512\n"
MF "v[88:91],a[72:75],a[20:23],v[88:91]\n"
EX "v82,v82\n"
DR "a[72:75],v6 offset:64\n"
MF "v[92:95],a[76:79],a[20:23],v[92:95]\n"
EX "v83,v83\n"
DR "a[76:79],v6 offset:576\n"
MF "v[88:91],a[80:83],a[24:27],v[88:91]\n"
CV "v116,v80,v81\n"
CV "v117,v82,v83\n"
DR "a[80:83],v6 offset:128\n"
MF "v[92:95],a[84:87],a[24:27],v[92:95]\n"
EX "v84,v84\n"
DR "a[84:87],v6 offset:640\n"
MF "v[88:91],a[88:91],a[28:31],v[88:91]\n"
EX "v85,v85\n"
DR "a[88:91],v6 offset:192\n"
MF "v[92:95],a[92:95],a[28:31],v[92:95]\n"
EX "v86,v86\n"
DR "a[92:95],v6 offset:704\n"
MF "v[172:175],a[96:99],v[120:123],v[172:175]\n"
EX "v87,v87\n"
DT "a[96:97],v7 offset:0\n"
DT "a[98:99],v7 offset:512\n"
MF "v[176:179],a[100:103],v[120:123],v[176:179]\n"
CV "v118,v84,v85\n"
CV "v119,v86,v87\n"
DT "a[100:101],v7 offset:32\n"
DT "a[102:103],v7 offset:544\n"
MF "v[180:183],a[104:107],v[120:123],v[180:183]\n"
DT "a[104:105],v7 offset:64\n"
DT "a[106:107],v7 offset:576\n"
MF "v[184:187],a[108:111],v[120:123],v[184:187]\n"
DT "a[108:109],v7 offset:96\n"
DT "a[110:111],v7 offset:608\n"
MF "v[188:191],a[112:115],v[120:123],v[188:191]\n"
DT "a[112:113],v7 offset:128\n"
DT "a[114:115],v7 offset:640\n"
MF "v[192:195],a[116:119],v[120:123],v[192:195]\n"
DT "a[116:117],v7 offset:160\n"
DT "a[118:119],v7 offset:672\n"
"v_max3_f32 v128,v88,v89,v90\n"
"v_max3_f32 v128,v128,v91,v92\n"
MF "v[196:199],a[120:123],v[120:123],v[196:199]\n"
DT "a[120:121],v7 offset:192\n"
DT "a[122:123],v7 offset:704\n"
"v_max3_f32 v128,v128,v93,v94\n"
"v_max_f32 v128,v128,v95\n"
MF "v[200:203],a[124:127],v[120:123],v[200:203]\n"
DT "a[124:125],v7 offset:224\n"
DT "a[126:127],v7 offset:736\n"
"v_cmp_lt_f32 vcc,s53,v128\n"
MF "v[204:207],a[48:51],v[120:123],v[204:207]\n"
"s_cbranch_vccnz Ls342_%=\n"
"Ls343_%=:\n"
WT "vmcnt(21)\n"
MF "a[128:131],a[212:215],v[32:35],a[128:131]\n"
MF "a[164:167],a[212:215],v[48:51],a[164:167]\n"
"s_add_u32 s44,s57,0\n"
MF "a[200:203],a[212:215],v[64:67],a[200:203]\n"
DT "a[212:213],v21 offset:9216\n"
BL "v[44:47],v244,s[28:31],s44 offen offset:3072 sc1\n"
MF "a[132:135],a[216:219],v[32:35],a[132:135]\n"
DT "a[214:215],v21 offset:13824\n"
MF "a[168:171],a[216:219],v[48:51],a[168:171]\n"
BL "v[60:63],v245,s[28:31],s44 offen offset:3072 sc1\n"
MF "a[204:207],a[216:219],v[64:67],a[204:207]\n"
DT "a[216:217],v21 offset:9248\n"
MF "a[136:139],a[220:223],v[32:35],a[136:139]\n"
DT "a[218:219],v21 offset:13856\n"
BL "v[76:79],v246,s[28:31],s44 offen offset:3072 sc1\n"
MF "a[172:175],a[220:223],v[48:51],a[172:175]\n"
"s_add_u32 m0,s76,84480\n"
"s_add_u32 s67,s70,0\n"
DT "a[220:221],v22 offset:9216\n"
MF "a[140:143],a[224:227],v[32:35],a[140:143]\n"
DT "a[222:223],v22 offset:13824\n"
BL "v12,s[24:27],s67 offen offset:0 lds\n"
MF "a[176:179],a[224:227],v[48:51],a[176:179]\n"
DT "a[224:225],v22 offset:9248\n"
MF "a[144:147],a[228:231],v[32:35],a[144:147]\n"
DT "a[226:227],v22 offset:13856\n"
BL "v13,s[24:27],s67 offen offset:1024 lds\n"
MF "a[180:183],a[228:231],v[48:51],a[180:183]\n"
DT "a[228:229],v23 offset:9216\n"
MF "a[148:151],a[232:235],v[32:35],a[148:151]\n"
DT "a[230:231],v23 offset:13824\n"
BL "v14,s[24:27],s67 offen offset:2048 lds\n"
MF "a[184:187],a[232:235],v[48:51],a[184:187]\n"
"s_add_u32 m0,s76,87552\n"
"s_add_u32 s67,s70,0\n"
DT "a[232:233],v23 offset:9248\n"
MF "a[152:155],a[236:239],v[32:35],a[152:155]\n"
DT "a[234:235],v23 offset:13856\n"
BL "v15,s[24:27],s67 offen offset:0 lds\n"
MF "a[188:191],a[236:239],v[48:51],a[188:191]\n"
DT "a[236:237],v24 offset:9216\n"
MF "a[156:159],a[240:243],v[32:35],a[156:159]\n"
DT "a[238:239],v24 offset:13824\n"
BL "v16,s[24:27],s67 offen offset:1024 lds\n"
MF "a[192:195],a[240:243],v[48:51],a[192:195]\n"
DT "a[240:241],v24 offset:9248\n"
MF "a[160:163],a[244:247],v[32:35],a[160:163]\n"
DT "a[242:243],v24 offset:13856\n"
BL "v17,s[24:27],s67 offen offset:2048 lds\n"
MF "a[196:199],a[244:247],v[48:51],a[196:199]\n"
DT "a[244:245],v25 offset:9216\n"
DT "a[246:247],v25 offset:13824\n"
WT "lgkmcnt(15)\n"
MF "v[80:83],a[64:67],a[0:3],v[104:107]\n"
EX "v88,v88\n"
MF "v[84:87],a[68:71],a[0:3],v[104:107]\n"
EX "v89,v89\n"
MF "v[80:83],a[72:75],a[4:7],v[80:83]\n"
EX "v90,v90\n"
MF "v[84:87],a[76:79],a[4:7],v[84:87]\n"
EX "v91,v91\n"
MF "v[80:83],a[80:83],a[8:11],v[80:83]\n"
CV "v120,v88,v89\n"
CV "v121,v90,v91\n"
MF "v[84:87],a[84:87],a[8:11],v[84:87]\n"
EX "v92,v92\n"
MF "v[80:83],a[88:91],a[12:15],v[80:83]\n"
EX "v93,v93\n"
MF "v[84:87],a[92:95],a[12:15],v[84:87]\n"
EX "v94,v94\n"
WT "lgkmcnt(0)\n"
MF "v[136:139],a[96:99],v[116:119],v[136:139]\n"
EX "v95,v95\n"
MF "v[140:143],a[100:103],v[116:119],v[140:143]\n"
CV "v122,v92,v93\n"
CV "v123,v94,v95\n"
MF "v[144:147],a[104:107],v[116:119],v[144:147]\n"
"s_add_u32 m0,s50,s48\n"
"s_add_u32 m0,m0,8448\n"
MF "v[148:151],a[108:111],v[116:119],v[148:151]\n"
BL "v10,s[16:19],s79 offen nt lds\n"
MF "v[152:155],a[112:115],v[116:119],v[152:155]\n"
BL "v11,s[16:19],s79 offen offset:1056 nt lds\n"
"s_add_u32 s51,s51,8192\n"
"s_mov_b32 s79,s78\n"
"s_xor_b32 s78,s51,s41\n"
MF "v[156:159],a[116:119],v[116:119],v[156:159]\n"
"v_add_u32 v6,s49,v4\n"
"s_mov_b32 s50,s42\n"
"v_max3_f32 v128,v80,v81,v82\n"
"v_max3_f32 v128,v128,v83,v84\n"
MF "v[160:163],a[120:123],v[116:119],v[160:163]\n"
"v_add_u32 v7,s49,v5\n"
"s_mov_b32 s42,s49\n"
"v_max3_f32 v128,v128,v85,v86\n"
"v_max_f32 v128,v128,v87\n"
MF "v[164:167],a[124:127],v[116:119],v[164:167]\n"
"s_add_u32 s49,s49,16896\n"
"s_cmp_eq_u32 s49,84480\n"
"s_cselect_b32 s49,0,s49\n"
"v_cmp_lt_f32 vcc,s53,v128\n"
MF "v[168:171],a[48:51],v[116:119],v[168:171]\n"
"s_cbranch_vccnz Ls344_%=\n"
"Ls345_%=:\n"
WT "vmcnt(25)\n"
WT "lgkmcnt(0)\n"
"s_barrier\n"
MF "v[88:91],a[64:67],a[16:19],v[108:111]\n"
EX "v80,v80\n"
DR "a[64:67],v6 offset:0\n"
MF "v[92:95],a[68:71],a[16:19],v[108:111]\n"
EX "v81,v81\n"
DR "a[68:71],v6 offset:512\n"
MF "v[88:91],a[72:75],a[20:23],v[88:91]\n"
EX "v82,v82\n"
DR "a[72:75],v6 offset:64\n"
MF "v[92:95],a[76:79],a[20:23],v[92:95]\n"
EX "v83,v83\n"
DR "a[76:79],v6 offset:576\n"
MF "v[88:91],a[80:83],a[24:27],v[88:91]\n"
CV "v116,v80,v81\n"
CV "v117,v82,v83\n"
DR "a[80:83],v6 offset:128\n"
MF "v[92:95],a[84:87],a[24:27],v[92:95]\n"
EX "v84,v84\n"
DR "a[84:87],v6 offset:640\n"
MF "v[88:91],a[88:91],a[28:31],v[88:91]\n"
EX "v85,v85\n"
DR "a[88:91],v6 offset:192\n"
MF "v[92:95],a[92:95],a[28:31],v[92:95]\n"
EX "v86,v86\n"
DR "a[92:95],v6 offset:704\n"
MF "v[172:175],a[96:99],v[120:123],v[172:175]\n"
EX "v87,v87\n"
DT "a[96:97],v7 offset:0\n"
DT "a[98:99],v7 offset:512\n"
MF "v[176:179],a[100:103],v[120:123],v[176:179]\n"
CV "v118,v84,v85\n"
CV "v119,v86,v87\n"
DT "a[100:101],v7 offset:32\n"
DT "a[102:103],v7 offset:544\n"
MF "v[180:183],a[104:107],v[120:123],v[180:183]\n"
DT "a[104:105],v7 offset:64\n"
DT "a[106:107],v7 offset:576\n"
MF "v[184:187],a[108:111],v[120:123],v[184:187]\n"
DT "a[108:109],v7 offset:96\n"
DT "a[110:111],v7 offset:608\n"
MF "v[188:191],a[112:115],v[120:123],v[188:191]\n"
DT "a[112:113],v7 offset:128\n"
DT "a[114:115],v7 offset:640\n"
MF "v[192:195],a[116:119],v[120:123],v[192:195]\n"
DT "a[116:117],v7 offset:160\n"
DT "a[118:119],v7 offset:672\n"
"v_max3_f32 v128,v88,v89,v90\n"
"v_max3_f32 v128,v128,v91,v92\n"
MF "v[196:199],a[120:123],v[120:123],v[196:199]\n"
DT "a[120:121],v7 offset:192\n"
DT "a[122:123],v7 offset:704\n"
"v_max3_f32 v128,v128,v93,v94\n"
"v_max_f32 v128,v128,v95\n"
MF "v[200:203],a[124:127],v[120:123],v[200:203]\n"
DT "a[124:125],v7 offset:224\n"
DT "a[126:127],v7 offset:736\n"
"v_cmp_lt_f32 vcc,s53,v128\n"
MF "v[204:207],a[48:51],v[120:123],v[204:207]\n"
"s_cbranch_vccnz Ls346_%=\n"
"Ls347_%=:\n"
WT "vmcnt(22)\n"
MF "a[128:131],a[212:215],v[36:39],a[128:131]\n"
MF "a[164:167],a[212:215],v[52:55],a[164:167]\n"
"s_add_u32 s44,s58,0\n"
MF "a[200:203],a[212:215],v[68:71],a[200:203]\n"
DT "a[212:213],v21 offset:18432\n"
BL "v[32:35],v244,s[28:31],s44 offen offset:0 sc1\n"
MF "a[132:135],a[216:219],v[36:39],a[132:135]\n"
DT "a[214:215],v21 offset:23040\n"
MF "a[168:171],a[216:219],v[52:55],a[168:171]\n"
MF "a[204:207],a[216:219],v[68:71],a[204:207]\n"
DT "a[216:217],v21 offset:18464\n"
BL "v[48:51],v245,s[28:31],s44 offen offset:0 sc1\n"
MF "a[136:139],a[220:223],v[36:39],a[136:139]\n"
DT "a[218:219],v21 offset:23072\n"
MF "a[172:175],a[220:223],v[52:55],a[172:175]\n"
DT "a[220:221],v22 offset:18432\n"
MF "a[140:143],a[224:227],v[36:39],a[140:143]\n"
DT "a[222:223],v22 offset:23040\n"
BL "v[64:67],v246,s[28:31],s44 offen offset:0 sc1\n"
MF "a[176:179],a[224:227],v[52:55],a[176:179]\n"
"s_add_u32 m0,s76,90624\n"
DT "a[224:225],v22 offset:18464\n"
MF "a[144:147],a[228:231],v[36:39],a[144:147]\n"
"s_add_u32 s67,s70,0\n"
DT "a[226:227],v22 offset:23072\n"
MF "a[180:183],a[228:231],v[52:55],a[180:183]\n"
DT "a[228:229],v23 offset:18432\n"
BL "v18,s[24:27],s67 offen offset:0 lds\n"
MF "a[148:151],a[232:235],v[36:39],a[148:151]\n"
DT "a[230:231],v23 offset:23040\n"
MF "a[184:187],a[232:235],v[52:55],a[184:187]\n"
DT "a[232:233],v23 offset:18464\n"
MF "a[152:155],a[236:239],v[36:39],a[152:155]\n"
DT "a[234:235],v23 offset:23072\n"
BL "v19,s[24:27],s67 offen offset:1024 lds\n"
MF "a[188:191],a[236:239],v[52:55],a[188:191]\n"
DT "a[236:237],v24 offset:18432\n"
MF "a[156:159],a[240:243],v[36:39],a[156:159]\n"
DT "a[238:239],v24 offset:23040\n"
MF "a[192:195],a[240:243],v[52:55],a[192:195]\n"
DT "a[240:241],v24 offset:18464\n"
BL "v20,s[24:27],s67 offen offset:2048 lds\n"
MF "a[160:163],a[244:247],v[36:39],a[160:163]\n"
DT "a[242:243],v24 offset:23072\n"
MF "a[196:199],a[244:247],v[52:55],a[196:199]\n"
DT "a[244:245],v25 offset:18432\n"
DT "a[246:247],v25 offset:23040\n"
WT "lgkmcnt(15)\n"
MF "v[80:83],a[64:67],a[0:3],v[104:107]\n"
EX "v88,v88\n"
MF "v[84:87],a[68:71],a[0:3],v[104:107]\n"
EX "v89,v89\n"
MF "v[80:83],a[72:75],a[4:7],v[80:83]\n"
EX "v90,v90\n"
MF "v[84:87],a[76:79],a[4:7],v[84:87]\n"
EX "v91,v91\n"
MF "v[80:83],a[80:83],a[8:11],v[80:83]\n"
CV "v120,v88,v89\n"
CV "v121,v90,v91\n"
MF "v[84:87],a[84:87],a[8:11],v[84:87]\n"
EX "v92,v92\n"
MF "v[80:83],a[88:91],a[12:15],v[80:83]\n"
EX "v93,v93\n"
MF "v[84:87],a[92:95],a[12:15],v[84:87]\n"
EX "v94,v94\n"
WT "lgkmcnt(0)\n"
MF "v[136:139],a[96:99],v[116:119],v[136:139]\n"
EX "v95,v95\n"
MF "v[140:143],a[100:103],v[116:119],v[140:143]\n"
CV "v122,v92,v93\n"
CV "v123,v94,v95\n"
MF "v[144:147],a[104:107],v[116:119],v[144:147]\n"
"v_add_u32 v6,s49,v4\n"
"s_mov_b32 s50,s42\n"
MF "v[148:151],a[108:111],v[116:119],v[148:151]\n"
MF "v[152:155],a[112:115],v[116:119],v[152:155]\n"
"v_add_u32 v7,s49,v5\n"
"s_mov_b32 s42,s49\n"
MF "v[156:159],a[116:119],v[116:119],v[156:159]\n"
"v_max3_f32 v128,v80,v81,v82\n"
"v_max3_f32 v128,v128,v83,v84\n"
MF "v[160:163],a[120:123],v[116:119],v[160:163]\n"
"s_add_u32 s49,s49,16896\n"
"s_cmp_eq_u32 s49,84480\n"
"s_cselect_b32 s49,0,s49\n"
"v_max3_f32 v128,v128,v85,v86\n"
"v_max_f32 v128,v128,v87\n"
MF "v[164:167],a[124:127],v[116:119],v[164:167]\n"
"v_cmp_lt_f32 vcc,s53,v128\n"
MF "v[168:171],a[48:51],v[116:119],v[168:171]\n"
"s_cbranch_vccnz Ls348_%=\n"
"Ls349_%=:\n"
WT "vmcnt(24)\n"
"s_barrier\n"
MF "v[88:91],a[64:67],a[16:19],v[108:111]\n"
EX "v80,v80\n"
DR "a[64:67],v6 offset:0\n"
MF "v[92:95],a[68:71],a[16:19],v[108:111]\n"
EX "v81,v81\n"
DR "a[68:71],v6 offset:512\n"
MF "v[88:91],a[72:75],a[20:23],v[88:91]\n"
EX "v82,v82\n"
DR "a[72:75],v6 offset:64\n"
MF "v[92:95],a[76:79],a[20:23],v[92:95]\n"
EX "v83,v83\n"
DR "a[76:79],v6 offset:576\n"
MF "v[88:91],a[80:83],a[24:27],v[88:91]\n"
CV "v116,v80,v81\n"
CV "v117,v82,v83\n"
DR "a[80:83],v6 offset:128\n"
MF "v[92:95],a[84:87],a[24:27],v[92:95]\n"
EX "v84,v84\n"
DR "a[84:87],v6 offset:640\n"
MF "v[88:91],a[88:91],a[28:31],v[88:91]\n"
EX "v85,v85\n"
DR "a[88:91],v6 offset:192\n"
MF "v[92:95],a[92:95],a[28:31],v[92:95]\n"
EX "v86,v86\n"
DR "a[92:95],v6 offset:704\n"
MF "v[172:175],a[96:99],v[120:123],v[172:175]\n"
EX "v87,v87\n"
DT "a[96:97],v7 offset:0\n"
DT "a[98:99],v7 offset:512\n"
MF "v[176:179],a[100:103],v[120:123],v[176:179]\n"
CV "v118,v84,v85\n"
CV "v119,v86,v87\n"
DT "a[100:101],v7 offset:32\n"
DT "a[102:103],v7 offset:544\n"
MF "v[180:183],a[104:107],v[120:123],v[180:183]\n"
DT "a[104:105],v7 offset:64\n"
DT "a[106:107],v7 offset:576\n"
MF "v[184:187],a[108:111],v[120:123],v[184:187]\n"
DT "a[108:109],v7 offset:96\n"
DT "a[110:111],v7 offset:608\n"
MF "v[188:191],a[112:115],v[120:123],v[188:191]\n"
DT "a[112:113],v7 offset:128\n"
DT "a[114:115],v7 offset:640\n"
MF "v[192:195],a[116:119],v[120:123],v[192:195]\n"
DT "a[116:117],v7 offset:160\n"
DT "a[118:119],v7 offset:672\n"
"v_max3_f32 v128,v88,v89,v90\n"
"v_max3_f32 v128,v128,v91,v92\n"
MF "v[196:199],a[120:123],v[120:123],v[196:199]\n"
DT "a[120:121],v7 offset:192\n"
DT "a[122:123],v7 offset:704\n"
"v_max3_f32 v128,v128,v93,v94\n"
"v_max_f32 v128,v128,v95\n"
MF "v[200:203],a[124:127],v[120:123],v[200:203]\n"
DT "a[124:125],v7 offset:224\n"
DT "a[126:127],v7 offset:736\n"
"v_cmp_lt_f32 vcc,s53,v128\n"
MF "v[204:207],a[48:51],v[120:123],v[204:207]\n"
"s_cbranch_vccnz Ls350_%=\n"
"Ls351_%=:\n"
WT "vmcnt(21)\n"
MF "a[128:131],a[212:215],v[40:43],a[128:131]\n"
MF "a[164:167],a[212:215],v[56:59],a[164:167]\n"
"s_add_u32 s44,s58,0\n"
MF "a[200:203],a[212:215],v[72:75],a[200:203]\n"
DT "a[212:213],v21 offset:27648\n"
BL "v[36:39],v244,s[28:31],s44 offen offset:1024 sc1\n"
MF "a[132:135],a[216:219],v[40:43],a[132:135]\n"
DT "a[214:215],v21 offset:32256\n"
MF "a[168:171],a[216:219],v[56:59],a[168:171]\n"
MF "a[204:207],a[216:219],v[72:75],a[204:207]\n"
DT "a[216:217],v21 offset:27680\n"
MF "a[136:139],a[220:223],v[40:43],a[136:139]\n"
DT "a[218:219],v21 offset:32288\n"
"v_subrev_u32 v21,36864,v21\n"
MF "a[172:175],a[220:223],v[56:59],a[172:175]\n"
DT "a[220:221],v22 offset:27648\n"
MF "a[140:143],a[224:227],v[40:43],a[140:143]\n"
DT "a[222:223],v22 offset:32256\n"
BL "v[52:55],v245,s[28:31],s44 offen offset:1024 sc1\n"
MF "a[176:179],a[224:227],v[56:59],a[176:179]\n"
DT "a[224:225],v22 offset:27680\n"
MF "a[144:147],a[228:231],v[40:43],a[144:147]\n"
DT "a[226:227],v22 offset:32288\n"
"v_subrev_u32 v22,36864,v22\n"
MF "a[180:183],a[228:231],v[56:59],a[180:183]\n"
DT "a[228:229],v23 offset:27648\n"
MF "a[148:151],a[232:235],v[40:43],a[148:151]\n"
DT "a[230:231],v23 offset:32256\n"
MF "a[184:187],a[232:235],v[56:59],a[184:187]\n"
DT "a[232:233],v23 offset:27680\n"
MF "a[152:155],a[236:239],v[40:43],a[152:155]\n"
DT "a[234:235],v23 offset:32288\n"
BL "v[68:71],v246,s[28:31],s44 offen offset:1024 sc1\n"
"v_subrev_u32 v23,36864,v23\n"
MF "a[188:191],a[236:239],v[56:59],a[188:191]\n"
DT "a[236:237],v24 offset:27648\n"
MF "a[156:159],a[240:243],v[40:43],a[156:159]\n"
DT "a[238:239],v24 offset:32256\n"
MF "a[192:195],a[240:243],v[56:59],a[192:195]\n"
DT "a[240:241],v24 offset:27680\n"
MF "a[160:163],a[244:247],v[40:43],a[160:163]\n"
DT "a[242:243],v24 offset:32288\n"
"v_subrev_u32 v24,36864,v24\n"
MF "a[196:199],a[244:247],v[56:59],a[196:199]\n"
DT "a[244:245],v25 offset:27648\n"
DT "a[246:247],v25 offset:32256\n"
"v_subrev_u32 v25,36864,v25\n"
WT "lgkmcnt(15)\n"
MF "v[80:83],a[64:67],a[0:3],v[104:107]\n"
EX "v88,v88\n"
MF "v[84:87],a[68:71],a[0:3],v[104:107]\n"
EX "v89,v89\n"
MF "v[80:83],a[72:75],a[4:7],v[80:83]\n"
EX "v90,v90\n"
MF "v[84:87],a[76:79],a[4:7],v[84:87]\n"
EX "v91,v91\n"
MF "v[80:83],a[80:83],a[8:11],v[80:83]\n"
CV "v120,v88,v89\n"
CV "v121,v90,v91\n"
MF "v[84:87],a[84:87],a[8:11],v[84:87]\n"
EX "v92,v92\n"
MF "v[80:83],a[88:91],a[12:15],v[80:83]\n"
EX "v93,v93\n"
MF "v[84:87],a[92:95],a[12:15],v[84:87]\n"
EX "v94,v94\n"
WT "lgkmcnt(0)\n"
MF "v[136:139],a[96:99],v[116:119],v[136:139]\n"
EX "v95,v95\n"
MF "v[140:143],a[100:103],v[116:119],v[140:143]\n"
CV "v122,v92,v93\n"
CV "v123,v94,v95\n"
MF "v[144:147],a[104:107],v[116:119],v[144:147]\n"
"v_add_u32 v6,s49,v4\n"
"s_mov_b32 s50,s42\n"
MF "v[148:151],a[108:111],v[116:119],v[148:151]\n"
MF "v[152:155],a[112:115],v[116:119],v[152:155]\n"
"v_add_u32 v7,s49,v5\n"
"s_mov_b32 s42,s49\n"
MF "v[156:159],a[116:119],v[116:119],v[156:159]\n"
"v_max3_f32 v128,v80,v81,v82\n"
"v_max3_f32 v128,v128,v83,v84\n"
MF "v[160:163],a[120:123],v[116:119],v[160:163]\n"
"s_add_u32 s49,s49,16896\n"
"s_cmp_eq_u32 s49,84480\n"
"s_cselect_b32 s49,0,s49\n"
"v_max3_f32 v128,v128,v85,v86\n"
"v_max_f32 v128,v128,v87\n"
MF "v[164:167],a[124:127],v[116:119],v[164:167]\n"
"v_cmp_lt_f32 vcc,s53,v128\n"
MF "v[168:171],a[48:51],v[116:119],v[168:171]\n"
"s_cbranch_vccnz Ls352_%=\n"
"Ls353_%=:\n"
WT "vmcnt(20)\n"
WT "vmcnt(3)\n"
"s_barrier\n"
MF "v[88:91],a[64:67],a[16:19],v[108:111]\n"
EX "v80,v80\n"
DR "a[64:67],v6 offset:0\n"
MF "v[92:95],a[68:71],a[16:19],v[108:111]\n"
EX "v81,v81\n"
DR "a[68:71],v6 offset:512\n"
MF "v[88:91],a[72:75],a[20:23],v[88:91]\n"
EX "v82,v82\n"
DR "a[72:75],v6 offset:64\n"
MF "v[92:95],a[76:79],a[20:23],v[92:95]\n"
EX "v83,v83\n"
DR "a[76:79],v6 offset:576\n"
MF "v[88:91],a[80:83],a[24:27],v[88:91]\n"
CV "v116,v80,v81\n"
CV "v117,v82,v83\n"
DR "a[80:83],v6 offset:128\n"
MF "v[92:95],a[84:87],a[24:27],v[92:95]\n"
EX "v84,v84\n"
DR "a[84:87],v6 offset:640\n"
MF "v[88:91],a[88:91],a[28:31],v[88:91]\n"
EX "v85,v85\n"
DR "a[88:91],v6 offset:192\n"
MF "v[92:95],a[92:95],a[28:31],v[92:95]\n"
EX "v86,v86\n"
DR "a[92:95],v6 offset:704\n"
MF "v[172:175],a[96:99],v[120:123],v[172:175]\n"
EX "v87,v87\n"
DT "a[96:97],v7 offset:0\n"
DT "a[98:99],v7 offset:512\n"
MF "v[176:179],a[100:103],v[120:123],v[176:179]\n"
CV "v118,v84,v85\n"
CV "v119,v86,v87\n"
DT "a[100:101],v7 offset:32\n"
DT "a[102:103],v7 offset:544\n"
MF "v[180:183],a[104:107],v[120:123],v[180:183]\n"
DT "a[104:105],v7 offset:64\n"
DT "a[106:107],v7 offset:576\n"
MF "v[184:187],a[108:111],v[120:123],v[184:187]\n"
DT "a[108:109],v7 offset:96\n"
DT "a[110:111],v7 offset:608\n"
MF "v[188:191],a[112:115],v[120:123],v[188:191]\n"
DT "a[112:113],v7 offset:128\n"
DT "a[114:115],v7 offset:640\n"
MF "v[192:195],a[116:119],v[120:123],v[192:195]\n"
DT "a[116:117],v7 offset:160\n"
DT "a[118:119],v7 offset:672\n"
"v_max3_f32 v128,v88,v89,v90\n"
"v_max3_f32 v128,v128,v91,v92\n"
MF "v[196:199],a[120:123],v[120:123],v[196:199]\n"
DT "a[120:121],v7 offset:192\n"
DT "a[122:123],v7 offset:704\n"
"v_max3_f32 v128,v128,v93,v94\n"
"v_max_f32 v128,v128,v95\n"
MF "v[200:203],a[124:127],v[120:123],v[200:203]\n"
DT "a[124:125],v7 offset:224\n"
DT "a[126:127],v7 offset:736\n"
"v_cmp_lt_f32 vcc,s53,v128\n"
MF "v[204:207],a[48:51],v[120:123],v[204:207]\n"
"s_cbranch_vccnz Ls354_%=\n"
"Ls355_%=:\n"
WT "vmcnt(17)\n"
MF "a[128:131],a[212:215],v[44:47],a[128:131]\n"
MF "a[164:167],a[212:215],v[60:63],a[164:167]\n"
"s_add_u32 s44,s58,0\n"
MF "a[200:203],a[212:215],v[76:79],a[200:203]\n"
DT "a[212:213],v21 offset:0\n"
BL "v[40:43],v244,s[28:31],s44 offen offset:2048 sc1\n"
MF "a[132:135],a[216:219],v[44:47],a[132:135]\n"
DT "a[214:215],v21 offset:4608\n"
MF "a[168:171],a[216:219],v[60:63],a[168:171]\n"
MF "a[204:207],a[216:219],v[76:79],a[204:207]\n"
DT "a[216:217],v21 offset:32\n"
MF "a[136:139],a[220:223],v[44:47],a[136:139]\n"
DT "a[218:219],v21 offset:4640\n"
MF "a[172:175],a[220:223],v[60:63],a[172:175]\n"
DT "a[220:221],v22 offset:0\n"
MF "a[140:143],a[224:227],v[44:47],a[140:143]\n"
DT "a[222:223],v22 offset:4608\n"
BL "v[56:59],v245,s[28:31],s44 offen offset:2048 sc1\n"
MF "a[176:179],a[224:227],v[60:63],a[176:179]\n"
DT "a[224:225],v22 offset:32\n"
MF "a[144:147],a[228:231],v[44:47],a[144:147]\n"
DT "a[226:227],v22 offset:4640\n"
MF "a[180:183],a[228:231],v[60:63],a[180:183]\n"
DT "a[228:229],v23 offset:0\n"
MF "a[148:151],a[232:235],v[44:47],a[148:151]\n"
DT "a[230:231],v23 offset:4608\n"
MF "a[184:187],a[232:235],v[60:63],a[184:187]\n"
DT "a[232:233],v23 offset:32\n"
MF "a[152:155],a[236:239],v[44:47],a[152:155]\n"
DT "a[234:235],v23 offset:4640\n"
BL "v[72:75],v246,s[28:31],s44 offen offset:2048 sc1\n"
MF "a[188:191],a[236:239],v[60:63],a[188:191]\n"
DT "a[236:237],v24 offset:0\n"
MF "a[156:159],a[240:243],v[44:47],a[156:159]\n"
DT "a[238:239],v24 offset:4608\n"
MF "a[192:195],a[240:243],v[60:63],a[192:195]\n"
DT "a[240:241],v24 offset:32\n"
MF "a[160:163],a[244:247],v[44:47],a[160:163]\n"
DT "a[242:243],v24 offset:4640\n"
MF "a[196:199],a[244:247],v[60:63],a[196:199]\n"
DT "a[244:245],v25 offset:0\n"
DT "a[246:247],v25 offset:4608\n"
WT "lgkmcnt(15)\n"
MF "v[80:83],a[64:67],a[0:3],v[104:107]\n"
EX "v88,v88\n"
MF "v[84:87],a[68:71],a[0:3],v[104:107]\n"
EX "v89,v89\n"
"s_add_u32 s44,s58,0\n"
BL "v[44:47],v244,s[28:31],s44 offen offset:3072 sc1\n"
MF "v[80:83],a[72:75],a[4:7],v[80:83]\n"
EX "v90,v90\n"
BL "v[60:63],v245,s[28:31],s44 offen offset:3072 sc1\n"
MF "v[84:87],a[76:79],a[4:7],v[84:87]\n"
EX "v91,v91\n"
BL "v[76:79],v246,s[28:31],s44 offen offset:3072 sc1\n"
MF "v[80:83],a[80:83],a[8:11],v[80:83]\n"
CV "v120,v88,v89\n"
CV "v121,v90,v91\n"
"s_add_u32 m0,s76,121344\n"
"s_add_u32 s67,s68,147456\n"
BL "v12,s[24:27],s67 offen offset:0 lds\n"
MF "v[84:87],a[84:87],a[8:11],v[84:87]\n"
EX "v92,v92\n"
BL "v13,s[24:27],s67 offen offset:1024 lds\n"
MF "v[80:83],a[88:91],a[12:15],v[80:83]\n"
EX "v93,v93\n"
BL "v14,s[24:27],s67 offen offset:2048 lds\n"
MF "v[84:87],a[92:95],a[12:15],v[84:87]\n"
EX "v94,v94\n"
WT "lgkmcnt(0)\n"
"s_add_u32 m0,s76,124416\n"
"s_add_u32 s67,s68,147456\n"
BL "v15,s[24:27],s67 offen offset:0 lds\n"
MF "v[136:139],a[96:99],v[116:119],v[136:139]\n"
EX "v95,v95\n"
BL "v16,s[24:27],s67 offen offset:1024 lds\n"
MF "v[140:143],a[100:103],v[116:119],v[140:143]\n"
CV "v122,v92,v93\n"
CV "v123,v94,v95\n"
BL "v17,s[24:27],s67 offen offset:2048 lds\n"
MF "v[144:147],a[104:107],v[116:119],v[144:147]\n"
"s_add_u32 m0,s76,127488\n"
"s_add_u32 s67,s68,147456\n"
BL "v18,s[24:27],s67 offen offset:0 lds\n"
MF "v[148:151],a[108:111],v[116:119],v[148:151]\n"
BL "v19,s[24:27],s67 offen offset:1024 lds\n"
MF "v[152:155],a[112:115],v[116:119],v[152:155]\n"
BL "v20,s[24:27],s67 offen offset:2048 lds\n"
MF "v[156:159],a[116:119],v[116:119],v[156:159]\n"
"v_add_u32 v6,s49,v4\n"
"s_mov_b32 s50,s42\n"
"v_max3_f32 v128,v80,v81,v82\n"
"v_max3_f32 v128,v128,v83,v84\n"
MF "v[160:163],a[120:123],v[116:119],v[160:163]\n"
"v_add_u32 v7,s49,v5\n"
"s_mov_b32 s42,s49\n"
"v_max3_f32 v128,v128,v85,v86\n"
"v_max_f32 v128,v128,v87\n"
MF "v[164:167],a[124:127],v[116:119],v[164:167]\n"
"s_add_u32 s49,s49,16896\n"
"s_cmp_eq_u32 s49,84480\n"
"s_cselect_b32 s49,0,s49\n"
"v_cmp_lt_f32 vcc,s53,v128\n"
MF "v[168:171],a[48:51],v[116:119],v[168:171]\n"
"s_cbranch_vccnz Ls356_%=\n"
"Ls357_%=:\n"
WT "vmcnt(24)\n"
"s_barrier\n"
MF "v[88:91],a[64:67],a[16:19],v[108:111]\n"
EX "v80,v80\n"
MF "v[92:95],a[68:71],a[16:19],v[108:111]\n"
EX "v81,v81\n"
MF "v[88:91],a[72:75],a[20:23],v[88:91]\n"
EX "v82,v82\n"
MF "v[92:95],a[76:79],a[20:23],v[92:95]\n"
EX "v83,v83\n"
MF "v[88:91],a[80:83],a[24:27],v[88:91]\n"
CV "v116,v80,v81\n"
CV "v117,v82,v83\n"
MF "v[92:95],a[84:87],a[24:27],v[92:95]\n"
EX "v84,v84\n"
MF "v[88:91],a[88:91],a[28:31],v[88:91]\n"
EX "v85,v85\n"
MF "v[92:95],a[92:95],a[28:31],v[92:95]\n"
EX "v86,v86\n"
MF "v[172:175],a[96:99],v[120:123],v[172:175]\n"
EX "v87,v87\n"
DT "a[96:97],v7 offset:0\n"
DT "a[98:99],v7 offset:512\n"
MF "v[176:179],a[100:103],v[120:123],v[176:179]\n"
CV "v118,v84,v85\n"
CV "v119,v86,v87\n"
DT "a[100:101],v7 offset:32\n"
DT "a[102:103],v7 offset:544\n"
MF "v[180:183],a[104:107],v[120:123],v[180:183]\n"
DT "a[104:105],v7 offset:64\n"
DT "a[106:107],v7 offset:576\n"
MF "v[184:187],a[108:111],v[120:123],v[184:187]\n"
DT "a[108:109],v7 offset:96\n"
DT "a[110:111],v7 offset:608\n"
MF "v[188:191],a[112:115],v[120:123],v[188:191]\n"
DT "a[112:113],v7 offset:128\n"
DT "a[114:115],v7 offset:640\n"
MF "v[192:195],a[116:119],v[120:123],v[192:195]\n"
DT "a[116:117],v7 offset:160\n"
DT "a[118:119],v7 offset:672\n"
"v_max3_f32 v128,v88,v89,v90\n"
"v_max3_f32 v128,v128,v91,v92\n"
MF "v[196:199],a[120:123],v[120:123],v[196:199]\n"
DT "a[120:121],v7 offset:192\n"
DT "a[122:123],v7 offset:704\n"
"v_max3_f32 v128,v128,v93,v94\n"
"v_max_f32 v128,v128,v95\n"
MF "v[200:203],a[124:127],v[120:123],v[200:203]\n"
DT "a[124:125],v7 offset:224\n"
DT "a[126:127],v7 offset:736\n"
"v_cmp_lt_f32 vcc,s53,v128\n"
MF "v[204:207],a[48:51],v[120:123],v[204:207]\n"
"s_cbranch_vccnz Ls358_%=\n"
"Ls359_%=:\n"
WT "lgkmcnt(0)\n"
MF "v[136:139],a[96:99],v[116:119],v[136:139]\n"
EX "v88,v88\n"
MF "v[140:143],a[100:103],v[116:119],v[140:143]\n"
EX "v89,v89\n"
MF "v[144:147],a[104:107],v[116:119],v[144:147]\n"
EX "v90,v90\n"
MF "v[148:151],a[108:111],v[116:119],v[148:151]\n"
EX "v91,v91\n"
MF "v[152:155],a[112:115],v[116:119],v[152:155]\n"
CV "v120,v88,v89\n"
CV "v121,v90,v91\n"
MF "v[156:159],a[116:119],v[116:119],v[156:159]\n"
EX "v92,v92\n"
MF "v[160:163],a[120:123],v[116:119],v[160:163]\n"
EX "v93,v93\n"
MF "v[164:167],a[124:127],v[116:119],v[164:167]\n"
EX "v94,v94\n"
MF "v[168:171],a[48:51],v[116:119],v[168:171]\n"
EX "v95,v95\n"
CV "v122,v92,v93\n"
CV "v123,v94,v95\n"
"s_nop 1\n"
WT "vmcnt(24)\n"
"s_barrier\n"
MF "v[172:175],a[96:99],v[120:123],v[172:175]\n"
MF "v[176:179],a[100:103],v[120:123],v[176:179]\n"
MF "v[180:183],a[104:107],v[120:123],v[180:183]\n"
MF "v[184:187],a[108:111],v[120:123],v[184:187]\n"
MF "v[188:191],a[112:115],v[120:123],v[188:191]\n"
MF "v[192:195],a[116:119],v[120:123],v[192:195]\n"
MF "v[196:199],a[120:123],v[120:123],v[196:199]\n"
MF "v[200:203],a[124:127],v[120:123],v[200:203]\n"
MF "v[204:207],a[48:51],v[120:123],v[204:207]\n"
"s_nop 1\n"
"v_rcp_f32 v130,v168\n"
"s_add_u32 s66,s56,36864\n"
PM "v[136:137],v[136:137],v[130:131] op_sel_hi:[1,0]\n"
PM "v[138:139],v[138:139],v[130:131] op_sel_hi:[1,0]\n"
PM "v[140:141],v[140:141],v[130:131] op_sel_hi:[1,0]\n"
PM "v[142:143],v[142:143],v[130:131] op_sel_hi:[1,0]\n"
CV "v80,v136,v137\n"
CV "v81,v138,v139\n"
CV "v82,v140,v141\n"
CV "v83,v142,v143\n"
"buffer_store_dwordx4 v[80:83],v244,s[28:31],s66 offen offset:0 sc1\n"
PM "v[144:145],v[144:145],v[130:131] op_sel_hi:[1,0]\n"
PM "v[146:147],v[146:147],v[130:131] op_sel_hi:[1,0]\n"
PM "v[148:149],v[148:149],v[130:131] op_sel_hi:[1,0]\n"
PM "v[150:151],v[150:151],v[130:131] op_sel_hi:[1,0]\n"
CV "v84,v144,v145\n"
CV "v85,v146,v147\n"
CV "v86,v148,v149\n"
CV "v87,v150,v151\n"
"buffer_store_dwordx4 v[84:87],v244,s[28:31],s66 offen offset:1024 sc1\n"
PM "v[152:153],v[152:153],v[130:131] op_sel_hi:[1,0]\n"
PM "v[154:155],v[154:155],v[130:131] op_sel_hi:[1,0]\n"
PM "v[156:157],v[156:157],v[130:131] op_sel_hi:[1,0]\n"
PM "v[158:159],v[158:159],v[130:131] op_sel_hi:[1,0]\n"
CV "v88,v152,v153\n"
CV "v89,v154,v155\n"
CV "v90,v156,v157\n"
CV "v91,v158,v159\n"
"buffer_store_dwordx4 v[88:91],v244,s[28:31],s66 offen offset:2048 sc1\n"
PM "v[160:161],v[160:161],v[130:131] op_sel_hi:[1,0]\n"
PM "v[162:163],v[162:163],v[130:131] op_sel_hi:[1,0]\n"
PM "v[164:165],v[164:165],v[130:131] op_sel_hi:[1,0]\n"
PM "v[166:167],v[166:167],v[130:131] op_sel_hi:[1,0]\n"
CV "v92,v160,v161\n"
CV "v93,v162,v163\n"
CV "v94,v164,v165\n"
CV "v95,v166,v167\n"
"buffer_store_dwordx4 v[92:95],v244,s[28:31],s66 offen offset:3072 sc1\n"
"v_rcp_f32 v130,v204\n"
"s_add_u32 s66,s56,36864\n"
PM "v[172:173],v[172:173],v[130:131] op_sel_hi:[1,0]\n"
PM "v[174:175],v[174:175],v[130:131] op_sel_hi:[1,0]\n"
PM "v[176:177],v[176:177],v[130:131] op_sel_hi:[1,0]\n"
PM "v[178:179],v[178:179],v[130:131] op_sel_hi:[1,0]\n"
CV "v96,v172,v173\n"
CV "v97,v174,v175\n"
CV "v98,v176,v177\n"
CV "v99,v178,v179\n"
"buffer_store_dwordx4 v[96:99],v245,s[28:31],s66 offen offset:0 sc1\n"
PM "v[180:181],v[180:181],v[130:131] op_sel_hi:[1,0]\n"
PM "v[182:183],v[182:183],v[130:131] op_sel_hi:[1,0]\n"
PM "v[184:185],v[184:185],v[130:131] op_sel_hi:[1,0]\n"
PM "v[186:187],v[186:187],v[130:131] op_sel_hi:[1,0]\n"
CV "v100,v180,v181\n"
CV "v101,v182,v183\n"
CV "v102,v184,v185\n"
CV "v103,v186,v187\n"
"buffer_store_dwordx4 v[100:103],v245,s[28:31],s66 offen offset:1024 sc1\n"
PM "v[188:189],v[188:189],v[130:131] op_sel_hi:[1,0]\n"
PM "v[190:191],v[190:191],v[130:131] op_sel_hi:[1,0]\n"
PM "v[192:193],v[192:193],v[130:131] op_sel_hi:[1,0]\n"
PM "v[194:195],v[194:195],v[130:131] op_sel_hi:[1,0]\n"
CV "v104,v188,v189\n"
CV "v105,v190,v191\n"
CV "v106,v192,v193\n"
CV "v107,v194,v195\n"
"buffer_store_dwordx4 v[104:107],v245,s[28:31],s66 offen offset:2048 sc1\n"
PM "v[196:197],v[196:197],v[130:131] op_sel_hi:[1,0]\n"
PM "v[198:199],v[198:199],v[130:131] op_sel_hi:[1,0]\n"
PM "v[200:201],v[200:201],v[130:131] op_sel_hi:[1,0]\n"
PM "v[202:203],v[202:203],v[130:131] op_sel_hi:[1,0]\n"
CV "v108,v196,v197\n"
CV "v109,v198,v199\n"
CV "v110,v200,v201\n"
CV "v111,v202,v203\n"
"buffer_store_dwordx4 v[108:111],v245,s[28:31],s66 offen offset:3072 sc1\n"
"v_mov_b32 v128,0\n"
"ds_write_b32 v251,v128 offset:16\n"
"s_branch LO15_%=\n"
"LE8_%=:\n"
WT "vmcnt(0)\n"
        : "+v"(tid)
        : "s"(blockIdx.x), "s"((uint32_t)ka), "s"((uint32_t)(ka >> 32)), "s"((uint32_t)(uintptr_t)smem)
        : "v1","v2","v3","v4","v5","v6","v7","v8","v9","v10","v11","v12","v13","v14","v15","v16","v17","v18","v19","v20","v21","v22","v23","v24","v25","v26","v27","v28","v29","v30","v31","v32","v33","v34","v35","v36","v37","v38","v39","v40","v41","v42","v43","v44","v45","v46","v47","v48","v49","v50","v51","v52","v53","v54","v55","v56","v57","v58","v59","v60","v61","v62","v63","v64","v65","v66","v67","v68","v69","v70","v71","v72","v73","v74","v75","v76","v77","v78","v79","v80","v81","v82","v83","v84","v85","v86","v87","v88","v89","v90","v91","v92","v93","v94","v95","v96","v97","v98","v99","v100","v101","v102","v103","v104","v105","v106","v107","v108","v109","v110","v111","v112","v113","v114","v115","v116","v117","v118","v119","v120","v121","v122","v123","v124","v125","v126","v127","v128","v129","v130","v131","v132","v133","v134","v135","v136","v137","v138","v139","v140","v141","v142","v143","v144","v145","v146","v147","v148","v149","v150","v151","v152","v153","v154","v155","v156","v157","v158","v159","v160","v161","v162","v163","v164","v165","v166","v167","v168","v169","v170","v171","v172","v173","v174","v175","v176","v177","v178","v179","v180","v181","v182","v183","v184","v185","v186","v187","v188","v189","v190","v191","v192","v193","v194","v195","v196","v197","v198","v199","v200","v201","v202","v203","v204","v205","v206","v207","v208","v209","v210","v211","v212","v213","v214","v215","v216","v217","v218","v219","v220","v221","v222","v223","v224","v225","v226","v227","v228","v229","v230","v231","v232","v233","v234","v235","v236","v237","v238","v239","v240","v241","v242","v243","v244","v245","v246","v247","v248","v249","v250","v251","v252","v253","v254","v255","a0","a1","a2","a3","a4","a5","a6","a7","a8","a9","a10","a11","a12","a13","a14","a15","a16","a17","a18","a19","a20","a21","a22","a23","a24","a25","a26","a27","a28","a29","a30","a31","a32","a33","a34","a35","a36","a37","a38","a39","a40","a41","a42","a43","a44","a45","a46","a47","a48","a49","a50","a51","a52","a53","a54","a55","a56","a57","a58","a59","a60","a61","a62","a63","a64","a65","a66","a67","a68","a69","a70","a71","a72","a73","a74","a75","a76","a77","a78","a79","a80","a81","a82","a83","a84","a85","a86","a87","a88","a89","a90","a91","a92","a93","a94","a95","a96","a97","a98","a99","a100","a101","a102","a103","a104","a105","a106","a107","a108","a109","a110","a111","a112","a113","a114","a115","a116","a117","a118","a119","a120","a121","a122","a123","a124","a125","a126","a127","a128","a129","a130","a131","a132","a133","a134","a135","a136","a137","a138","a139","a140","a141","a142","a143","a144","a145","a146","a147","a148","a149","a150","a151","a152","a153","a154","a155","a156","a157","a158","a159","a160","a161","a162","a163","a164","a165","a166","a167","a168","a169","a170","a171","a172","a173","a174","a175","a176","a177","a178","a179","a180","a181","a182","a183","a184","a185","a186","a187","a188","a189","a190","a191","a192","a193","a194","a195","a196","a197","a198","a199","a200","a201","a202","a203","a204","a205","a206","a207","a208","a209","a210","a211","a212","a213","a214","a215","a216","a217","a218","a219","a220","a221","a222","a223","a224","a225","a226","a227","a228","a229","a230","a231","a232","a233","a234","a235","a236","a237","a238","a239","a240","a241","a242","a243","a244","a245","a246","a247","a248","a249","a250","a251","a252","a253","a254","a255","s8","s9","s10","s11","s12","s13","s14","s15","s16","s17","s18","s19","s20","s21","s22","s23","s24","s25","s26","s27","s28","s29","s30","s31","s33","s34","s35","s36","s37","s38","s39","s40","s41","s42","s43","s44","s45","s46","s47","s48","s49","s50","s51","s52","s53","s54","s55","s56","s57","s58","s59","s60","s61","s62","s63","s64","s65","s66","s67","s68","s69","s70","s71","s72","s73","s74","s75","s76","s77","s78","s79","s80","s81","s82","s83","s84","s85","s86","s87","s88","s89","s90","s91","s92","s93","s94","s95","s96","s97","s98","s99", "vcc", "scc", "memory");
}

extern "C" void kernel_launch(void* const* d_in, const int* in_sizes, int n_in, void* d_out, int out_size,
                              void* d_ws, size_t ws_size, hipStream_t stream) {
    KArgs a;
    for (int i = 0; i < 10; ++i) a.in[i] = d_in[i];
    a.out = d_out;
    a.ws = d_ws;
    fused_block_kernel<<<256, 256, 0, stream>>>(a);
}
```
